```python
import torch

def _softmax(x, axis=-1):
    return torch.softmax(x, dim=axis)

def _gelu(x):
    return torch.nn.functional.gelu(x, approximate='tanh')

def _silu(x):
    return torch.nn.functional.silu(x)

def _sigmoid(x):
    return torch.sigmoid(x)

def _layernorm(x, axis=-1, eps=1e-05):
    m = x.mean(dim=axis, keepdim=True)
    v = x.var(dim=axis, keepdim=True, unbiased=False)
    return (x - m) / torch.sqrt(v + eps)

def _scatter_add(tgt, idx, src, axis=0):
    return tgt.clone().index_add_(axis, idx, src)

def _segment_sum(src, seg, n):
    out = torch.zeros((n,) + src.shape[1:], dtype=src.dtype)
    return out.index_add_(0, seg, src)

def _pad(x, pw):
    flat = tuple((v for pair in reversed(pw) for v in pair))
    return torch.nn.functional.pad(x, flat)
kernel_name = 'parallel_sum__spectral_fnet__mixer_gmlp__prompt'

def setup_inputs():

    _w = lambda a: (lambda t: t.to(torch.bfloat16) if t.is_floating_point() else t)(torch.as_tensor(a))

    def _A_setup():
        B, S, D = (1, 2048, 512)
        ns = torch.arange(S)[:, None] * torch.arange(S)[None, :]
        nd = torch.arange(D)[:, None] * torch.arange(D)[None, :]
        Ws_re = torch.cos(-2.0 * torch.pi * ns / S).to(float)
        Ws_im = torch.sin(-2.0 * torch.pi * ns / S).to(float)
        Wd_re = torch.cos(-2.0 * torch.pi * nd / D).to(float)
        Wd_im = torch.sin(-2.0 * torch.pi * nd / D).to(float)
        return {'x': _w((lambda *s: torch.randn(*s, dtype=torch.bfloat16) * 0.05)(B, S, D)), 'dft_seq_re': _w(Ws_re), 'dft_seq_im': _w(Ws_im), 'dft_hid_re': _w(Wd_re), 'dft_hid_im': _w(Wd_im)}
    _A_in = _A_setup()

    def _B_setup():
        B, S, D, F = (1, 2048, 512, 1792)
        Fh = F // 2
        return {'x': _w((lambda *s: torch.randn(*s, dtype=torch.bfloat16) * 0.05)(B, S, D)), 'w_in': _w((lambda *s: torch.randn(*s, dtype=torch.bfloat16) * 0.8)(D, 2 * Fh)), 'w_sp': _w(0.01 * (lambda *s: torch.randn(*s, dtype=torch.bfloat16) * 0.8)(S, S)), 'b_sp': _w(1.0 + 0.01 * (lambda *s: torch.randn(*s, dtype=torch.bfloat16) * 0.8)(S)), 'w_out': _w((lambda *s: torch.randn(*s, dtype=torch.bfloat16) * 0.8)(Fh, D))}
    _B_in = _B_setup()
    return {'A_x': _A_in['x'], 'A_dft_seq_re': _A_in['dft_seq_re'], 'A_dft_seq_im': _A_in['dft_seq_im'], 'A_dft_hid_re': _A_in['dft_hid_re'], 'A_dft_hid_im': _A_in['dft_hid_im'], 'B_x': _B_in['x'], 'B_w_in': _B_in['w_in'], 'B_w_sp': _B_in['w_sp'], 'B_b_sp': _B_in['b_sp'], 'B_w_out': _B_in['w_out']}

def reference(A_x, A_dft_seq_re, A_dft_seq_im, A_dft_hid_re, A_dft_hid_im, B_x, B_w_in, B_w_sp, B_b_sp, B_w_out):

    def _A_ref(x, dft_seq_re, dft_seq_im, dft_hid_re, dft_hid_im):
        a = torch.einsum('bsd,ed->bse', x, dft_hid_re)
        b = torch.einsum('bsd,ed->bse', x, dft_hid_im)
        y = torch.einsum('ts,bse->bte', dft_seq_re, a) - torch.einsum('ts,bse->bte', dft_seq_im, b)
        return y

    def _B_ref(x, w_in, w_sp, b_sp, w_out):
        B, S, D = x.shape
        F = w_in.shape[-1]
        Fh = F // 2
        z = _gelu(torch.einsum('bsd,df->bsf', x, w_in))
        z1 = z[..., :Fh].clone()
        z2 = _layernorm(z[..., Fh:], axis=-1)
        g = torch.einsum('ts,bsf->btf', w_sp, z2) + b_sp[None, :, None]
        return torch.einsum('bsf,fd->bsd', z1 * g, w_out)
    x = A_x
    xn = _layernorm(x, axis=-1)
    return x + _A_ref(xn, A_dft_seq_re, A_dft_seq_im, A_dft_hid_re, A_dft_hid_im) + _B_ref(xn, B_w_in, B_w_sp, B_b_sp, B_w_out)
```

```cpp
#include <hip/hip_runtime.h>
#include <hip/hip_bf16.h>
#include <stdint.h>

typedef unsigned short u16;
__device__ __forceinline__ float bf2f(u16 v) { return __uint_as_float(((unsigned)v) << 16); }
__device__ __forceinline__ u16 f2bf(float f) { __hip_bfloat16 b = __float2bfloat16(f); return *(u16*)&b; }

__global__ void ln_k(const u16* in, int ld, int off, int n, u16* out) {
  int r = blockIdx.x; __shared__ float red[256];
  float s = 0; for (int i = threadIdx.x; i < n; i += 256) s += bf2f(in[(long)r * ld + off + i]);
  red[threadIdx.x] = s; __syncthreads();
  for (int o = 128; o > 0; o >>= 1) { if (threadIdx.x < o) red[threadIdx.x] += red[threadIdx.x + o]; __syncthreads(); }
  float m = red[0] / n; __syncthreads();
  s = 0; for (int i = threadIdx.x; i < n; i += 256) { float d = bf2f(in[(long)r * ld + off + i]) - m; s += d * d; }
  red[threadIdx.x] = s; __syncthreads();
  for (int o = 128; o > 0; o >>= 1) { if (threadIdx.x < o) red[threadIdx.x] += red[threadIdx.x + o]; __syncthreads(); }
  float rs = rsqrtf(red[0] / n + 1e-5f);
  for (int i = threadIdx.x; i < n; i += 256) out[(long)r * n + i] = f2bf((bf2f(in[(long)r * ld + off + i]) - m) * rs);
}
__global__ void gemm_k(const u16* A, long sam, long sak, const u16* B, long sbk, long sbn, float* C, int M, int N, int K) {
  __shared__ float As[16][65], Bs[16][65];
  int tx = threadIdx.x & 15, ty = threadIdx.x >> 4;
  int bm = blockIdx.y * 64, bn = blockIdx.x * 64;
  float acc[4][4] = {};
  for (int k0 = 0; k0 < K; k0 += 16) {
    for (int i = threadIdx.x; i < 1024; i += 256) {
      int kk = i & 15, mm = i >> 4;
      As[kk][mm] = bf2f(A[(bm + mm) * sam + (k0 + kk) * sak]);
      Bs[kk][mm] = bf2f(B[(k0 + kk) * sbk + (bn + mm) * sbn]);
    }
    __syncthreads();
    for (int kk = 0; kk < 16; ++kk) {
      float a[4], b[4];
      for (int i = 0; i < 4; ++i) { a[i] = As[kk][ty * 4 + i]; b[i] = Bs[kk][tx * 4 + i]; }
      for (int i = 0; i < 4; ++i) for (int j = 0; j < 4; ++j) acc[i][j] += a[i] * b[j];
    }
    __syncthreads();
  }
  for (int i = 0; i < 4; ++i) for (int j = 0; j < 4; ++j) C[(long)(bm + ty * 4 + i) * N + bn + tx * 4 + j] = acc[i][j];
}
__global__ void cvt_k(const float* in, u16* out, long n, int gelu) {
  long i = blockIdx.x * 256L + threadIdx.x; if (i >= n) return;
  float v = in[i];
  if (gelu) { v = bf2f(f2bf(v)); float u = 0.7978845608028654f * (v + 0.044715f * v * v * v); v = 0.5f * v * (1.f + tanhf(u)); }
  out[i] = f2bf(v);
}
__global__ void h_k(const float* g, const u16* z, const u16* bsp, u16* h) {
  long i = blockIdx.x * 256L + threadIdx.x; if (i >= 2048L * 896) return;
  int s = i / 896, f = i % 896;
  float gg = bf2f(f2bf(bf2f(f2bf(g[i])) + bf2f(bsp[s])));
  h[i] = f2bf(gg * bf2f(z[(long)s * 1792 + f]));
}
__global__ void fin_k(const u16* x, const float* y1, const float* y2, const float* ob, u16* out) {
  long i = blockIdx.x * 256L + threadIdx.x; if (i >= 2048L * 512) return;
  out[i] = f2bf(bf2f(x[i]) + y1[i] - y2[i] + ob[i]);
}

extern "C" void kernel_launch(void* const* d_in, const int* in_sizes, int n_in,
                              void* d_out, int out_size, void* d_ws, size_t ws_size,
                              hipStream_t stream) {
  const u16* x = (const u16*)d_in[0]; const u16* wsr = (const u16*)d_in[1]; const u16* wsi = (const u16*)d_in[2];
  const u16* wdr = (const u16*)d_in[3]; const u16* wdi = (const u16*)d_in[4];
  const u16* win = (const u16*)d_in[6]; const u16* wsp = (const u16*)d_in[7]; const u16* bsp = (const u16*)d_in[8]; const u16* wout = (const u16*)d_in[9];
  char* ws = (char*)d_ws;
  u16* xn = (u16*)ws; ws += 2048 * 512 * 2;
  u16* a = (u16*)ws; ws += 2048 * 512 * 2;
  u16* b = (u16*)ws; ws += 2048 * 512 * 2;
  u16* z = (u16*)ws; ws += 2048 * 1792 * 2;
  u16* z2 = (u16*)ws; ws += 2048 * 896 * 2;
  u16* h = (u16*)ws; ws += 2048 * 896 * 2;
  float* t = (float*)ws; ws += 2048 * 1792 * 4;
  float* y1 = (float*)ws; ws += 2048 * 512 * 4;
  float* y2 = (float*)ws; ws += 2048 * 512 * 4;
  float* ob = (float*)ws; ws += 2048 * 512 * 4;
  ln_k<<<2048, 256, 0, stream>>>(x, 512, 0, 512, xn);
  gemm_k<<<dim3(8, 32), 256, 0, stream>>>(xn, 512, 1, wdr, 1, 512, t, 2048, 512, 512);
  cvt_k<<<2048 * 512 / 256, 256, 0, stream>>>(t, a, 2048 * 512, 0);
  gemm_k<<<dim3(8, 32), 256, 0, stream>>>(xn, 512, 1, wdi, 1, 512, t, 2048, 512, 512);
  cvt_k<<<2048 * 512 / 256, 256, 0, stream>>>(t, b, 2048 * 512, 0);
  gemm_k<<<dim3(28, 32), 256, 0, stream>>>(xn, 512, 1, win, 1792, 1, t, 2048, 1792, 512);
  cvt_k<<<2048 * 1792 / 256, 256, 0, stream>>>(t, z, 2048 * 1792, 1);
  ln_k<<<2048, 256, 0, stream>>>(z, 1792, 896, 896, z2);
  gemm_k<<<dim3(8, 32), 256, 0, stream>>>(wsr, 2048, 1, a, 512, 1, y1, 2048, 512, 2048);
  gemm_k<<<dim3(8, 32), 256, 0, stream>>>(wsi, 2048, 1, b, 512, 1, y2, 2048, 512, 2048);
  gemm_k<<<dim3(14, 32), 256, 0, stream>>>(wsp, 2048, 1, z2, 896, 1, t, 2048, 896, 2048);
  h_k<<<2048 * 896 / 256, 256, 0, stream>>>(t, z, bsp, h);
  gemm_k<<<dim3(8, 32), 256, 0, stream>>>(h, 896, 1, wout, 512, 1, ob, 2048, 512, 896);
  fin_k<<<2048 * 512 / 256, 256, 0, stream>>>(x, y1, y2, ob, (u16*)d_out);
}
```

```cpp
#include <hip/hip_runtime.h>
#include <hip/hip_bf16.h>
#include <stdint.h>

typedef unsigned short u16;
typedef short bf16x8 __attribute__((ext_vector_type(8)));
typedef short s16x4 __attribute__((ext_vector_type(4)));
typedef float f32x4 __attribute__((ext_vector_type(4)));
typedef float f32x2 __attribute__((ext_vector_type(2)));
typedef unsigned u32x4 __attribute__((ext_vector_type(4)));
typedef unsigned u32x2 __attribute__((ext_vector_type(2)));
typedef __bf16 bf16x2v __attribute__((ext_vector_type(2)));
#define LAS __attribute__((address_space(3)))
#define WAIT_V(n) asm volatile("s_waitcnt vmcnt(%0)" ::"n"(n) : "memory")
#define WL0() __builtin_amdgcn_s_waitcnt(0xC07F)
#define BAR() do { asm volatile("" ::: "memory"); __builtin_amdgcn_s_barrier(); asm volatile("" ::: "memory"); } while (0)
#define SCHED() __builtin_amdgcn_sched_barrier(0)
#define MFMA16(a, b, c) __builtin_amdgcn_mfma_f32_16x16x32_bf16(a, b, c, 0, 0, 0)
#define GLDS(rsrc, ldsaddr, voff, soff) \
  asm volatile("s_mov_b32 m0, %0\n\ts_nop 0\n\tbuffer_load_dwordx4 %1, %2, %3 offen lds" ::"s"((unsigned)(ldsaddr)), "v"((unsigned)(voff)), "s"(rsrc), "s"((unsigned)(soff)))

#define STAMP_DECL
#define STAMP(i)
#define STAMP_OUT(ws, kid, n)
constexpr int S = 2048, D = 512, F = 1792, FH = 896;
constexpr size_t WS_AT = 0;
constexpr size_t WS_BT = WS_AT + (size_t)D * S * 2;
constexpr size_t WS_Z2T = WS_BT + (size_t)D * S * 2;
constexpr size_t WS_Z1 = WS_Z2T + (size_t)FH * S * 2;
constexpr size_t WS_H = WS_Z1 + (size_t)FH * S * 2;
constexpr size_t WS_Y = WS_H + (size_t)FH * S * 2;
constexpr size_t WS_WO = WS_Y + (size_t)2 * D * S * 2;
constexpr size_t WS_ST = WS_WO + (size_t)FH * D * 2;

__device__ __forceinline__ unsigned cvt_pk(float lo, float hi) {
  f32x2 v = {lo, hi};
  bf16x2v b = __builtin_convertvector(v, bf16x2v);
  return __builtin_bit_cast(unsigned, b);
}
__device__ __forceinline__ float bf_lo(unsigned u) { return __uint_as_float(u << 16); }
__device__ __forceinline__ float bf_hi(unsigned u) { return __uint_as_float(u & 0xffff0000u); }
__device__ __forceinline__ float dot2(unsigned a, unsigned b, float c) {
  return __builtin_amdgcn_fdot2_f32_bf16(__builtin_bit_cast(bf16x2v, a), __builtin_bit_cast(bf16x2v, b), c, false);
}
__device__ __forceinline__ float gelu_t(float v) {
  const float c1 = -2.0f * 0.7978845608028654f * 1.4426950408889634f;
  const float c2 = c1 * 0.044715f;
  float e = __builtin_amdgcn_exp2f(v * (c1 + c2 * v * v));
  return v * __builtin_amdgcn_rcpf(1.0f + e);
}
__device__ __forceinline__ float xsum16(float v) {
  v += __builtin_bit_cast(float, __builtin_amdgcn_update_dpp(0, __builtin_bit_cast(int, v), 0x128, 0xf, 0xf, false));
  v += __builtin_bit_cast(float, __builtin_amdgcn_update_dpp(0, __builtin_bit_cast(int, v), 0x124, 0xf, 0xf, false));
  v += __builtin_bit_cast(float, __builtin_amdgcn_update_dpp(0, __builtin_bit_cast(int, v), 0x122, 0xf, 0xf, false));
  v += __builtin_bit_cast(float, __builtin_amdgcn_update_dpp(0, __builtin_bit_cast(int, v), 0x121, 0xf, 0xf, false));
  return v;
}
__device__ __forceinline__ float xsumq(float v) {
  v += __shfl_xor(v, 16);
  v += __shfl_xor(v, 32);
  return v;
}
__device__ __forceinline__ int sigma(int e) { return (e & ~31) + (((e >> 2) & 1) << 4) + (((e >> 3) & 3) << 2) + (e & 3); }

struct K1Args {
  const u16* x; const u16* wdr; const u16* wdi; const u16* win;
  char* ws;
};
constexpr int K1_SLOT = 32768;
template <int N> struct IC { static constexpr int value = N; };

template <int TYPE>
__device__ __forceinline__ void k1_body(const K1Args& a, char* smem, int pm, int grp, int jj) {
  const int tid = threadIdx.x, lane = tid & 63;
  const int wid = __builtin_amdgcn_readfirstlane(tid >> 6);
  const int wm = wid & 3, wn = wid >> 2;
  STAMP_DECL STAMP(0);
  const unsigned sb = (unsigned)(size_t)(LAS char*)smem;
  const int T0 = (TYPE == 2) ? (42 + 11 * grp) : 3 * (grp * 7 + jj);
  const int TT = (TYPE == 2) ? T0 + 8 : T0;
  const int col0 = grp * FH + jj * 128;

  auto rX = __builtin_amdgcn_make_buffer_rsrc((void*)(a.x + (size_t)pm * 128 * D), 0, 128 * D * 2, 0x00020000);
  auto rW = (TYPE == 2) ? __builtin_amdgcn_make_buffer_rsrc((void*)(a.wdi + (size_t)(T0 - 32) * 16 * D), 0, 128 * D * 2, 0x00020000)
                        : __builtin_amdgcn_make_buffer_rsrc((void*)(a.win + col0), 0, (D * F - col0) * 2, 0x00020000);
  auto rDr = __builtin_amdgcn_make_buffer_rsrc((void*)a.wdr, 0, D * D * 2, 0x00020000);
  auto rDi = __builtin_amdgcn_make_buffer_rsrc((void*)a.wdi, 0, D * D * 2, 0x00020000);
  const int r8 = lane >> 3, p8 = lane & 7;
  const unsigned sw_e = (p8 ^ (r8 >> 1)) << 4, sw_o = (p8 ^ ((r8 >> 1) | 4)) << 4;
  const unsigned xrow = (8 * (r8 >> 2) + (r8 & 3)) * (D * 2);
  const unsigned vx_e = xrow + sw_e, vx_o = xrow + sw_o;
  const unsigned vr_e = r8 * (D * 2) + sw_e, vr_o = r8 * (D * 2) + sw_o;
  const unsigned vw = (lane >> 4) * (F * 2) + (((lane & 15) ^ (((lane >> 4) << 2) | (wid & 3))) << 4);
  const unsigned sx = (32 * (wid >> 1) + 4 * (wid & 1)) * (D * 2);

  auto stage = [&](int st) {
    const unsigned slot = sb + (st % 5) * K1_SLOT;
    if (st < 8) {
      GLDS(rX, slot + (2 * wid) * 1024, vx_e, sx + st * 128);
      GLDS(rX, slot + (2 * wid + 1) * 1024, vx_o, sx + 16 * (D * 2) + st * 128);
      if (TYPE == 2) {
        GLDS(rW, slot + 16384 + (2 * wid) * 1024, vr_e, (2 * wid) * 8 * (D * 2) + st * 128);
        GLDS(rW, slot + 16384 + (2 * wid + 1) * 1024, vr_o, (2 * wid + 1) * 8 * (D * 2) + st * 128);
      } else {
        GLDS(rW, slot + 16384 + wid * 1024, vw, (st * 64 + 4 * wid) * (F * 2));
        GLDS(rW, slot + 16384 + (wid + 8) * 1024, vw, (st * 64 + 4 * (wid + 8)) * (F * 2));
      }
    } else {
#pragma unroll
      for (int i = 0; i < 3; ++i) {
        const int u = wid * 3 + i, kb = u / 6, g6 = u % 6;
        const int T = TT + (g6 >> 1);
        const unsigned so = ((T & 31) * 16 + (g6 & 1) * 8) * (D * 2) + ((st - 8) * 4 + kb) * 128;
        const unsigned vo = (g6 & 1) ? vr_o : vr_e;
        if (T >= 32) GLDS(rDi, slot + u * 1024, vo, so); else GLDS(rDr, slot + u * 1024, vo, so);
      }
    }
  };

  const int fr = lane & 15, fq = lane >> 4;
  const unsigned fsw = (fq ^ ((fr >> 1) & 7)) << 4;
  const unsigned xa0 = (32 * wm + fr) * 128 + fsw;
  unsigned wb0, wb1;
  if (TYPE == 2) {
    wb0 = 16384 + (64 * wn + 16 * wm + fr) * 128 + fsw;
    wb1 = 0;
  } else {
    const int q4 = fr >> 2, pp = lane & 3;
    const int ch = 8 * wn + 2 * wm + (pp >> 1);
    wb0 = 16384 + 256 * (8 * fq + q4) + 16 * (ch ^ ((q4 << 2) | ((2 * fq) & 3))) + 8 * (pp & 1);
    wb1 = 16384 + 256 * (8 * fq + 4 + q4) + 16 * (ch ^ ((q4 << 2) | ((2 * fq + 1) & 3))) + 8 * (pp & 1);
  }

  bf16x8 xf[16][2];
  f32x4 acc[2][4], acct[2][2];
#pragma unroll
  for (int m = 0; m < 2; ++m) {
#pragma unroll
    for (int n = 0; n < 4; ++n) acc[m][n] = f32x4{0, 0, 0, 0};
    acct[m][0] = f32x4{0, 0, 0, 0}; acct[m][1] = f32x4{0, 0, 0, 0};
  }
  float xs[2] = {0, 0}, xq[2] = {0, 0}, cs = 0, cst[2] = {0, 0};
  const unsigned ONES = 0x3f803f80u;

#pragma unroll
  for (int st = 0; st < 5; ++st) stage(st);
  STAMP(1);

#pragma unroll
  for (int st = 0; st < 8; ++st) {
    if (st == 0) WAIT_V(16);
    else if (st <= 4) WAIT_V(12);
    else if (st == 5) WAIT_V(11);
    else if (st == 6) WAIT_V(10);
    else WAIT_V(6);
    WL0();
    BAR();
    if (st == 0) STAMP(2);
    if (st == 4) STAMP(3);
    if (st >= 1 && st + 4 <= 9) stage(st + 4);
    const unsigned slot = sb + (st % 5) * K1_SLOT;
#pragma unroll
    for (int ks = 0; ks < 2; ++ks) {
      bf16x8 wf[4];
#pragma unroll
      for (int m = 0; m < 2; ++m) xf[st * 2 + ks][m] = *(const LAS bf16x8*)(size_t)(slot + ((xa0 + m * 2048) ^ (ks * 64)));
#pragma unroll
      for (int n = 0; n < 4; ++n) {
        if (TYPE == 2) {
          wf[n] = *(const LAS bf16x8*)(size_t)(slot + ((wb0 ^ (n * 2048)) ^ (ks * 64)));
        } else {
          s16x4 lo = __builtin_amdgcn_ds_read_tr16_b64_v4i16((LAS s16x4*)(size_t)(slot + (wb0 ^ (n * 32)) + ks * 8192));
          s16x4 hi = __builtin_amdgcn_ds_read_tr16_b64_v4i16((LAS s16x4*)(size_t)(slot + (wb1 ^ (n * 32)) + ks * 8192));
          wf[n] = __builtin_shufflevector(lo, hi, 0, 1, 2, 3, 4, 5, 6, 7);
        }
      }
#pragma unroll
      for (int m = 0; m < 2; ++m) {
#pragma unroll
        for (int n = 0; n < 4; ++n)
          acc[m][n] = (TYPE == 0) ? MFMA16(wf[n], xf[st * 2 + ks][m], acc[m][n]) : MFMA16(xf[st * 2 + ks][m], wf[n], acc[m][n]);
        u32x4 xv = __builtin_bit_cast(u32x4, xf[st * 2 + ks][m]);
#pragma unroll
        for (int i = 0; i < 4; ++i) { xs[m] = dot2(xv[i], ONES, xs[m]); xq[m] = dot2(xv[i], xv[i], xq[m]); }
      }
      u32x4 wv = __builtin_bit_cast(u32x4, wf[0]);
#pragma unroll
      for (int i = 0; i < 4; ++i) cs = dot2(wv[i], ONES, cs);
    }
  }
  STAMP(4);
  float mean[2], rstd[2];
#pragma unroll
  for (int m = 0; m < 2; ++m) {
    float s1 = xsumq(xs[m]), s2 = xsumq(xq[m]);
    mean[m] = s1 * (1.0f / D);
    float var = fmaxf(s2 * (1.0f / D) - mean[m] * mean[m], 0.0f);
    rstd[m] = __builtin_amdgcn_rsqf(var + 1e-5f);
  }
  cs = xsumq(cs);
  LAS float* scr = (LAS float*)(size_t)sb;
  LAS float* scw = (LAS float*)(size_t)(sb + 1024 + wid * 256);
  LAS float* scz = (LAS float*)(size_t)(sb + 4096);
  if (fq == 0) {
    scr[64 * wn + 16 * wm + fr] = cs;
    scw[fr] = mean[0]; scw[16 + fr] = mean[1]; scw[32 + fr] = rstd[0]; scw[48 + fr] = rstd[1];
  }
  WAIT_V(3);
  WL0();
  BAR();
  STAMP(5);
  f32x4 mq[2], rq[2];
#pragma unroll
  for (int m = 0; m < 2; ++m) { mq[m] = *(LAS f32x4*)(scw + 16 * m + 4 * fq); rq[m] = *(LAS f32x4*)(scw + 32 + 16 * m + 4 * fq); }

  const int s0 = pm * 128 + 32 * wm;
  u32x4 zp[4];
  if (TYPE == 0) {
    u16* z1 = (u16*)(a.ws + WS_Z1);
#pragma unroll
    for (int n = 0; n < 4; ++n) {
      const int tile = 4 * wn + (n ^ wm);
      f32x4 u = *(LAS f32x4*)(scr + 16 * tile + 4 * fq);
#pragma unroll
      for (int m = 0; m < 2; ++m) {
        float v[4];
#pragma unroll
        for (int j = 0; j < 4; ++j) v[j] = gelu_t(rstd[m] * (acc[m][n][j] - mean[m] * u[j]));
        const int sl = 8 * (fr >> 2) + 4 * m + (fr & 3);
        const int t = s0 + sl, c = (jj * 128 + 16 * tile) / 8 + (fq >> 1);
        u32x2 o = {cvt_pk(v[0], v[1]), cvt_pk(v[2], v[3])};
        *(u32x2*)(z1 + ((size_t)((t >> 4) * 112 + c) * 16 + (t & 15)) * 8 + 4 * (fq & 1)) = o;
      }
    }
  } else {
    float ps[2][4], pq[2][4];
#pragma unroll
    for (int m = 0; m < 2; ++m)
#pragma unroll
      for (int j = 0; j < 4; ++j) { ps[m][j] = 0; pq[m][j] = 0; }
#pragma unroll
    for (int n = 0; n < 4; ++n) {
      const int tile = 4 * wn + (n ^ wm);
      const float u = scr[16 * tile + fr];
#pragma unroll
      for (int m = 0; m < 2; ++m) {
        float v[4];
#pragma unroll
        for (int j = 0; j < 4; ++j) {
          v[j] = rq[m][j] * (acc[m][n][j] - mq[m][j] * u);
          if (TYPE == 1) v[j] = gelu_t(v[j]);
        }
        unsigned lo = cvt_pk(v[0], v[1]), hi = cvt_pk(v[2], v[3]);
        zp[n][2 * m] = lo; zp[n][2 * m + 1] = hi;
        if (TYPE == 1) {
          float r0 = bf_lo(lo), r1 = bf_hi(lo), r2 = bf_lo(hi), r3 = bf_hi(hi);
          ps[m][0] += r0; ps[m][1] += r1; ps[m][2] += r2; ps[m][3] += r3;
          pq[m][0] += r0 * r0; pq[m][1] += r1 * r1; pq[m][2] += r2 * r2; pq[m][3] += r3 * r3;
        }
      }
      if (TYPE == 2) {
        const int e = (T0 - 32 + tile) * 16 + fr;
        *(u32x4*)((u16*)(a.ws + WS_BT) + (size_t)sigma(e) * S + s0 + 8 * fq) = zp[n];
      } else {
        const int f = jj * 128 + tile * 16 + fr;
        *(u32x4*)((u16*)(a.ws + WS_Z2T) + (size_t)sigma(f) * S + s0 + 8 * fq) = zp[n];
      }
    }
    if (TYPE == 1) {
#pragma unroll
      for (int m = 0; m < 2; ++m)
#pragma unroll
        for (int j = 0; j < 4; ++j) { ps[m][j] = xsum16(ps[m][j]); pq[m][j] = xsum16(pq[m][j]); }
      if (fr == 0) {
#pragma unroll
        for (int m = 0; m < 2; ++m)
#pragma unroll
          for (int j = 0; j < 4; ++j) {
            const int sl = 32 * wm + 8 * fq + 4 * m + j;
            *(LAS f32x2*)(scz + (wn * 128 + sl) * 2) = f32x2{ps[m][j], pq[m][j]};
          }
      }
    }
  }
  STAMP(6);
  auto tail = [&](auto NT) {
#pragma unroll
    for (int st = 8; st < 10; ++st) {
      if (st == 9) {
        if (TYPE == 0) WAIT_V(8); else WAIT_V(4);
        WL0();
        BAR();
      }
      const unsigned slot = sb + (st % 5) * K1_SLOT;
      const unsigned tb = slot + (32 * wn + fr) * 128 + fsw;
#pragma unroll
      for (int kb = 0; kb < 4; ++kb)
#pragma unroll
        for (int ks = 0; ks < 2; ++ks) {
          const int kk = (st - 8) * 8 + kb * 2 + ks;
#pragma unroll
          for (int n = 0; n < NT.value; ++n) {
            bf16x8 w0 = *(const LAS bf16x8*)(size_t)((tb ^ (ks * 64)) + kb * 6144 + n * 2048);
            acct[0][n] = MFMA16(xf[kk][0], w0, acct[0][n]);
            acct[1][n] = MFMA16(xf[kk][1], w0, acct[1][n]);
            u32x4 wv = __builtin_bit_cast(u32x4, w0);
#pragma unroll
            for (int i = 0; i < 4; ++i) cst[n] = dot2(wv[i], ONES, cst[n]);
          }
        }
    }
  };
  if (wn == 0) tail(IC<2>{}); else tail(IC<1>{});
  STAMP(7);
  if (TYPE == 1 && tid < 128) {
    f32x2 p0 = *(LAS f32x2*)(scz + tid * 2), p1 = *(LAS f32x2*)(scz + (128 + tid) * 2);
    *(f32x2*)((float*)(a.ws + WS_ST) + ((size_t)jj * S + pm * 128 + tid) * 2) = p0 + p1;
  }
#pragma unroll
  for (int n = 0; n < 2; ++n) {
    if (n == 1 && wn != 0) break;
    const float u = xsumq(cst[n]);
    u32x4 o;
#pragma unroll
    for (int m = 0; m < 2; ++m) {
      float v[4];
#pragma unroll
      for (int j = 0; j < 4; ++j) v[j] = rq[m][j] * (acct[m][n][j] - mq[m][j] * u);
      o[2 * m] = cvt_pk(v[0], v[1]); o[2 * m + 1] = cvt_pk(v[2], v[3]);
    }
    const int T = TT + 2 * wn + n;
    const int e = (T & 31) * 16 + fr;
    u16* dst = (u16*)(a.ws + (T >= 32 ? WS_BT : WS_AT));
    *(u32x4*)(dst + (size_t)sigma(e) * S + s0 + 8 * fq) = o;
  }
  STAMP(8);
  STAMP_OUT(a.ws, 0, 9);
}

__global__ __launch_bounds__(512, 2) void k1(K1Args a) {
  extern __shared__ __attribute__((aligned(1024))) char smem[];
  const int x = blockIdx.x & 7, i = blockIdx.x >> 3;
  const int pm = 4 * (x >> 1) + (i >> 3), grp = x & 1, jj = i & 7;
  if (jj == 7) k1_body<2>(a, smem, pm, grp, jj);
  else if (grp == 0) k1_body<0>(a, smem, pm, grp, jj);
  else k1_body<1>(a, smem, pm, grp, jj);
}

__global__ __launch_bounds__(256) void k1b(char* ws) {
  const int sc = blockIdx.x & 15, fb = blockIdx.x >> 4;
  const int c = threadIdx.x & 15, r = threadIdx.x >> 4;
  const int s = sc * 128 + c * 8;
  const float* st = (const float*)(ws + WS_ST);
  float mu[8], rs[8];
#pragma unroll
  for (int i = 0; i < 8; ++i) {
    float a = 0, b = 0;
#pragma unroll
    for (int j = 0; j < 7; ++j) { f32x2 p = *(const f32x2*)(st + ((size_t)j * S + s + i) * 2); a += p[0]; b += p[1]; }
    mu[i] = a * (1.0f / FH);
    rs[i] = __builtin_amdgcn_rsqf(fmaxf(b * (1.0f / FH) - mu[i] * mu[i], 0.f) + 1e-5f);
  }
  u16* z = (u16*)(ws + WS_Z2T);
  for (int f = fb * 56 + r; f < fb * 56 + 56; f += 16) {
    u32x4 v = *(u32x4*)(z + (size_t)f * S + s), o;
#pragma unroll
    for (int i = 0; i < 4; ++i) o[i] = cvt_pk((bf_lo(v[i]) - mu[2 * i]) * rs[2 * i], (bf_hi(v[i]) - mu[2 * i + 1]) * rs[2 * i + 1]);
    *(u32x4*)(z + (size_t)f * S + s) = o;
  }
}

struct K2Args {
  const u16* wsr; const u16* wsi; const u16* wsp; const u16* bsp; const u16* wout;
  char* ws;
};
constexpr int K2_STAGE = 32768, K2_NKT = S / 64;

__global__ __launch_bounds__(512, 2) void k2(K2Args a) {
  extern __shared__ __attribute__((aligned(1024))) char smem[];
  const int tid = threadIdx.x, lane = tid & 63;
  const int wid = __builtin_amdgcn_readfirstlane(tid >> 6);
  int mat, tm, tn;
  {
    const int x = blockIdx.x & 7, i = blockIdx.x >> 3;
    if (x < 4) { mat = x >> 1; tm = (x & 1) * 8 + (i >> 2); tn = i & 3; }
    else {
      mat = 2; tm = (x - 4) * 4 + (i / 7); tn = i % 7;
      if (i >= 28) {
        const int sbk = (x - 4) * 4 + (i - 28);
        u16* wo = (u16*)(a.ws + WS_WO);
        const int ep = tid & 255, half = tid >> 8;
        for (int cc = half; cc < 7; cc += 2) {
          const int c = sbk * 7 + cc;
          unsigned v[8];
#pragma unroll
          for (int r = 0; r < 8; ++r) v[r] = *(const unsigned*)(a.wout + (size_t)(c * 8 + r) * D + 2 * ep);
          u32x4 lo, hi;
#pragma unroll
          for (int r = 0; r < 4; ++r) {
            lo[r] = (v[2 * r] & 0xffffu) | (v[2 * r + 1] << 16);
            hi[r] = (v[2 * r] >> 16) | (v[2 * r + 1] & 0xffff0000u);
          }
          const int rho = sigma(2 * ep);
          u16* dst = wo + ((size_t)((rho >> 4) * 112 + c) * 16 + (rho & 15)) * 8;
          *(u32x4*)dst = lo;
          *(u32x4*)(dst + 8) = hi;
        }
        return;
      }
    }
  }
  const unsigned sb = (unsigned)(size_t)(LAS char*)smem;
  if (wid >= 4) {
    const u16* Pp = (mat == 0 ? a.wsr : mat == 1 ? a.wsi : a.wsp) + (size_t)tm * 128 * S;
    const u16* Qp = (const u16*)(a.ws + (mat == 0 ? WS_AT : mat == 1 ? WS_BT : WS_Z2T)) + (size_t)tn * 128 * S;
    auto rP = __builtin_amdgcn_make_buffer_rsrc((void*)Pp, 0, 128 * S * 2, 0x00020000);
    auto rQ = __builtin_amdgcn_make_buffer_rsrc((void*)Qp, 0, 128 * S * 2, 0x00020000);
    const int lw = wid - 4;
    const int r8 = lane >> 3, p = lane & 7;
    const unsigned vo_e = r8 * (S * 2) + ((p ^ (r8 >> 1)) << 4);
    const unsigned vo_o = r8 * (S * 2) + ((p ^ ((r8 >> 1) | 4)) << 4);
    auto stage = [&](int kt, int slot) {
#pragma unroll
      for (int j = 0; j < 4; ++j) {
        const int g = lw * 4 + j;
        const unsigned so = kt * 128 + g * 8 * (S * 2);
        const unsigned la = sb + slot * K2_STAGE + g * 1024;
        GLDS(rP, la, (j & 1) ? vo_o : vo_e, so);
        GLDS(rQ, la + 16384, (j & 1) ? vo_o : vo_e, so);
      }
    };
    stage(0, 0); stage(1, 1);
    WAIT_V(8); BAR();
    stage(2, 2); stage(3, 3);
    int slot = 0;
    for (int kt = 0; kt < K2_NKT - 4; ++kt) {
      WAIT_V(16);
      BAR();
      stage(kt + 4, slot);
      slot = (slot + 1) & 3;
    }
    WAIT_V(16); BAR();
    WAIT_V(8); BAR();
    WAIT_V(0); BAR();
    return;
  }
  const int wp = wid >> 1, wq = wid & 1;
  typedef const LAS bf16x8* lp;
  f32x4 acc[4][4];
#pragma unroll
  for (int m = 0; m < 4; ++m)
#pragma unroll
    for (int n = 0; n < 4; ++n) acc[m][n] = f32x4{0, 0, 0, 0};
  const int fr = lane & 15, fq = lane >> 4, sw = (fr >> 1) & 7;
  unsigned ra[2][2], rb[2][2];
  ra[0][0] = sb + (wp * 64 + fr) * 128 + ((fq ^ sw) << 4);
  rb[0][0] = sb + 16384 + (wq * 64 + fr) * 128 + ((fq ^ sw) << 4);
  ra[0][1] = ra[0][0] ^ 64; rb[0][1] = rb[0][0] ^ 64;
  ra[1][0] = ra[0][0] + 65536; ra[1][1] = ra[0][1] + 65536; rb[1][0] = rb[0][0] + 65536; rb[1][1] = rb[0][1] + 65536;
  asm volatile("" : "+v"(ra[1][0]), "+v"(ra[1][1]), "+v"(rb[1][0]), "+v"(rb[1][1]));
  bf16x8 a0[4], b0[4], a1[4], b1[4];
#define K2_RD(AF, BF, sl, ks, i) do { const unsigned o_ = ((sl) & 1) * K2_STAGE; \
    if ((i) == 0) AF[0] = *(lp)(size_t)(ra[(sl) >> 1][ks] + o_); \
    else if ((i) <= 4) BF[(i) - 1] = *(lp)(size_t)(rb[(sl) >> 1][ks] + o_ + ((i) - 1) * 2048); \
    else AF[(i) - 4] = *(lp)(size_t)(ra[(sl) >> 1][ks] + o_ + ((i) - 4) * 2048); } while (0)
#define K2_MM(AF, BF, i) acc[(i) >> 2][(i) & 3] = MFMA16(BF[(i) & 3], AF[(i) >> 2], acc[(i) >> 2][(i) & 3])
#define K2_STEP(s, LAST) do { \
    SCHED(); WL0(); SCHED(); \
    _Pragma("unroll") for (int i = 0; i < 16; ++i) { K2_MM(a0, b0, i); SCHED(); if (i < 8) K2_RD(a1, b1, s, 1, i); SCHED(); } \
    WL0(); \
    if (!(LAST)) BAR(); \
    SCHED(); \
    _Pragma("unroll") for (int i = 0; i < 16; ++i) { K2_MM(a1, b1, i); SCHED(); if (!(LAST) && i < 8) K2_RD(a0, b0, ((s) + 1) & 3, 0, i); SCHED(); } \
  } while (0)
  BAR();
#pragma unroll
  for (int i = 0; i < 8; ++i) K2_RD(a0, b0, 0, 0, i);
  for (int kt = 0; kt < K2_NKT - 4; kt += 4) { K2_STEP(0, 0); K2_STEP(1, 0); K2_STEP(2, 0); K2_STEP(3, 0); }
  K2_STEP(0, 0); K2_STEP(1, 0); K2_STEP(2, 0); K2_STEP(3, 1);
  SCHED();
  if (mat < 2) {
    u16* y = (u16*)(a.ws + WS_Y) + (size_t)mat * S * D;
#pragma unroll
    for (int ni = 0; ni < 4; ++ni)
#pragma unroll
      for (int P = 0; P < 2; ++P) {
        const f32x4 c0 = acc[ni][2 * P], c1 = acc[ni][2 * P + 1];
        u32x4 v = {cvt_pk(c0[0], c0[1]), cvt_pk(c0[2], c0[3]), cvt_pk(c1[0], c1[1]), cvt_pk(c1[2], c1[3])};
        const int tt = 8 * tm + 4 * wp + ni, c = 16 * tn + 8 * wq + 4 * P;
        *(u32x4*)(y + (size_t)(tt * 64 + c) * 128 + lane * 8) = v;
      }
  } else {
    const u16* z1 = (const u16*)(a.ws + WS_Z1);
    u16* h = (u16*)(a.ws + WS_H);
#pragma unroll
    for (int ni = 0; ni < 4; ++ni) {
      const int tt = 8 * tm + 4 * wp + ni;
      const float bias = __uint_as_float(((unsigned)a.bsp[tt * 16 + fr]) << 16);
#pragma unroll
      for (int P = 0; P < 2; ++P) {
        const f32x4 c0 = acc[ni][2 * P], c1 = acc[ni][2 * P + 1];
        const int c = 16 * tn + 8 * wq + 4 * P;
        const size_t off = (size_t)(tt * 112 + c) * 128 + lane * 8;
        const u32x4 z = *(const u32x4*)(z1 + off);
        u32x4 v = {cvt_pk(bf_lo(z[0]) * (c0[0] + bias), bf_hi(z[0]) * (c0[1] + bias)), cvt_pk(bf_lo(z[1]) * (c0[2] + bias), bf_hi(z[1]) * (c0[3] + bias)),
                   cvt_pk(bf_lo(z[2]) * (c1[0] + bias), bf_hi(z[2]) * (c1[1] + bias)), cvt_pk(bf_lo(z[3]) * (c1[2] + bias), bf_hi(z[3]) * (c1[3] + bias))};
        *(u32x4*)(h + off) = v;
      }
    }
  }
}

struct K3Args {
  const u16* x; char* ws; u16* out;
};
constexpr int K3_STAGE = 32768;

__global__ __launch_bounds__(512, 2) void k3(K3Args a) {
  extern __shared__ __attribute__((aligned(1024))) char smem[];
  const int tid = threadIdx.x, lane = tid & 63;
  const int wid = __builtin_amdgcn_readfirstlane(tid >> 6);
  const int x = blockIdx.x & 7, i = blockIdx.x >> 3;
  const int tm = x * 4 + (i >> 3), tn = i & 7;
  const unsigned sb = (unsigned)(size_t)(LAS char*)smem;
  if (wid >= 4) {
    const int lw = wid - 4;
    auto rH = __builtin_amdgcn_make_buffer_rsrc((void*)(a.ws + WS_H + (size_t)(tm * 4 + lw) * 112 * 256), 0, 112 * 256, 0x00020000);
    auto rW = __builtin_amdgcn_make_buffer_rsrc((void*)(a.ws + WS_WO + (size_t)(tn * 4 + lw) * 112 * 256), 0, 112 * 256, 0x00020000);
    const unsigned vo = lane * 16;
    auto stage = [&](int st) {
      const unsigned la = sb + (st % 5) * K3_STAGE + lw * 4096;
#pragma unroll
      for (int j = 0; j < 4; ++j) {
        GLDS(rH, la + j * 1024, vo, st * 4096 + j * 1024);
        GLDS(rW, la + 16384 + j * 1024, vo, st * 4096 + j * 1024);
      }
    };
    stage(0); stage(1);
    WAIT_V(8); BAR();
    stage(2); stage(3); stage(4);
    WAIT_V(24); BAR();
    stage(5);
    WAIT_V(24); BAR();
    stage(6);
    WAIT_V(24); BAR();
    WAIT_V(16); BAR();
    WAIT_V(8); BAR();
    WAIT_V(0); BAR();
    return;
  }
  const int wp = wid >> 1, wq = wid & 1;
  const int fr = lane & 15, fq = lane >> 4;
  typedef const LAS bf16x8* lp;
  f32x4 acc[2][2];
#pragma unroll
  for (int m = 0; m < 2; ++m) { acc[m][0] = f32x4{0, 0, 0, 0}; acc[m][1] = f32x4{0, 0, 0, 0}; }
  u32x4 ex[2], e1[2], e2[2];
  const u16* y = (const u16*)(a.ws + WS_Y);
#pragma unroll
  for (int ni = 0; ni < 2; ++ni) {
    const int tt = 4 * tm + 2 * wp + ni, c = 8 * tn + 4 * wq;
    e1[ni] = *(const u32x4*)(y + (size_t)(tt * 64 + c) * 128 + lane * 8);
    e2[ni] = *(const u32x4*)(y + (size_t)S * D + (size_t)(tt * 64 + c) * 128 + lane * 8);
    ex[ni] = *(const u32x4*)(a.x + (size_t)(tt * 16 + fr) * D + (c + fq) * 8);
  }
  const unsigned hb = sb + wp * 8192 + lane * 16, wb = sb + 16384 + wq * 8192 + lane * 16;
#pragma unroll
  for (int st = 0; st < 7; ++st) {
    WL0();
    BAR();
    const unsigned so = (st % 5) * K3_STAGE;
#pragma unroll
    for (int ks = 0; ks < 4; ++ks) {
      bf16x8 hf[2], wf[2];
#pragma unroll
      for (int j = 0; j < 2; ++j) {
        hf[j] = *(lp)(size_t)(hb + so + j * 4096 + ks * 1024);
        wf[j] = *(lp)(size_t)(wb + so + j * 4096 + ks * 1024);
      }
#pragma unroll
      for (int ni = 0; ni < 2; ++ni)
#pragma unroll
        for (int mi = 0; mi < 2; ++mi) acc[ni][mi] = MFMA16(wf[mi], hf[ni], acc[ni][mi]);
    }
  }
#pragma unroll
  for (int ni = 0; ni < 2; ++ni) {
    const int tt = 4 * tm + 2 * wp + ni, c = 8 * tn + 4 * wq;
    u32x4 o;
#pragma unroll
    for (int k = 0; k < 4; ++k) {
      const float lo = bf_lo(ex[ni][k]) + bf_lo(e1[ni][k]) - bf_lo(e2[ni][k]) + acc[ni][k >> 1][(k & 1) * 2];
      const float hi = bf_hi(ex[ni][k]) + bf_hi(e1[ni][k]) - bf_hi(e2[ni][k]) + acc[ni][k >> 1][(k & 1) * 2 + 1];
      o[k] = cvt_pk(lo, hi);
    }
    *(u32x4*)(a.out + (size_t)(tt * 16 + fr) * D + (c + fq) * 8) = o;
  }
}

extern "C" void kernel_launch(void* const* d_in, const int* in_sizes, int n_in, void* d_out, int out_size, void* d_ws, size_t ws_size,
                              hipStream_t stream) {
  (void)in_sizes; (void)n_in; (void)out_size; (void)ws_size;
  (void)hipFuncSetAttribute((const void*)k1, hipFuncAttributeMaxDynamicSharedMemorySize, 5 * K1_SLOT);
  (void)hipFuncSetAttribute((const void*)k2, hipFuncAttributeMaxDynamicSharedMemorySize, 4 * K2_STAGE);
  (void)hipFuncSetAttribute((const void*)k3, hipFuncAttributeMaxDynamicSharedMemorySize, 5 * K3_STAGE);
  K1Args a1{(const u16*)d_in[0], (const u16*)d_in[3], (const u16*)d_in[4], (const u16*)d_in[6], (char*)d_ws};
  K2Args a2{(const u16*)d_in[1], (const u16*)d_in[2], (const u16*)d_in[7], (const u16*)d_in[8], (const u16*)d_in[9], (char*)d_ws};
  K3Args a3{(const u16*)d_in[0], (char*)d_ws, (u16*)d_out};
  k1<<<256, 512, 5 * K1_SLOT, stream>>>(a1);
  k1b<<<256, 256, 0, stream>>>((char*)d_ws);
  k2<<<256, 512, 4 * K2_STAGE, stream>>>(a2);
  k3<<<256, 512, 5 * K3_STAGE, stream>>>(a3);
}
```

```cpp
#include <hip/hip_runtime.h>
#include <hip/hip_bf16.h>
#include <stdint.h>

typedef unsigned short u16;
typedef short bf16x8 __attribute__((ext_vector_type(8)));
typedef short s16x4 __attribute__((ext_vector_type(4)));
typedef float f32x4 __attribute__((ext_vector_type(4)));
typedef float f32x2 __attribute__((ext_vector_type(2)));
typedef unsigned u32x4 __attribute__((ext_vector_type(4)));
typedef unsigned u32x2 __attribute__((ext_vector_type(2)));
typedef __bf16 bf16x2v __attribute__((ext_vector_type(2)));
#define LAS __attribute__((address_space(3)))
#define WAIT_V(n) asm volatile("s_waitcnt vmcnt(%0)" ::"n"(n) : "memory")
#define WL0() __builtin_amdgcn_s_waitcnt(0xC07F)
#define BAR() do { asm volatile("" ::: "memory"); __builtin_amdgcn_s_barrier(); asm volatile("" ::: "memory"); } while (0)
#define SCHED() __builtin_amdgcn_sched_barrier(0)
#define MFMA16(a, b, c) __builtin_amdgcn_mfma_f32_16x16x32_bf16(a, b, c, 0, 0, 0)
#define GLDS(rsrc, ldsaddr, voff, soff) \
  asm volatile("s_mov_b32 m0, %0\n\ts_nop 0\n\tbuffer_load_dwordx4 %1, %2, %3 offen lds" ::"s"((unsigned)(ldsaddr)), "v"((unsigned)(voff)), "s"(rsrc), "s"((unsigned)(soff)))

#define STAMP_DECL
#define STAMP(i)
#define STAMPF(i)
#define STAMP_OUT(ws, kid, n)
#define K2_KXMASK 3
#define K3_ROT 1
#define STSC " sc1"
#define K2_KX0 0
#define K1_KXMASK 1
constexpr int S = 2048, D = 512, F = 1792, FH = 896;
constexpr size_t WS_AT = 0;
constexpr size_t WS_BT = WS_AT + (size_t)D * S * 2;
constexpr size_t WS_Z2T = WS_BT + (size_t)D * S * 2;
constexpr size_t WS_Z1 = WS_Z2T + (size_t)FH * S * 2;
constexpr size_t WS_H = WS_Z1 + (size_t)FH * S * 2;
constexpr size_t WS_Y = WS_H + (size_t)FH * S * 2;
constexpr size_t WS_WO = WS_Y + (size_t)2 * D * S * 2;
constexpr size_t WS_PAY = WS_WO + (size_t)FH * D * 2;
constexpr size_t WS_REQ = WS_PAY + 16 * 16 * 1024;
constexpr size_t WS_FLG = WS_REQ + 16 * 16 * 64;
constexpr size_t WS_FP = WS_FLG + 16 * 16 * 64;

__device__ __forceinline__ unsigned cvt_pk(float lo, float hi) {
  f32x2 v = {lo, hi};
  bf16x2v b = __builtin_convertvector(v, bf16x2v);
  return __builtin_bit_cast(unsigned, b);
}
__device__ __forceinline__ float bf_lo(unsigned u) { return __uint_as_float(u << 16); }
__device__ __forceinline__ float bf_hi(unsigned u) { return __uint_as_float(u & 0xffff0000u); }
__device__ __forceinline__ float dot2(unsigned a, unsigned b, float c) {
  return __builtin_amdgcn_fdot2_f32_bf16(__builtin_bit_cast(bf16x2v, a), __builtin_bit_cast(bf16x2v, b), c, false);
}
__device__ __forceinline__ float gelu_t(float v) {
  const float c1 = -2.0f * 0.7978845608028654f * 1.4426950408889634f;
  const float c2 = c1 * 0.044715f;
  float e = __builtin_amdgcn_exp2f(v * (c1 + c2 * v * v));
  return v * __builtin_amdgcn_rcpf(1.0f + e);
}
__device__ __forceinline__ float xsum16(float v) {
  v += __builtin_bit_cast(float, __builtin_amdgcn_update_dpp(0, __builtin_bit_cast(int, v), 0x128, 0xf, 0xf, false));
  v += __builtin_bit_cast(float, __builtin_amdgcn_update_dpp(0, __builtin_bit_cast(int, v), 0x124, 0xf, 0xf, false));
  v += __builtin_bit_cast(float, __builtin_amdgcn_update_dpp(0, __builtin_bit_cast(int, v), 0x122, 0xf, 0xf, false));
  v += __builtin_bit_cast(float, __builtin_amdgcn_update_dpp(0, __builtin_bit_cast(int, v), 0x121, 0xf, 0xf, false));
  return v;
}
__device__ __forceinline__ float xsumq(float v) {
  v += __shfl_xor(v, 16);
  v += __shfl_xor(v, 32);
  return v;
}
__device__ __forceinline__ int sigma(int e) { return (e & ~31) + (((e >> 2) & 1) << 4) + (((e >> 3) & 3) << 2) + (e & 3); }

struct K1Args {
  const u16* x; const u16* wdr; const u16* wdi; const u16* win; const u16* wout;
  char* ws;
};
template <int N> struct IC { static constexpr int value = N; };
constexpr int K1_LDS = 163840;
constexpr int K1_RING = 32768, K1_SCR = 49152, K1_P2 = 65536;

__device__ __forceinline__ f32x2 gelu2(f32x2 v) {
  const float c1 = -2.0f * 0.7978845608028654f * 1.4426950408889634f;
  const float c2 = c1 * 0.044715f;
  f32x2 w = (v * v) * c2 + c1;
  f32x2 t = v * w;
  f32x2 e = {__builtin_amdgcn_exp2f(t[0]), __builtin_amdgcn_exp2f(t[1])};
  e = e + 1.0f;
  f32x2 r = {__builtin_amdgcn_rcpf(e[0]), __builtin_amdgcn_rcpf(e[1])};
  return v * r;
}
__device__ __forceinline__ float sel4(f32x4 v, int i) { const float a = (i & 1) ? v[1] : v[0], b = (i & 1) ? v[3] : v[2]; return (i & 2) ? b : a; }
#define LDG_SC1_32(dst, ptr) asm volatile("global_load_dword %0, %1, off sc1" : "=v"(dst) : "v"(ptr) : "memory")
#define LDG_SC1_128(dst, ptr, off) asm volatile("global_load_dwordx4 %0, %1, off offset:%2 sc1" : "=v"(dst) : "v"(ptr), "n"(off))
#define STG_SC1_32(ptr, val) asm volatile("global_store_dword %0, %1, off sc1" ::"v"(ptr), "v"(val) : "memory")
#define STG_SC1_128(ptr, val) asm volatile("global_store_dwordx4 %0, %1, off sc1\n\ts_nop 1" ::"v"(ptr), "v"(val) : "memory")

__device__ __forceinline__ void GLDSI(__amdgpu_buffer_rsrc_t rsrc, unsigned mbase, int mimm, unsigned voff, unsigned soff) {
  asm volatile("s_add_i32 m0, %0, %1\n\ts_nop 0\n\tbuffer_load_dwordx4 %2, %3, %4 offen lds" ::"s"(mbase), "n"(mimm), "v"(voff), "s"(rsrc), "s"(soff) : "scc");
}
__device__ __forceinline__ void ldg_sc1_32(unsigned& dst, const unsigned* p) { LDG_SC1_32(dst, p); }
__device__ __forceinline__ void stg_sc1_32(unsigned* p, unsigned v) { STG_SC1_32(p, v); }
__device__ __forceinline__ void wait_v0() { WAIT_V(0); }
__device__ __forceinline__ void wait_vn(int n) { WAIT_V(n); }
__device__ __forceinline__ void gather14(__amdgpu_buffer_rsrc_t rP, unsigned voff) {
#pragma unroll
  for (int i = 0; i < 14; ++i)
    asm volatile("s_mov_b32 m0, %0\n\ts_nop 0\n\tbuffer_load_dwordx4 %1, %2, %3 offen offset:%4 sc1 lds" ::"n"(K1_P2 + (i >> 2) * 4096), "v"(voff), "s"(rP), "s"((unsigned)((i >> 2) * 4096)), "n"((i & 3) * 1024) : "memory");
}
__device__ __forceinline__ void word_to_lds(__amdgpu_buffer_rsrc_t r, int ldsaddr, unsigned voff, unsigned soff) {
  asm volatile("s_mov_b32 m0, %0\n\ts_nop 0\n\tbuffer_load_dword %1, %2, %3 offen sc1 lds" ::"n"(ldsaddr), "v"(voff), "s"(r), "s"(soff) : "memory");
}
__device__ __forceinline__ void bstore16(u32x4 v, __amdgpu_buffer_rsrc_t r, unsigned voff, unsigned soff) {
  asm volatile("buffer_store_dwordx4 %0, %1, %2, %3 offen" STSC "\n\ts_nop 1" ::"v"(v), "v"(voff), "s"(r), "s"(soff) : "memory");
}
__device__ __forceinline__ void pin(unsigned& x) { asm volatile("" : "+v"(x)); }
#define OPAQUE(x) asm volatile("" : "+v"(x))

template <bool ZB>
__device__ __forceinline__ void k1_body(const K1Args& a, int pm, int cb, int kx) {
  const int tid = threadIdx.x, lane = tid & 63;
  const int wid = __builtin_amdgcn_readfirstlane(tid >> 6);
  const int wm = wid & 3, wn = wid >> 2;
  STAMP_DECL STAMP(0);
  const int T0 = ZB ? 3 * cb : 42 + 11 * (cb - 14);
  const int TT = ZB ? T0 : T0 + 8;

  auto rX = __builtin_amdgcn_make_buffer_rsrc((void*)(a.x + (size_t)pm * 128 * D), 0, 128 * D * 2, 0x00020000);
  auto rW = ZB ? __builtin_amdgcn_make_buffer_rsrc((void*)(a.win + 64 * cb), 0, (D * F - 64 * cb) * 2, 0x00020000)
               : __builtin_amdgcn_make_buffer_rsrc((void*)(a.wdi + (size_t)(T0 - 32) * 16 * D), 0, 128 * D * 2, 0x00020000);
  const int r8 = lane >> 3, p8 = lane & 7;
  const unsigned sw_e = (p8 ^ (r8 >> 1)) << 4;
  const unsigned vx_e = (32 * (wid >> 1) + 4 * (wid & 1) + 8 * (r8 >> 2) + (r8 & 3)) * (D * 2) + sw_e;
  const unsigned vx_o = (vx_e ^ 64) + 16 * (D * 2);
  const unsigned vw2 = ZB ? (8 * wid + r8) * (F * 2) + ((p8 ^ ((((r8 >> 1) & 1) | ((wid & 1) << 1)) << 1)) << 4)
                          : (64 + 8 * wid + r8) * (D * 2) + (sw_e ^ ((wid & 1) << 6));
  const unsigned vw1 = ZB ? vw2 + FH * 2 : vw2 - 64 * (D * 2);
  constexpr unsigned WKB = ZB ? 64 * F * 2 : 128;
  const unsigned mb1 = wid * 1024, mb2 = wid * 2048;
  auto piece1 = [&](int st, int p) {
    const unsigned kb = st ^ kx;
    const int base = K1_RING + (st & 3) * 32768;
    if (p == 0) GLDSI(rX, mb2, base, vx_e, kb * 128);
    else if (p == 1) GLDSI(rW, mb1, base + 16384, vw1, kb * WKB);
    else if (p == 2) GLDSI(rX, mb2, base + 1024, vx_o, kb * 128);
    else GLDSI(rW, mb1, base + 24576, vw2, kb * WKB);
  };
  piece1(0, 0); piece1(0, 1); piece1(0, 2); piece1(0, 3);
  SCHED();

  unsigned f0 = 0, rq0 = 0;
  unsigned* const flg_in = (unsigned*)(a.ws + WS_FLG) + (pm * 16 + cb) * 16 + (lane & 15);
  unsigned* const req_in = (unsigned*)(a.ws + WS_REQ) + (pm * 16 + cb) * 16 + (lane & 15);
  auto piece3 = [&](int p) {
    const unsigned vr_e = r8 * (D * 2) + sw_e, vr_o = (vr_e ^ 64) + 8 * (D * 2);
    const int T = TT + (p >> 1);
    auto rD = __builtin_amdgcn_make_buffer_rsrc((void*)((T >= 32 ? a.wdi : a.wdr) + (size_t)(T & 31) * 16 * D), 0, 16 * D * 2, 0x00020000);
    GLDSI(rD, wid * 6144, p * 1024, (p & 1) ? vr_o : vr_e, (wid ^ kx) * 128);
  };
  auto rS = __builtin_amdgcn_make_buffer_rsrc((void*)(a.ws + WS_REQ + (size_t)(pm * 16 + cb) * 64), 0, (unsigned)(WS_FLG - WS_REQ) + 64, 0x00020000);

  const int fr = lane & 15, fq = lane >> 4;
  const int kh = wm >> 1;
  const unsigned fsw = ((fq ^ ((fr >> 1) & 7)) << 4) ^ (kh << 6);
  unsigned xa[2][2][2], wb[2][2][2];
#pragma unroll
  for (int m = 0; m < 2; ++m) { xa[0][m][0] = K1_RING + (32 * wm + 16 * (m ^ wn) + fr) * 128 + fsw; xa[0][m][1] = xa[0][m][0] ^ 64; }
#pragma unroll
  for (int n = 0; n < 2; ++n) {
    const int tile = 2 * wn + (n ^ (wm & 1));
    if (ZB) {
      const int q4 = fr >> 2, pp = lane & 3;
      const int sp = ((q4 >> 1) & 1) | ((fq & 1) << 1);
      const int ch = (2 * tile + (pp >> 1)) ^ (sp << 1);
      wb[0][n][0] = K1_RING + 16384 + 128 * (8 * fq + q4) + 16 * ch + 8 * (pp & 1) + kh * 4096;
      wb[0][n][1] = wb[0][n][0] ^ 4096;
    } else {
      wb[0][n][0] = K1_RING + 16384 + (16 * tile + fr) * 128 + fsw;
      wb[0][n][1] = wb[0][n][0] ^ 64;
    }
  }
#pragma unroll
  for (int i = 0; i < 2; ++i)
#pragma unroll
    for (int ks = 0; ks < 2; ++ks) {
      xa[1][i][ks] = xa[0][i][ks] + 65536; wb[1][i][ks] = wb[0][i][ks] + 65536;
      OPAQUE(xa[0][i][ks]); OPAQUE(wb[0][i][ks]); OPAQUE(xa[1][i][ks]); OPAQUE(wb[1][i][ks]);
    }
  auto ldw = [&](unsigned addr) -> bf16x8 {
    if (ZB) {
      s16x4 lo = __builtin_amdgcn_ds_read_tr16_b64_v4i16((LAS s16x4*)(size_t)(addr));
      s16x4 hi = __builtin_amdgcn_ds_read_tr16_b64_v4i16((LAS s16x4*)(size_t)(addr + 512));
      return __builtin_shufflevector(lo, hi, 0, 1, 2, 3, 4, 5, 6, 7);
    } else {
      return *(const LAS bf16x8*)(size_t)(addr);
    }
  };
  SCHED();
  piece1(1, 0); piece1(1, 1); piece1(1, 2); piece1(1, 3);
  if (ZB && wid == 7) word_to_lds(rS, 0, (lane & 15) * 4, (unsigned)(WS_FLG - WS_REQ));
  STAMP(1);

  bf16x8 xf[16][2], wf[2][4];
  const f32x4 Z4 = {0, 0, 0, 0};
  f32x4 acc1[2][2] = {{Z4, Z4}, {Z4, Z4}}, acc2[2][2] = {{Z4, Z4}, {Z4, Z4}}, acct[2][2] = {{Z4, Z4}, {Z4, Z4}};
  f32x4 gram = Z4, rsum = Z4, cs1 = Z4, cs2 = Z4;
  const bf16x8 ONES = {0x3f80, 0x3f80, 0x3f80, 0x3f80, 0x3f80, 0x3f80, 0x3f80, 0x3f80};

  auto mm = [&](int kk, int i) {
    const int ks = kk & 1;
    if (i == 0) gram = MFMA16(xf[kk][0], xf[kk][0], gram);
    else if (i == 1) rsum = MFMA16(xf[kk][0], ONES, rsum);
    else if (i < 6) { const int m = (i - 2) >> 1, n = i & 1; acc1[m][n] = MFMA16(xf[kk][m], wf[ks][n], acc1[m][n]); }
    else if (i < 10) { const int m = (i - 6) >> 1, n = i & 1; acc2[m][n] = ZB ? MFMA16(wf[ks][2 + n], xf[kk][m], acc2[m][n]) : MFMA16(xf[kk][m], wf[ks][2 + n], acc2[m][n]); }
    else if (i == 10) cs1 = MFMA16(ONES, wf[0][0], cs1);
    else cs2 = MFMA16(ONES, wf[0][2], cs2);
  };
  auto rd = [&](int kk, int i) {
    const int st = kk >> 1, ks = kk & 1, wi = (st >> 1) & 1, so = (st & 1) * 32768;
    if (i == 0 || i == 3) xf[kk][i & 1] = *(const LAS bf16x8*)(size_t)(xa[wi][i & 1][ks] + so);
    else if (i < 3) wf[ks][i - 1] = ldw(wb[wi][i - 1][ks] + so);
    else wf[ks][i - 2] = ldw(wb[wi][i - 4][ks] + so + 8192);
  };
#pragma unroll
  for (int st = 0; st < 8; ++st) {
    wait_vn(st <= 6 ? 4 : 6);
    WL0();
    BAR();
    if (st == 0) STAMPF(2);
    SCHED();
#pragma unroll
    for (int i = 0; i < 10; ++i) {
      if (st > 0) mm(2 * st - 1, i);
      SCHED();
      if (i < 6) rd(2 * st, i);
      if (st + 2 < 8) { if (i == 3) piece1(st + 2, 0); if (i == 8) piece1(st + 2, 1); }
      if (st == 6) { if (i == 2) piece3(0); if (i == 6) piece3(1); if (i == 9) piece3(2); }
      SCHED();
    }
    WL0();
    SCHED();
#pragma unroll
    for (int i = 0; i < 12; ++i) {
      mm(2 * st, i);
      SCHED();
      if (i < 6) rd(2 * st + 1, i);
      if (st + 2 < 8) { if (i == 3) piece1(st + 2, 2); if (i == 9) piece1(st + 2, 3); }
      if (st == 6) { if (i == 3) piece3(3); if (i == 7) piece3(4); if (i == 10) piece3(5); }
      SCHED();
    }
    if (ZB && st == 3 && wid == 7) {
      f0 = *(LAS unsigned*)(size_t)(lane * 4) + 1;
      stg_sc1_32((unsigned*)(a.ws + WS_REQ) + (pm * 16 + (lane & 15)) * 16 + cb, f0);
    }
    if (ZB && st == 7 && wid == 7) word_to_lds(rS, K1_SCR + 7168, (lane & 15) * 4, 0);
    SCHED();
  }
#pragma unroll
  for (int i = 0; i < 10; ++i) mm(15, i);
  STAMP(3);
  unsigned sZ0 = ((pm * 8 + 2 * wm) * 112 + 8 * cb + 4 * wn) * 256 + 128 * wn, sZ1 = sZ0 ^ 128;
  asm volatile("" : "+s"(sZ0), "+s"(sZ1));
  LAS float* const s_r = (LAS float*)(size_t)(K1_SCR);
  LAS float* const s_c = (LAS float*)(size_t)(K1_SCR + 512);
  LAS float* const s_u1 = (LAS float*)(size_t)(K1_SCR + 1024);
  LAS float* const s_u2 = (LAS float*)(size_t)(K1_SCR + 1536);
  LAS float* const s_z = (LAS float*)(size_t)(K1_SCR + 2048);
  LAS float* const s_n = (LAS float*)(size_t)(K1_SCR + 4096);
  LAS float* const s_u3 = (LAS float*)(size_t)(K1_SCR + 5120);
  {
    const float s1 = sel4(rsum, fr & 3), s2 = sel4(gram, fr & 3);
    const float mean = s1 * (1.0f / D);
    const float rstd = __builtin_amdgcn_rsqf(fmaxf(s2 * (1.0f / D) - mean * mean, 0.0f) + 1e-5f);
    if ((fr >> 2) == fq) { s_r[32 * wm + 16 * wn + fr] = rstd; s_c[32 * wm + 16 * wn + fr] = -mean * rstd; }
    if (fq < 2) s_u1[128 * fq + 64 * kh + 16 * (2 * wn + (wm & 1)) + fr] = fq ? cs2[0] : cs1[0];
  }
  const int s0 = pm * 128 + 32 * wm;
  auto rT = __builtin_amdgcn_make_buffer_rsrc((void*)(a.ws + WS_AT), 0, (2 * D + FH) * S * 2, 0x00020000);
  const unsigned vT = ((fr >> 2) & 1) * 65536 + fq * 256 + (4 * (fr >> 3) + (fr & 3)) * 16;
  const unsigned tb0 = fr * 128 + fsw, tb1 = tb0 ^ 64;
  WAIT_V(0);
  WL0();
  BAR();
  STAMP(4);
  f32x4 rq[2], cq[2];
  auto ld_rc = [&]() {
#pragma unroll
    for (int m = 0; m < 2; ++m) {
      rq[m] = *(LAS f32x4*)(s_r + 32 * wm + 16 * (m ^ wn) + 4 * fq);
      cq[m] = *(LAS f32x4*)(s_c + 32 * wm + 16 * (m ^ wn) + 4 * fq);
    }
  };
  ld_rc();
  auto store_t = [&](int T, f32x4 v0, f32x4 v1) {
    const unsigned a0 = cvt_pk(v0[0], v0[1]), a1 = cvt_pk(v0[2], v0[3]), b0 = cvt_pk(v1[0], v1[1]), b1 = cvt_pk(v1[2], v1[3]);
    u32x4 o = wn ? u32x4{b0, b1, a0, a1} : u32x4{a0, a1, b0, b1};
    bstore16(o, rT, vT, (T >> 1) * 131072 + (T & 1) * 128 + s0 * 32);
  };
  auto lin_store = [&](f32x4 c0, f32x4 c1, float u, int T) { store_t(T, rq[0] * c0 + cq[0] * u, rq[1] * c1 + cq[1] * u); };
  float u1[2];
#pragma unroll
  for (int n = 0; n < 2; ++n) { const int ui = 16 * (2 * wn + (n ^ (wm & 1))) + fr; u1[n] = s_u1[ui] + s_u1[64 + ui]; }
  {
    const unsigned cb0 = tb0 + wid * 6144, cb1 = cb0 ^ 64;
    f32x4 cs3[3];
#pragma unroll
    for (int t = 0; t < 3; ++t) {
      cs3[t] = MFMA16(ONES, *(const LAS bf16x8*)(size_t)(cb0 + t * 2048), Z4);
      cs3[t] = MFMA16(ONES, *(const LAS bf16x8*)(size_t)(cb1 + t * 2048), cs3[t]);
    }
    if (ZB) {
      f32x4 ps[2], pq[2];
#pragma unroll
      for (int n = 0; n < 2; ++n)
#pragma unroll
        for (int m = 0; m < 2; ++m) {
          f32x4 v = rq[m] * acc1[m][n] + cq[m] * u1[n];
          f32x2 g0 = gelu2(f32x2{v[0], v[1]}), g1 = gelu2(f32x2{v[2], v[3]});
          v = f32x4{g0[0], g0[1], g1[0], g1[1]};
          acc1[m][n] = v;
          if (n == 0) { ps[m] = v; pq[m] = v * v; } else { ps[m] += v; pq[m] += v * v; }
        }
      float t[8], h[4];
#define DPP2(d, x, y, ctl, m0, m1) "v_add_f32_dpp " d ", " x ", " x " " ctl " row_mask:0xf bank_mask:" m0 "\n\tv_add_f32_dpp " d ", " y ", " y " " ctl " row_mask:0xf bank_mask:" m1 "\n\t"
      asm volatile("s_nop 1\n\t" DPP2("%0", "%8", "%16", "row_ror:8", "0x3", "0xc") DPP2("%1", "%9", "%17", "row_ror:8", "0x3", "0xc")
                   DPP2("%2", "%10", "%18", "row_ror:8", "0x3", "0xc") DPP2("%3", "%11", "%19", "row_ror:8", "0x3", "0xc")
                   DPP2("%4", "%12", "%20", "row_ror:8", "0x3", "0xc") DPP2("%5", "%13", "%21", "row_ror:8", "0x3", "0xc")
                   DPP2("%6", "%14", "%22", "row_ror:8", "0x3", "0xc") DPP2("%7", "%15", "%23", "row_ror:8", "0x3", "0xc") "s_nop 1"
                   : "=&v"(t[0]), "=&v"(t[1]), "=&v"(t[2]), "=&v"(t[3]), "=&v"(t[4]), "=&v"(t[5]), "=&v"(t[6]), "=&v"(t[7])
                   : "v"(ps[0][0]), "v"(pq[0][0]), "v"(ps[0][1]), "v"(pq[0][1]), "v"(ps[0][2]), "v"(pq[0][2]), "v"(ps[0][3]), "v"(pq[0][3]),
                     "v"(ps[1][0]), "v"(pq[1][0]), "v"(ps[1][1]), "v"(pq[1][1]), "v"(ps[1][2]), "v"(pq[1][2]), "v"(ps[1][3]), "v"(pq[1][3]));
      asm volatile("s_nop 1\n\t" DPP2("%0", "%4", "%8", "row_half_mirror", "0x5", "0xa") DPP2("%1", "%5", "%9", "row_half_mirror", "0x5", "0xa")
                   DPP2("%2", "%6", "%10", "row_half_mirror", "0x5", "0xa") DPP2("%3", "%7", "%11", "row_half_mirror", "0x5", "0xa") "s_nop 1\n\t"
                   "v_add_f32_dpp %0, %0, %0 quad_perm:[1,0,3,2] row_mask:0xf bank_mask:0xf\n\tv_add_f32_dpp %1, %1, %1 quad_perm:[1,0,3,2] row_mask:0xf bank_mask:0xf\n\t"
                   "v_add_f32_dpp %2, %2, %2 quad_perm:[1,0,3,2] row_mask:0xf bank_mask:0xf\n\tv_add_f32_dpp %3, %3, %3 quad_perm:[1,0,3,2] row_mask:0xf bank_mask:0xf\n\t"
                   "v_add_f32_dpp %0, %0, %0 quad_perm:[2,3,0,1] row_mask:0xf bank_mask:0xf\n\tv_add_f32_dpp %1, %1, %1 quad_perm:[2,3,0,1] row_mask:0xf bank_mask:0xf\n\t"
                   "v_add_f32_dpp %2, %2, %2 quad_perm:[2,3,0,1] row_mask:0xf bank_mask:0xf\n\tv_add_f32_dpp %3, %3, %3 quad_perm:[2,3,0,1] row_mask:0xf bank_mask:0xf"
                   : "=&v"(h[0]), "=&v"(h[1]), "=&v"(h[2]), "=&v"(h[3])
                   : "v"(t[0]), "v"(t[1]), "v"(t[2]), "v"(t[3]), "v"(t[4]), "v"(t[5]), "v"(t[6]), "v"(t[7]));
      if ((fr & 3) == 0) *(LAS f32x4*)(s_z + (wn * 128 + 32 * wm + 8 * fq + 4 * ((fr >> 3) ^ wn) + 2 * ((fr >> 2) & 1)) * 2) = f32x4{h[0], h[1], h[2], h[3]};
    } else {
#pragma unroll
      for (int n = 0; n < 2; ++n) {
        const int tile = 2 * wn + (n ^ (wm & 1)), ui = 16 * tile + fr;
        lin_store(acc1[0][n], acc1[1][n], u1[n], T0 + tile);
        lin_store(acc2[0][n], acc2[1][n], s_u2[ui] + s_u2[64 + ui], T0 + 4 + tile);
      }
    }
    if (fq < 3) s_u3[(16 * fq + fr) * 8 + wid] = fq == 0 ? cs3[0][0] : fq == 1 ? cs3[1][0] : cs3[2][0];
  }
  WL0();
  BAR();
  STAMP(5);
  unsigned* const flg_out = (unsigned*)(a.ws + WS_FLG) + (pm * 16 + (lane & 15)) * 16 + cb;
  if (ZB && wid == 7) {
    __builtin_amdgcn_s_setprio(2);
    f32x4 p = *(LAS f32x4*)(s_z + lane * 4) + *(LAS f32x4*)(s_z + 256 + lane * 4);
    STG_SC1_128((float*)(a.ws + WS_PAY) + (pm * 16 + cb) * 256 + lane * 4, p);
  }
  STAMP(6);
  f32x2 gv[2], gt[2];
  auto gelu_item = [&](int it, auto&& pre, auto&& post) {
    const int p = (it / 10) * 2 + (it & 1), sg = (it % 10) >> 1, b = it & 1;
    const float c1 = -2.0f * 0.7978845608028654f * 1.4426950408889634f, c2 = c1 * 0.044715f;
    if (sg == 0) gv[b] = pre(p);
    else if (sg == 1) gt[b] = gv[b] * ((gv[b] * gv[b]) * c2 + c1);
    else if (sg == 2) gt[b] = f32x2{__builtin_amdgcn_exp2f(gt[b][0]), __builtin_amdgcn_exp2f(gt[b][1])};
    else if (sg == 3) { const f32x2 d = gt[b] + 1.0f; gt[b] = f32x2{__builtin_amdgcn_rcpf(d[0]), __builtin_amdgcn_rcpf(d[1])}; }
    else post(p, gv[b] * gt[b]);
  };
  auto pass3 = [&](auto NT, auto&& fill) {
    constexpr int t0 = NT.value == 2 ? 0 : 2;
    constexpr int NF = 16 * NT.value, PD = 4;
    bf16x8 w[8];
    auto rd3 = [&](int f) {
      const int kk = f / NT.value, n = f % NT.value;
      w[f & 7] = *(const LAS bf16x8*)(size_t)(((kk & 1) ? tb1 : tb0) + (kk >> 1) * 6144 + (t0 + n) * 2048);
    };
#pragma unroll
    for (int f = 0; f < PD; ++f) rd3(f);
    float u3[NT.value];
#pragma unroll
    for (int n = 0; n < NT.value; ++n) {
      const f32x4 uu = *(LAS f32x4*)(s_u3 + (16 * (t0 + n) + fr) * 8) + *(LAS f32x4*)(s_u3 + (16 * (t0 + n) + fr) * 8 + 4);
      u3[n] = (uu[0] + uu[1]) + (uu[2] + uu[3]);
    }
    SCHED();
#pragma unroll
    for (int f = 0; f < NF; ++f) {
      const int kk = f / NT.value, n = f % NT.value;
      acct[0][n] = MFMA16(xf[kk][0], w[f & 7], acct[0][n]);
      SCHED();
      if (f + PD < NF) rd3(f + PD);
      if (ZB && f == NF - 3) ld_rc();
      fill(2 * f);
      SCHED();
      acct[1][n] = MFMA16(xf[kk][1], w[f & 7], acct[1][n]);
      SCHED();
      fill(2 * f + 1);
      SCHED();
    }
#pragma unroll
    for (int n = 0; n < NT.value; ++n) lin_store(acct[0][n], acct[1][n], u3[n], TT + t0 + n);
  };
  auto rP = __builtin_amdgcn_make_buffer_rsrc((void*)(a.ws + WS_PAY + (size_t)pm * 16 * 1024), 0, 14 * 1024, 0x00020000);
  if (ZB) {
    auto rZ = __builtin_amdgcn_make_buffer_rsrc((void*)(a.ws + WS_Z1), 0, FH * S * 2, 0x00020000);
    const unsigned vZ = (fr >> 3) * (112 * 256) + (2 * ((fq & 1) ^ (wm & 1)) + (fq >> 1)) * 256 + (fr & 7) * 16;
    f32x4 u[2];
    float r[2], c[2];
#pragma unroll
    for (int n = 0; n < 2; ++n) {
      const int ui = 16 * (2 * wn + (n ^ (wm & 1))) + 4 * fq;
      u[n] = *(LAS f32x4*)(s_u2 + ui) + *(LAS f32x4*)(s_u2 + 64 + ui);
      r[n] = s_r[32 * wm + 16 * (n ^ wn) + fr]; c[n] = s_c[32 * wm + 16 * (n ^ wn) + fr];
    }
    unsigned pk[2][2];
    auto pre = [&](int p) -> f32x2 {
      const int m = p >> 2, n = (p >> 1) & 1, h = p & 1;
      return f32x2{acc2[m][n][2 * h], acc2[m][n][2 * h + 1]} * r[m] + f32x2{u[n][2 * h], u[n][2 * h + 1]} * c[m];
    };
    auto post = [&](int p, f32x2 g) {
      const int m = p >> 2, n = (p >> 1) & 1, h = p & 1;
      pk[n][h] = cvt_pk(g[0], g[1]);
      if ((p & 3) == 3) {
        auto w0 = __builtin_amdgcn_permlane16_swap(pk[0][0], pk[1][0], false, false);
        auto w1 = __builtin_amdgcn_permlane16_swap(pk[0][1], pk[1][1], false, false);
        bstore16(u32x4{w0[0], w1[0], w0[1], w1[1]}, rZ, vZ, m ? sZ1 : sZ0);
      }
    };
    int itn = 0;
    if (wn == 0) {
      pass3(IC<2>{}, [&](int slot) { if (slot >= 2 && itn < 40) gelu_item(itn++, pre, post); });
    } else {
      pass3(IC<1>{}, [&](int slot) {
        if (slot >= 2 && itn < 40) gelu_item(itn++, pre, post);
        if (slot >= 12 && slot < 22) gelu_item(itn++, pre, post);
        if (slot == 10 && wid == 7) {
          rq0 = *(LAS unsigned*)(size_t)(K1_SCR + 7168 + lane * 4);
          wait_v0();
          stg_sc1_32(flg_out, rq0);
        }
      });
    }
  } else {
    auto fill = [&](int) {};
    if (wn == 0) pass3(IC<2>{}, fill); else pass3(IC<1>{}, fill);
  }
  STAMP(7);
  if (ZB) {
    if (wid == 7) {
      for (unsigned spin = 0; spin < (1u << 22); ++spin) {
        unsigned fl, rr;
        LDG_SC1_32(fl, flg_in);
        LDG_SC1_32(rr, req_in);
        asm volatile("s_waitcnt vmcnt(0)" : "+v"(fl), "+v"(rr)::"memory");
        if (__all((lane & 15) >= 14 || fl == f0)) break;
        if (__any(rr != rq0)) { rq0 = rr; STG_SC1_32(flg_out, rq0); }
      }
      unsigned rr0;
      LDG_SC1_32(rr0, req_in);
      const float* pay = (const float*)(a.ws + WS_PAY) + pm * 16 * 256 + lane * 4;
      f32x4 pv[14];
#pragma unroll
      for (int i = 0; i < 14; ++i) { const float* pp = pay + (i >> 2) * 1024; LDG_SC1_128(pv[i], pp, (i & 3) * 1024); }
      asm volatile("s_waitcnt vmcnt(0)" : "+v"(rr0), "+v"(pv[0]), "+v"(pv[1]), "+v"(pv[2]), "+v"(pv[3]), "+v"(pv[4]), "+v"(pv[5]), "+v"(pv[6]), "+v"(pv[7]), "+v"(pv[8]),
                   "+v"(pv[9]), "+v"(pv[10]), "+v"(pv[11]), "+v"(pv[12]), "+v"(pv[13])::"memory");
      if (__any(rr0 != rq0)) STG_SC1_32(flg_out, rr0);
      f32x4 t = ((pv[0] + pv[1]) + (pv[2] + pv[3])) + ((pv[4] + pv[5]) + (pv[6] + pv[7])) + (((pv[8] + pv[9]) + (pv[10] + pv[11])) + (pv[12] + pv[13]));
      const float m0 = t[0] * (1.0f / FH), m1 = t[2] * (1.0f / FH);
      const float r0 = __builtin_amdgcn_rsqf(fmaxf(t[1] * (1.0f / FH) - m0 * m0, 0.0f) + 1e-5f);
      const float r1 = __builtin_amdgcn_rsqf(fmaxf(t[3] * (1.0f / FH) - m1 * m1, 0.0f) + 1e-5f);
      *(LAS f32x2*)(s_n + 2 * lane) = f32x2{r0, r1};
      *(LAS f32x2*)(s_n + 128 + 2 * lane) = f32x2{-m0 * r0, -m1 * r1};
    }
    WL0();
    BAR();
    STAMP(8);
    f32x4 r2[2], c2[2];
#pragma unroll
    for (int m = 0; m < 2; ++m) {
      r2[m] = *(LAS f32x4*)(s_n + 32 * wm + 8 * fq + 4 * (m ^ wn));
      c2[m] = *(LAS f32x4*)(s_n + 128 + 32 * wm + 8 * fq + 4 * (m ^ wn));
    }
#pragma unroll
    for (int n = 0; n < 2; ++n) store_t(64 + 4 * cb + 2 * wn + (n ^ (wm & 1)), acc1[0][n] * r2[0] + c2[0], acc1[1][n] * r2[1] + c2[1]);
  } else {
    STAMP(8);
    const int sbk = pm * 2 + (cb - 14);
    if (sbk < 28) {
      u16* wo = (u16*)(a.ws + WS_WO);
      const int ep = tid & 255;
      unsigned v[2][8];
#pragma unroll
      for (int cc = 0; cc < 2; ++cc)
#pragma unroll
        for (int r = 0; r < 8; ++r) v[cc][r] = *(const unsigned*)(a.wout + (size_t)((sbk * 4 + wn * 2 + cc) * 8 + r) * D + 2 * ep);
      const int rho = sigma(2 * ep);
#pragma unroll
      for (int cc = 0; cc < 2; ++cc) {
        u32x4 lo, hi;
#pragma unroll
        for (int r = 0; r < 4; ++r) {
          lo[r] = (v[cc][2 * r] & 0xffffu) | (v[cc][2 * r + 1] << 16);
          hi[r] = (v[cc][2 * r] >> 16) | (v[cc][2 * r + 1] & 0xffff0000u);
        }
        u16* dst = wo + ((size_t)((rho >> 4) * 112 + sbk * 4 + wn * 2 + cc) * 16 + (rho & 15)) * 8;
        *(u32x4*)dst = lo;
        *(u32x4*)(dst + 8) = hi;
      }
    } else if (sbk == 31 && wid == 0) {
      *(u32x4*)(a.ws + WS_FP + lane * 16) = u32x4{0, 0, 0, 0};
    }
  }
  STAMP(9);
  STAMP_OUT(a.ws, 0, 10);
}

__global__ __launch_bounds__(512, 2) void k1(K1Args a) {
  extern __shared__ __attribute__((aligned(1024))) char smem[];
  asm volatile("" ::"v"(smem));
  const int x = blockIdx.x & 7, i = blockIdx.x >> 3;
  const int pm = 4 * (x >> 1) + (i >> 3), cb = 2 * (i & 7) + (x & 1);
  const int kx = ((i & 7) ^ (2 * (i >> 3))) & K1_KXMASK;
  if (cb >= 14) k1_body<false>(a, pm, cb, kx);
  else k1_body<true>(a, pm, cb, kx);
}

struct K2Args {
  const u16* wsr; const u16* wsi; const u16* wsp; const u16* bsp; const u16* x;
  char* ws; u16* out;
};
constexpr int K2_STAGE = 32768, K2_NKT = S / 64, K2_LDS = 163840, K2_E = 131072, K2_X = 114688;

__device__ __forceinline__ void bstore16_sc1(u32x4 v, __amdgpu_buffer_rsrc_t r, unsigned voff, unsigned soff) {
  asm volatile("buffer_store_dwordx4 %0, %1, %2, %3 offen sc1\n\ts_nop 1" ::"v"(v), "v"(voff), "s"(r), "s"(soff) : "memory");
}

__global__ __launch_bounds__(512, 2) void k2(K2Args a) {
  extern __shared__ __attribute__((aligned(1024))) char smem[];
  asm volatile("" ::"v"(smem));
  const int tid = threadIdx.x, lane = tid & 63;
  const int wid = __builtin_amdgcn_readfirstlane(tid >> 6);
  STAMP_DECL STAMP(0);
  const int bx = blockIdx.x & 7, bi = blockIdx.x >> 3;
  int mat, tm, tn;
  if (bx < 4) { mat = bx >> 1; tm = (bx & 1) * 8 + (bi >> 2); tn = bi & 3; }
  else { mat = bi >= 28 ? 3 : 2; tm = (bx - 4) * 4 + (bi / 7); tn = bi % 7; }
  const int tm3 = bx * 4 + (bi >> 3), tn3 = bi & 7;
  const int fr = lane & 15, fq = lane >> 4;
  const unsigned vl = lane * 16;
  typedef const LAS bf16x8* lp;
  if (wid >= 4) {
    const int lw = wid - 4;
    auto rWo = __builtin_amdgcn_make_buffer_rsrc((void*)(a.ws + WS_WO + (size_t)tn3 * 4 * 112 * 256), 0, 4 * 112 * 256, 0x00020000);
    auto wpieces = [&](int sl) {
      const unsigned b = sl * 32768 + lw * (sl < 3 ? 8192 : 4096);
#pragma unroll
      for (int j = 0; j < (sl < 3 ? 8 : 4); ++j) GLDSI(rWo, b, j * 1024, vl, b + j * 1024);
    };
    if (mat < 3) {
      const u16* Pp = (mat == 0 ? a.wsr : mat == 1 ? a.wsi : a.wsp) + (size_t)tm * 128 * S;
      auto rP = __builtin_amdgcn_make_buffer_rsrc((void*)Pp, 0, 128 * S * 2, 0x00020000);
      auto rQ = __builtin_amdgcn_make_buffer_rsrc((void*)(a.ws + WS_AT + (size_t)(mat * 4 + tn) * 128 * S * 2), 0, 128 * S * 2, 0x00020000);
      const int kx = (tm ^ tn) & K2_KXMASK;
      const int r8 = lane >> 3, p = lane & 7;
      const unsigned vo_e = (lw * 32 + r8) * (S * 2) + ((p ^ (r8 >> 1)) << 4);
      const unsigned vo_o = (vo_e ^ 64) + 8 * (S * 2);
      const unsigned vq = vl + lw * 131072;
      auto stage = [&](int kt, int slot) {
        const unsigned kk = K2_KX0 && kt < 4 ? kt : kt ^ kx;
        const unsigned mb = slot * K2_STAGE + lw * 4096;
#pragma unroll
        for (int j = 0; j < 4; ++j) {
          GLDSI(rP, mb, j * 1024, (j & 1) ? vo_o : vo_e, kk * 128 + (j >> 1) * 16 * (S * 2));
          GLDSI(rQ, mb, 16384 + j * 1024, vq, kk * 2048 + (j >> 1) * 65536 + (j & 1) * 1024);
        }
      };
      stage(0, 0); stage(1, 1);
      WAIT_V(8); BAR();
      stage(2, 2); stage(3, 3);
      int slot = 0;
      for (int kt = 0; kt < K2_NKT - 4; ++kt) {
        WAIT_V(16);
        BAR();
        stage(kt + 4, slot);
        slot = (slot + 1) & 3;
      }
      if (mat == 2) {
        auto rZ = __builtin_amdgcn_make_buffer_rsrc((void*)(a.ws + WS_Z1 + (size_t)((8 * tm + 2 * lw) * 112 + 16 * tn) * 256), 0, 112 * 256 + 4096, 0x00020000);
#pragma unroll
        for (int j = 0; j < 8; ++j) GLDSI(rZ, lw * 8192, K2_E + j * 1024, vl, (j >> 2) * (112 * 256) + (j & 3) * 1024);
        WAIT_V(24); BAR(); wpieces(0);
        WAIT_V(24); BAR(); wpieces(1);
        WAIT_V(24); BAR(); wpieces(2);
        WAIT_V(24); BAR(); wpieces(3);
      } else {
        WAIT_V(16); BAR(); wpieces(0);
        WAIT_V(16); BAR(); wpieces(1);
        WAIT_V(16); BAR(); wpieces(2);
        BAR(); wpieces(3);
      }
    } else {
      wpieces(0); wpieces(1); wpieces(2); wpieces(3);
    }
    STAMP(1); STAMP(2); STAMP(3);
  } else if (mat < 3) {
    const int wp = wid >> 1, wq = wid & 1;
    f32x4 acc[4][4];
#pragma unroll
    for (int m = 0; m < 4; ++m)
#pragma unroll
      for (int n = 0; n < 4; ++n) acc[m][n] = f32x4{0, 0, 0, 0};
    unsigned bv[4];
#pragma unroll
    for (int ni = 0; ni < 4; ++ni) bv[ni] = a.bsp[(8 * tm + 4 * wp + ni) * 16 + fr];
    const int sw = (fr >> 1) & 7;
    unsigned ra[2][2], rb[2];
    ra[0][0] = (wp * 64 + fr) * 128 + ((fq ^ sw) << 4);
    rb[0] = 16384 + wq * 8192 + vl;
    ra[0][1] = ra[0][0] ^ 64;
    ra[1][0] = ra[0][0] + 65536; ra[1][1] = ra[0][1] + 65536; rb[1] = rb[0] + 65536;
    asm volatile("" : "+v"(ra[1][0]), "+v"(ra[1][1]), "+v"(rb[1]), "+v"(bv[0]), "+v"(bv[1]), "+v"(bv[2]), "+v"(bv[3]));
    bf16x8 a0[4], b0[4], a1[4], b1[4];
#define K2_RD(AF, BF, sl, ks, i) do { const unsigned o_ = ((sl) & 1) * K2_STAGE; \
    if ((i) == 0) AF[0] = *(lp)(size_t)(ra[(sl) >> 1][ks] + o_); \
    else if ((i) <= 4) BF[(i) - 1] = *(lp)(size_t)(rb[(sl) >> 1] + o_ + ((i) - 1) * 2048 + (ks) * 1024); \
    else AF[(i) - 4] = *(lp)(size_t)(ra[(sl) >> 1][ks] + o_ + ((i) - 4) * 2048); } while (0)
#define K2_MM(AF, BF, i) acc[(i) >> 2][(i) & 3] = MFMA16(BF[(i) & 3], AF[(i) >> 2], acc[(i) >> 2][(i) & 3])
#define K2_STEP(s, LAST) do { \
    SCHED(); WL0(); SCHED(); \
    _Pragma("unroll") for (int i = 0; i < 16; ++i) { K2_MM(a0, b0, i); SCHED(); if (i < 8) K2_RD(a1, b1, s, 1, i); SCHED(); } \
    WL0(); \
    BAR(); \
    SCHED(); \
    _Pragma("unroll") for (int i = 0; i < 16; ++i) { K2_MM(a1, b1, i); SCHED(); if (!(LAST) && i < 8) K2_RD(a0, b0, ((s) + 1) & 3, 0, i); SCHED(); } \
  } while (0)
    BAR();
    STAMP(1);
#pragma unroll
    for (int i = 0; i < 8; ++i) K2_RD(a0, b0, 0, 0, i);
    for (int kt = 0; kt < K2_NKT - 4; kt += 4) { K2_STEP(0, 0); K2_STEP(1, 0); K2_STEP(2, 0); K2_STEP(3, 0); }
    K2_STEP(0, 0); K2_STEP(1, 0); K2_STEP(2, 0); K2_STEP(3, 1);
    SCHED();
    STAMP(2);
    if (mat < 2) {
      auto rY = __builtin_amdgcn_make_buffer_rsrc((void*)(a.ws + WS_Y + (size_t)mat * S * D * 2), 0, S * D * 2, 0x00020000);
      const unsigned so = ((8 * tm + 4 * wp) * 64 + 16 * tn + 8 * wq) * 256;
#pragma unroll
      for (int ni = 0; ni < 4; ++ni)
#pragma unroll
        for (int P = 0; P < 2; ++P) {
          const f32x4 c0 = acc[ni][2 * P], c1 = acc[ni][2 * P + 1];
          u32x4 v = {cvt_pk(c0[0], c0[1]), cvt_pk(c0[2], c0[3]), cvt_pk(c1[0], c1[1]), cvt_pk(c1[2], c1[3])};
          bstore16_sc1(v, rY, vl, so + (ni * 64 + 4 * P) * 256);
        }
    } else {
      auto rH = __builtin_amdgcn_make_buffer_rsrc((void*)(a.ws + WS_H), 0, S * FH * 2, 0x00020000);
      const unsigned so = ((8 * tm + 4 * wp) * 112 + 16 * tn + 8 * wq) * 256;
      const unsigned za = K2_E + wp * 16384 + wq * 2048 + (fq * 16 + ((fr & 4) << 1) + ((fr & 8) >> 1) + (fr & 3)) * 16;
#pragma unroll
      for (int ni = 0; ni < 4; ++ni) {
        const float bias = __uint_as_float(bv[ni] << 16);
#pragma unroll
        for (int P = 0; P < 2; ++P) {
          const f32x4 c0 = acc[ni][2 * P] + bias, c1 = acc[ni][2 * P + 1] + bias;
          const u32x4 z = *(const LAS u32x4*)(size_t)(za + ni * 4096 + P * 1024);
          u32x4 v = {cvt_pk(bf_lo(z[0]) * c0[0], bf_hi(z[0]) * c0[1]), cvt_pk(bf_lo(z[1]) * c0[2], bf_hi(z[1]) * c0[3]),
                     cvt_pk(bf_lo(z[2]) * c1[0], bf_hi(z[2]) * c1[1]), cvt_pk(bf_lo(z[3]) * c1[2], bf_hi(z[3]) * c1[3])};
          bstore16_sc1(v, rH, vl, so + (ni * 112 + 4 * P) * 256);
        }
      }
    }
    STAMP(3);
    WAIT_V(0);
  } else {
    STAMP(1); STAMP(2); STAMP(3);
  }
  const int ti = wid & 3, kh = wid >> 2;
  const int tt3 = 4 * tm3 + ti, c3 = 8 * tn3 + 4 * kh;
  const u32x4 ex = *(const u32x4*)(a.x + (size_t)(tt3 * 16 + fr) * D + (c3 + fq) * 8);
  BAR();
  STAMP(4);
  if (wid == 4) {
    unsigned* fp = (unsigned*)(a.ws + WS_FP);
    if (mat < 3 && lane == 0) STG_SC1_32(fp + tm * 16 + (mat == 2 ? tn : 8 + 4 * mat + tn), 1u);
    const unsigned* pp = fp + (tm3 >> 1) * 16 + fr;
    const bool need = fr < 7 || (fr >= 8 && (fr & 3) == (tn3 >> 1));
    for (unsigned spin = 0; spin < (1u << 22); ++spin) {
      unsigned fl;
      LDG_SC1_32(fl, pp);
      asm volatile("s_waitcnt vmcnt(0)" : "+v"(fl)::"memory");
      if (__all(!need || fl == 1u)) break;
      __builtin_amdgcn_s_sleep(1);
    }
  }
  if (wid >= 4) WAIT_V(0);
  BAR();
  STAMP(5);
  {
    auto rH = __builtin_amdgcn_make_buffer_rsrc((void*)(a.ws + WS_H + (size_t)(tt3 * 112 + 56 * kh) * 256), 0, 56 * 256, 0x00020000);
    auto rY = __builtin_amdgcn_make_buffer_rsrc((void*)(a.ws + WS_Y + (size_t)(tt3 * 64 + c3) * 256), 0, S * D * 2 + 1024, 0x00020000);
    u32x4 hf[14];
#pragma unroll
    for (int k = 0; k < 14; ++k) hf[k] = __builtin_amdgcn_raw_buffer_load_b128(rH, vl + (k & 3) * 1024, (k >> 2) * 4096, 16);
    const u32x4 e1 = __builtin_amdgcn_raw_buffer_load_b128(rY, vl, 0, 16), e2 = __builtin_amdgcn_raw_buffer_load_b128(rY, vl, S * D * 2, 16);
    SCHED();
    const unsigned wa0 = kh * (2 * 28672 + 14336) + vl, wa1 = (kh ^ 1) * (2 * 28672) + kh * 14336 + vl;
    f32x4 acc[4] = {f32x4{0, 0, 0, 0}, f32x4{0, 0, 0, 0}, f32x4{0, 0, 0, 0}, f32x4{0, 0, 0, 0}};
#pragma unroll
    for (int k = 0; k < 14; ++k) {
#pragma unroll
      for (int mi = 0; mi < 4; ++mi) {
        const bf16x8 wf = *(lp)(size_t)((mi < 2 ? wa0 : wa1) + (mi & 1) * 28672 + k * 1024);
        acc[mi] = MFMA16(wf, __builtin_bit_cast(bf16x8, hf[k]), acc[mi]);
      }
    }
    STAMP(6);
    LAS f32x4* xb = (LAS f32x4*)(size_t)(K2_X);
    xb[(wid * 2) * 64 + lane] = acc[2];
    xb[(wid * 2 + 1) * 64 + lane] = acc[3];
    WL0();
    BAR();
    const f32x4 c0 = acc[0] + xb[((wid ^ 4) * 2) * 64 + lane], c1 = acc[1] + xb[((wid ^ 4) * 2 + 1) * 64 + lane];
    u32x4 o;
#pragma unroll
    for (int k = 0; k < 4; ++k) {
      const f32x4 c = (k >> 1) ? c1 : c0;
      const float lo = bf_lo(ex[k]) + bf_lo(e1[k]) - bf_lo(e2[k]) + c[(k & 1) * 2];
      const float hi = bf_hi(ex[k]) + bf_hi(e1[k]) - bf_hi(e2[k]) + c[(k & 1) * 2 + 1];
      o[k] = cvt_pk(lo, hi);
    }
    *(u32x4*)(a.out + (size_t)(tt3 * 16 + fr) * D + (c3 + fq) * 8) = o;
  }
  STAMP(7);
  STAMP_OUT(a.ws, 1, 8);
}

extern "C" void kernel_launch(void* const* d_in, const int* in_sizes, int n_in, void* d_out, int out_size, void* d_ws, size_t ws_size,
                              hipStream_t stream) {
  (void)in_sizes; (void)n_in; (void)out_size; (void)ws_size;
  (void)hipFuncSetAttribute((const void*)k1, hipFuncAttributeMaxDynamicSharedMemorySize, K1_LDS);
  (void)hipFuncSetAttribute((const void*)k2, hipFuncAttributeMaxDynamicSharedMemorySize, K2_LDS);
  K1Args a1{(const u16*)d_in[0], (const u16*)d_in[3], (const u16*)d_in[4], (const u16*)d_in[6], (const u16*)d_in[9], (char*)d_ws};
  K2Args a2{(const u16*)d_in[1], (const u16*)d_in[2], (const u16*)d_in[7], (const u16*)d_in[8], (const u16*)d_in[0], (char*)d_ws, (u16*)d_out};
  k1<<<256, 512, K1_LDS, stream>>>(a1);
  k2<<<256, 512, K2_LDS, stream>>>(a2);
}
```

```cpp
#include <hip/hip_runtime.h>
#include <hip/hip_bf16.h>
#include <stdint.h>

typedef unsigned short u16;
typedef short bf16x8 __attribute__((ext_vector_type(8)));
typedef short s16x4 __attribute__((ext_vector_type(4)));
typedef float f32x4 __attribute__((ext_vector_type(4)));
typedef float f32x2 __attribute__((ext_vector_type(2)));
typedef unsigned u32x4 __attribute__((ext_vector_type(4)));
typedef __bf16 bf16x2v __attribute__((ext_vector_type(2)));
typedef __amdgpu_buffer_rsrc_t rsrc_t;
#define LAS __attribute__((address_space(3)))
#define WAIT_V(n) asm volatile("s_waitcnt vmcnt(%0)" ::"n"(n) : "memory")
#define WL0() __builtin_amdgcn_s_waitcnt(0xC07F)
#define BAR() do { asm volatile("" ::: "memory"); __builtin_amdgcn_s_barrier(); asm volatile("" ::: "memory"); } while (0)
#define SCHED() __builtin_amdgcn_sched_barrier(0)
#define MFMA16(a, b, c) __builtin_amdgcn_mfma_f32_16x16x32_bf16(a, b, c, 0, 0, 0)
#define RSRC(p, n) __builtin_amdgcn_make_buffer_rsrc((void*)(p), 0, (unsigned)(n), 0x00020000)
#define OPAQUE(x) asm volatile("" : "+v"(x))

#define STAMP_DECL
#define STAMP(i)
#define STAMP_OUT(ws, kid, n)
#define K2_KXMASK 3
#define K1_KXMASK 3
#define DWARM 0
#define CODEWARM 4
#define PF_LIST 0x642810u
#define PF_N 6

constexpr int S = 2048, D = 512, F = 1792, FH = 896;
constexpr size_t WS_Q = 0;
constexpr size_t WS_Z1 = WS_Q + (size_t)(2 * D + FH) * S * 2;
constexpr size_t WS_H = WS_Z1 + (size_t)FH * S * 2;
constexpr size_t WS_Y = WS_H + (size_t)FH * S * 2;
constexpr size_t WS_PAY = WS_Y + (size_t)2 * D * S * 2;
constexpr size_t WS_G = WS_PAY + 16 * 16 * 1024;
constexpr unsigned G_F1 = 0;
constexpr unsigned G_FP = 32768;
constexpr unsigned G_FX = 32768 + 64;
constexpr unsigned G_ALL = 65536;
constexpr unsigned G_REQ = 66560;
constexpr unsigned G_ANS = 67584;
constexpr unsigned G_END = 68608;

constexpr int LDS_BYTES = 163840;
constexpr int P1_RING = 32768, P1_SCR = 81920;
constexpr int P2_PB = 65536, P2_Z = 131072, P3_X = 114688;
constexpr unsigned SPIN = 1u << 20;

struct Args {
  const u16* x; const u16* wdr; const u16* wdi; const u16* win; const u16* wout;
  const u16* wsr; const u16* wsi; const u16* wsp; const u16* bsp;
  char* ws; u16* out;
};

__device__ __forceinline__ unsigned cvt_pk(float lo, float hi) {
  f32x2 v = {lo, hi};
  return __builtin_bit_cast(unsigned, __builtin_convertvector(v, bf16x2v));
}
__device__ __forceinline__ float bf_lo(unsigned u) { return __uint_as_float(u << 16); }
__device__ __forceinline__ float bf_hi(unsigned u) { return __uint_as_float(u & 0xffff0000u); }
constexpr float GC1 = -2.0f * 0.7978845608028654f * 1.4426950408889634f, GC2 = GC1 * 0.044715f;
__device__ __forceinline__ f32x2 gelu2(f32x2 v) {
  const f32x2 t = v * ((v * v) * GC2 + GC1);
  const f32x2 e = f32x2{__builtin_amdgcn_exp2f(t[0]), __builtin_amdgcn_exp2f(t[1])} + 1.0f;
  return v * f32x2{__builtin_amdgcn_rcpf(e[0]), __builtin_amdgcn_rcpf(e[1])};
}
__device__ __forceinline__ float sel4(f32x4 v, int i) { const float a = (i & 1) ? v[1] : v[0], b = (i & 1) ? v[3] : v[2]; return (i & 2) ? b : a; }

__device__ __forceinline__ void dma16(rsrc_t r, unsigned mbase, int mimm, unsigned voff, unsigned soff) {
  asm volatile("s_add_i32 m0, %0, %1\n\ts_nop 0\n\tbuffer_load_dwordx4 %2, %3, %4 offen lds" ::"s"(mbase), "n"(mimm), "v"(voff), "s"(r), "s"(soff) : "scc");
}
__device__ __forceinline__ void dma16_sc1(rsrc_t r, unsigned mbase, int mimm, unsigned voff, unsigned soff) {
  asm volatile("s_add_i32 m0, %0, %1\n\ts_nop 0\n\tbuffer_load_dwordx4 %2, %3, %4 offen sc1 lds" ::"s"(mbase), "n"(mimm), "v"(voff), "s"(r), "s"(soff) : "scc");
}
__device__ __forceinline__ void dma4_sc1(rsrc_t r, int ldsaddr, unsigned voff, unsigned soff) {
  asm volatile("s_mov_b32 m0, %0\n\ts_nop 0\n\tbuffer_load_dword %1, %2, %3 offen sc1 lds" ::"n"(ldsaddr), "v"(voff), "s"(r), "s"(soff) : "memory");
}
__device__ __forceinline__ void touch(rsrc_t r, int junk, unsigned voff, unsigned soff) {
  asm volatile("s_mov_b32 m0, %0\n\ts_nop 0\n\tbuffer_load_dword %1, %2, %3 offen lds" ::"n"(junk), "v"(voff), "s"(r), "s"(soff) : "memory");
}
__device__ __forceinline__ void st16_sc1(u32x4 v, rsrc_t r, unsigned voff, unsigned soff) {
  asm volatile("buffer_store_dwordx4 %0, %1, %2, %3 offen sc1\n\ts_nop 1" ::"v"(v), "v"(voff), "s"(r), "s"(soff) : "memory");
}
__device__ __forceinline__ void st4_sc1(unsigned v, rsrc_t r, unsigned voff, unsigned soff) {
  asm volatile("buffer_store_dword %0, %1, %2, %3 offen sc1" ::"v"(v), "v"(voff), "s"(r), "s"(soff) : "memory");
}
__device__ __forceinline__ unsigned ld4_sc1(rsrc_t r, unsigned voff, unsigned soff) {
  unsigned v;
  asm volatile("buffer_load_dword %0, %1, %2, %3 offen sc1\n\ts_waitcnt vmcnt(0)" : "=&v"(v) : "v"(voff), "s"(r), "s"(soff) : "memory");
  return v;
}
__device__ __forceinline__ u32x4 ld16_sc1(rsrc_t r, unsigned voff, unsigned soff) {
  u32x4 v;
  asm volatile("buffer_load_dwordx4 %0, %1, %2, %3 offen sc1\n\ts_waitcnt vmcnt(0)" : "=&v"(v) : "v"(voff), "s"(r), "s"(soff) : "memory");
  return v;
}
__device__ __forceinline__ void wait_vn(int n) { WAIT_V(n); }
__device__ __forceinline__ bf16x8 ld_tr(unsigned addr) {
  s16x4 lo = __builtin_amdgcn_ds_read_tr16_b64_v4i16((LAS s16x4*)(size_t)(addr));
  s16x4 hi = __builtin_amdgcn_ds_read_tr16_b64_v4i16((LAS s16x4*)(size_t)(addr + 512));
  return __builtin_shufflevector(lo, hi, 0, 1, 2, 3, 4, 5, 6, 7);
}
__device__ __forceinline__ bf16x8 ld_frag(unsigned addr) { return *(const LAS bf16x8*)(size_t)(addr); }
__device__ __forceinline__ void wait_go(rsrc_t rG, unsigned nonce, unsigned seen) {
  for (unsigned spin = 0; seen != nonce && spin < SPIN; ++spin) {
    seen = __builtin_amdgcn_readfirstlane(ld4_sc1(rG, 0, G_ANS + blockIdx.x * 4));
    __builtin_amdgcn_s_sleep(1);
  }
}

template <bool ZB>
__device__ __forceinline__ void phase1(const Args& a, int pm, int cb, int kx, const u16* Pp, int kx2) {
  const int tid = threadIdx.x, lane = tid & 63;
  const int wid = __builtin_amdgcn_readfirstlane(tid >> 6);
  const int wm = wid & 3, wn = wid >> 2;
  STAMP_DECL STAMP(0);
  const rsrc_t rX = RSRC(a.x + (size_t)pm * 128 * D, 128 * D * 2);
  const rsrc_t rW = ZB ? RSRC(a.win + 64 * cb, (D * F - 64 * cb) * 2) : RSRC(a.wdi + (size_t)384 * D, 128 * D * 2);
  const int r8 = lane >> 3, p8 = lane & 7;
  const unsigned sw_e = (p8 ^ (r8 >> 1)) << 4;
  const unsigned vx_e = (32 * (wid >> 1) + 4 * (wid & 1) + 8 * (r8 >> 2) + (r8 & 3)) * (D * 2) + sw_e;
  const unsigned vx_o = (vx_e ^ 64) + 16 * (D * 2);
  const unsigned vw2 = ZB ? (8 * wid + r8) * (F * 2) + ((p8 ^ ((((r8 >> 1) & 1) | ((wid & 1) << 1)) << 1)) << 4)
                          : (64 + 8 * wid + r8) * (D * 2) + (sw_e ^ ((wid & 1) << 6));
  const unsigned vw1 = ZB ? vw2 + FH * 2 : vw2 - 64 * (D * 2);
  constexpr unsigned WKB = ZB ? 64 * F * 2 : 128;
  const unsigned mb1 = wid * 1024, mb2 = wid * 2048;
  auto piece1 = [&](int st, int p) {
    const unsigned kb = st ^ kx;
    const int base = P1_RING + (st & 3) * 32768;
    if (p == 0) dma16(rX, mb2, base, vx_e, kb * 128);
    else if (p == 1) dma16(rW, mb1, base + 16384, vw1, kb * WKB);
    else if (p == 2) dma16(rX, mb2, base + 1024, vx_o, kb * 128);
    else dma16(rW, mb1, base + 24576, vw2, kb * WKB);
  };
  piece1(0, 0); piece1(0, 1); piece1(0, 2); piece1(0, 3);
  SCHED();

  const int fr = lane & 15, fq = lane >> 4;
  const int kh = wm >> 1;
  const unsigned fsw = ((fq ^ ((fr >> 1) & 7)) << 4) ^ (kh << 6);
  unsigned xa[2][2][2], wb[2][2][2];
#pragma unroll
  for (int m = 0; m < 2; ++m) { xa[0][m][0] = P1_RING + (32 * wm + 16 * (m ^ wn) + fr) * 128 + fsw; xa[0][m][1] = xa[0][m][0] ^ 64; }
#pragma unroll
  for (int n = 0; n < 2; ++n) {
    const int tile = 2 * wn + (n ^ (wm & 1));
    if (ZB) {
      const int q4 = fr >> 2, pp = lane & 3;
      const int sp = ((q4 >> 1) & 1) | ((fq & 1) << 1);
      const int ch = (2 * tile + (pp >> 1)) ^ (sp << 1);
      wb[0][n][0] = P1_RING + 16384 + 128 * (8 * fq + q4) + 16 * ch + 8 * (pp & 1) + kh * 4096;
      wb[0][n][1] = wb[0][n][0] ^ 4096;
    } else {
      wb[0][n][0] = P1_RING + 16384 + (16 * tile + fr) * 128 + fsw;
      wb[0][n][1] = wb[0][n][0] ^ 64;
    }
  }
#pragma unroll
  for (int i = 0; i < 2; ++i)
#pragma unroll
    for (int ks = 0; ks < 2; ++ks) {
      xa[1][i][ks] = xa[0][i][ks] + 65536; wb[1][i][ks] = wb[0][i][ks] + 65536;
      OPAQUE(xa[0][i][ks]); OPAQUE(wb[0][i][ks]); OPAQUE(xa[1][i][ks]); OPAQUE(wb[1][i][ks]);
    }
  auto ldw = [&](unsigned addr) -> bf16x8 { return ZB ? ld_tr(addr) : ld_frag(addr); };
  SCHED();
  piece1(1, 0); piece1(1, 1); piece1(1, 2); piece1(1, 3);
  const rsrc_t rG = RSRC(a.ws + WS_G, G_END);
  if (wid == 7) dma4_sc1(rG, 0, 0, G_ANS + blockIdx.x * 4);
  unsigned nonce = 0;
  STAMP(1);

  bf16x8 xf[16][2], wf[2][4];
  const f32x4 Z4 = {0, 0, 0, 0};
  f32x4 acc1[2][2] = {{Z4, Z4}, {Z4, Z4}}, acc2[2][2] = {{Z4, Z4}, {Z4, Z4}};
  f32x4 gram = Z4, rsum = Z4, cs1 = Z4, cs2 = Z4;
  const bf16x8 ONES = {0x3f80, 0x3f80, 0x3f80, 0x3f80, 0x3f80, 0x3f80, 0x3f80, 0x3f80};

  auto mm = [&](int kk, int i) {
    const int ks = kk & 1;
    if (i == 0) gram = MFMA16(xf[kk][0], xf[kk][0], gram);
    else if (i == 1) rsum = MFMA16(xf[kk][0], ONES, rsum);
    else if (i < 6) { const int m = (i - 2) >> 1, n = i & 1; acc1[m][n] = MFMA16(xf[kk][m], wf[ks][n], acc1[m][n]); }
    else if (i < 10) { const int m = (i - 6) >> 1, n = i & 1; acc2[m][n] = ZB ? MFMA16(wf[ks][2 + n], xf[kk][m], acc2[m][n]) : MFMA16(xf[kk][m], wf[ks][2 + n], acc2[m][n]); }
    else if (i == 10) cs1 = MFMA16(ONES, wf[0][0], cs1);
    else cs2 = MFMA16(ONES, wf[0][2], cs2);
  };
  auto rd = [&](int kk, int i) {
    const int st = kk >> 1, ks = kk & 1, wi = (st >> 1) & 1, so = (st & 1) * 32768;
    if (i == 0 || i == 3) xf[kk][i & 1] = ld_frag(xa[wi][i & 1][ks] + so);
    else if (i < 3) wf[ks][i - 1] = ldw(wb[wi][i - 1][ks] + so);
    else wf[ks][i - 2] = ldw(wb[wi][i - 4][ks] + so + 8192);
  };
  auto piece3 = [&](int p) {
    const rsrc_t rD = RSRC((cb < 8 ? a.wdr : a.wdi) + (size_t)(cb & 7) * 64 * D, 64 * D * 2);
    const unsigned ve = r8 * (D * 2) + sw_e, vo = (ve ^ 64) + 8 * (D * 2);
    dma16(rD, wid * 8192, p * 1024, (p & 1) ? vo : ve, (wid ^ kx) * 128 + (p >> 1) * 16 * (D * 2));
  };
  auto earlyP = [&](int k) {
    const rsrc_t rP = RSRC(Pp, 128 * S * 2);
    const unsigned ve = (16 * wid + r8) * (S * 2) + sw_e, vo = (ve ^ 64) + 8 * (S * 2);
    dma16(rP, mb2, P2_PB + (k + 2) * 16384, ve, (k ^ kx2) * 128);
    dma16(rP, mb2, P2_PB + (k + 2) * 16384 + 1024, vo, (k ^ kx2) * 128);
  };
#pragma unroll
  for (int st = 0; st < 8; ++st) {
    wait_vn(st <= 6 ? 4 : ZB ? 8 : 0);
    WL0();
    BAR();
    if (st == 0) STAMP(2);
    SCHED();
#pragma unroll
    for (int i = 0; i < 10; ++i) {
      if (st > 0) mm(2 * st - 1, i);
      SCHED();
      if (i < 6) rd(2 * st, i);
      if (st + 2 < 8) { if (i == 3) piece1(st + 2, 0); if (i == 8) piece1(st + 2, 1); }
      if (ZB && st == 6 && (i % 3) == 0) piece3(i / 3);
      if (st == 7 && i == 7) earlyP(0);
      SCHED();
    }
    WL0();
    SCHED();
#pragma unroll
    for (int i = 0; i < 12; ++i) {
      mm(2 * st, i);
      SCHED();
      if (i < 6) rd(2 * st + 1, i);
      if (st + 2 < 8) { if (i == 3) piece1(st + 2, 2); if (i == 9) piece1(st + 2, 3); }
      if (ZB && st == 6 && (i % 3) == 2) piece3(4 + i / 3);
      if (st == 7 && i == 8) earlyP(1);
      SCHED();
    }
    if (st == 2 && wid == 7) {
      nonce = __builtin_amdgcn_readfirstlane(*(LAS unsigned*)(size_t)(lane * 4)) + 1;
      st4_sc1(nonce, rG, 0, G_REQ + blockIdx.x * 4);
    }
    if (st == 6 && wid == 7) dma4_sc1(rG, P1_SCR + 7168, 0, G_ANS + blockIdx.x * 4);
    SCHED();
  }
#pragma unroll
  for (int i = 0; i < 10; ++i) mm(15, i);
  STAMP(3);
  unsigned sZ0 = ((pm * 8 + 2 * wm) * 112 + 8 * cb + 4 * wn) * 256 + 128 * wn, sZ1 = sZ0 ^ 128;
  asm volatile("" : "+s"(sZ0), "+s"(sZ1));
  LAS float* const s_r = (LAS float*)(size_t)(P1_SCR);
  LAS float* const s_c = (LAS float*)(size_t)(P1_SCR + 512);
  LAS float* const s_u1 = (LAS float*)(size_t)(P1_SCR + 1024);
  LAS float* const s_u2 = (LAS float*)(size_t)(P1_SCR + 1536);
  LAS float* const s_z = (LAS float*)(size_t)(P1_SCR + 2048);
  LAS float* const s_n = (LAS float*)(size_t)(P1_SCR + 4096);
  LAS float* const s_u3 = (LAS float*)(size_t)(P1_SCR + 5120);
  {
    const float s1 = sel4(rsum, fr & 3), s2 = sel4(gram, fr & 3);
    const float mean = s1 * (1.0f / D);
    const float rstd = __builtin_amdgcn_rsqf(fmaxf(s2 * (1.0f / D) - mean * mean, 0.0f) + 1e-5f);
    if ((fr >> 2) == fq) { s_r[32 * wm + 16 * wn + fr] = rstd; s_c[32 * wm + 16 * wn + fr] = -mean * rstd; }
    if (fq < 2) s_u1[128 * fq + 64 * kh + 16 * (2 * wn + (wm & 1)) + fr] = fq ? cs2[0] : cs1[0];
  }
  const int s0 = pm * 128 + 32 * wm;
  const rsrc_t rT = RSRC(a.ws + WS_Q, (2 * D + FH) * S * 2);
  const unsigned vT = ((fr >> 2) & 1) * 65536 + fq * 256 + (4 * (fr >> 3) + (fr & 3)) * 16;
  const unsigned tb0 = fr * 128 + fsw, tb1 = tb0 ^ 64;
  WAIT_V(4);
  WL0();
  BAR();
  STAMP(4);
  earlyP(2); earlyP(3);
  f32x4 rq[2], cq[2];
  auto ld_rc = [&]() {
#pragma unroll
    for (int m = 0; m < 2; ++m) {
      rq[m] = *(LAS f32x4*)(s_r + 32 * wm + 16 * (m ^ wn) + 4 * fq);
      cq[m] = *(LAS f32x4*)(s_c + 32 * wm + 16 * (m ^ wn) + 4 * fq);
    }
  };
  ld_rc();
  auto store_t = [&](int T, f32x4 v0, f32x4 v1) {
    const unsigned a0 = cvt_pk(v0[0], v0[1]), a1 = cvt_pk(v0[2], v0[3]), b0 = cvt_pk(v1[0], v1[1]), b1 = cvt_pk(v1[2], v1[3]);
    const u32x4 o = wn ? u32x4{b0, b1, a0, a1} : u32x4{a0, a1, b0, b1};
    st16_sc1(o, rT, vT, (T >> 1) * 131072 + (T & 1) * 128 + s0 * 32);
  };
  auto lin_store = [&](f32x4 c0, f32x4 c1, float u, int T) { store_t(T, rq[0] * c0 + cq[0] * u, rq[1] * c1 + cq[1] * u); };
  float u1[2];
#pragma unroll
  for (int n = 0; n < 2; ++n) { const int ui = 16 * (2 * wn + (n ^ (wm & 1))) + fr; u1[n] = s_u1[ui] + s_u1[64 + ui]; }
  if (!ZB) {
#pragma unroll
    for (int n = 0; n < 2; ++n) {
      const int tile = 2 * wn + (n ^ (wm & 1)), ui = 16 * tile + fr;
      lin_store(acc1[0][n], acc1[1][n], u1[n], 56 + tile);
      lin_store(acc2[0][n], acc2[1][n], s_u2[ui] + s_u2[64 + ui], 60 + tile);
    }
    if (wid == 7) wait_go(rG, nonce, __builtin_amdgcn_readfirstlane(*(LAS unsigned*)(size_t)(P1_SCR + 7168 + lane * 4)));
    STAMP(5); STAMP(6); STAMP(7); STAMP(8); STAMP(9);
    STAMP_OUT(a.ws, 0, 10);
    return;
  }
  {
    const unsigned cb0 = tb0 + wid * 8192, cb1 = cb0 ^ 64;
    f32x4 cs3[4];
#pragma unroll
    for (int t = 0; t < 4; ++t) {
      cs3[t] = MFMA16(ONES, ld_frag(cb0 + t * 2048), Z4);
      cs3[t] = MFMA16(ONES, ld_frag(cb1 + t * 2048), cs3[t]);
    }
    f32x4 ps[2], pq[2];
#pragma unroll
    for (int n = 0; n < 2; ++n)
#pragma unroll
      for (int m = 0; m < 2; ++m) {
        f32x4 v = rq[m] * acc1[m][n] + cq[m] * u1[n];
        const f32x2 g0 = gelu2(f32x2{v[0], v[1]}), g1 = gelu2(f32x2{v[2], v[3]});
        v = f32x4{g0[0], g0[1], g1[0], g1[1]};
        acc1[m][n] = v;
        if (n == 0) { ps[m] = v; pq[m] = v * v; } else { ps[m] += v; pq[m] += v * v; }
      }
    float t[8], h[4];
#define DPP2(d, x, y, ctl, m0, m1) "v_add_f32_dpp " d ", " x ", " x " " ctl " row_mask:0xf bank_mask:" m0 "\n\tv_add_f32_dpp " d ", " y ", " y " " ctl " row_mask:0xf bank_mask:" m1 "\n\t"
#define DPPQ(d, ctl) "v_add_f32_dpp " d ", " d ", " d " " ctl " row_mask:0xf bank_mask:0xf\n\t"
    asm volatile("s_nop 1\n\t" DPP2("%0", "%8", "%16", "row_ror:8", "0x3", "0xc") DPP2("%1", "%9", "%17", "row_ror:8", "0x3", "0xc")
                 DPP2("%2", "%10", "%18", "row_ror:8", "0x3", "0xc") DPP2("%3", "%11", "%19", "row_ror:8", "0x3", "0xc")
                 DPP2("%4", "%12", "%20", "row_ror:8", "0x3", "0xc") DPP2("%5", "%13", "%21", "row_ror:8", "0x3", "0xc")
                 DPP2("%6", "%14", "%22", "row_ror:8", "0x3", "0xc") DPP2("%7", "%15", "%23", "row_ror:8", "0x3", "0xc") "s_nop 1"
                 : "=&v"(t[0]), "=&v"(t[1]), "=&v"(t[2]), "=&v"(t[3]), "=&v"(t[4]), "=&v"(t[5]), "=&v"(t[6]), "=&v"(t[7])
                 : "v"(ps[0][0]), "v"(pq[0][0]), "v"(ps[0][1]), "v"(pq[0][1]), "v"(ps[0][2]), "v"(pq[0][2]), "v"(ps[0][3]), "v"(pq[0][3]),
                   "v"(ps[1][0]), "v"(pq[1][0]), "v"(ps[1][1]), "v"(pq[1][1]), "v"(ps[1][2]), "v"(pq[1][2]), "v"(ps[1][3]), "v"(pq[1][3]));
    asm volatile("s_nop 1\n\t" DPP2("%0", "%4", "%8", "row_half_mirror", "0x5", "0xa") DPP2("%1", "%5", "%9", "row_half_mirror", "0x5", "0xa")
                 DPP2("%2", "%6", "%10", "row_half_mirror", "0x5", "0xa") DPP2("%3", "%7", "%11", "row_half_mirror", "0x5", "0xa") "s_nop 1\n\t"
                 DPPQ("%0", "quad_perm:[1,0,3,2]") DPPQ("%1", "quad_perm:[1,0,3,2]") DPPQ("%2", "quad_perm:[1,0,3,2]") DPPQ("%3", "quad_perm:[1,0,3,2]")
                 DPPQ("%0", "quad_perm:[2,3,0,1]") DPPQ("%1", "quad_perm:[2,3,0,1]") DPPQ("%2", "quad_perm:[2,3,0,1]") DPPQ("%3", "quad_perm:[2,3,0,1]") "s_nop 0"
                 : "=&v"(h[0]), "=&v"(h[1]), "=&v"(h[2]), "=&v"(h[3])
                 : "v"(t[0]), "v"(t[1]), "v"(t[2]), "v"(t[3]), "v"(t[4]), "v"(t[5]), "v"(t[6]), "v"(t[7]));
    if ((fr & 3) == 0) *(LAS f32x4*)(s_z + (wn * 128 + 32 * wm + 8 * fq + 4 * ((fr >> 3) ^ wn) + 2 * ((fr >> 2) & 1)) * 2) = f32x4{h[0], h[1], h[2], h[3]};
    s_u3[lane * 8 + wid] = sel4(f32x4{cs3[0][0], cs3[1][0], cs3[2][0], cs3[3][0]}, fq);
  }
  WL0();
  BAR();
  STAMP(5);
  const rsrc_t rPay = RSRC(a.ws + WS_PAY + (size_t)pm * 16 * 1024, 14 * 1024);
  if (wid == 7) {
    __builtin_amdgcn_s_setprio(2);
    const f32x4 p = *(LAS f32x4*)(s_z + lane * 4) + *(LAS f32x4*)(s_z + 256 + lane * 4);
    st16_sc1(__builtin_bit_cast(u32x4, p), rPay, lane * 16, cb * 1024);
  }
  STAMP(6);
  {
    const rsrc_t rZ = RSRC(a.ws + WS_Z1, FH * S * 2);
    const unsigned vZ = (fr >> 3) * (112 * 256) + (2 * ((fq & 1) ^ (wm & 1)) + (fq >> 1)) * 256 + (fr & 7) * 16;
    f32x4 u[2];
    float r[2], c[2], u3[2];
    f32x4 acct[2][2] = {{Z4, Z4}, {Z4, Z4}};
    const unsigned t30 = tb0 + wn * 4096, t31 = tb1 + wn * 4096;
    bf16x8 w[8];
    auto rd3 = [&](int f) { const int kk = f >> 1; w[f & 7] = ld_frag(((kk & 1) ? t31 : t30) + (kk >> 1) * 8192 + (f & 1) * 2048); };
#pragma unroll
    for (int f = 0; f < 4; ++f) rd3(f);
#pragma unroll
    for (int n = 0; n < 2; ++n) {
      const int ui = 16 * (2 * wn + (n ^ (wm & 1))) + 4 * fq;
      u[n] = *(LAS f32x4*)(s_u2 + ui) + *(LAS f32x4*)(s_u2 + 64 + ui);
      r[n] = s_r[32 * wm + 16 * (n ^ wn) + fr]; c[n] = s_c[32 * wm + 16 * (n ^ wn) + fr];
      const f32x4 uu = *(LAS f32x4*)(s_u3 + (32 * wn + 16 * n + fr) * 8) + *(LAS f32x4*)(s_u3 + (32 * wn + 16 * n + fr) * 8 + 4);
      u3[n] = (uu[0] + uu[1]) + (uu[2] + uu[3]);
    }
    unsigned pk[2][2];
    f32x2 gv[2], gt[2];
    auto item = [&](int it) {
      const int p = (it / 10) * 2 + (it & 1), sg = (it % 10) >> 1, b = it & 1;
      const int m = p >> 2, n = (p >> 1) & 1, h = p & 1;
      if (sg == 0) gv[b] = f32x2{acc2[m][n][2 * h], acc2[m][n][2 * h + 1]} * r[m] + f32x2{u[n][2 * h], u[n][2 * h + 1]} * c[m];
      else if (sg == 1) gt[b] = gv[b] * ((gv[b] * gv[b]) * GC2 + GC1);
      else if (sg == 2) gt[b] = f32x2{__builtin_amdgcn_exp2f(gt[b][0]), __builtin_amdgcn_exp2f(gt[b][1])};
      else if (sg == 3) { const f32x2 d = gt[b] + 1.0f; gt[b] = f32x2{__builtin_amdgcn_rcpf(d[0]), __builtin_amdgcn_rcpf(d[1])}; }
      else {
        const f32x2 g = gv[b] * gt[b];
        pk[n][h] = cvt_pk(g[0], g[1]);
        if ((p & 3) == 3) {
          auto w0 = __builtin_amdgcn_permlane16_swap(pk[0][0], pk[1][0], false, false);
          auto w1 = __builtin_amdgcn_permlane16_swap(pk[0][1], pk[1][1], false, false);
          st16_sc1(u32x4{w0[0], w1[0], w0[1], w1[1]}, rZ, vZ, m ? sZ1 : sZ0);
        }
      }
    };
    SCHED();
#pragma unroll
    for (int f = 0; f < 32; ++f) {
      const int kk = f >> 1, n = f & 1;
      acct[0][n] = MFMA16(xf[kk][0], w[f & 7], acct[0][n]);
      SCHED();
      if (f + 4 < 32) rd3(f + 4);
      if (f == 29) ld_rc();
      if (f >= 1 && f <= 20) item(2 * f - 2);
      if (f == 6 && wid == 7) {
        const unsigned seen = __builtin_amdgcn_readfirstlane(*(LAS unsigned*)(size_t)(P1_SCR + 7168 + lane * 4));
        wait_vn(0);
        wait_go(rG, nonce, seen);
        st4_sc1(1u, rG, fr * 128, G_FX + pm * 2048 + cb * 4);
      }
      SCHED();
      acct[1][n] = MFMA16(xf[kk][1], w[f & 7], acct[1][n]);
      SCHED();
      if (f >= 1 && f <= 20) item(2 * f - 1);
      SCHED();
    }
#pragma unroll
    for (int n = 0; n < 2; ++n) lin_store(acct[0][n], acct[1][n], u3[n], 4 * cb + 2 * wn + n);
  }
  STAMP(7);
  if (wid == 7) {
    for (unsigned spin = 0; spin < SPIN; ++spin) {
      const unsigned fl = ld4_sc1(rG, fr * 4, G_FX + (pm * 16 + cb) * 128);
      if (__all(fr >= 14 || fl == 1u)) break;
    }
    f32x4 pv[14];
#pragma unroll
    for (int i = 0; i < 14; ++i) pv[i] = __builtin_bit_cast(f32x4, __builtin_amdgcn_raw_buffer_load_b128(rPay, lane * 16 + (i & 3) * 1024, (i >> 2) * 4096, 16));
    const f32x4 t = ((pv[0] + pv[1]) + (pv[2] + pv[3])) + ((pv[4] + pv[5]) + (pv[6] + pv[7])) + (((pv[8] + pv[9]) + (pv[10] + pv[11])) + (pv[12] + pv[13]));
    const float m0 = t[0] * (1.0f / FH), m1 = t[2] * (1.0f / FH);
    const float r0 = __builtin_amdgcn_rsqf(fmaxf(t[1] * (1.0f / FH) - m0 * m0, 0.0f) + 1e-5f);
    const float r1 = __builtin_amdgcn_rsqf(fmaxf(t[3] * (1.0f / FH) - m1 * m1, 0.0f) + 1e-5f);
    *(LAS f32x2*)(s_n + 2 * lane) = f32x2{r0, r1};
    *(LAS f32x2*)(s_n + 128 + 2 * lane) = f32x2{-m0 * r0, -m1 * r1};
    __builtin_amdgcn_s_setprio(0);
  }
  WL0();
  BAR();
  STAMP(8);
  {
    f32x4 r2[2], c2[2];
#pragma unroll
    for (int m = 0; m < 2; ++m) {
      r2[m] = *(LAS f32x4*)(s_n + 32 * wm + 8 * fq + 4 * (m ^ wn));
      c2[m] = *(LAS f32x4*)(s_n + 128 + 32 * wm + 8 * fq + 4 * (m ^ wn));
    }
#pragma unroll
    for (int n = 0; n < 2; ++n) store_t(64 + 4 * cb + 2 * wn + (n ^ (wm & 1)), acc1[0][n] * r2[0] + c2[0], acc1[1][n] * r2[1] + c2[1]);
  }
  STAMP(9);
  STAMP_OUT(a.ws, 0, 10);
}

__device__ __forceinline__ void warm_window(const Args& a, int junk, int rb, int w, int wv, int lane) {
  if (w < 8) {
    const unsigned vo = (rb * 128 + wv * 32 + (lane >> 2)) * (S * 2) + (lane & 3) * 128;
#pragma unroll
    for (int m = 0; m < 3; ++m) {
      const rsrc_t r = RSRC(m == 0 ? a.wsr : m == 1 ? a.wsi : a.wsp, S * S * 2);
      touch(r, junk, vo, w * 512);
      touch(r, junk, vo, w * 512 + 16 * (S * 2));
    }
  } else {
    const rsrc_t r = RSRC(a.wout + (size_t)rb * 56 * D, 56 * D * 2);
    touch(r, junk, lane * 128, wv * 16384);
    touch(r, junk, lane * 128, wv * 16384 + 8192);
  }
}

__global__ __launch_bounds__(512, 2) void fused(Args a) {
  extern __shared__ __attribute__((aligned(1024))) char smem[];
  asm volatile("" ::"v"(smem));
  const int bx = blockIdx.x & 7, bi = blockIdx.x >> 3;
  if (bi < CODEWARM && threadIdx.x < 64) {
    unsigned long long pc;
    asm volatile("s_getpc_b64 %0" : "=s"(pc));
    touch(RSRC(pc & ~127ull, CODEWARM * 8192), 1024, threadIdx.x * 128, bi * 8192);
  }
  const int pm = 4 * (bx >> 1) + (bi >> 3), cb = 2 * (bi & 7) + (bx & 1);
  int mat, tm, tn;
  if (bx & 1) { mat = 2; tm = pm; tn = bi & 7; }
  else { mat = bx >> 2; tm = ((bx >> 1) & 1) * 8 + (bi >> 2); tn = bi & 3; }
  const int kx = (tm ^ tn) & K2_KXMASK;
  const u16* Pp = (mat == 0 ? a.wsr : mat == 1 ? a.wsi : a.wsp) + (size_t)tm * 128 * S;
  {
    const int kx1 = ((bi & 7) ^ (2 * (bi >> 3))) & K1_KXMASK;
    if (cb < 14) phase1<true>(a, pm, cb, kx1, Pp, kx);
    else if (cb == 14) phase1<false>(a, pm, cb, kx1, Pp, kx);
  }
  int tid = threadIdx.x;
  asm volatile("" : "+v"(tid));
  const int lane = tid & 63;
  const int wid = __builtin_amdgcn_readfirstlane(tid >> 6);
  STAMP_DECL STAMP(0);
  const int tm3 = bx * 4 + (bi >> 3), tn3 = bi & 7;
  const int fr = lane & 15, fq = lane >> 4;
  const unsigned vl = lane * 16;
  const rsrc_t rG = RSRC(a.ws + WS_G, G_END);
  const bool work2 = cb != 15;
  if (blockIdx.x == 255) {
#pragma unroll
    for (int i = 0; i < 8; ++i) st16_sc1(u32x4{0, 0, 0, 0}, rG, vl, wid * 8192 + i * 1024);
    st16_sc1(u32x4{0, 0, 0, 0}, rG, vl, G_ALL);
  }
  if (work2 || blockIdx.x == 255) {
    WAIT_V(0);
    WL0();
    BAR();
  }
  if (wid >= 4) {
    const int lw = wid - 4;
    const int r8 = lane >> 3, p = lane & 7;
    if (wid == 7) {
      if (!work2) {
        if (blockIdx.x == 255) {
          for (unsigned spin = 0; spin < SPIN; ++spin) {
            const u32x4 rq = ld16_sc1(rG, vl, G_REQ);
            st16_sc1(rq, rG, vl, G_ANS);
            const u32x4 fl = ld16_sc1(rG, vl, G_ALL);
            if (__all((fl[0] & fl[1] & fl[2] & (lane == 63 ? 1u : fl[3])) == 1u)) break;
          }
        } else {
          const unsigned nonce = __builtin_amdgcn_readfirstlane(ld4_sc1(rG, 0, G_ANS + blockIdx.x * 4)) + 1;
          st4_sc1(nonce, rG, 0, G_REQ + blockIdx.x * 4);
          wait_go(rG, nonce, nonce - 1);
        }
      }
      const unsigned box = (fq & 1) * 128 + (cb >> 1) * 16 + fr;
      st4_sc1(1u, rG, fq < 2 ? box * 128 + (pm * 2 + (cb & 1)) * 4 : G_ALL + blockIdx.x * 4, 0);
    }
    const rsrc_t rP = RSRC(Pp, 128 * S * 2);
    const unsigned vo_e = (lw * 32 + r8) * (S * 2) + ((p ^ (r8 >> 1)) << 4);
    const unsigned vo_o = (vo_e ^ 64) + 8 * (S * 2);
    auto stageP = [&](int kt, unsigned poff) {
      const unsigned mb = poff + lw * 4096, so = (kt ^ kx) * 128;
#pragma unroll
      for (int j = 0; j < 4; ++j) dma16(rP, mb, P2_PB + j * 1024, (j & 1) ? vo_o : vo_e, so + (j >> 1) * 16 * (S * 2));
    };
    if (work2) {
      BAR();
      stageP(4, 0); stageP(5, 16384);
    }
    const rsrc_t rWo = RSRC(a.wout + 64 * tn3, (FH * D - 64 * tn3) * 2);
    const unsigned vw_e = (32 * lw + r8) * (D * 2) + ((p ^ (((r8 >> 1) & 1) << 1)) << 4), vw_o = (vw_e ^ 64) + 8 * (D * 2);
    u32x4 wreg[12];
#pragma unroll
    for (int i = 0; i < 12; ++i) {
      const int rg = i < 8 ? 2 + (i >> 2) : 6;
      wreg[i] = __builtin_amdgcn_raw_buffer_load_b128(rWo, (i & 1) ? vw_o : vw_e, (rg * 128 + ((i >> 1) & 1) * 16) * (D * 2), 0);
    }
    auto wput = [&](int rg) {
#pragma unroll
      for (int j = 0; j < 4; ++j) *(LAS u32x4*)(size_t)(rg * 16384 + lw * 4096 + j * 1024 + vl) = wreg[(rg == 6 ? 8 : (rg - 2) * 4) + j];
    };
    auto wdma = [&](int rg) {
#pragma unroll
      for (int j = 0; j < 4; ++j) dma16(rWo, lw * 4096, rg * 16384 + j * 1024, (j & 1) ? vw_o : vw_e, (rg * 128 + (j >> 1) * 16) * (D * 2));
    };
    if (work2) {
      if (wid == 7) {
        const unsigned box = (mat * 4 + tn) * 16 + tm;
        for (unsigned spin = 0; spin < SPIN; ++spin) {
          const unsigned fl = ld4_sc1(rG, (lane & 31) * 4, G_F1 + box * 128);
          if (__all(fl == 1u || (box >= 112 && box < 128 && (lane & 1)))) break;
        }
      }
      BAR();
      STAMP(1);
      const rsrc_t rQ = RSRC(a.ws + WS_Q + (size_t)(mat * 4 + tn) * 128 * S * 2, 128 * S * 2);
      const unsigned vq = vl + lw * 131072;
      auto stageQ = [&](int kt) {
        const unsigned mb = (kt & 3) * 16384 + lw * 4096, so = (kt ^ kx) * 2048;
#pragma unroll
        for (int j = 0; j < 4; ++j) dma16_sc1(rQ, mb, j * 1024, vq, so + (j >> 1) * 65536 + (j & 1) * 1024);
      };
#pragma unroll
      for (int i = 0; i < 12; ++i) OPAQUE(wreg[i]);
      stageQ(0); stageQ(1);
      WAIT_V(4); BAR();
      stageQ(2); stageQ(3);
      WAIT_V(8); BAR(); stageQ(4); stageP(6, 2 * 16384);
      WAIT_V(12); BAR(); stageQ(5); stageP(7, 3 * 16384);
      WAIT_V(16); BAR(); stageQ(6); stageP(8, 4 * 16384);
      unsigned poff = 5 * 16384;
      for (int kt = 3; kt < 26; ++kt) {
        WAIT_V(20);
        BAR();
        stageQ(kt + 4); stageP(kt + 6, poff);
        poff = poff == 5 * 16384 ? 0 : poff + 16384;
      }
      if (mat == 2) {
        const rsrc_t rZ = RSRC(a.ws + WS_Z1 + (size_t)((8 * tm + lw) * 112 + 16 * tn) * 256, 4 * 112 * 256 + 4096);
        auto stageZ = [&](int h) {
#pragma unroll
          for (int j = 0; j < 4; ++j) dma16_sc1(rZ, lw * 4096, P2_Z + h * 16384 + j * 1024, vl, h * (4 * 112 * 256) + j * 1024);
        };
        WAIT_V(20); BAR(); stageQ(30); stageZ(0);
        WAIT_V(20); BAR(); stageQ(31); stageZ(1);
        WAIT_V(20); BAR(); wdma(0); wdma(4);
        WAIT_V(20); BAR();
        WAIT_V(12); BAR(); wput(2); wput(6);
        WAIT_V(8); BAR(); wput(3);
      } else {
        WAIT_V(20); BAR(); stageQ(30);
        WAIT_V(16); BAR(); stageQ(31);
        WAIT_V(12); BAR(); wdma(0); wdma(4);
        WAIT_V(12); BAR(); wdma(1); wdma(5);
        WAIT_V(16); BAR(); wput(2); wput(6);
        BAR(); wput(3);
      }
    } else {
      STAMP(1);
      wdma(0); wdma(4); wput(2); wput(3); wput(6);
    }
    STAMP(2); STAMP(3);
    WL0();
    BAR();
    if (wid == 4 && work2) {
      const unsigned box = mat == 2 ? fr : (fr & 8) + 2 * tn + (fr & 1);
      st4_sc1(1u, rG, box * 128, G_FP + tm * 2048 + (mat == 2 ? tn : 8 + mat) * 4);
    }
    BAR();
    if (mat == 2) { wdma(1); wdma(5); }
  } else if (work2) {
    BAR();
    if (DWARM && cb == 14) { warm_window(a, 0, pm, 2, wid, lane); WAIT_V(0); }
    const int wp = wid >> 1, wq = wid & 1;
    f32x4 acc[4][4];
#pragma unroll
    for (int m = 0; m < 4; ++m)
#pragma unroll
      for (int n = 0; n < 4; ++n) acc[m][n] = f32x4{0, 0, 0, 0};
    unsigned bv[4];
#pragma unroll
    for (int ni = 0; ni < 4; ++ni) bv[ni] = a.bsp[(8 * tm + 4 * wp + ni) * 16 + fr];
    const int sw = (fr >> 1) & 7;
    const unsigned ra = P2_PB + (wp * 64 + fr) * 128 + ((fq ^ sw) << 4);
    const unsigned rb = wq * 8192 + vl;
    asm volatile("" : "+v"(bv[0]), "+v"(bv[1]), "+v"(bv[2]), "+v"(bv[3]));
    bf16x8 a0[4], b0[4], a1[4], b1[4];
    unsigned poff = 2 * 16384;
    unsigned pa0 = ra + poff, pa1 = pa0 ^ 64;
#define K2_RD(AF, BF, pa, sl, ks, i) do { \
    if ((i) == 0) AF[0] = ld_frag(pa); \
    else if ((i) <= 4) BF[(i) - 1] = ld_frag(rb + (sl) * 16384 + ((i) - 1) * 2048 + (ks) * 1024); \
    else AF[(i) - 4] = ld_frag(pa + ((i) - 4) * 2048); } while (0)
#define K2_MM(AF, BF, i) acc[(i) >> 2][(i) & 3] = MFMA16(BF[(i) & 3], AF[(i) >> 2], acc[(i) >> 2][(i) & 3])
#define K2_STEP(s, LAST) do { \
    SCHED(); WL0(); SCHED(); \
    _Pragma("unroll") for (int i = 0; i < 16; ++i) { K2_MM(a0, b0, i); SCHED(); if (i < 8) K2_RD(a1, b1, pa1, s, 1, i); \
      if (i == 10) { poff = poff == 5 * 16384 ? 0 : poff + 16384; pa0 = ra + poff; } SCHED(); } \
    WL0(); \
    BAR(); \
    SCHED(); \
    _Pragma("unroll") for (int i = 0; i < 16; ++i) { K2_MM(a1, b1, i); SCHED(); if (!(LAST) && i < 8) K2_RD(a0, b0, pa0, ((s) + 1) & 3, 0, i); \
      if (i == 10) pa1 = pa0 ^ 64; SCHED(); } \
  } while (0)
    BAR();
    BAR();
    STAMP(1);
#pragma unroll
    for (int i = 0; i < 8; ++i) K2_RD(a0, b0, pa0, 0, 0, i);
    for (int kt = 0; kt < 28; kt += 4) { K2_STEP(0, 0); K2_STEP(1, 0); K2_STEP(2, 0); K2_STEP(3, 0); }
    K2_STEP(0, 0); K2_STEP(1, 0); K2_STEP(2, 0); K2_STEP(3, 1);
    SCHED();
    STAMP(2);
    if (mat < 2) {
      const rsrc_t rY = RSRC(a.ws + WS_Y + (size_t)mat * S * D * 2, S * D * 2);
      const unsigned so = ((8 * tm + 4 * wp) * 64 + 16 * tn + 8 * wq) * 256;
#pragma unroll
      for (int ni = 0; ni < 4; ++ni)
#pragma unroll
        for (int P = 0; P < 2; ++P) {
          const f32x4 c0 = acc[ni][2 * P], c1 = acc[ni][2 * P + 1];
          const u32x4 v = {cvt_pk(c0[0], c0[1]), cvt_pk(c0[2], c0[3]), cvt_pk(c1[0], c1[1]), cvt_pk(c1[2], c1[3])};
          st16_sc1(v, rY, vl, so + (ni * 64 + 4 * P) * 256);
        }
    } else {
      const rsrc_t rH = RSRC(a.ws + WS_H, S * FH * 2);
      const unsigned so = ((8 * tm + 4 * wp) * 112 + 16 * tn + 8 * wq) * 256;
      const unsigned za = P2_Z + wp * 16384 + wq * 2048 + (fq * 16 + ((fr & 4) << 1) + ((fr & 8) >> 1) + (fr & 3)) * 16;
#pragma unroll
      for (int ni = 0; ni < 4; ++ni) {
        const float bias = __uint_as_float(bv[ni] << 16);
#pragma unroll
        for (int P = 0; P < 2; ++P) {
          const f32x4 c0 = acc[ni][2 * P] + bias, c1 = acc[ni][2 * P + 1] + bias;
          const u32x4 z = *(const LAS u32x4*)(size_t)(za + ni * 4096 + P * 1024);
          const u32x4 v = {cvt_pk(bf_lo(z[0]) * c0[0], bf_hi(z[0]) * c0[1]), cvt_pk(bf_lo(z[1]) * c0[2], bf_hi(z[1]) * c0[3]),
                           cvt_pk(bf_lo(z[2]) * c1[0], bf_hi(z[2]) * c1[1]), cvt_pk(bf_lo(z[3]) * c1[2], bf_hi(z[3]) * c1[3])};
          st16_sc1(v, rH, vl, so + (ni * 112 + 4 * P) * 256);
        }
      }
    }
    STAMP(3);
    WAIT_V(0);
    BAR();
    BAR();
  } else {
    STAMP(1);
    for (int i = 0; i < PF_N && blockIdx.x != 255; ++i) {
      warm_window(a, P2_Z, pm, (PF_LIST >> (4 * i)) & 15, wid, lane);
      WAIT_V(6);
    }
    STAMP(2);
    WAIT_V(0);
    STAMP(3);
    BAR();
    BAR();
  }
  const int ti = (wid + tn3) & 3, kh = (wid >> 2) ^ (tn3 >> 2);
  const int tt3 = 4 * tm3 + ti;
  const int cq = 2 * (fq & 1) + (fq >> 1);
  const int c3 = 8 * tn3 + 4 * kh;
  STAMP(4);
  {
    const u32x4 ex = *(const u32x4*)(a.x + (size_t)(tt3 * 16 + fr) * D + (c3 + cq) * 8);
    if (wid == 4) {
      for (unsigned spin = 0; spin < SPIN; ++spin) {
        const unsigned fl = ld4_sc1(rG, fr * 4, G_FP + (tm3 * 8 + tn3) * 128);
        if (__all(fl == 1u || fr < 8 || fr > 9)) break;
      }
    }
    BAR();
    const rsrc_t rY = RSRC(a.ws + WS_Y + (size_t)(tt3 * 64 + c3) * 256, S * D * 2 + 1024);
    const unsigned vy = cq * 256 + fr * 16;
    const u32x4 e1 = __builtin_amdgcn_raw_buffer_load_b128(rY, vy, 0, 16), e2 = __builtin_amdgcn_raw_buffer_load_b128(rY, vy, S * D * 2, 16);
    if (wid == 4) {
      for (unsigned spin = 0; spin < SPIN; ++spin) {
        const unsigned fl = ld4_sc1(rG, fr * 4, G_FP + (tm3 * 8 + tn3) * 128);
        if (__all(fl == 1u || fr > 6)) break;
      }
    }
    if (wid >= 4) WAIT_V(0);
    BAR();
    STAMP(5);
    const rsrc_t rH = RSRC(a.ws + WS_H + (size_t)(tt3 * 112 + 56 * kh) * 256, 56 * 256);
    u32x4 hf[14];
#pragma unroll
    for (int k = 0; k < 14; ++k) hf[k] = __builtin_amdgcn_raw_buffer_load_b128(rH, vl + (k & 3) * 1024, (k >> 2) * 4096, 16);
    SCHED();
    const int q4 = fr >> 2, pp = lane & 3;
    const int sp = ((q4 >> 1) & 1) | ((fq & 1) << 1);
    const unsigned wa = kh * (14 * 4096) + 128 * (8 * fq + q4) + 16 * ((4 * kh + (pp >> 1)) ^ (sp << 1)) + 8 * (pp & 1);
    f32x4 acc[4] = {f32x4{0, 0, 0, 0}, f32x4{0, 0, 0, 0}, f32x4{0, 0, 0, 0}, f32x4{0, 0, 0, 0}};
#pragma unroll
    for (int k = 0; k < 14; ++k)
#pragma unroll
      for (int mi = 0; mi < 4; ++mi) acc[mi] = MFMA16(ld_tr((wa ^ (mi * 32)) + k * 4096), __builtin_bit_cast(bf16x8, hf[k]), acc[mi]);
    STAMP(6);
    LAS f32x4* xb = (LAS f32x4*)(size_t)(P3_X);
    xb[(wid * 2) * 64 + lane] = acc[2];
    xb[(wid * 2 + 1) * 64 + lane] = acc[3];
    WL0();
    BAR();
    const f32x4 s0 = acc[0] + xb[((wid ^ 4) * 2) * 64 + lane], s1 = acc[1] + xb[((wid ^ 4) * 2 + 1) * 64 + lane];
    f32x4 c0, c1;
#pragma unroll
    for (int j = 0; j < 4; ++j) {
      auto w = __builtin_amdgcn_permlane16_swap(__float_as_uint(s0[j]), __float_as_uint(s1[j]), false, false);
      c0[j] = __uint_as_float(w[0]); c1[j] = __uint_as_float(w[1]);
    }
    u32x4 o;
#pragma unroll
    for (int k = 0; k < 4; ++k) {
      const f32x4 c = (k >> 1) ? c1 : c0;
      const float lo = bf_lo(ex[k]) + bf_lo(e1[k]) - bf_lo(e2[k]) + c[(k & 1) * 2];
      const float hi = bf_hi(ex[k]) + bf_hi(e1[k]) - bf_hi(e2[k]) + c[(k & 1) * 2 + 1];
      o[k] = cvt_pk(lo, hi);
    }
    __builtin_nontemporal_store(o, (u32x4*)(a.out + (size_t)(tt3 * 16 + fr) * D + (c3 + cq) * 8));
  }
  STAMP(7);
  STAMP_OUT(a.ws, 1, 8);
}

extern "C" void kernel_launch(void* const* d_in, const int* in_sizes, int n_in, void* d_out, int out_size, void* d_ws, size_t ws_size,
                              hipStream_t stream) {
  (void)in_sizes; (void)n_in; (void)out_size; (void)ws_size;
  (void)hipFuncSetAttribute((const void*)fused, hipFuncAttributeMaxDynamicSharedMemorySize, LDS_BYTES);
  Args a{(const u16*)d_in[0], (const u16*)d_in[3], (const u16*)d_in[4], (const u16*)d_in[6], (const u16*)d_in[9],
         (const u16*)d_in[1], (const u16*)d_in[2], (const u16*)d_in[7], (const u16*)d_in[8], (char*)d_ws, (u16*)d_out};
  fused<<<256, 512, LDS_BYTES, stream>>>(a);
}
```

```cpp
#include <hip/hip_runtime.h>
#include <hip/hip_bf16.h>
#include <stdint.h>

typedef unsigned short u16;
typedef short bf16x8 __attribute__((ext_vector_type(8)));
typedef short s16x4 __attribute__((ext_vector_type(4)));
typedef float f32x4 __attribute__((ext_vector_type(4)));
typedef float f32x2 __attribute__((ext_vector_type(2)));
typedef unsigned u32x4 __attribute__((ext_vector_type(4)));
typedef __bf16 bf16x2v __attribute__((ext_vector_type(2)));
typedef __amdgpu_buffer_rsrc_t rsrc_t;
#define LAS __attribute__((address_space(3)))
#define WAIT_V(n) asm volatile("s_waitcnt vmcnt(%0)" ::"n"(n) : "memory")
#define WL0() __builtin_amdgcn_s_waitcnt(0xC07F)
#define BAR() do { asm volatile("" ::: "memory"); __builtin_amdgcn_s_barrier(); asm volatile("" ::: "memory"); } while (0)
#define SCHED() __builtin_amdgcn_sched_barrier(0)
#define MFMA16(a, b, c) __builtin_amdgcn_mfma_f32_16x16x32_bf16(a, b, c, 0, 0, 0)
#define RSRC(p, n) __builtin_amdgcn_make_buffer_rsrc((void*)(p), 0, (unsigned)(n), 0x00020000)
#define OPAQUE(x) asm volatile("" : "+v"(x))

#define STAMP_DECL
#define STAMP(i)
#define STAMP_OUT(ws, kid, n)
#define K2_KXMASK 3
#define K1_KXMASK 3
#define DWARM 0
#define CODEWARM 5
#define NOPARK 0
#define BULKLATE 0
#define ZNB 0
#define GATHER_AT 18
#define WINSTART 1
#define PF_LIST 0x753810u
#define PF_N 6

constexpr int S = 2048, D = 512, F = 1792, FH = 896;
constexpr size_t WS_Q = 0;
constexpr size_t WS_Z1 = WS_Q + (size_t)(2 * D + FH) * S * 2;
constexpr size_t WS_H = WS_Z1 + (size_t)FH * S * 2;
constexpr size_t WS_Y = WS_H + (size_t)FH * S * 2;
constexpr size_t WS_G = WS_Y + (size_t)2 * D * S * 2;
constexpr unsigned G_F1 = 0;
constexpr unsigned G_FP = 32768;
constexpr unsigned G_FZ = 32768 + 64;
constexpr unsigned G_PAY = 65536;
constexpr unsigned G_ALL = G_PAY + 16 * 14336;
constexpr unsigned G_REQ = G_ALL + 1024;
constexpr unsigned G_ANS = G_REQ + 1024;
constexpr unsigned G_END = G_ANS + 1024;

constexpr int LDS_BYTES = 163840;
constexpr int P1_RING = 32768, P1_SCR = 81920;
constexpr int P2_PB = 65536, P2_Z = 131072, P3_X = 114688;
constexpr unsigned SPIN = 1u << 20;

struct Args {
  const u16* x; const u16* wdr; const u16* wdi; const u16* win; const u16* wout;
  const u16* wsr; const u16* wsi; const u16* wsp; const u16* bsp;
  char* ws; u16* out;
};

__device__ __forceinline__ unsigned cvt_pk(float lo, float hi) {
  f32x2 v = {lo, hi};
  return __builtin_bit_cast(unsigned, __builtin_convertvector(v, bf16x2v));
}
__device__ __forceinline__ float bf_lo(unsigned u) { return __uint_as_float(u << 16); }
__device__ __forceinline__ float bf_hi(unsigned u) { return __uint_as_float(u & 0xffff0000u); }
constexpr float GC1 = -2.0f * 0.7978845608028654f * 1.4426950408889634f, GC2 = GC1 * 0.044715f;
__device__ __forceinline__ f32x2 gelu2(f32x2 v) {
  const f32x2 t = v * ((v * v) * GC2 + GC1);
  const f32x2 e = f32x2{__builtin_amdgcn_exp2f(t[0]), __builtin_amdgcn_exp2f(t[1])} + 1.0f;
  return v * f32x2{__builtin_amdgcn_rcpf(e[0]), __builtin_amdgcn_rcpf(e[1])};
}
__device__ __forceinline__ void dot_sq(float& c, unsigned a) { asm volatile("v_dot2c_f32_bf16 %0, %1, %1" : "+v"(c) : "v"(a)); }
__device__ __forceinline__ void dot_sum(float& c, unsigned a) { asm volatile("v_dot2c_f32_bf16 %0, 0x3f803f80, %1" : "+v"(c) : "v"(a)); }
__device__ __forceinline__ float sum_rows(float v) {
  auto a = __builtin_amdgcn_permlane32_swap(__float_as_uint(v), __float_as_uint(v), false, false);
  v = __uint_as_float(a[0]) + __uint_as_float(a[1]);
  auto b = __builtin_amdgcn_permlane16_swap(__float_as_uint(v), __float_as_uint(v), false, false);
  return __uint_as_float(b[0]) + __uint_as_float(b[1]);
}
__device__ __forceinline__ float sel4(f32x4 v, int i) { const float a = (i & 1) ? v[1] : v[0], b = (i & 1) ? v[3] : v[2]; return (i & 2) ? b : a; }

__device__ __forceinline__ void dma16(rsrc_t r, unsigned mbase, int mimm, unsigned voff, unsigned soff) {
  asm volatile("s_add_i32 m0, %0, %1\n\ts_nop 0\n\tbuffer_load_dwordx4 %2, %3, %4 offen lds" ::"s"(mbase), "n"(mimm), "v"(voff), "s"(r), "s"(soff) : "scc");
}
__device__ __forceinline__ void dma16_sc1(rsrc_t r, unsigned mbase, int mimm, unsigned voff, unsigned soff) {
  asm volatile("s_add_i32 m0, %0, %1\n\ts_nop 0\n\tbuffer_load_dwordx4 %2, %3, %4 offen sc1 lds" ::"s"(mbase), "n"(mimm), "v"(voff), "s"(r), "s"(soff) : "scc");
}
__device__ __forceinline__ void dma4_sc1(rsrc_t r, int ldsaddr, unsigned voff, unsigned soff) {
  asm volatile("s_mov_b32 m0, %0\n\ts_nop 0\n\tbuffer_load_dword %1, %2, %3 offen sc1 lds" ::"n"(ldsaddr), "v"(voff), "s"(r), "s"(soff) : "memory");
}
__device__ __forceinline__ void touch(rsrc_t r, int junk, unsigned voff, unsigned soff) {
  asm volatile("s_mov_b32 m0, %0\n\ts_nop 0\n\tbuffer_load_dword %1, %2, %3 offen lds" ::"n"(junk), "v"(voff), "s"(r), "s"(soff) : "memory");
}
__device__ __forceinline__ void st16_sc1(u32x4 v, rsrc_t r, unsigned voff, unsigned soff) {
  asm volatile("buffer_store_dwordx4 %0, %1, %2, %3 offen sc1\n\ts_nop 1" ::"v"(v), "v"(voff), "s"(r), "s"(soff) : "memory");
}
__device__ __forceinline__ void st4_sc1(unsigned v, rsrc_t r, unsigned voff, unsigned soff) {
  asm volatile("buffer_store_dword %0, %1, %2, %3 offen sc1" ::"v"(v), "v"(voff), "s"(r), "s"(soff) : "memory");
}
__device__ __forceinline__ unsigned ld4_sc1(rsrc_t r, unsigned voff, unsigned soff) {
  unsigned v;
  asm volatile("buffer_load_dword %0, %1, %2, %3 offen sc1\n\ts_waitcnt vmcnt(0)" : "=&v"(v) : "v"(voff), "s"(r), "s"(soff) : "memory");
  return v;
}
__device__ __forceinline__ u32x4 ld16_sc1(rsrc_t r, unsigned voff, unsigned soff) {
  u32x4 v;
  asm volatile("buffer_load_dwordx4 %0, %1, %2, %3 offen sc1\n\ts_waitcnt vmcnt(0)" : "=&v"(v) : "v"(voff), "s"(r), "s"(soff) : "memory");
  return v;
}
__device__ __forceinline__ void ld16_async(u32x4& v, rsrc_t r, unsigned voff, unsigned soff) {
  asm volatile("buffer_load_dwordx4 %0, %1, %2, %3 offen" : "=v"(v) : "v"(voff), "s"(r), "s"(soff) : "memory");
}
__device__ __forceinline__ void wait_vn(int n) { WAIT_V(n); }
__device__ __forceinline__ bf16x8 ld_tr(unsigned addr) {
  s16x4 lo = __builtin_amdgcn_ds_read_tr16_b64_v4i16((LAS s16x4*)(size_t)(addr));
  s16x4 hi = __builtin_amdgcn_ds_read_tr16_b64_v4i16((LAS s16x4*)(size_t)(addr + 512));
  return __builtin_shufflevector(lo, hi, 0, 1, 2, 3, 4, 5, 6, 7);
}
__device__ __forceinline__ bf16x8 ld_frag(unsigned addr) { return *(const LAS bf16x8*)(size_t)(addr); }
__device__ __forceinline__ void wait_go(rsrc_t rG, unsigned nonce, unsigned seen) {
  for (unsigned spin = 0; seen != nonce && spin < SPIN; ++spin) {
    seen = __builtin_amdgcn_readfirstlane(ld4_sc1(rG, 0, G_ANS + blockIdx.x * 4));
    __builtin_amdgcn_s_sleep(1);
  }
}

template <bool ZB>
__device__ __forceinline__ void phase1(const Args& a, int pm, int cb, int kx, const u16* Pp, int kx2, f32x4 (&z1v)[2][2]) {
  const int tid = threadIdx.x, lane = tid & 63;
  const int wid = __builtin_amdgcn_readfirstlane(tid >> 6);
  const int wm = wid & 3, wn = wid >> 2;
  STAMP_DECL STAMP(0);
  const rsrc_t rX = RSRC(a.x + (size_t)pm * 128 * D, 128 * D * 2);
  const rsrc_t rW = ZB ? RSRC(a.win + 64 * cb, (D * F - 64 * cb) * 2) : RSRC(a.wdi + (size_t)384 * D, 128 * D * 2);
  const int r8 = lane >> 3, p8 = lane & 7;
  const unsigned sw_e = (p8 ^ (r8 >> 1)) << 4;
  const unsigned vx_e = (32 * (wid >> 1) + 4 * (wid & 1) + 8 * (r8 >> 2) + (r8 & 3)) * (D * 2) + sw_e;
  const unsigned vx_o = (vx_e ^ 64) + 16 * (D * 2);
  const unsigned vw2 = ZB ? (8 * wid + r8) * (F * 2) + ((p8 ^ ((((r8 >> 1) & 1) | ((wid & 1) << 1)) << 1)) << 4)
                          : (64 + 8 * wid + r8) * (D * 2) + (sw_e ^ ((wid & 1) << 6));
  const unsigned vw1 = ZB ? vw2 + FH * 2 : vw2 - 64 * (D * 2);
  constexpr unsigned WKB = ZB ? 64 * F * 2 : 128;
  const unsigned mb1 = wid * 1024, mb2 = wid * 2048;
  auto piece1 = [&](int st, int p) {
    const unsigned kb = st ^ kx;
    const int base = P1_RING + (st & 3) * 32768;
    if (p == 0) dma16(rX, mb2, base, vx_e, kb * 128);
    else if (p == 1) dma16(rW, mb1, base + 16384, vw1, kb * WKB);
    else if (p == 2) dma16(rX, mb2, base + 1024, vx_o, kb * 128);
    else dma16(rW, mb1, base + 24576, vw2, kb * WKB);
  };
  piece1(0, 0); piece1(0, 1); piece1(0, 2); piece1(0, 3);
  SCHED();

  const int fr = lane & 15, fq = lane >> 4;
  const int kh = wm >> 1;
  const unsigned fsw = ((fq ^ ((fr >> 1) & 7)) << 4) ^ (kh << 6);
  unsigned xa[2][2][2], wb[2][2][2];
#pragma unroll
  for (int m = 0; m < 2; ++m) { xa[0][m][0] = P1_RING + (32 * wm + 16 * (m ^ wn) + fr) * 128 + fsw; xa[0][m][1] = xa[0][m][0] ^ 64; }
#pragma unroll
  for (int n = 0; n < 2; ++n) {
    const int tile = 2 * wn + (n ^ (wm & 1));
    if (ZB) {
      const int q4 = fr >> 2, pp = lane & 3;
      const int sp = ((q4 >> 1) & 1) | ((fq & 1) << 1);
      const int ch = (2 * tile + (pp >> 1)) ^ (sp << 1);
      wb[0][n][0] = P1_RING + 16384 + 128 * (8 * fq + q4) + 16 * ch + 8 * (pp & 1) + kh * 4096;
      wb[0][n][1] = wb[0][n][0] ^ 4096;
    } else {
      wb[0][n][0] = P1_RING + 16384 + (16 * tile + fr) * 128 + fsw;
      wb[0][n][1] = wb[0][n][0] ^ 64;
    }
  }
#pragma unroll
  for (int i = 0; i < 2; ++i)
#pragma unroll
    for (int ks = 0; ks < 2; ++ks) {
      xa[1][i][ks] = xa[0][i][ks] + 65536; wb[1][i][ks] = wb[0][i][ks] + 65536;
      OPAQUE(xa[0][i][ks]); OPAQUE(wb[0][i][ks]); OPAQUE(xa[1][i][ks]); OPAQUE(wb[1][i][ks]);
    }
  auto ldw = [&](unsigned addr) -> bf16x8 { return ZB ? ld_tr(addr) : ld_frag(addr); };
  SCHED();
  piece1(1, 0); piece1(1, 1); piece1(1, 2); piece1(1, 3);
  const rsrc_t rG = RSRC(a.ws + WS_G, G_END);
  if (wid == 7) dma4_sc1(rG, 0, 0, G_ANS + blockIdx.x * 4);
  unsigned nonce = 0;
  STAMP(1);

  bf16x8 xf[16][2], wf[2][4];
  const f32x4 Z4 = {0, 0, 0, 0};
  f32x4 acc1[2][2] = {{Z4, Z4}, {Z4, Z4}}, acc2[2][2] = {{Z4, Z4}, {Z4, Z4}};
  f32x4 gram = Z4, rsum = Z4, cs1 = Z4, cs2 = Z4;
  const bf16x8 ONES = {0x3f80, 0x3f80, 0x3f80, 0x3f80, 0x3f80, 0x3f80, 0x3f80, 0x3f80};

  auto mm = [&](int kk, int i) {
    const int ks = kk & 1;
    if (i == 0) gram = MFMA16(xf[kk][0], xf[kk][0], gram);
    else if (i == 1) rsum = MFMA16(xf[kk][0], ONES, rsum);
    else if (i < 6) { const int m = (i - 2) >> 1, n = i & 1; acc1[m][n] = MFMA16(xf[kk][m], wf[ks][n], acc1[m][n]); }
    else if (i < 10) { const int m = (i - 6) >> 1, n = i & 1; acc2[m][n] = ZB ? MFMA16(wf[ks][2 + n], xf[kk][m], acc2[m][n]) : MFMA16(xf[kk][m], wf[ks][2 + n], acc2[m][n]); }
    else if (i == 10) cs1 = MFMA16(ONES, wf[0][0], cs1);
    else cs2 = MFMA16(ONES, wf[0][2], cs2);
  };
  auto rd = [&](int kk, int i) {
    const int st = kk >> 1, ks = kk & 1, wi = (st >> 1) & 1, so = (st & 1) * 32768;
    if (i == 0 || i == 3) xf[kk][i & 1] = ld_frag(xa[wi][i & 1][ks] + so);
    else if (i < 3) wf[ks][i - 1] = ldw(wb[wi][i - 1][ks] + so);
    else wf[ks][i - 2] = ldw(wb[wi][i - 4][ks] + so + 8192);
  };
  auto piece3 = [&](int p) {
    const rsrc_t rD = RSRC((cb < 8 ? a.wdr : a.wdi) + (size_t)(cb & 7) * 64 * D, 64 * D * 2);
    const unsigned ve = r8 * (D * 2) + sw_e, vo = (ve ^ 64) + 8 * (D * 2);
    dma16(rD, wid * 8192, p * 1024, (p & 1) ? vo : ve, (wid ^ kx) * 128 + (p >> 1) * 16 * (D * 2));
  };
  auto earlyP = [&](int k) {
    const rsrc_t rP = RSRC(Pp, 128 * S * 2);
    const unsigned ve = (16 * wid + r8) * (S * 2) + sw_e, vo = (ve ^ 64) + 8 * (S * 2);
    dma16(rP, mb2, P2_PB + (k + 2) * 16384, ve, (k ^ kx2) * 128);
    dma16(rP, mb2, P2_PB + (k + 2) * 16384 + 1024, vo, (k ^ kx2) * 128);
  };
#pragma unroll
  for (int st = 0; st < 8; ++st) {
    wait_vn(st <= 6 ? 4 : ZB ? 8 : 0);
    WL0();
    BAR();
    if (st == 0) STAMP(2);
    SCHED();
#pragma unroll
    for (int i = 0; i < 10; ++i) {
      if (st > 0) mm(2 * st - 1, i);
      SCHED();
      if (i < 6) rd(2 * st, i);
      if (st + 2 < 8) { if (i == 3) piece1(st + 2, 0); if (i == 8) piece1(st + 2, 1); }
      if (ZB && st == 6 && (i % 3) == 0) piece3(i / 3);
      if (st == 7 && i == 7) earlyP(0);
      SCHED();
    }
    WL0();
    SCHED();
#pragma unroll
    for (int i = 0; i < 12; ++i) {
      mm(2 * st, i);
      SCHED();
      if (i < 6) rd(2 * st + 1, i);
      if (st + 2 < 8) { if (i == 3) piece1(st + 2, 2); if (i == 9) piece1(st + 2, 3); }
      if (ZB && st == 6 && (i % 3) == 2) piece3(4 + i / 3);
      if (st == 7 && i == 8) earlyP(1);
      SCHED();
    }
    if (st == 2 && wid == 7) {
      nonce = __builtin_amdgcn_readfirstlane(*(LAS unsigned*)(size_t)(lane * 4)) + 1;
      st4_sc1(nonce, rG, 0, G_REQ + blockIdx.x * 4);
    }
    SCHED();
  }
  LAS float* const s_r = (LAS float*)(size_t)(P1_SCR);
  LAS float* const s_c = (LAS float*)(size_t)(P1_SCR + 512);
  LAS float* const s_u1 = (LAS float*)(size_t)(P1_SCR + 1024);
  LAS float* const s_u2 = (LAS float*)(size_t)(P1_SCR + 1536);
  LAS float* const s_z = (LAS float*)(size_t)(P1_SCR + 2048);
  LAS float* const s_n = (LAS float*)(size_t)(P1_SCR + 4096);
  LAS float* const s_u3 = (LAS float*)(size_t)(P1_SCR + 5120);
  const unsigned tb0 = fr * 128 + fsw, tb1 = tb0 ^ 64;
  WAIT_V(4);
  {
    const unsigned cb0 = tb0 + wid * 8192, cb1 = cb0 ^ 64;
    bf16x8 c3[8];
    f32x4 cs3[4];
    if (ZB) {
#pragma unroll
      for (int t = 0; t < 4; ++t) { c3[2 * t] = ld_frag(cb0 + t * 2048); c3[2 * t + 1] = ld_frag(cb1 + t * 2048); }
    }
    mm(15, 0); mm(15, 1);
    SCHED();
    if (ZB) {
#pragma unroll
      for (int t = 0; t < 4; ++t) cs3[t] = MFMA16(ONES, c3[2 * t], Z4);
#pragma unroll
      for (int t = 0; t < 4; ++t) cs3[t] = MFMA16(ONES, c3[2 * t + 1], cs3[t]);
    }
    SCHED();
#pragma unroll
    for (int i = 2; i < 10; ++i) mm(15, i);
    STAMP(3);
    unsigned i01 = 32 * wm + 16 * wn, i23 = 64 * kh + 16 * (2 * wn + (wm & 1));
    asm volatile("" : "+s"(i01), "+s"(i23));
    const float s1 = sel4(rsum, fr & 3), s2 = sel4(gram, fr & 3);
    const float mean = s1 * (1.0f / D);
    const float rstd = __builtin_amdgcn_rsqf(fmaxf(s2 * (1.0f / D) - mean * mean, 0.0f) + 1e-5f);
    if ((fr >> 2) == fq) { s_r[i01 + fr] = rstd; s_c[i01 + fr] = -mean * rstd; }
    if (fq < 2) s_u1[128 * fq + i23 + fr] = fq ? cs2[0] : cs1[0];
    if (ZB) s_u3[lane * 8 + wid] = sel4(f32x4{cs3[0][0], cs3[1][0], cs3[2][0], cs3[3][0]}, fq);
  }
  const int s0 = pm * 128 + 32 * wm;
  const rsrc_t rT = RSRC(a.ws + WS_Q, (2 * D + FH) * S * 2);
  const unsigned vT = ((fr >> 2) & 1) * 65536 + fq * 256 + (4 * (fr >> 3) + (fr & 3)) * 16;
  WL0();
  BAR();
  STAMP(4);
  if (wid == 7) dma4_sc1(rG, P1_SCR + 7168, 0, G_ANS + blockIdx.x * 4);
  earlyP(2); earlyP(3);
  f32x4 rq[2], cq[2];
  auto ld_rc = [&]() {
#pragma unroll
    for (int m = 0; m < 2; ++m) {
      rq[m] = *(LAS f32x4*)(s_r + 32 * wm + 16 * (m ^ wn) + 4 * fq);
      cq[m] = *(LAS f32x4*)(s_c + 32 * wm + 16 * (m ^ wn) + 4 * fq);
    }
  };
  ld_rc();
  auto store_t = [&](int T, f32x4 v0, f32x4 v1) {
    const unsigned a0 = cvt_pk(v0[0], v0[1]), a1 = cvt_pk(v0[2], v0[3]), b0 = cvt_pk(v1[0], v1[1]), b1 = cvt_pk(v1[2], v1[3]);
    const u32x4 o = wn ? u32x4{b0, b1, a0, a1} : u32x4{a0, a1, b0, b1};
    st16_sc1(o, rT, vT, (T >> 1) * 131072 + (T & 1) * 128 + s0 * 32);
  };
  auto lin_store = [&](f32x4 c0, f32x4 c1, float u, int T) { store_t(T, rq[0] * c0 + cq[0] * u, rq[1] * c1 + cq[1] * u); };
  float u1[2];
#pragma unroll
  for (int n = 0; n < 2; ++n) { const int ui = 16 * (2 * wn + (n ^ (wm & 1))) + fr; u1[n] = s_u1[ui] + s_u1[64 + ui]; }
  if (!ZB) {
#pragma unroll
    for (int n = 0; n < 2; ++n) {
      const int tile = 2 * wn + (n ^ (wm & 1)), ui = 16 * tile + fr;
      lin_store(acc1[0][n], acc1[1][n], u1[n], 56 + tile);
      lin_store(acc2[0][n], acc2[1][n], s_u2[ui] + s_u2[64 + ui], 60 + tile);
    }
    if (wid == 7) { WAIT_V(0); wait_go(rG, nonce, __builtin_amdgcn_readfirstlane(*(LAS unsigned*)(size_t)(P1_SCR + 7168 + lane * 4))); }
    STAMP(5); STAMP(6); STAMP(7); STAMP(8); STAMP(9);
    STAMP_OUT(a.ws, 0, 10);
    return;
  }
  {
    f32x4 ps[2], pq[2];
#pragma unroll
    for (int n = 0; n < 2; ++n)
#pragma unroll
      for (int m = 0; m < 2; ++m) {
        f32x4 v = rq[m] * acc1[m][n] + cq[m] * u1[n];
        const f32x2 g0 = gelu2(f32x2{v[0], v[1]}), g1 = gelu2(f32x2{v[2], v[3]});
        v = f32x4{g0[0], g0[1], g1[0], g1[1]};
        acc1[m][n] = v;
        if (n == 0) { ps[m] = v; pq[m] = v * v; } else { ps[m] += v; pq[m] += v * v; }
      }
    float t[8], h[4];
#define DPP2(d, x, y, ctl, m0, m1) "v_add_f32_dpp " d ", " x ", " x " " ctl " row_mask:0xf bank_mask:" m0 "\n\tv_add_f32_dpp " d ", " y ", " y " " ctl " row_mask:0xf bank_mask:" m1 "\n\t"
#define DPPQ(d, ctl) "v_add_f32_dpp " d ", " d ", " d " " ctl " row_mask:0xf bank_mask:0xf\n\t"
    asm volatile("s_nop 1\n\t" DPP2("%0", "%8", "%16", "row_ror:8", "0x3", "0xc") DPP2("%1", "%9", "%17", "row_ror:8", "0x3", "0xc")
                 DPP2("%2", "%10", "%18", "row_ror:8", "0x3", "0xc") DPP2("%3", "%11", "%19", "row_ror:8", "0x3", "0xc")
                 DPP2("%4", "%12", "%20", "row_ror:8", "0x3", "0xc") DPP2("%5", "%13", "%21", "row_ror:8", "0x3", "0xc")
                 DPP2("%6", "%14", "%22", "row_ror:8", "0x3", "0xc") DPP2("%7", "%15", "%23", "row_ror:8", "0x3", "0xc") "s_nop 1"
                 : "=&v"(t[0]), "=&v"(t[1]), "=&v"(t[2]), "=&v"(t[3]), "=&v"(t[4]), "=&v"(t[5]), "=&v"(t[6]), "=&v"(t[7])
                 : "v"(ps[0][0]), "v"(pq[0][0]), "v"(ps[0][1]), "v"(pq[0][1]), "v"(ps[0][2]), "v"(pq[0][2]), "v"(ps[0][3]), "v"(pq[0][3]),
                   "v"(ps[1][0]), "v"(pq[1][0]), "v"(ps[1][1]), "v"(pq[1][1]), "v"(ps[1][2]), "v"(pq[1][2]), "v"(ps[1][3]), "v"(pq[1][3]));
    asm volatile("s_nop 1\n\t" DPP2("%0", "%4", "%8", "row_half_mirror", "0x5", "0xa") DPP2("%1", "%5", "%9", "row_half_mirror", "0x5", "0xa")
                 DPP2("%2", "%6", "%10", "row_half_mirror", "0x5", "0xa") DPP2("%3", "%7", "%11", "row_half_mirror", "0x5", "0xa") "s_nop 1\n\t"
                 DPPQ("%0", "quad_perm:[1,0,3,2]") DPPQ("%1", "quad_perm:[1,0,3,2]") DPPQ("%2", "quad_perm:[1,0,3,2]") DPPQ("%3", "quad_perm:[1,0,3,2]")
                 DPPQ("%0", "quad_perm:[2,3,0,1]") DPPQ("%1", "quad_perm:[2,3,0,1]") DPPQ("%2", "quad_perm:[2,3,0,1]") DPPQ("%3", "quad_perm:[2,3,0,1]") "s_nop 0"
                 : "=&v"(h[0]), "=&v"(h[1]), "=&v"(h[2]), "=&v"(h[3])
                 : "v"(t[0]), "v"(t[1]), "v"(t[2]), "v"(t[3]), "v"(t[4]), "v"(t[5]), "v"(t[6]), "v"(t[7]));
    if ((fr & 3) == 0) *(LAS f32x4*)(s_z + (wn * 128 + 32 * wm + 8 * fq + 4 * ((fr >> 3) ^ wn) + 2 * ((fr >> 2) & 1)) * 2) = f32x4{h[0], h[1], h[2], h[3]};
  }
  WL0();
  BAR();
  STAMP(5);
  if (wid == 7) {
    __builtin_amdgcn_s_setprio(2);
    const f32x4 p = *(LAS f32x4*)(s_z + lane * 4) + *(LAS f32x4*)(s_z + 256 + lane * 4);
    WAIT_V(4);
    wait_go(rG, nonce, __builtin_amdgcn_readfirstlane(*(LAS unsigned*)(size_t)(P1_SCR + 7168 + lane * 4)));
    st16_sc1(__builtin_bit_cast(u32x4, p) | 1u, rG, lane * 16, G_PAY + (pm * 14 + cb) * 1024);
  }
  STAMP(6);
  {
    float u3[2];
    f32x4 acct[2][2] = {{Z4, Z4}, {Z4, Z4}};
    const unsigned t30 = tb0 + wn * 4096, t31 = tb1 + wn * 4096;
    bf16x8 w[8];
    auto rd3 = [&](int f) { const int kk = f >> 1; w[f & 7] = ld_frag(((kk & 1) ? t31 : t30) + (kk >> 1) * 8192 + (f & 1) * 2048); };
#pragma unroll
    for (int f = 0; f < 4; ++f) rd3(f);
    SCHED();
#pragma unroll
    for (int f = 0; f < 32; ++f) {
      const int kk = f >> 1, n = f & 1;
      acct[0][n] = MFMA16(xf[kk][0], w[f & 7], acct[0][n]);
      SCHED();
      if (f + 4 < 32) rd3(f + 4);
      if (f == 8 || f == 12) {
        const int m = (f - 8) >> 2;
        const float r = s_r[32 * wm + 16 * (m ^ wn) + fr], c = s_c[32 * wm + 16 * (m ^ wn) + fr];
#pragma unroll
        for (int q = 0; q < 2; ++q) {
          const int ui = 16 * (2 * wn + (q ^ (wm & 1))) + 4 * fq;
          z1v[m][q] = acc2[m][q] * r + (*(LAS f32x4*)(s_u2 + ui) + *(LAS f32x4*)(s_u2 + 64 + ui)) * c;
        }
      }
      if (f == GATHER_AT && wid == 7) {
#pragma unroll
        for (int i = 0; i < 14; ++i) dma16_sc1(rG, 0, P2_PB + i * 1024, lane * 16, G_PAY + pm * 14336 + i * 1024);
      }
      if (f == 20 || f == 24) {
        const int q = (f - 20) >> 2;
        const f32x4 uu = *(LAS f32x4*)(s_u3 + (32 * wn + 16 * q + fr) * 8) + *(LAS f32x4*)(s_u3 + (32 * wn + 16 * q + fr) * 8 + 4);
        u3[q] = (uu[0] + uu[1]) + (uu[2] + uu[3]);
      }
      SCHED();
      acct[1][n] = MFMA16(xf[kk][1], w[f & 7], acct[1][n]);
      SCHED();
    }
    if (wid != 7) {
#pragma unroll
      for (int n = 0; n < 2; ++n) lin_store(acct[0][n], acct[1][n], u3[n], 4 * cb + 2 * wn + n);
    }
    STAMP(7);
  if (wid == 7) {
    f32x4 t;
    for (unsigned spin = 0; spin < SPIN; ++spin) {
      WAIT_V(0);
      u32x4 al = *(LAS u32x4*)(size_t)(P2_PB + lane * 16);
      t = __builtin_bit_cast(f32x4, al);
#pragma unroll
      for (int i = 1; i < 14; ++i) { const u32x4 pv = *(LAS u32x4*)(size_t)(P2_PB + i * 1024 + lane * 16); al &= pv; t += __builtin_bit_cast(f32x4, pv); }
      if (__all((al[0] & al[1] & al[2] & al[3] & 1u) != 0)) break;
      WL0();
#pragma unroll
      for (int i = 0; i < 14; ++i) dma16_sc1(rG, 0, P2_PB + i * 1024, lane * 16, G_PAY + pm * 14336 + i * 1024);
    }
    const float m0 = t[0] * (1.0f / FH), m1 = t[2] * (1.0f / FH);
    const float r0 = __builtin_amdgcn_rsqf(fmaxf(t[1] * (1.0f / FH) - m0 * m0, 0.0f) + 1e-5f);
    const float r1 = __builtin_amdgcn_rsqf(fmaxf(t[3] * (1.0f / FH) - m1 * m1, 0.0f) + 1e-5f);
    *(LAS f32x2*)(s_n + 2 * lane) = f32x2{r0, r1};
    *(LAS f32x2*)(s_n + 128 + 2 * lane) = f32x2{-m0 * r0, -m1 * r1};
    __builtin_amdgcn_s_setprio(0);
  }
  WL0();
  BAR();
  if (wid == 7) {
#pragma unroll
    for (int n = 0; n < 2; ++n) lin_store(acct[0][n], acct[1][n], u3[n], 4 * cb + 2 * wn + n);
  }
  }
  STAMP(8);
  {
    f32x4 r2[2], c2[2];
#pragma unroll
    for (int m = 0; m < 2; ++m) {
      r2[m] = *(LAS f32x4*)(s_n + 32 * wm + 8 * fq + 4 * (m ^ wn));
      c2[m] = *(LAS f32x4*)(s_n + 128 + 32 * wm + 8 * fq + 4 * (m ^ wn));
    }
#pragma unroll
    for (int n = 0; n < 2; ++n) store_t(64 + 4 * cb + 2 * wn + (n ^ (wm & 1)), acc1[0][n] * r2[0] + c2[0], acc1[1][n] * r2[1] + c2[1]);
  }
  STAMP(9);
  STAMP_OUT(a.ws, 0, 10);
}

__device__ __forceinline__ void warm_window(const Args& a, int junk, int rb, int w, int wv, int lane) {
  if (w < 8) {
    const unsigned vo = (rb * 128 + wv * 32 + (lane >> 2)) * (S * 2) + (lane & 3) * 128;
#pragma unroll
    for (int m = 0; m < 3; ++m) {
      const rsrc_t r = RSRC(m == 0 ? a.wsr : m == 1 ? a.wsi : a.wsp, S * S * 2);
      touch(r, junk, vo, w * 512);
      touch(r, junk, vo, w * 512 + 16 * (S * 2));
    }
  } else {
    const rsrc_t r = RSRC(a.wout + (size_t)rb * 56 * D, 56 * D * 2);
    touch(r, junk, lane * 128, wv * 16384);
    touch(r, junk, lane * 128, wv * 16384 + 8192);
  }
}

__global__ __launch_bounds__(512, 2) void fused(Args a) {
  extern __shared__ __attribute__((aligned(1024))) char smem[];
  asm volatile("" ::"v"(smem));
  const int bx = blockIdx.x & 7, bi = blockIdx.x >> 3;
  if (bi < CODEWARM && threadIdx.x < 64) {
    unsigned long long pc;
    asm volatile("s_getpc_b64 %0" : "=s"(pc));
    touch(RSRC(pc & ~127ull, CODEWARM * 8192), 1024, threadIdx.x * 128, bi * 8192);
  }
  const int pm = 4 * (bx >> 1) + (bi >> 3), cb = 2 * (bi & 7) + (bx & 1);
  int mat, tm, tn;
  if (bx & 1) { mat = 2; tm = pm; tn = bi & 7; }
  else { mat = bx >> 2; tm = ((bx >> 1) & 1) * 8 + (bi >> 2); tn = bi & 3; }
  const int kx = (tm ^ tn) & K2_KXMASK;
  const u16* Pp = (mat == 0 ? a.wsr : mat == 1 ? a.wsi : a.wsp) + (size_t)tm * 128 * S;
  f32x4 z1v[2][2];
  {
    const int kx1 = ((bi & 7) ^ (2 * (bi >> 3))) & K1_KXMASK;
    if (__builtin_expect(cb < 14, 1)) phase1<true>(a, pm, cb, kx1, Pp, kx, z1v);
    else if (cb == 14) phase1<false>(a, pm, cb, kx1, Pp, kx, z1v);
  }
  int tid = threadIdx.x;
  asm volatile("" : "+v"(tid));
  const int lane = tid & 63;
  const int wid = __builtin_amdgcn_readfirstlane(tid >> 6);
  STAMP_DECL STAMP(0);
  const int tm3 = bx * 4 + (bi >> 3), tn3 = bi & 7;
  const int fr = lane & 15, fq = lane >> 4;
  const unsigned vl = lane * 16;
  const rsrc_t rG = RSRC(a.ws + WS_G, G_END);
  const bool work2 = cb != 15;
  auto z1_finish = [&]() {
    if (cb < 14) {
      const int wm = wid & 3, wn = wid >> 2;
      const rsrc_t rZ = RSRC(a.ws + WS_Z1, FH * S * 2);
      const unsigned vZ = (fr >> 3) * (112 * 256) + (2 * ((fq & 1) ^ (wm & 1)) + (fq >> 1)) * 256 + (fr & 7) * 16;
      const unsigned sZ = ((pm * 8 + 2 * wm) * 112 + 8 * cb + 4 * wn) * 256;
#pragma unroll
      for (int m = 0; m < 2; ++m) {
        unsigned pk[2][2];
#pragma unroll
        for (int n = 0; n < 2; ++n) {
          const f32x2 g0 = gelu2(f32x2{z1v[m][n][0], z1v[m][n][1]}), g1 = gelu2(f32x2{z1v[m][n][2], z1v[m][n][3]});
          pk[n][0] = cvt_pk(g0[0], g0[1]); pk[n][1] = cvt_pk(g1[0], g1[1]);
        }
        auto w0 = __builtin_amdgcn_permlane16_swap(pk[0][0], pk[1][0], false, false);
        auto w1 = __builtin_amdgcn_permlane16_swap(pk[0][1], pk[1][1], false, false);
        st16_sc1(u32x4{w0[0], w1[0], w0[1], w1[1]}, rZ, vZ, sZ + 128 * (m ^ wn));
      }
    }
  };
  if (blockIdx.x == 255) {
    for (int i = 0; i < 36; ++i) st16_sc1(u32x4{0, 0, 0, 0}, rG, vl, (wid * 36 + i) * 1024);
    st16_sc1(u32x4{0, 0, 0, 0}, rG, vl, G_ALL);
  }
  unsigned bv[4];
#pragma unroll
  for (int ni = 0; ni < 4; ++ni) bv[ni] = a.bsp[(8 * tm + 4 * ((wid >> 1) & 1) + ni) * 16 + fr];
  asm volatile("" : "+v"(bv[0]), "+v"(bv[1]), "+v"(bv[2]), "+v"(bv[3]));
  if (work2 || blockIdx.x == 255) {
    WAIT_V(0);
    WL0();
    BAR();
  }
  if (wid >= 4) {
    const int lw = wid - 4;
    const int r8 = lane >> 3, p = lane & 7;
    if (wid == 7) {
      if (!work2) {
        if (blockIdx.x == 255) {
          for (unsigned spin = 0; spin < SPIN; ++spin) {
            const u32x4 rq = ld16_sc1(rG, vl, G_REQ);
            st16_sc1(rq, rG, vl, G_ANS);
            const u32x4 fl = ld16_sc1(rG, vl, G_ALL);
            if (__all((fl[0] & fl[1] & fl[2] & (lane == 63 ? 1u : fl[3])) == 1u)) break;
          }
        } else {
          const unsigned nonce = __builtin_amdgcn_readfirstlane(ld4_sc1(rG, 0, G_ANS + blockIdx.x * 4)) + 1;
          st4_sc1(nonce, rG, 0, G_REQ + blockIdx.x * 4);
          wait_go(rG, nonce, nonce - 1);
        }
      }
      const unsigned box = (fq & 1) * 128 + (cb >> 1) * 16 + fr;
      st4_sc1(1u, rG, fq < 2 ? box * 128 + (pm * 2 + (cb & 1)) * 4 : G_ALL + blockIdx.x * 4, 0);
    }
    const rsrc_t rP = RSRC(Pp, 128 * S * 2);
    const unsigned vo_e = (lw * 32 + r8) * (S * 2) + ((p ^ (r8 >> 1)) << 4);
    const unsigned vo_o = (vo_e ^ 64) + 8 * (S * 2);
    auto stageP = [&](int kt, unsigned poff) {
      const unsigned mb = poff + lw * 4096, so = (kt ^ kx) * 128;
#pragma unroll
      for (int j = 0; j < 4; ++j) dma16(rP, mb, P2_PB + j * 1024, (j & 1) ? vo_o : vo_e, so + (j >> 1) * 16 * (S * 2));
    };
    if (work2) {
      BAR();
      if (!BULKLATE) { stageP(4, 0); stageP(5, 16384); }
    }
    const rsrc_t rWo = RSRC(a.wout + 64 * tn3, (FH * D - 64 * tn3) * 2);
    const unsigned vw_e = (32 * lw + r8) * (D * 2) + ((p ^ (((r8 >> 1) & 1) << 1)) << 4), vw_o = (vw_e ^ 64) + 8 * (D * 2);
    u32x4 wreg[12];
    auto wpark = [&]() {
#pragma unroll
      for (int i = 0; i < 12; ++i) {
        const int rg = i < 8 ? 2 + (i >> 2) : 6;
        ld16_async(wreg[i], rWo, (i & 1) ? vw_o : vw_e, (rg * 128 + ((i >> 1) & 1) * 16) * (D * 2));
      }
    };
    if (!BULKLATE && work2) wpark();
    auto wput = [&](int rg) {
#pragma unroll
      for (int j = 0; j < 4 && !NOPARK; ++j) *(LAS u32x4*)(size_t)(rg * 16384 + lw * 4096 + j * 1024 + vl) = wreg[(rg == 6 ? 8 : (rg - 2) * 4) + j];
    };
    auto wdma = [&](int rg) {
#pragma unroll
      for (int j = 0; j < 4; ++j) dma16(rWo, lw * 4096, rg * 16384 + j * 1024, (j & 1) ? vw_o : vw_e, (rg * 128 + (j >> 1) * 16) * (D * 2));
    };
    if (work2) {
      const unsigned box = (mat * 4 + tn) * 16 + tm;
      bool allq = true;
      if (wid == 7) {
        for (unsigned spin = 0; spin < SPIN; ++spin) {
          const unsigned fl = ld4_sc1(rG, (lane & 31) * 4, G_F1 + box * 128);
          const bool ok = fl == 1u || (box >= 112 && box < 128 && (lane & 1));
          allq = __all(ok);
          if (__all(ok || (WINSTART && (lane & 31) > 3))) break;
        }
      }
      BAR();
      STAMP(1);
      const rsrc_t rQ = RSRC(a.ws + WS_Q + (size_t)(mat * 4 + tn) * 128 * S * 2, 128 * S * 2);
      const unsigned vq = vl + lw * 131072;
      auto stageQ = [&](int kt) {
        const unsigned mb = (kt & 3) * 16384 + lw * 4096, so = (kt ^ kx) * 2048;
#pragma unroll
        for (int j = 0; j < 4; ++j) dma16_sc1(rQ, mb, j * 1024, vq, so + (j >> 1) * 65536 + (j & 1) * 1024);
      };
      stageQ(0); stageQ(1);
      z1_finish();
      WAIT_V(4); BAR();
      stageQ(2); stageQ(3);
      for (unsigned spin = 0; !allq && spin < SPIN; ++spin)
        allq = __all(ld4_sc1(rG, (lane & 31) * 4, G_F1 + box * 128) == 1u || (box >= 112 && box < 128 && (lane & 1)));
      if (BULKLATE) {
        stageP(4, 0); stageP(5, 16384);
        WAIT_V(16); BAR(); stageQ(4); stageP(6, 2 * 16384);
        WAIT_V(20); BAR(); stageQ(5); stageP(7, 3 * 16384);
        WAIT_V(24); BAR(); stageQ(6); stageP(8, 4 * 16384);
        wpark();
      } else {
        WAIT_V(8); BAR(); stageQ(4); stageP(6, 2 * 16384);
        WAIT_V(12); BAR(); stageQ(5); stageP(7, 3 * 16384);
        WAIT_V(16); BAR(); stageQ(6); stageP(8, 4 * 16384);
      }
      unsigned poff = 5 * 16384;
      for (int kt = 3; kt < 26; ++kt) {
        WAIT_V(20);
        BAR();
        if (kt == 3 && wid == 7 && cb < 14) st4_sc1(1u, rG, 0, G_FZ + (pm * 8 + (cb >> 1)) * 128 + (cb & 1) * 4);
        stageQ(kt + 4); stageP(kt + 6, poff);
        poff = poff == 5 * 16384 ? 0 : poff + 16384;
      }
      if (mat == 2) {
        const rsrc_t rZ = RSRC(a.ws + WS_Z1 + (size_t)((8 * tm + lw) * 112 + 16 * tn) * 256, 4 * 112 * 256 + 4096);
        auto stageZ = [&](int h) {
#pragma unroll
          for (int j = 0; j < 4; ++j) dma16_sc1(rZ, lw * 4096, P2_Z + h * 16384 + j * 1024, vl, h * (4 * 112 * 256) + j * 1024);
        };
        if (wid == 7) {
          for (unsigned spin = 0; spin < SPIN; ++spin)
            if (__all(ld4_sc1(rG, (lane & 1) * 4, G_FZ + (tm * 8 + tn) * 128) == 1u)) break;
        }
        WAIT_V(20); BAR(); stageQ(30); stageZ(0);
        WAIT_V(20); BAR(); stageQ(31); stageZ(1);
        WAIT_V(20); BAR(); wdma(0); wdma(4);
        WAIT_V(20); BAR(); wdma(1); wdma(5);
        WAIT_V(20); BAR(); wput(2); wput(6);
        WAIT_V(16); BAR(); wput(3);
      } else {
        WAIT_V(20); BAR(); stageQ(30);
        WAIT_V(16); BAR(); stageQ(31);
        WAIT_V(12); BAR(); wdma(0); wdma(4);
        WAIT_V(12); BAR(); wdma(1); wdma(5);
        WAIT_V(16); BAR(); wput(2); wput(6);
        BAR(); wput(3);
      }
    } else {
      STAMP(1);
      wdma(0); wdma(4); wdma(1); wdma(5); wdma(2); wdma(3); wdma(6);
    }
    STAMP(2); STAMP(3);
    WAIT_V(0);
  } else if (work2) {
    const int wp = wid >> 1, wq = wid & 1;
    f32x4 acc[4][4];
#pragma unroll
    for (int ni = 0; ni < 4; ++ni) {
      const float bias = mat == 2 ? __uint_as_float(bv[ni] << 16) : 0.0f;
#pragma unroll
      for (int n = 0; n < 4; ++n) acc[ni][n] = f32x4{bias, bias, bias, bias};
    }
    BAR();
    if (DWARM && cb == 14) warm_window(a, 0, pm, 2, wid, lane);
    const int sw = (fr >> 1) & 7;
    const unsigned ra = P2_PB + (wp * 64 + fr) * 128 + ((fq ^ sw) << 4);
    const unsigned rb = wq * 8192 + vl;
    bf16x8 a0[4], b0[4], a1[4], b1[4];
    unsigned poff = 2 * 16384;
    unsigned pa0 = ra + poff, pa1 = pa0 ^ 64;
#define K2_RD(AF, BF, pa, sl, ks, i) do { \
    if ((i) == 0) AF[0] = ld_frag(pa); \
    else if ((i) <= 4) BF[(i) - 1] = ld_frag(rb + (sl) * 16384 + ((i) - 1) * 2048 + (ks) * 1024); \
    else AF[(i) - 4] = ld_frag(pa + ((i) - 4) * 2048); } while (0)
#define K2_MM(AF, BF, i) acc[(i) >> 2][(i) & 3] = MFMA16(BF[(i) & 3], AF[(i) >> 2], acc[(i) >> 2][(i) & 3])
#define K2_STEP(s, LAST) do { \
    SCHED(); WL0(); SCHED(); \
    _Pragma("unroll") for (int i = 0; i < 16; ++i) { K2_MM(a0, b0, i); SCHED(); if (i < 8) K2_RD(a1, b1, pa1, s, 1, i); \
      if (i == 10) { poff = poff == 5 * 16384 ? 0 : poff + 16384; pa0 = ra + poff; } SCHED(); } \
    WL0(); \
    if ((s) == 3) WAIT_V(0); \
    BAR(); \
    SCHED(); \
    _Pragma("unroll") for (int i = 0; i < 16; ++i) { K2_MM(a1, b1, i); SCHED(); if (!(LAST) && i < 8) K2_RD(a0, b0, pa0, ((s) + 1) & 3, 0, i); \
      if (i == 10) pa1 = pa0 ^ 64; SCHED(); } \
  } while (0)
    if (DWARM && cb == 14) WAIT_V(0);
    BAR();
    z1_finish();
    BAR();
    STAMP(1);
#pragma unroll
    for (int i = 0; i < 8; ++i) K2_RD(a0, b0, pa0, 0, 0, i);
    for (int kt = 0; kt < 28; kt += 4) { K2_STEP(0, 0); K2_STEP(1, 0); K2_STEP(2, 0); K2_STEP(3, 0); }
    K2_STEP(0, 0); K2_STEP(1, 0); K2_STEP(2, 0); K2_STEP(3, 1);
    SCHED();
    STAMP(2);
    if (mat < 2) {
      const rsrc_t rY = RSRC(a.ws + WS_Y + (size_t)mat * S * D * 2, S * D * 2);
      const unsigned so = ((8 * tm + 4 * wp) * 64 + 16 * tn + 8 * wq) * 256;
#pragma unroll
      for (int ni = 0; ni < 4; ++ni)
#pragma unroll
        for (int P = 0; P < 2; ++P) {
          const f32x4 c0 = acc[ni][2 * P], c1 = acc[ni][2 * P + 1];
          const u32x4 v = {cvt_pk(c0[0], c0[1]), cvt_pk(c0[2], c0[3]), cvt_pk(c1[0], c1[1]), cvt_pk(c1[2], c1[3])};
          st16_sc1(v, rY, vl, so + (ni * 64 + 4 * P) * 256);
        }
    } else {
      const rsrc_t rH = RSRC(a.ws + WS_H, S * FH * 2);
      const unsigned so = ((8 * tm + 4 * wp) * 112 + 16 * tn + 8 * wq) * 256;
      const unsigned za = P2_Z + wp * 16384 + wq * 2048 + (fq * 16 + ((fr & 4) << 1) + ((fr & 8) >> 1) + (fr & 3)) * 16;
#pragma unroll
      for (int ni = 0; ni < 4; ++ni) {
#pragma unroll
        for (int P = 0; P < 2; ++P) {
          const f32x4 c0 = acc[ni][2 * P], c1 = acc[ni][2 * P + 1];
          const u32x4 z = *(const LAS u32x4*)(size_t)(za + ni * 4096 + P * 1024);
          const u32x4 v = {cvt_pk(bf_lo(z[0]) * c0[0], bf_hi(z[0]) * c0[1]), cvt_pk(bf_lo(z[1]) * c0[2], bf_hi(z[1]) * c0[3]),
                           cvt_pk(bf_lo(z[2]) * c1[0], bf_hi(z[2]) * c1[1]), cvt_pk(bf_lo(z[3]) * c1[2], bf_hi(z[3]) * c1[3])};
          st16_sc1(v, rH, vl, so + (ni * 112 + 4 * P) * 256);
        }
      }
    }
    STAMP(3);
    WAIT_V(0);
  } else {
    STAMP(1);
    for (int i = 0; i < PF_N && blockIdx.x != 255; ++i) {
      warm_window(a, P2_Z, pm, (PF_LIST >> (4 * i)) & 15, wid, lane);
      WAIT_V(6);
    }
    STAMP(2);
    WAIT_V(0);
    STAMP(3);
  }
  WL0();
  BAR();
  STAMP(4);
  {
    const int wr = (wid + tn3) & 7, tp = wr >> 2, kq = wr & 3;
    const int tt3 = 4 * tm3 + 2 * tp + (kq & 1);
    const int cq = 2 * (fq & 1) + (fq >> 1);
    const int c3 = 8 * tn3 + 2 * (kq & 2);
    if (wid == 4 && work2) {
      const unsigned box = mat == 2 ? fr : (fr & 8) + 2 * tn + (fr & 1);
      st4_sc1(1u, rG, box * 128, G_FP + tm * 2048 + (mat == 2 ? tn : 8 + mat) * 4);
    }
    const int q4 = fr >> 2, pp = lane & 3;
    const int sp = ((q4 >> 1) & 1) | ((fq & 1) << 1);
    const unsigned wa = kq * 4096 + 128 * (8 * fq + q4) + 16 * ((2 * (kq & 2) + (pp >> 1)) ^ (sp << 1)) + 8 * (pp & 1);
    bf16x8 wf[7][4];
    auto ld_w = [&]() {
#pragma unroll
      for (int mi = 0; mi < 4; ++mi) {
        unsigned lo = wa ^ (mi * 32), hi = lo + 65536;
        OPAQUE(hi);
#pragma unroll
        for (int g = 0; g < 7; ++g) wf[g][mi] = ld_tr((g < 4 ? lo : hi) + (g & 3) * 16384);
      }
    };
    if (wid == 4) {
      for (unsigned spin = 0; spin < SPIN; ++spin) {
        const unsigned fl = ld4_sc1(rG, fr * 4, G_FP + (tm3 * 8 + tn3) * 128);
        if (__all(fl == 1u || fr == 7 || fr > 9)) break;
      }
    } else {
      ld_w();
    }
    BAR();
    STAMP(5);
    const rsrc_t rH = RSRC(a.ws + WS_H + (size_t)((4 * tm3 + 2 * tp) * 112 + 4 * kq) * 256, 2 * 112 * 256);
    const rsrc_t rY = RSRC(a.ws + WS_Y + (size_t)(tt3 * 64 + c3) * 256, S * D * 2 + 1024);
    const unsigned vh0 = vl + (kq & 1) * (112 * 256), vh1 = vl + ((kq & 1) ^ 1) * (112 * 256);
    u32x4 hf[7][2];
#pragma unroll
    for (int g = 0; g < 7; ++g) {
      hf[g][0] = __builtin_amdgcn_raw_buffer_load_b128(rH, vh0, g * 4096, 16);
      hf[g][1] = __builtin_amdgcn_raw_buffer_load_b128(rH, vh1, g * 4096, 16);
    }
    const unsigned vy = cq * 256 + fr * 16;
    const u32x4 e1 = __builtin_amdgcn_raw_buffer_load_b128(rY, vy, 0, 16), e2 = __builtin_amdgcn_raw_buffer_load_b128(rY, vy, S * D * 2, 16);
    const u32x4 ex = *(const u32x4*)(a.x + (size_t)(tt3 * 16 + fr) * D + (c3 + cq) * 8);
    SCHED();
    if (wid == 4) ld_w();
    f32x4 acc[2][4];
#pragma unroll
    for (int g = 0; g < 7; ++g)
#pragma unroll
      for (int tl = 0; tl < 2; ++tl)
#pragma unroll
        for (int mi = 0; mi < 4; ++mi) acc[tl][mi] = MFMA16(wf[g][mi], __builtin_bit_cast(bf16x8, hf[g][tl]), (g ? acc[tl][mi] : f32x4{0, 0, 0, 0}));
    STAMP(6);
    LAS f32x4* xb = (LAS f32x4*)(size_t)(P3_X) + lane;
#pragma unroll
    for (int d = 1; d < 4; ++d) {
      xb[((wr * 3 + d - 1) * 2) * 64] = acc[d & 1][d & 2];
      xb[((wr * 3 + d - 1) * 2 + 1) * 64] = acc[d & 1][(d & 2) + 1];
    }
    WL0();
    BAR();
    f32x4 s0 = acc[0][0], s1 = acc[0][1];
#pragma unroll
    for (int d = 1; d < 4; ++d) {
      s0 += xb[(((wr ^ d) * 3 + d - 1) * 2) * 64];
      s1 += xb[(((wr ^ d) * 3 + d - 1) * 2 + 1) * 64];
    }
    f32x4 c0, c1;
#pragma unroll
    for (int j = 0; j < 4; ++j) {
      auto w = __builtin_amdgcn_permlane16_swap(__float_as_uint(s0[j]), __float_as_uint(s1[j]), false, false);
      c0[j] = __uint_as_float(w[0]); c1[j] = __uint_as_float(w[1]);
    }
    u32x4 o;
#pragma unroll
    for (int k = 0; k < 4; ++k) {
      const f32x4 c = (k >> 1) ? c1 : c0;
      const float lo = bf_lo(ex[k]) + bf_lo(e1[k]) - bf_lo(e2[k]) + c[(k & 1) * 2];
      const float hi = bf_hi(ex[k]) + bf_hi(e1[k]) - bf_hi(e2[k]) + c[(k & 1) * 2 + 1];
      o[k] = cvt_pk(lo, hi);
    }
    __builtin_nontemporal_store(o, (u32x4*)(a.out + (size_t)(tt3 * 16 + fr) * D + (c3 + cq) * 8));
  }
  STAMP(7);
  STAMP_OUT(a.ws, 1, 8);
}

extern "C" void kernel_launch(void* const* d_in, const int* in_sizes, int n_in, void* d_out, int out_size, void* d_ws, size_t ws_size,
                              hipStream_t stream) {
  (void)in_sizes; (void)n_in; (void)out_size; (void)ws_size;
  (void)hipFuncSetAttribute((const void*)fused, hipFuncAttributeMaxDynamicSharedMemorySize, LDS_BYTES);
  Args a{(const u16*)d_in[0], (const u16*)d_in[3], (const u16*)d_in[4], (const u16*)d_in[6], (const u16*)d_in[9],
         (const u16*)d_in[1], (const u16*)d_in[2], (const u16*)d_in[7], (const u16*)d_in[8], (char*)d_ws, (u16*)d_out};
  fused<<<256, 512, LDS_BYTES, stream>>>(a);
}
```

```cpp
#define PROBE 2
#define PBLK 73
#include <hip/hip_runtime.h>
#include <hip/hip_bf16.h>
#include <stdint.h>

typedef unsigned short u16;
typedef short bf16x8 __attribute__((ext_vector_type(8)));
typedef short s16x4 __attribute__((ext_vector_type(4)));
typedef float f32x4 __attribute__((ext_vector_type(4)));
typedef float f32x2 __attribute__((ext_vector_type(2)));
typedef unsigned u32x4 __attribute__((ext_vector_type(4)));
typedef __bf16 bf16x2v __attribute__((ext_vector_type(2)));
typedef __amdgpu_buffer_rsrc_t rsrc_t;
#define LAS __attribute__((address_space(3)))
#define WAIT_V(n) asm volatile("s_waitcnt vmcnt(%0)" ::"n"(n) : "memory")
#define WL0() __builtin_amdgcn_s_waitcnt(0xC07F)
#define BAR() do { asm volatile("" ::: "memory"); __builtin_amdgcn_s_barrier(); asm volatile("" ::: "memory"); } while (0)
#define SCHED() __builtin_amdgcn_sched_barrier(0)
#define MFMA16(a, b, c) __builtin_amdgcn_mfma_f32_16x16x32_bf16(a, b, c, 0, 0, 0)
#define RSRC(p, n) __builtin_amdgcn_make_buffer_rsrc((void*)(p), 0, (unsigned)(n), 0x00020000)
#define OPAQUE(x) asm volatile("" : "+v"(x))

#define STAMP_DECL
#define STAMP(i) do { if (KID * 16 + (i) == PROBE) asm volatile("s_memrealtime %0\n\ts_waitcnt lgkmcnt(0)" : "=s"(ptime) :: "memory"); } while (0)
#define STAMP_OUT(ws, kid, n)
#define K2_KXMASK 3
#define K1_KXMASK 3
#define DWARM 0
#define CODEWARM 5
#define NOPARK 0
#define BULKLATE 0
#define ZNB 0
#define GATHER_AT 18
#define WINSTART 1
#define PF_LIST 0x753810u
#define PF_N 6

constexpr int S = 2048, D = 512, F = 1792, FH = 896;
constexpr size_t WS_Q = 0;
constexpr size_t WS_Z1 = WS_Q + (size_t)(2 * D + FH) * S * 2;
constexpr size_t WS_H = WS_Z1 + (size_t)FH * S * 2;
constexpr size_t WS_Y = WS_H + (size_t)FH * S * 2;
constexpr size_t WS_G = WS_Y + (size_t)2 * D * S * 2;
constexpr unsigned G_F1 = 0;
constexpr unsigned G_FP = 32768;
constexpr unsigned G_FZ = 32768 + 64;
constexpr unsigned G_PAY = 65536;
constexpr unsigned G_ALL = G_PAY + 16 * 14336;
constexpr unsigned G_REQ = G_ALL + 1024;
constexpr unsigned G_ANS = G_REQ + 1024;
constexpr unsigned G_END = G_ANS + 1024;

constexpr int LDS_BYTES = 163840;
constexpr int P1_RING = 32768, P1_SCR = 81920;
constexpr int P2_PB = 65536, P2_Z = 131072, P3_X = 114688;
constexpr unsigned SPIN = 1u << 20;

struct Args {
  const u16* x; const u16* wdr; const u16* wdi; const u16* win; const u16* wout;
  const u16* wsr; const u16* wsi; const u16* wsp; const u16* bsp;
  char* ws; u16* out;
};

__device__ __forceinline__ unsigned cvt_pk(float lo, float hi) {
  f32x2 v = {lo, hi};
  return __builtin_bit_cast(unsigned, __builtin_convertvector(v, bf16x2v));
}
__device__ __forceinline__ float bf_lo(unsigned u) { return __uint_as_float(u << 16); }
__device__ __forceinline__ float bf_hi(unsigned u) { return __uint_as_float(u & 0xffff0000u); }
constexpr float GC1 = -2.0f * 0.7978845608028654f * 1.4426950408889634f, GC2 = GC1 * 0.044715f;
__device__ __forceinline__ f32x2 gelu2(f32x2 v) {
  const f32x2 t = v * ((v * v) * GC2 + GC1);
  const f32x2 e = f32x2{__builtin_amdgcn_exp2f(t[0]), __builtin_amdgcn_exp2f(t[1])} + 1.0f;
  return v * f32x2{__builtin_amdgcn_rcpf(e[0]), __builtin_amdgcn_rcpf(e[1])};
}
__device__ __forceinline__ void dot_sq(float& c, unsigned a) { asm volatile("v_dot2c_f32_bf16 %0, %1, %1" : "+v"(c) : "v"(a)); }
__device__ __forceinline__ void dot_sum(float& c, unsigned a) { asm volatile("v_dot2c_f32_bf16 %0, 0x3f803f80, %1" : "+v"(c) : "v"(a)); }
__device__ __forceinline__ float sum_rows(float v) {
  auto a = __builtin_amdgcn_permlane32_swap(__float_as_uint(v), __float_as_uint(v), false, false);
  v = __uint_as_float(a[0]) + __uint_as_float(a[1]);
  auto b = __builtin_amdgcn_permlane16_swap(__float_as_uint(v), __float_as_uint(v), false, false);
  return __uint_as_float(b[0]) + __uint_as_float(b[1]);
}
__device__ __forceinline__ float sel4(f32x4 v, int i) { const float a = (i & 1) ? v[1] : v[0], b = (i & 1) ? v[3] : v[2]; return (i & 2) ? b : a; }

__device__ __forceinline__ void dma16(rsrc_t r, unsigned mbase, int mimm, unsigned voff, unsigned soff) {
  asm volatile("s_add_i32 m0, %0, %1\n\ts_nop 0\n\tbuffer_load_dwordx4 %2, %3, %4 offen lds" ::"s"(mbase), "n"(mimm), "v"(voff), "s"(r), "s"(soff) : "scc");
}
__device__ __forceinline__ void dma16_sc1(rsrc_t r, unsigned mbase, int mimm, unsigned voff, unsigned soff) {
  asm volatile("s_add_i32 m0, %0, %1\n\ts_nop 0\n\tbuffer_load_dwordx4 %2, %3, %4 offen sc1 lds" ::"s"(mbase), "n"(mimm), "v"(voff), "s"(r), "s"(soff) : "scc");
}
__device__ __forceinline__ void dma4_sc1(rsrc_t r, int ldsaddr, unsigned voff, unsigned soff) {
  asm volatile("s_mov_b32 m0, %0\n\ts_nop 0\n\tbuffer_load_dword %1, %2, %3 offen sc1 lds" ::"n"(ldsaddr), "v"(voff), "s"(r), "s"(soff) : "memory");
}
__device__ __forceinline__ void touch(rsrc_t r, int junk, unsigned voff, unsigned soff) {
  asm volatile("s_mov_b32 m0, %0\n\ts_nop 0\n\tbuffer_load_dword %1, %2, %3 offen lds" ::"n"(junk), "v"(voff), "s"(r), "s"(soff) : "memory");
}
__device__ __forceinline__ void st16_sc1(u32x4 v, rsrc_t r, unsigned voff, unsigned soff) {
  asm volatile("buffer_store_dwordx4 %0, %1, %2, %3 offen sc1\n\ts_nop 1" ::"v"(v), "v"(voff), "s"(r), "s"(soff) : "memory");
}
__device__ __forceinline__ void st4_sc1(unsigned v, rsrc_t r, unsigned voff, unsigned soff) {
  asm volatile("buffer_store_dword %0, %1, %2, %3 offen sc1" ::"v"(v), "v"(voff), "s"(r), "s"(soff) : "memory");
}
__device__ __forceinline__ unsigned ld4_sc1(rsrc_t r, unsigned voff, unsigned soff) {
  unsigned v;
  asm volatile("buffer_load_dword %0, %1, %2, %3 offen sc1\n\ts_waitcnt vmcnt(0)" : "=&v"(v) : "v"(voff), "s"(r), "s"(soff) : "memory");
  return v;
}
__device__ __forceinline__ u32x4 ld16_sc1(rsrc_t r, unsigned voff, unsigned soff) {
  u32x4 v;
  asm volatile("buffer_load_dwordx4 %0, %1, %2, %3 offen sc1\n\ts_waitcnt vmcnt(0)" : "=&v"(v) : "v"(voff), "s"(r), "s"(soff) : "memory");
  return v;
}
__device__ __forceinline__ void ld16_async(u32x4& v, rsrc_t r, unsigned voff, unsigned soff) {
  asm volatile("buffer_load_dwordx4 %0, %1, %2, %3 offen" : "=v"(v) : "v"(voff), "s"(r), "s"(soff) : "memory");
}
__device__ __forceinline__ void wait_vn(int n) { WAIT_V(n); }
__device__ __forceinline__ bf16x8 ld_tr(unsigned addr) {
  s16x4 lo = __builtin_amdgcn_ds_read_tr16_b64_v4i16((LAS s16x4*)(size_t)(addr));
  s16x4 hi = __builtin_amdgcn_ds_read_tr16_b64_v4i16((LAS s16x4*)(size_t)(addr + 512));
  return __builtin_shufflevector(lo, hi, 0, 1, 2, 3, 4, 5, 6, 7);
}
__device__ __forceinline__ bf16x8 ld_frag(unsigned addr) { return *(const LAS bf16x8*)(size_t)(addr); }
__device__ __forceinline__ void wait_go(rsrc_t rG, unsigned nonce, unsigned seen) {
  for (unsigned spin = 0; seen != nonce && spin < SPIN; ++spin) {
    seen = __builtin_amdgcn_readfirstlane(ld4_sc1(rG, 0, G_ANS + blockIdx.x * 4));
    __builtin_amdgcn_s_sleep(1);
  }
}

template <bool ZB>
__device__ __forceinline__ void phase1(const Args& a, int pm, int cb, int kx, const u16* Pp, int kx2, f32x4 (&z1v)[2][2], unsigned long long& ptime) {
  constexpr int KID = 0;
  const int tid = threadIdx.x, lane = tid & 63;
  const int wid = __builtin_amdgcn_readfirstlane(tid >> 6);
  const int wm = wid & 3, wn = wid >> 2;
  STAMP_DECL STAMP(0);
  const rsrc_t rX = RSRC(a.x + (size_t)pm * 128 * D, 128 * D * 2);
  const rsrc_t rW = ZB ? RSRC(a.win + 64 * cb, (D * F - 64 * cb) * 2) : RSRC(a.wdi + (size_t)384 * D, 128 * D * 2);
  const int r8 = lane >> 3, p8 = lane & 7;
  const unsigned sw_e = (p8 ^ (r8 >> 1)) << 4;
  const unsigned vx_e = (32 * (wid >> 1) + 4 * (wid & 1) + 8 * (r8 >> 2) + (r8 & 3)) * (D * 2) + sw_e;
  const unsigned vx_o = (vx_e ^ 64) + 16 * (D * 2);
  const unsigned vw2 = ZB ? (8 * wid + r8) * (F * 2) + ((p8 ^ ((((r8 >> 1) & 1) | ((wid & 1) << 1)) << 1)) << 4)
                          : (64 + 8 * wid + r8) * (D * 2) + (sw_e ^ ((wid & 1) << 6));
  const unsigned vw1 = ZB ? vw2 + FH * 2 : vw2 - 64 * (D * 2);
  constexpr unsigned WKB = ZB ? 64 * F * 2 : 128;
  const unsigned mb1 = wid * 1024, mb2 = wid * 2048;
  auto piece1 = [&](int st, int p) {
    const unsigned kb = st ^ kx;
    const int base = P1_RING + (st & 3) * 32768;
    if (p == 0) dma16(rX, mb2, base, vx_e, kb * 128);
    else if (p == 1) dma16(rW, mb1, base + 16384, vw1, kb * WKB);
    else if (p == 2) dma16(rX, mb2, base + 1024, vx_o, kb * 128);
    else dma16(rW, mb1, base + 24576, vw2, kb * WKB);
  };
  piece1(0, 0); piece1(0, 1); piece1(0, 2); piece1(0, 3);
  SCHED();

  const int fr = lane & 15, fq = lane >> 4;
  const int kh = wm >> 1;
  const unsigned fsw = ((fq ^ ((fr >> 1) & 7)) << 4) ^ (kh << 6);
  unsigned xa[2][2][2], wb[2][2][2];
#pragma unroll
  for (int m = 0; m < 2; ++m) { xa[0][m][0] = P1_RING + (32 * wm + 16 * (m ^ wn) + fr) * 128 + fsw; xa[0][m][1] = xa[0][m][0] ^ 64; }
#pragma unroll
  for (int n = 0; n < 2; ++n) {
    const int tile = 2 * wn + (n ^ (wm & 1));
    if (ZB) {
      const int q4 = fr >> 2, pp = lane & 3;
      const int sp = ((q4 >> 1) & 1) | ((fq & 1) << 1);
      const int ch = (2 * tile + (pp >> 1)) ^ (sp << 1);
      wb[0][n][0] = P1_RING + 16384 + 128 * (8 * fq + q4) + 16 * ch + 8 * (pp & 1) + kh * 4096;
      wb[0][n][1] = wb[0][n][0] ^ 4096;
    } else {
      wb[0][n][0] = P1_RING + 16384 + (16 * tile + fr) * 128 + fsw;
      wb[0][n][1] = wb[0][n][0] ^ 64;
    }
  }
#pragma unroll
  for (int i = 0; i < 2; ++i)
#pragma unroll
    for (int ks = 0; ks < 2; ++ks) {
      xa[1][i][ks] = xa[0][i][ks] + 65536; wb[1][i][ks] = wb[0][i][ks] + 65536;
      OPAQUE(xa[0][i][ks]); OPAQUE(wb[0][i][ks]); OPAQUE(xa[1][i][ks]); OPAQUE(wb[1][i][ks]);
    }
  auto ldw = [&](unsigned addr) -> bf16x8 { return ZB ? ld_tr(addr) : ld_frag(addr); };
  SCHED();
  piece1(1, 0); piece1(1, 1); piece1(1, 2); piece1(1, 3);
  const rsrc_t rG = RSRC(a.ws + WS_G, G_END);
  if (wid == 7) dma4_sc1(rG, 0, 0, G_ANS + blockIdx.x * 4);
  unsigned nonce = 0;
  STAMP(1);

  bf16x8 xf[16][2], wf[2][4];
  const f32x4 Z4 = {0, 0, 0, 0};
  f32x4 acc1[2][2] = {{Z4, Z4}, {Z4, Z4}}, acc2[2][2] = {{Z4, Z4}, {Z4, Z4}};
  f32x4 gram = Z4, rsum = Z4, cs1 = Z4, cs2 = Z4;
  const bf16x8 ONES = {0x3f80, 0x3f80, 0x3f80, 0x3f80, 0x3f80, 0x3f80, 0x3f80, 0x3f80};

  auto mm = [&](int kk, int i) {
    const int ks = kk & 1;
    if (i == 0) gram = MFMA16(xf[kk][0], xf[kk][0], gram);
    else if (i == 1) rsum = MFMA16(xf[kk][0], ONES, rsum);
    else if (i < 6) { const int m = (i - 2) >> 1, n = i & 1; acc1[m][n] = MFMA16(xf[kk][m], wf[ks][n], acc1[m][n]); }
    else if (i < 10) { const int m = (i - 6) >> 1, n = i & 1; acc2[m][n] = ZB ? MFMA16(wf[ks][2 + n], xf[kk][m], acc2[m][n]) : MFMA16(xf[kk][m], wf[ks][2 + n], acc2[m][n]); }
    else if (i == 10) cs1 = MFMA16(ONES, wf[0][0], cs1);
    else cs2 = MFMA16(ONES, wf[0][2], cs2);
  };
  auto rd = [&](int kk, int i) {
    const int st = kk >> 1, ks = kk & 1, wi = (st >> 1) & 1, so = (st & 1) * 32768;
    if (i == 0 || i == 3) xf[kk][i & 1] = ld_frag(xa[wi][i & 1][ks] + so);
    else if (i < 3) wf[ks][i - 1] = ldw(wb[wi][i - 1][ks] + so);
    else wf[ks][i - 2] = ldw(wb[wi][i - 4][ks] + so + 8192);
  };
  auto piece3 = [&](int p) {
    const rsrc_t rD = RSRC((cb < 8 ? a.wdr : a.wdi) + (size_t)(cb & 7) * 64 * D, 64 * D * 2);
    const unsigned ve = r8 * (D * 2) + sw_e, vo = (ve ^ 64) + 8 * (D * 2);
    dma16(rD, wid * 8192, p * 1024, (p & 1) ? vo : ve, (wid ^ kx) * 128 + (p >> 1) * 16 * (D * 2));
  };
  auto earlyP = [&](int k) {
    const rsrc_t rP = RSRC(Pp, 128 * S * 2);
    const unsigned ve = (16 * wid + r8) * (S * 2) + sw_e, vo = (ve ^ 64) + 8 * (S * 2);
    dma16(rP, mb2, P2_PB + (k + 2) * 16384, ve, (k ^ kx2) * 128);
    dma16(rP, mb2, P2_PB + (k + 2) * 16384 + 1024, vo, (k ^ kx2) * 128);
  };
#pragma unroll
  for (int st = 0; st < 8; ++st) {
    wait_vn(st <= 6 ? 4 : ZB ? 8 : 0);
    WL0();
    BAR();
    if (st == 0) STAMP(2);
    SCHED();
#pragma unroll
    for (int i = 0; i < 10; ++i) {
      if (st > 0) mm(2 * st - 1, i);
      SCHED();
      if (i < 6) rd(2 * st, i);
      if (st + 2 < 8) { if (i == 3) piece1(st + 2, 0); if (i == 8) piece1(st + 2, 1); }
      if (ZB && st == 6 && (i % 3) == 0) piece3(i / 3);
      if (st == 7 && i == 7) earlyP(0);
      SCHED();
    }
    WL0();
    SCHED();
#pragma unroll
    for (int i = 0; i < 12; ++i) {
      mm(2 * st, i);
      SCHED();
      if (i < 6) rd(2 * st + 1, i);
      if (st + 2 < 8) { if (i == 3) piece1(st + 2, 2); if (i == 9) piece1(st + 2, 3); }
      if (ZB && st == 6 && (i % 3) == 2) piece3(4 + i / 3);
      if (st == 7 && i == 8) earlyP(1);
      SCHED();
    }
    if (st == 2 && wid == 7) {
      nonce = __builtin_amdgcn_readfirstlane(*(LAS unsigned*)(size_t)(lane * 4)) + 1;
      st4_sc1(nonce, rG, 0, G_REQ + blockIdx.x * 4);
    }
    SCHED();
  }
  LAS float* const s_r = (LAS float*)(size_t)(P1_SCR);
  LAS float* const s_c = (LAS float*)(size_t)(P1_SCR + 512);
  LAS float* const s_u1 = (LAS float*)(size_t)(P1_SCR + 1024);
  LAS float* const s_u2 = (LAS float*)(size_t)(P1_SCR + 1536);
  LAS float* const s_z = (LAS float*)(size_t)(P1_SCR + 2048);
  LAS float* const s_n = (LAS float*)(size_t)(P1_SCR + 4096);
  LAS float* const s_u3 = (LAS float*)(size_t)(P1_SCR + 5120);
  const unsigned tb0 = fr * 128 + fsw, tb1 = tb0 ^ 64;
  WAIT_V(4);
  {
    const unsigned cb0 = tb0 + wid * 8192, cb1 = cb0 ^ 64;
    bf16x8 c3[8];
    f32x4 cs3[4];
    if (ZB) {
#pragma unroll
      for (int t = 0; t < 4; ++t) { c3[2 * t] = ld_frag(cb0 + t * 2048); c3[2 * t + 1] = ld_frag(cb1 + t * 2048); }
    }
    mm(15, 0); mm(15, 1);
    SCHED();
    if (ZB) {
#pragma unroll
      for (int t = 0; t < 4; ++t) cs3[t] = MFMA16(ONES, c3[2 * t], Z4);
#pragma unroll
      for (int t = 0; t < 4; ++t) cs3[t] = MFMA16(ONES, c3[2 * t + 1], cs3[t]);
    }
    SCHED();
#pragma unroll
    for (int i = 2; i < 10; ++i) mm(15, i);
    STAMP(3);
    unsigned i01 = 32 * wm + 16 * wn, i23 = 64 * kh + 16 * (2 * wn + (wm & 1));
    asm volatile("" : "+s"(i01), "+s"(i23));
    const float s1 = sel4(rsum, fr & 3), s2 = sel4(gram, fr & 3);
    const float mean = s1 * (1.0f / D);
    const float rstd = __builtin_amdgcn_rsqf(fmaxf(s2 * (1.0f / D) - mean * mean, 0.0f) + 1e-5f);
    if ((fr >> 2) == fq) { s_r[i01 + fr] = rstd; s_c[i01 + fr] = -mean * rstd; }
    if (fq < 2) s_u1[128 * fq + i23 + fr] = fq ? cs2[0] : cs1[0];
    if (ZB) s_u3[lane * 8 + wid] = sel4(f32x4{cs3[0][0], cs3[1][0], cs3[2][0], cs3[3][0]}, fq);
  }
  const int s0 = pm * 128 + 32 * wm;
  const rsrc_t rT = RSRC(a.ws + WS_Q, (2 * D + FH) * S * 2);
  const unsigned vT = ((fr >> 2) & 1) * 65536 + fq * 256 + (4 * (fr >> 3) + (fr & 3)) * 16;
  WL0();
  BAR();
  STAMP(4);
  if (wid == 7) dma4_sc1(rG, P1_SCR + 7168, 0, G_ANS + blockIdx.x * 4);
  earlyP(2); earlyP(3);
  f32x4 rq[2], cq[2];
  auto ld_rc = [&]() {
#pragma unroll
    for (int m = 0; m < 2; ++m) {
      rq[m] = *(LAS f32x4*)(s_r + 32 * wm + 16 * (m ^ wn) + 4 * fq);
      cq[m] = *(LAS f32x4*)(s_c + 32 * wm + 16 * (m ^ wn) + 4 * fq);
    }
  };
  ld_rc();
  auto store_t = [&](int T, f32x4 v0, f32x4 v1) {
    const unsigned a0 = cvt_pk(v0[0], v0[1]), a1 = cvt_pk(v0[2], v0[3]), b0 = cvt_pk(v1[0], v1[1]), b1 = cvt_pk(v1[2], v1[3]);
    const u32x4 o = wn ? u32x4{b0, b1, a0, a1} : u32x4{a0, a1, b0, b1};
    st16_sc1(o, rT, vT, (T >> 1) * 131072 + (T & 1) * 128 + s0 * 32);
  };
  auto lin_store = [&](f32x4 c0, f32x4 c1, float u, int T) { store_t(T, rq[0] * c0 + cq[0] * u, rq[1] * c1 + cq[1] * u); };
  float u1[2];
#pragma unroll
  for (int n = 0; n < 2; ++n) { const int ui = 16 * (2 * wn + (n ^ (wm & 1))) + fr; u1[n] = s_u1[ui] + s_u1[64 + ui]; }
  if (!ZB) {
#pragma unroll
    for (int n = 0; n < 2; ++n) {
      const int tile = 2 * wn + (n ^ (wm & 1)), ui = 16 * tile + fr;
      lin_store(acc1[0][n], acc1[1][n], u1[n], 56 + tile);
      lin_store(acc2[0][n], acc2[1][n], s_u2[ui] + s_u2[64 + ui], 60 + tile);
    }
    if (wid == 7) { WAIT_V(0); wait_go(rG, nonce, __builtin_amdgcn_readfirstlane(*(LAS unsigned*)(size_t)(P1_SCR + 7168 + lane * 4))); }
    STAMP(5); STAMP(6); STAMP(7); STAMP(8); STAMP(9);
    STAMP_OUT(a.ws, 0, 10);
    return;
  }
  {
    f32x4 ps[2], pq[2];
#pragma unroll
    for (int n = 0; n < 2; ++n)
#pragma unroll
      for (int m = 0; m < 2; ++m) {
        f32x4 v = rq[m] * acc1[m][n] + cq[m] * u1[n];
        const f32x2 g0 = gelu2(f32x2{v[0], v[1]}), g1 = gelu2(f32x2{v[2], v[3]});
        v = f32x4{g0[0], g0[1], g1[0], g1[1]};
        acc1[m][n] = v;
        if (n == 0) { ps[m] = v; pq[m] = v * v; } else { ps[m] += v; pq[m] += v * v; }
      }
    float t[8], h[4];
#define DPP2(d, x, y, ctl, m0, m1) "v_add_f32_dpp " d ", " x ", " x " " ctl " row_mask:0xf bank_mask:" m0 "\n\tv_add_f32_dpp " d ", " y ", " y " " ctl " row_mask:0xf bank_mask:" m1 "\n\t"
#define DPPQ(d, ctl) "v_add_f32_dpp " d ", " d ", " d " " ctl " row_mask:0xf bank_mask:0xf\n\t"
    asm volatile("s_nop 1\n\t" DPP2("%0", "%8", "%16", "row_ror:8", "0x3", "0xc") DPP2("%1", "%9", "%17", "row_ror:8", "0x3", "0xc")
                 DPP2("%2", "%10", "%18", "row_ror:8", "0x3", "0xc") DPP2("%3", "%11", "%19", "row_ror:8", "0x3", "0xc")
                 DPP2("%4", "%12", "%20", "row_ror:8", "0x3", "0xc") DPP2("%5", "%13", "%21", "row_ror:8", "0x3", "0xc")
                 DPP2("%6", "%14", "%22", "row_ror:8", "0x3", "0xc") DPP2("%7", "%15", "%23", "row_ror:8", "0x3", "0xc") "s_nop 1"
                 : "=&v"(t[0]), "=&v"(t[1]), "=&v"(t[2]), "=&v"(t[3]), "=&v"(t[4]), "=&v"(t[5]), "=&v"(t[6]), "=&v"(t[7])
                 : "v"(ps[0][0]), "v"(pq[0][0]), "v"(ps[0][1]), "v"(pq[0][1]), "v"(ps[0][2]), "v"(pq[0][2]), "v"(ps[0][3]), "v"(pq[0][3]),
                   "v"(ps[1][0]), "v"(pq[1][0]), "v"(ps[1][1]), "v"(pq[1][1]), "v"(ps[1][2]), "v"(pq[1][2]), "v"(ps[1][3]), "v"(pq[1][3]));
    asm volatile("s_nop 1\n\t" DPP2("%0", "%4", "%8", "row_half_mirror", "0x5", "0xa") DPP2("%1", "%5", "%9", "row_half_mirror", "0x5", "0xa")
                 DPP2("%2", "%6", "%10", "row_half_mirror", "0x5", "0xa") DPP2("%3", "%7", "%11", "row_half_mirror", "0x5", "0xa") "s_nop 1\n\t"
                 DPPQ("%0", "quad_perm:[1,0,3,2]") DPPQ("%1", "quad_perm:[1,0,3,2]") DPPQ("%2", "quad_perm:[1,0,3,2]") DPPQ("%3", "quad_perm:[1,0,3,2]")
                 DPPQ("%0", "quad_perm:[2,3,0,1]") DPPQ("%1", "quad_perm:[2,3,0,1]") DPPQ("%2", "quad_perm:[2,3,0,1]") DPPQ("%3", "quad_perm:[2,3,0,1]") "s_nop 0"
                 : "=&v"(h[0]), "=&v"(h[1]), "=&v"(h[2]), "=&v"(h[3])
                 : "v"(t[0]), "v"(t[1]), "v"(t[2]), "v"(t[3]), "v"(t[4]), "v"(t[5]), "v"(t[6]), "v"(t[7]));
    if ((fr & 3) == 0) *(LAS f32x4*)(s_z + (wn * 128 + 32 * wm + 8 * fq + 4 * ((fr >> 3) ^ wn) + 2 * ((fr >> 2) & 1)) * 2) = f32x4{h[0], h[1], h[2], h[3]};
  }
  WL0();
  BAR();
  STAMP(5);
  if (wid == 7) {
    __builtin_amdgcn_s_setprio(2);
    const f32x4 p = *(LAS f32x4*)(s_z + lane * 4) + *(LAS f32x4*)(s_z + 256 + lane * 4);
    WAIT_V(4);
    wait_go(rG, nonce, __builtin_amdgcn_readfirstlane(*(LAS unsigned*)(size_t)(P1_SCR + 7168 + lane * 4)));
    st16_sc1(__builtin_bit_cast(u32x4, p) | 1u, rG, lane * 16, G_PAY + (pm * 14 + cb) * 1024);
  }
  STAMP(6);
  {
    float u3[2];
    f32x4 acct[2][2] = {{Z4, Z4}, {Z4, Z4}};
    const unsigned t30 = tb0 + wn * 4096, t31 = tb1 + wn * 4096;
    bf16x8 w[8];
    auto rd3 = [&](int f) { const int kk = f >> 1; w[f & 7] = ld_frag(((kk & 1) ? t31 : t30) + (kk >> 1) * 8192 + (f & 1) * 2048); };
#pragma unroll
    for (int f = 0; f < 4; ++f) rd3(f);
    SCHED();
#pragma unroll
    for (int f = 0; f < 32; ++f) {
      const int kk = f >> 1, n = f & 1;
      acct[0][n] = MFMA16(xf[kk][0], w[f & 7], acct[0][n]);
      SCHED();
      if (f + 4 < 32) rd3(f + 4);
      if (f == 8 || f == 12) {
        const int m = (f - 8) >> 2;
        const float r = s_r[32 * wm + 16 * (m ^ wn) + fr], c = s_c[32 * wm + 16 * (m ^ wn) + fr];
#pragma unroll
        for (int q = 0; q < 2; ++q) {
          const int ui = 16 * (2 * wn + (q ^ (wm & 1))) + 4 * fq;
          z1v[m][q] = acc2[m][q] * r + (*(LAS f32x4*)(s_u2 + ui) + *(LAS f32x4*)(s_u2 + 64 + ui)) * c;
        }
      }
      if (f == GATHER_AT && wid == 7) {
#pragma unroll
        for (int i = 0; i < 14; ++i) dma16_sc1(rG, 0, P2_PB + i * 1024, lane * 16, G_PAY + pm * 14336 + i * 1024);
      }
      if (f == 20 || f == 24) {
        const int q = (f - 20) >> 2;
        const f32x4 uu = *(LAS f32x4*)(s_u3 + (32 * wn + 16 * q + fr) * 8) + *(LAS f32x4*)(s_u3 + (32 * wn + 16 * q + fr) * 8 + 4);
        u3[q] = (uu[0] + uu[1]) + (uu[2] + uu[3]);
      }
      SCHED();
      acct[1][n] = MFMA16(xf[kk][1], w[f & 7], acct[1][n]);
      SCHED();
    }
    if (wid != 7) {
#pragma unroll
      for (int n = 0; n < 2; ++n) lin_store(acct[0][n], acct[1][n], u3[n], 4 * cb + 2 * wn + n);
    }
    STAMP(7);
  if (wid == 7) {
    f32x4 t;
    for (unsigned spin = 0; spin < SPIN; ++spin) {
      WAIT_V(0);
      u32x4 al = *(LAS u32x4*)(size_t)(P2_PB + lane * 16);
      t = __builtin_bit_cast(f32x4, al);
#pragma unroll
      for (int i = 1; i < 14; ++i) { const u32x4 pv = *(LAS u32x4*)(size_t)(P2_PB + i * 1024 + lane * 16); al &= pv; t += __builtin_bit_cast(f32x4, pv); }
      if (__all((al[0] & al[1] & al[2] & al[3] & 1u) != 0)) break;
      WL0();
#pragma unroll
      for (int i = 0; i < 14; ++i) dma16_sc1(rG, 0, P2_PB + i * 1024, lane * 16, G_PAY + pm * 14336 + i * 1024);
    }
    const float m0 = t[0] * (1.0f / FH), m1 = t[2] * (1.0f / FH);
    const float r0 = __builtin_amdgcn_rsqf(fmaxf(t[1] * (1.0f / FH) - m0 * m0, 0.0f) + 1e-5f);
    const float r1 = __builtin_amdgcn_rsqf(fmaxf(t[3] * (1.0f / FH) - m1 * m1, 0.0f) + 1e-5f);
    *(LAS f32x2*)(s_n + 2 * lane) = f32x2{r0, r1};
    *(LAS f32x2*)(s_n + 128 + 2 * lane) = f32x2{-m0 * r0, -m1 * r1};
    __builtin_amdgcn_s_setprio(0);
  }
  WL0();
  BAR();
  if (wid == 7) {
#pragma unroll
    for (int n = 0; n < 2; ++n) lin_store(acct[0][n], acct[1][n], u3[n], 4 * cb + 2 * wn + n);
  }
  }
  STAMP(8);
  {
    f32x4 r2[2], c2[2];
#pragma unroll
    for (int m = 0; m < 2; ++m) {
      r2[m] = *(LAS f32x4*)(s_n + 32 * wm + 8 * fq + 4 * (m ^ wn));
      c2[m] = *(LAS f32x4*)(s_n + 128 + 32 * wm + 8 * fq + 4 * (m ^ wn));
    }
#pragma unroll
    for (int n = 0; n < 2; ++n) store_t(64 + 4 * cb + 2 * wn + (n ^ (wm & 1)), acc1[0][n] * r2[0] + c2[0], acc1[1][n] * r2[1] + c2[1]);
  }
  STAMP(9);
  STAMP_OUT(a.ws, 0, 10);
}

__device__ __forceinline__ void warm_window(const Args& a, int junk, int rb, int w, int wv, int lane) {
  if (w < 8) {
    const unsigned vo = (rb * 128 + wv * 32 + (lane >> 2)) * (S * 2) + (lane & 3) * 128;
#pragma unroll
    for (int m = 0; m < 3; ++m) {
      const rsrc_t r = RSRC(m == 0 ? a.wsr : m == 1 ? a.wsi : a.wsp, S * S * 2);
      touch(r, junk, vo, w * 512);
      touch(r, junk, vo, w * 512 + 16 * (S * 2));
    }
  } else {
    const rsrc_t r = RSRC(a.wout + (size_t)rb * 56 * D, 56 * D * 2);
    touch(r, junk, lane * 128, wv * 16384);
    touch(r, junk, lane * 128, wv * 16384 + 8192);
  }
}

__global__ __launch_bounds__(512, 2) void fused(Args a) {
  extern __shared__ __attribute__((aligned(1024))) char smem[];
  asm volatile("" ::"v"(smem));
  constexpr int KID = 1;
  unsigned long long pstart = __builtin_amdgcn_s_memrealtime(), ptime = pstart;
  const int bx = blockIdx.x & 7, bi = blockIdx.x >> 3;
  if (bi < CODEWARM && threadIdx.x < 64) {
    unsigned long long pc;
    asm volatile("s_getpc_b64 %0" : "=s"(pc));
    touch(RSRC(pc & ~127ull, CODEWARM * 8192), 1024, threadIdx.x * 128, bi * 8192);
  }
  const int pm = 4 * (bx >> 1) + (bi >> 3), cb = 2 * (bi & 7) + (bx & 1);
  int mat, tm, tn;
  if (bx & 1) { mat = 2; tm = pm; tn = bi & 7; }
  else { mat = bx >> 2; tm = ((bx >> 1) & 1) * 8 + (bi >> 2); tn = bi & 3; }
  const int kx = (tm ^ tn) & K2_KXMASK;
  const u16* Pp = (mat == 0 ? a.wsr : mat == 1 ? a.wsi : a.wsp) + (size_t)tm * 128 * S;
  f32x4 z1v[2][2];
  {
    const int kx1 = ((bi & 7) ^ (2 * (bi >> 3))) & K1_KXMASK;
    if (__builtin_expect(cb < 14, 1)) phase1<true>(a, pm, cb, kx1, Pp, kx, z1v, ptime);
    else if (cb == 14) phase1<false>(a, pm, cb, kx1, Pp, kx, z1v, ptime);
  }
  int tid = threadIdx.x;
  asm volatile("" : "+v"(tid));
  const int lane = tid & 63;
  const int wid = __builtin_amdgcn_readfirstlane(tid >> 6);
  STAMP_DECL STAMP(0);
  const int tm3 = bx * 4 + (bi >> 3), tn3 = bi & 7;
  const int fr = lane & 15, fq = lane >> 4;
  const unsigned vl = lane * 16;
  const rsrc_t rG = RSRC(a.ws + WS_G, G_END);
  const bool work2 = cb != 15;
  auto z1_finish = [&]() {
    if (cb < 14) {
      const int wm = wid & 3, wn = wid >> 2;
      const rsrc_t rZ = RSRC(a.ws + WS_Z1, FH * S * 2);
      const unsigned vZ = (fr >> 3) * (112 * 256) + (2 * ((fq & 1) ^ (wm & 1)) + (fq >> 1)) * 256 + (fr & 7) * 16;
      const unsigned sZ = ((pm * 8 + 2 * wm) * 112 + 8 * cb + 4 * wn) * 256;
#pragma unroll
      for (int m = 0; m < 2; ++m) {
        unsigned pk[2][2];
#pragma unroll
        for (int n = 0; n < 2; ++n) {
          const f32x2 g0 = gelu2(f32x2{z1v[m][n][0], z1v[m][n][1]}), g1 = gelu2(f32x2{z1v[m][n][2], z1v[m][n][3]});
          pk[n][0] = cvt_pk(g0[0], g0[1]); pk[n][1] = cvt_pk(g1[0], g1[1]);
        }
        auto w0 = __builtin_amdgcn_permlane16_swap(pk[0][0], pk[1][0], false, false);
        auto w1 = __builtin_amdgcn_permlane16_swap(pk[0][1], pk[1][1], false, false);
        st16_sc1(u32x4{w0[0], w1[0], w0[1], w1[1]}, rZ, vZ, sZ + 128 * (m ^ wn));
      }
    }
  };
  if (blockIdx.x == 255) {
    for (int i = 0; i < 36; ++i) st16_sc1(u32x4{0, 0, 0, 0}, rG, vl, (wid * 36 + i) * 1024);
    st16_sc1(u32x4{0, 0, 0, 0}, rG, vl, G_ALL);
  }
  unsigned bv[4];
#pragma unroll
  for (int ni = 0; ni < 4; ++ni) bv[ni] = a.bsp[(8 * tm + 4 * ((wid >> 1) & 1) + ni) * 16 + fr];
  asm volatile("" : "+v"(bv[0]), "+v"(bv[1]), "+v"(bv[2]), "+v"(bv[3]));
  if (work2 || blockIdx.x == 255) {
    WAIT_V(0);
    WL0();
    BAR();
  }
  if (wid >= 4) {
    const int lw = wid - 4;
    const int r8 = lane >> 3, p = lane & 7;
    if (wid == 7) {
      if (!work2) {
        if (blockIdx.x == 255) {
          for (unsigned spin = 0; spin < SPIN; ++spin) {
            const u32x4 rq = ld16_sc1(rG, vl, G_REQ);
            st16_sc1(rq, rG, vl, G_ANS);
            const u32x4 fl = ld16_sc1(rG, vl, G_ALL);
            if (__all((fl[0] & fl[1] & fl[2] & (lane == 63 ? 1u : fl[3])) == 1u)) break;
          }
        } else {
          const unsigned nonce = __builtin_amdgcn_readfirstlane(ld4_sc1(rG, 0, G_ANS + blockIdx.x * 4)) + 1;
          st4_sc1(nonce, rG, 0, G_REQ + blockIdx.x * 4);
          wait_go(rG, nonce, nonce - 1);
        }
      }
      const unsigned box = (fq & 1) * 128 + (cb >> 1) * 16 + fr;
      st4_sc1(1u, rG, fq < 2 ? box * 128 + (pm * 2 + (cb & 1)) * 4 : G_ALL + blockIdx.x * 4, 0);
    }
    const rsrc_t rP = RSRC(Pp, 128 * S * 2);
    const unsigned vo_e = (lw * 32 + r8) * (S * 2) + ((p ^ (r8 >> 1)) << 4);
    const unsigned vo_o = (vo_e ^ 64) + 8 * (S * 2);
    auto stageP = [&](int kt, unsigned poff) {
      const unsigned mb = poff + lw * 4096, so = (kt ^ kx) * 128;
#pragma unroll
      for (int j = 0; j < 4; ++j) dma16(rP, mb, P2_PB + j * 1024, (j & 1) ? vo_o : vo_e, so + (j >> 1) * 16 * (S * 2));
    };
    if (work2) {
      BAR();
      if (!BULKLATE) { stageP(4, 0); stageP(5, 16384); }
    }
    const rsrc_t rWo = RSRC(a.wout + 64 * tn3, (FH * D - 64 * tn3) * 2);
    const unsigned vw_e = (32 * lw + r8) * (D * 2) + ((p ^ (((r8 >> 1) & 1) << 1)) << 4), vw_o = (vw_e ^ 64) + 8 * (D * 2);
    u32x4 wreg[12];
    auto wpark = [&]() {
#pragma unroll
      for (int i = 0; i < 12; ++i) {
        const int rg = i < 8 ? 2 + (i >> 2) : 6;
        ld16_async(wreg[i], rWo, (i & 1) ? vw_o : vw_e, (rg * 128 + ((i >> 1) & 1) * 16) * (D * 2));
      }
    };
    if (!BULKLATE && work2) wpark();
    auto wput = [&](int rg) {
#pragma unroll
      for (int j = 0; j < 4 && !NOPARK; ++j) *(LAS u32x4*)(size_t)(rg * 16384 + lw * 4096 + j * 1024 + vl) = wreg[(rg == 6 ? 8 : (rg - 2) * 4) + j];
    };
    auto wdma = [&](int rg) {
#pragma unroll
      for (int j = 0; j < 4; ++j) dma16(rWo, lw * 4096, rg * 16384 + j * 1024, (j & 1) ? vw_o : vw_e, (rg * 128 + (j >> 1) * 16) * (D * 2));
    };
    if (work2) {
      const unsigned box = (mat * 4 + tn) * 16 + tm;
      bool allq = true;
      if (wid == 7) {
        for (unsigned spin = 0; spin < SPIN; ++spin) {
          const unsigned fl = ld4_sc1(rG, (lane & 31) * 4, G_F1 + box * 128);
          const bool ok = fl == 1u || (box >= 112 && box < 128 && (lane & 1));
          allq = __all(ok);
          if (__all(ok || (WINSTART && (lane & 31) > 3))) break;
        }
      }
      BAR();
      STAMP(1);
      const rsrc_t rQ = RSRC(a.ws + WS_Q + (size_t)(mat * 4 + tn) * 128 * S * 2, 128 * S * 2);
      const unsigned vq = vl + lw * 131072;
      auto stageQ = [&](int kt) {
        const unsigned mb = (kt & 3) * 16384 + lw * 4096, so = (kt ^ kx) * 2048;
#pragma unroll
        for (int j = 0; j < 4; ++j) dma16_sc1(rQ, mb, j * 1024, vq, so + (j >> 1) * 65536 + (j & 1) * 1024);
      };
      stageQ(0); stageQ(1);
      z1_finish();
      WAIT_V(4); BAR();
      stageQ(2); stageQ(3);
      for (unsigned spin = 0; !allq && spin < SPIN; ++spin)
        allq = __all(ld4_sc1(rG, (lane & 31) * 4, G_F1 + box * 128) == 1u || (box >= 112 && box < 128 && (lane & 1)));
      if (BULKLATE) {
        stageP(4, 0); stageP(5, 16384);
        WAIT_V(16); BAR(); stageQ(4); stageP(6, 2 * 16384);
        WAIT_V(20); BAR(); stageQ(5); stageP(7, 3 * 16384);
        WAIT_V(24); BAR(); stageQ(6); stageP(8, 4 * 16384);
        wpark();
      } else {
        WAIT_V(8); BAR(); stageQ(4); stageP(6, 2 * 16384);
        WAIT_V(12); BAR(); stageQ(5); stageP(7, 3 * 16384);
        WAIT_V(16); BAR(); stageQ(6); stageP(8, 4 * 16384);
      }
      unsigned poff = 5 * 16384;
      for (int kt = 3; kt < 26; ++kt) {
        WAIT_V(20);
        BAR();
        if (kt == 3 && wid == 7 && cb < 14) st4_sc1(1u, rG, 0, G_FZ + (pm * 8 + (cb >> 1)) * 128 + (cb & 1) * 4);
        stageQ(kt + 4); stageP(kt + 6, poff);
        poff = poff == 5 * 16384 ? 0 : poff + 16384;
      }
      if (mat == 2) {
        const rsrc_t rZ = RSRC(a.ws + WS_Z1 + (size_t)((8 * tm + lw) * 112 + 16 * tn) * 256, 4 * 112 * 256 + 4096);
        auto stageZ = [&](int h) {
#pragma unroll
          for (int j = 0; j < 4; ++j) dma16_sc1(rZ, lw * 4096, P2_Z + h * 16384 + j * 1024, vl, h * (4 * 112 * 256) + j * 1024);
        };
        if (wid == 7) {
          for (unsigned spin = 0; spin < SPIN; ++spin)
            if (__all(ld4_sc1(rG, (lane & 1) * 4, G_FZ + (tm * 8 + tn) * 128) == 1u)) break;
        }
        WAIT_V(20); BAR(); stageQ(30); stageZ(0);
        WAIT_V(20); BAR(); stageQ(31); stageZ(1);
        WAIT_V(20); BAR(); wdma(0); wdma(4);
        WAIT_V(20); BAR(); wdma(1); wdma(5);
        WAIT_V(20); BAR(); wput(2); wput(6);
        WAIT_V(16); BAR(); wput(3);
      } else {
        WAIT_V(20); BAR(); stageQ(30);
        WAIT_V(16); BAR(); stageQ(31);
        WAIT_V(12); BAR(); wdma(0); wdma(4);
        WAIT_V(12); BAR(); wdma(1); wdma(5);
        WAIT_V(16); BAR(); wput(2); wput(6);
        BAR(); wput(3);
      }
    } else {
      STAMP(1);
      wdma(0); wdma(4); wdma(1); wdma(5); wdma(2); wdma(3); wdma(6);
    }
    STAMP(2); STAMP(3);
    WAIT_V(0);
  } else if (work2) {
    const int wp = wid >> 1, wq = wid & 1;
    f32x4 acc[4][4];
#pragma unroll
    for (int ni = 0; ni < 4; ++ni) {
      const float bias = mat == 2 ? __uint_as_float(bv[ni] << 16) : 0.0f;
#pragma unroll
      for (int n = 0; n < 4; ++n) acc[ni][n] = f32x4{bias, bias, bias, bias};
    }
    BAR();
    if (DWARM && cb == 14) warm_window(a, 0, pm, 2, wid, lane);
    const int sw = (fr >> 1) & 7;
    const unsigned ra = P2_PB + (wp * 64 + fr) * 128 + ((fq ^ sw) << 4);
    const unsigned rb = wq * 8192 + vl;
    bf16x8 a0[4], b0[4], a1[4], b1[4];
    unsigned poff = 2 * 16384;
    unsigned pa0 = ra + poff, pa1 = pa0 ^ 64;
#define K2_RD(AF, BF, pa, sl, ks, i) do { \
    if ((i) == 0) AF[0] = ld_frag(pa); \
    else if ((i) <= 4) BF[(i) - 1] = ld_frag(rb + (sl) * 16384 + ((i) - 1) * 2048 + (ks) * 1024); \
    else AF[(i) - 4] = ld_frag(pa + ((i) - 4) * 2048); } while (0)
#define K2_MM(AF, BF, i) acc[(i) >> 2][(i) & 3] = MFMA16(BF[(i) & 3], AF[(i) >> 2], acc[(i) >> 2][(i) & 3])
#define K2_STEP(s, LAST) do { \
    SCHED(); WL0(); SCHED(); \
    _Pragma("unroll") for (int i = 0; i < 16; ++i) { K2_MM(a0, b0, i); SCHED(); if (i < 8) K2_RD(a1, b1, pa1, s, 1, i); \
      if (i == 10) { poff = poff == 5 * 16384 ? 0 : poff + 16384; pa0 = ra + poff; } SCHED(); } \
    WL0(); \
    if ((s) == 3) WAIT_V(0); \
    BAR(); \
    SCHED(); \
    _Pragma("unroll") for (int i = 0; i < 16; ++i) { K2_MM(a1, b1, i); SCHED(); if (!(LAST) && i < 8) K2_RD(a0, b0, pa0, ((s) + 1) & 3, 0, i); \
      if (i == 10) pa1 = pa0 ^ 64; SCHED(); } \
  } while (0)
    if (DWARM && cb == 14) WAIT_V(0);
    BAR();
    z1_finish();
    BAR();
    STAMP(1);
#pragma unroll
    for (int i = 0; i < 8; ++i) K2_RD(a0, b0, pa0, 0, 0, i);
    for (int kt = 0; kt < 28; kt += 4) { K2_STEP(0, 0); K2_STEP(1, 0); K2_STEP(2, 0); K2_STEP(3, 0); }
    K2_STEP(0, 0); K2_STEP(1, 0); K2_STEP(2, 0); K2_STEP(3, 1);
    SCHED();
    STAMP(2);
    if (mat < 2) {
      const rsrc_t rY = RSRC(a.ws + WS_Y + (size_t)mat * S * D * 2, S * D * 2);
      const unsigned so = ((8 * tm + 4 * wp) * 64 + 16 * tn + 8 * wq) * 256;
#pragma unroll
      for (int ni = 0; ni < 4; ++ni)
#pragma unroll
        for (int P = 0; P < 2; ++P) {
          const f32x4 c0 = acc[ni][2 * P], c1 = acc[ni][2 * P + 1];
          const u32x4 v = {cvt_pk(c0[0], c0[1]), cvt_pk(c0[2], c0[3]), cvt_pk(c1[0], c1[1]), cvt_pk(c1[2], c1[3])};
          st16_sc1(v, rY, vl, so + (ni * 64 + 4 * P) * 256);
        }
    } else {
      const rsrc_t rH = RSRC(a.ws + WS_H, S * FH * 2);
      const unsigned so = ((8 * tm + 4 * wp) * 112 + 16 * tn + 8 * wq) * 256;
      const unsigned za = P2_Z + wp * 16384 + wq * 2048 + (fq * 16 + ((fr & 4) << 1) + ((fr & 8) >> 1) + (fr & 3)) * 16;
#pragma unroll
      for (int ni = 0; ni < 4; ++ni) {
#pragma unroll
        for (int P = 0; P < 2; ++P) {
          const f32x4 c0 = acc[ni][2 * P], c1 = acc[ni][2 * P + 1];
          const u32x4 z = *(const LAS u32x4*)(size_t)(za + ni * 4096 + P * 1024);
          const u32x4 v = {cvt_pk(bf_lo(z[0]) * c0[0], bf_hi(z[0]) * c0[1]), cvt_pk(bf_lo(z[1]) * c0[2], bf_hi(z[1]) * c0[3]),
                           cvt_pk(bf_lo(z[2]) * c1[0], bf_hi(z[2]) * c1[1]), cvt_pk(bf_lo(z[3]) * c1[2], bf_hi(z[3]) * c1[3])};
          st16_sc1(v, rH, vl, so + (ni * 112 + 4 * P) * 256);
        }
      }
    }
    STAMP(3);
    WAIT_V(0);
  } else {
    STAMP(1);
    for (int i = 0; i < PF_N && blockIdx.x != 255; ++i) {
      warm_window(a, P2_Z, pm, (PF_LIST >> (4 * i)) & 15, wid, lane);
      WAIT_V(6);
    }
    STAMP(2);
    WAIT_V(0);
    STAMP(3);
  }
  WL0();
  BAR();
  STAMP(4);
  {
    const int wr = (wid + tn3) & 7, tp = wr >> 2, kq = wr & 3;
    const int tt3 = 4 * tm3 + 2 * tp + (kq & 1);
    const int cq = 2 * (fq & 1) + (fq >> 1);
    const int c3 = 8 * tn3 + 2 * (kq & 2);
    if (wid == 4 && work2) {
      const unsigned box = mat == 2 ? fr : (fr & 8) + 2 * tn + (fr & 1);
      st4_sc1(1u, rG, box * 128, G_FP + tm * 2048 + (mat == 2 ? tn : 8 + mat) * 4);
    }
    const int q4 = fr >> 2, pp = lane & 3;
    const int sp = ((q4 >> 1) & 1) | ((fq & 1) << 1);
    const unsigned wa = kq * 4096 + 128 * (8 * fq + q4) + 16 * ((2 * (kq & 2) + (pp >> 1)) ^ (sp << 1)) + 8 * (pp & 1);
    bf16x8 wf[7][4];
    auto ld_w = [&]() {
#pragma unroll
      for (int mi = 0; mi < 4; ++mi) {
        unsigned lo = wa ^ (mi * 32), hi = lo + 65536;
        OPAQUE(hi);
#pragma unroll
        for (int g = 0; g < 7; ++g) wf[g][mi] = ld_tr((g < 4 ? lo : hi) + (g & 3) * 16384);
      }
    };
    if (wid == 4) {
      for (unsigned spin = 0; spin < SPIN; ++spin) {
        const unsigned fl = ld4_sc1(rG, fr * 4, G_FP + (tm3 * 8 + tn3) * 128);
        if (__all(fl == 1u || fr == 7 || fr > 9)) break;
      }
    } else {
      ld_w();
    }
    BAR();
    STAMP(5);
    const rsrc_t rH = RSRC(a.ws + WS_H + (size_t)((4 * tm3 + 2 * tp) * 112 + 4 * kq) * 256, 2 * 112 * 256);
    const rsrc_t rY = RSRC(a.ws + WS_Y + (size_t)(tt3 * 64 + c3) * 256, S * D * 2 + 1024);
    const unsigned vh0 = vl + (kq & 1) * (112 * 256), vh1 = vl + ((kq & 1) ^ 1) * (112 * 256);
    u32x4 hf[7][2];
#pragma unroll
    for (int g = 0; g < 7; ++g) {
      hf[g][0] = __builtin_amdgcn_raw_buffer_load_b128(rH, vh0, g * 4096, 16);
      hf[g][1] = __builtin_amdgcn_raw_buffer_load_b128(rH, vh1, g * 4096, 16);
    }
    const unsigned vy = cq * 256 + fr * 16;
    const u32x4 e1 = __builtin_amdgcn_raw_buffer_load_b128(rY, vy, 0, 16), e2 = __builtin_amdgcn_raw_buffer_load_b128(rY, vy, S * D * 2, 16);
    const u32x4 ex = *(const u32x4*)(a.x + (size_t)(tt3 * 16 + fr) * D + (c3 + cq) * 8);
    SCHED();
    if (wid == 4) ld_w();
    f32x4 acc[2][4];
#pragma unroll
    for (int g = 0; g < 7; ++g)
#pragma unroll
      for (int tl = 0; tl < 2; ++tl)
#pragma unroll
        for (int mi = 0; mi < 4; ++mi) acc[tl][mi] = MFMA16(wf[g][mi], __builtin_bit_cast(bf16x8, hf[g][tl]), (g ? acc[tl][mi] : f32x4{0, 0, 0, 0}));
    STAMP(6);
    LAS f32x4* xb = (LAS f32x4*)(size_t)(P3_X) + lane;
#pragma unroll
    for (int d = 1; d < 4; ++d) {
      xb[((wr * 3 + d - 1) * 2) * 64] = acc[d & 1][d & 2];
      xb[((wr * 3 + d - 1) * 2 + 1) * 64] = acc[d & 1][(d & 2) + 1];
    }
    WL0();
    BAR();
    f32x4 s0 = acc[0][0], s1 = acc[0][1];
#pragma unroll
    for (int d = 1; d < 4; ++d) {
      s0 += xb[(((wr ^ d) * 3 + d - 1) * 2) * 64];
      s1 += xb[(((wr ^ d) * 3 + d - 1) * 2 + 1) * 64];
    }
    f32x4 c0, c1;
#pragma unroll
    for (int j = 0; j < 4; ++j) {
      auto w = __builtin_amdgcn_permlane16_swap(__float_as_uint(s0[j]), __float_as_uint(s1[j]), false, false);
      c0[j] = __uint_as_float(w[0]); c1[j] = __uint_as_float(w[1]);
    }
    u32x4 o;
#pragma unroll
    for (int k = 0; k < 4; ++k) {
      const f32x4 c = (k >> 1) ? c1 : c0;
      const float lo = bf_lo(ex[k]) + bf_lo(e1[k]) - bf_lo(e2[k]) + c[(k & 1) * 2];
      const float hi = bf_hi(ex[k]) + bf_hi(e1[k]) - bf_hi(e2[k]) + c[(k & 1) * 2 + 1];
      o[k] = cvt_pk(lo, hi);
    }
    __builtin_nontemporal_store(o, (u32x4*)(a.out + (size_t)(tt3 * 16 + fr) * D + (c3 + cq) * 8));
  }
  STAMP(7);
  STAMP_OUT(a.ws, 1, 8);
  if (blockIdx.x == PBLK && threadIdx.x < 64) {
    const unsigned long long tend = __builtin_amdgcn_s_memrealtime() + 4 * (ptime - pstart);
    while (__builtin_amdgcn_s_memrealtime() < tend) __builtin_amdgcn_s_sleep(1);
  }
}

extern "C" void kernel_launch(void* const* d_in, const int* in_sizes, int n_in, void* d_out, int out_size, void* d_ws, size_t ws_size,
                              hipStream_t stream) {
  (void)in_sizes; (void)n_in; (void)out_size; (void)ws_size;
  (void)hipFuncSetAttribute((const void*)fused, hipFuncAttributeMaxDynamicSharedMemorySize, LDS_BYTES);
  Args a{(const u16*)d_in[0], (const u16*)d_in[3], (const u16*)d_in[4], (const u16*)d_in[6], (const u16*)d_in[9],
         (const u16*)d_in[1], (const u16*)d_in[2], (const u16*)d_in[7], (const u16*)d_in[8], (char*)d_ws, (u16*)d_out};
  fused<<<256, 512, LDS_BYTES, stream>>>(a);
}
```

```cpp
#define PROBE 23
#define PBLK 73
#define SKEWK 0
#define SKM 0
#include <hip/hip_runtime.h>
#include <hip/hip_bf16.h>
#include <stdint.h>

typedef unsigned short u16;
typedef short bf16x8 __attribute__((ext_vector_type(8)));
typedef short s16x4 __attribute__((ext_vector_type(4)));
typedef float f32x4 __attribute__((ext_vector_type(4)));
typedef float f32x2 __attribute__((ext_vector_type(2)));
typedef unsigned u32x4 __attribute__((ext_vector_type(4)));
typedef __bf16 bf16x2v __attribute__((ext_vector_type(2)));
typedef __amdgpu_buffer_rsrc_t rsrc_t;
#define LAS __attribute__((address_space(3)))
#define WAIT_V(n) asm volatile("s_waitcnt vmcnt(%0)" ::"n"(n) : "memory")
#define WL0() __builtin_amdgcn_s_waitcnt(0xC07F)
#define BAR() do { asm volatile("" ::: "memory"); __builtin_amdgcn_s_barrier(); asm volatile("" ::: "memory"); } while (0)
#define SCHED() __builtin_amdgcn_sched_barrier(0)
#define MFMA16(a, b, c) __builtin_amdgcn_mfma_f32_16x16x32_bf16(a, b, c, 0, 0, 0)
#define RSRC(p, n) __builtin_amdgcn_make_buffer_rsrc((void*)(p), 0, (unsigned)(n), 0x00020000)
#define OPAQUE(x) asm volatile("" : "+v"(x))

#define STAMP_DECL
#define STAMP(i) do { if (KID * 16 + (i) == PROBE) asm volatile("s_memrealtime %0\n\ts_waitcnt lgkmcnt(0)" : "=s"(ptime) :: "memory"); } while (0)
#define STAMP_OUT(ws, kid, n)
#define K2_KXMASK 3
#define K1_KXMASK 3
#define DWARM 0
#define CODEWARM 5
#define NOPARK 0
#define BULKLATE 0
#define ZNB 0
#define GATHER_AT 18
#define WINSTART 1
#define PF_LIST 0x753810u
#define PF_N 6

constexpr int S = 2048, D = 512, F = 1792, FH = 896;
constexpr size_t WS_Q = 0;
constexpr size_t WS_Z1 = WS_Q + (size_t)(2 * D + FH) * S * 2;
constexpr size_t WS_H = WS_Z1 + (size_t)FH * S * 2;
constexpr size_t WS_Y = WS_H + (size_t)FH * S * 2;
constexpr size_t WS_G = WS_Y + (size_t)2 * D * S * 2;
constexpr unsigned G_F1 = 0;
constexpr unsigned G_FP = 32768;
constexpr unsigned G_FZ = 32768 + 64;
constexpr unsigned G_PAY = 65536;
constexpr unsigned G_ALL = G_PAY + 16 * 14336;
constexpr unsigned G_REQ = G_ALL + 1024;
constexpr unsigned G_ANS = G_REQ + 1024;
constexpr unsigned G_END = G_ANS + 1024;

constexpr int LDS_BYTES = 163840;
constexpr int P1_RING = 32768, P1_SCR = 81920;
constexpr int P2_PB = 65536, P2_Z = 131072, P3_X = 114688;
constexpr unsigned SPIN = 1u << 20;

struct Args {
  const u16* x; const u16* wdr; const u16* wdi; const u16* win; const u16* wout;
  const u16* wsr; const u16* wsi; const u16* wsp; const u16* bsp;
  char* ws; u16* out;
};

__device__ __forceinline__ unsigned cvt_pk(float lo, float hi) {
  f32x2 v = {lo, hi};
  return __builtin_bit_cast(unsigned, __builtin_convertvector(v, bf16x2v));
}
__device__ __forceinline__ float bf_lo(unsigned u) { return __uint_as_float(u << 16); }
__device__ __forceinline__ float bf_hi(unsigned u) { return __uint_as_float(u & 0xffff0000u); }
constexpr float GC1 = -2.0f * 0.7978845608028654f * 1.4426950408889634f, GC2 = GC1 * 0.044715f;
__device__ __forceinline__ f32x2 gelu2(f32x2 v) {
  const f32x2 t = v * ((v * v) * GC2 + GC1);
  const f32x2 e = f32x2{__builtin_amdgcn_exp2f(t[0]), __builtin_amdgcn_exp2f(t[1])} + 1.0f;
  return v * f32x2{__builtin_amdgcn_rcpf(e[0]), __builtin_amdgcn_rcpf(e[1])};
}
__device__ __forceinline__ void dot_sq(float& c, unsigned a) { asm volatile("v_dot2c_f32_bf16 %0, %1, %1" : "+v"(c) : "v"(a)); }
__device__ __forceinline__ void dot_sum(float& c, unsigned a) { asm volatile("v_dot2c_f32_bf16 %0, 0x3f803f80, %1" : "+v"(c) : "v"(a)); }
__device__ __forceinline__ float sum_rows(float v) {
  auto a = __builtin_amdgcn_permlane32_swap(__float_as_uint(v), __float_as_uint(v), false, false);
  v = __uint_as_float(a[0]) + __uint_as_float(a[1]);
  auto b = __builtin_amdgcn_permlane16_swap(__float_as_uint(v), __float_as_uint(v), false, false);
  return __uint_as_float(b[0]) + __uint_as_float(b[1]);
}
__device__ __forceinline__ float sel4(f32x4 v, int i) { const float a = (i & 1) ? v[1] : v[0], b = (i & 1) ? v[3] : v[2]; return (i & 2) ? b : a; }

__device__ __forceinline__ void dma16(rsrc_t r, unsigned mbase, int mimm, unsigned voff, unsigned soff) {
  asm volatile("s_add_i32 m0, %0, %1\n\ts_nop 0\n\tbuffer_load_dwordx4 %2, %3, %4 offen lds" ::"s"(mbase), "n"(mimm), "v"(voff), "s"(r), "s"(soff) : "scc");
}
__device__ __forceinline__ void dma16_sc1(rsrc_t r, unsigned mbase, int mimm, unsigned voff, unsigned soff) {
  asm volatile("s_add_i32 m0, %0, %1\n\ts_nop 0\n\tbuffer_load_dwordx4 %2, %3, %4 offen sc1 lds" ::"s"(mbase), "n"(mimm), "v"(voff), "s"(r), "s"(soff) : "scc");
}
__device__ __forceinline__ void dma4_sc1(rsrc_t r, int ldsaddr, unsigned voff, unsigned soff) {
  asm volatile("s_mov_b32 m0, %0\n\ts_nop 0\n\tbuffer_load_dword %1, %2, %3 offen sc1 lds" ::"n"(ldsaddr), "v"(voff), "s"(r), "s"(soff) : "memory");
}
__device__ __forceinline__ void touch(rsrc_t r, int junk, unsigned voff, unsigned soff) {
  asm volatile("s_mov_b32 m0, %0\n\ts_nop 0\n\tbuffer_load_dword %1, %2, %3 offen lds" ::"n"(junk), "v"(voff), "s"(r), "s"(soff) : "memory");
}
__device__ __forceinline__ void st16_sc1(u32x4 v, rsrc_t r, unsigned voff, unsigned soff) {
  asm volatile("buffer_store_dwordx4 %0, %1, %2, %3 offen sc1\n\ts_nop 1" ::"v"(v), "v"(voff), "s"(r), "s"(soff) : "memory");
}
__device__ __forceinline__ void st4_sc1(unsigned v, rsrc_t r, unsigned voff, unsigned soff) {
  asm volatile("buffer_store_dword %0, %1, %2, %3 offen sc1" ::"v"(v), "v"(voff), "s"(r), "s"(soff) : "memory");
}
__device__ __forceinline__ unsigned ld4_sc1(rsrc_t r, unsigned voff, unsigned soff) {
  unsigned v;
  asm volatile("buffer_load_dword %0, %1, %2, %3 offen sc1\n\ts_waitcnt vmcnt(0)" : "=&v"(v) : "v"(voff), "s"(r), "s"(soff) : "memory");
  return v;
}
__device__ __forceinline__ u32x4 ld16_sc1(rsrc_t r, unsigned voff, unsigned soff) {
  u32x4 v;
  asm volatile("buffer_load_dwordx4 %0, %1, %2, %3 offen sc1\n\ts_waitcnt vmcnt(0)" : "=&v"(v) : "v"(voff), "s"(r), "s"(soff) : "memory");
  return v;
}
__device__ __forceinline__ void ld16_async(u32x4& v, rsrc_t r, unsigned voff, unsigned soff) {
  asm volatile("buffer_load_dwordx4 %0, %1, %2, %3 offen" : "=v"(v) : "v"(voff), "s"(r), "s"(soff) : "memory");
}
__device__ __forceinline__ void wait_vn(int n) { WAIT_V(n); }
__device__ __forceinline__ bf16x8 ld_tr(unsigned addr) {
  s16x4 lo = __builtin_amdgcn_ds_read_tr16_b64_v4i16((LAS s16x4*)(size_t)(addr));
  s16x4 hi = __builtin_amdgcn_ds_read_tr16_b64_v4i16((LAS s16x4*)(size_t)(addr + 512));
  return __builtin_shufflevector(lo, hi, 0, 1, 2, 3, 4, 5, 6, 7);
}
__device__ __forceinline__ bf16x8 ld_frag(unsigned addr) { return *(const LAS bf16x8*)(size_t)(addr); }
__device__ __forceinline__ void wait_go(rsrc_t rG, unsigned nonce, unsigned seen) {
  for (unsigned spin = 0; seen != nonce && spin < SPIN; ++spin) {
    seen = __builtin_amdgcn_readfirstlane(ld4_sc1(rG, 0, G_ANS + blockIdx.x * 4));
    __builtin_amdgcn_s_sleep(1);
  }
}

template <bool ZB>
__device__ __forceinline__ void phase1(const Args& a, int pm, int cb, int kx, const u16* Pp, int kx2, f32x4 (&z1v)[2][2], unsigned long long& ptime) {
  constexpr int KID = 0;
  const int tid = threadIdx.x, lane = tid & 63;
  const int wid = __builtin_amdgcn_readfirstlane(tid >> 6);
  const int wm = wid & 3, wn = wid >> 2;
  STAMP_DECL STAMP(0);
  const rsrc_t rX = RSRC(a.x + (size_t)pm * 128 * D, 128 * D * 2);
  const rsrc_t rW = ZB ? RSRC(a.win + 64 * cb, (D * F - 64 * cb) * 2) : RSRC(a.wdi + (size_t)384 * D, 128 * D * 2);
  const int r8 = lane >> 3, p8 = lane & 7;
  const unsigned sw_e = (p8 ^ (r8 >> 1)) << 4;
  const unsigned vx_e = (32 * (wid >> 1) + 4 * (wid & 1) + 8 * (r8 >> 2) + (r8 & 3)) * (D * 2) + sw_e;
  const unsigned vx_o = (vx_e ^ 64) + 16 * (D * 2);
  const unsigned vw2 = ZB ? (8 * wid + r8) * (F * 2) + ((p8 ^ ((((r8 >> 1) & 1) | ((wid & 1) << 1)) << 1)) << 4)
                          : (64 + 8 * wid + r8) * (D * 2) + (sw_e ^ ((wid & 1) << 6));
  const unsigned vw1 = ZB ? vw2 + FH * 2 : vw2 - 64 * (D * 2);
  constexpr unsigned WKB = ZB ? 64 * F * 2 : 128;
  const unsigned mb1 = wid * 1024, mb2 = wid * 2048;
  auto piece1 = [&](int st, int p) {
    const unsigned kb = st ^ kx;
    const int base = P1_RING + (st & 3) * 32768;
    if (p == 0) dma16(rX, mb2, base, vx_e, kb * 128);
    else if (p == 1) dma16(rW, mb1, base + 16384, vw1, kb * WKB);
    else if (p == 2) dma16(rX, mb2, base + 1024, vx_o, kb * 128);
    else dma16(rW, mb1, base + 24576, vw2, kb * WKB);
  };
  piece1(0, 0); piece1(0, 1); piece1(0, 2); piece1(0, 3);
  SCHED();

  const int fr = lane & 15, fq = lane >> 4;
  const int kh = wm >> 1;
  const unsigned fsw = ((fq ^ ((fr >> 1) & 7)) << 4) ^ (kh << 6);
  unsigned xa[2][2][2], wb[2][2][2];
#pragma unroll
  for (int m = 0; m < 2; ++m) { xa[0][m][0] = P1_RING + (32 * wm + 16 * (m ^ wn) + fr) * 128 + fsw; xa[0][m][1] = xa[0][m][0] ^ 64; }
#pragma unroll
  for (int n = 0; n < 2; ++n) {
    const int tile = 2 * wn + (n ^ (wm & 1));
    if (ZB) {
      const int q4 = fr >> 2, pp = lane & 3;
      const int sp = ((q4 >> 1) & 1) | ((fq & 1) << 1);
      const int ch = (2 * tile + (pp >> 1)) ^ (sp << 1);
      wb[0][n][0] = P1_RING + 16384 + 128 * (8 * fq + q4) + 16 * ch + 8 * (pp & 1) + kh * 4096;
      wb[0][n][1] = wb[0][n][0] ^ 4096;
    } else {
      wb[0][n][0] = P1_RING + 16384 + (16 * tile + fr) * 128 + fsw;
      wb[0][n][1] = wb[0][n][0] ^ 64;
    }
  }
#pragma unroll
  for (int i = 0; i < 2; ++i)
#pragma unroll
    for (int ks = 0; ks < 2; ++ks) {
      xa[1][i][ks] = xa[0][i][ks] + 65536; wb[1][i][ks] = wb[0][i][ks] + 65536;
      OPAQUE(xa[0][i][ks]); OPAQUE(wb[0][i][ks]); OPAQUE(xa[1][i][ks]); OPAQUE(wb[1][i][ks]);
    }
  auto ldw = [&](unsigned addr) -> bf16x8 { return ZB ? ld_tr(addr) : ld_frag(addr); };
  SCHED();
  piece1(1, 0); piece1(1, 1); piece1(1, 2); piece1(1, 3);
  const rsrc_t rG = RSRC(a.ws + WS_G, G_END);
  if (wid == 7) dma4_sc1(rG, 0, 0, G_ANS + blockIdx.x * 4);
  unsigned nonce = 0;
  STAMP(1);

  bf16x8 xf[16][2], wf[2][4];
  const f32x4 Z4 = {0, 0, 0, 0};
  f32x4 acc1[2][2] = {{Z4, Z4}, {Z4, Z4}}, acc2[2][2] = {{Z4, Z4}, {Z4, Z4}};
  f32x4 gram = Z4, rsum = Z4, cs1 = Z4, cs2 = Z4;
  const bf16x8 ONES = {0x3f80, 0x3f80, 0x3f80, 0x3f80, 0x3f80, 0x3f80, 0x3f80, 0x3f80};

  auto mm = [&](int kk, int i) {
    const int ks = kk & 1;
    if (i == 0) gram = MFMA16(xf[kk][0], xf[kk][0], gram);
    else if (i == 1) rsum = MFMA16(xf[kk][0], ONES, rsum);
    else if (i < 6) { const int m = (i - 2) >> 1, n = i & 1; acc1[m][n] = MFMA16(xf[kk][m], wf[ks][n], acc1[m][n]); }
    else if (i < 10) { const int m = (i - 6) >> 1, n = i & 1; acc2[m][n] = ZB ? MFMA16(wf[ks][2 + n], xf[kk][m], acc2[m][n]) : MFMA16(xf[kk][m], wf[ks][2 + n], acc2[m][n]); }
    else if (i == 10) cs1 = MFMA16(ONES, wf[0][0], cs1);
    else cs2 = MFMA16(ONES, wf[0][2], cs2);
  };
  auto rd = [&](int kk, int i) {
    const int st = kk >> 1, ks = kk & 1, wi = (st >> 1) & 1, so = (st & 1) * 32768;
    if (i == 0 || i == 3) xf[kk][i & 1] = ld_frag(xa[wi][i & 1][ks] + so);
    else if (i < 3) wf[ks][i - 1] = ldw(wb[wi][i - 1][ks] + so);
    else wf[ks][i - 2] = ldw(wb[wi][i - 4][ks] + so + 8192);
  };
  auto piece3 = [&](int p) {
    const rsrc_t rD = RSRC((cb < 8 ? a.wdr : a.wdi) + (size_t)(cb & 7) * 64 * D, 64 * D * 2);
    const unsigned ve = r8 * (D * 2) + sw_e, vo = (ve ^ 64) + 8 * (D * 2);
    dma16(rD, wid * 8192, p * 1024, (p & 1) ? vo : ve, (wid ^ kx) * 128 + (p >> 1) * 16 * (D * 2));
  };
  auto earlyP = [&](int k) {
    const rsrc_t rP = RSRC(Pp, 128 * S * 2);
    const unsigned ve = (16 * wid + r8) * (S * 2) + sw_e, vo = (ve ^ 64) + 8 * (S * 2);
    dma16(rP, mb2, P2_PB + (k + 2) * 16384, ve, (k ^ kx2) * 128);
    dma16(rP, mb2, P2_PB + (k + 2) * 16384 + 1024, vo, (k ^ kx2) * 128);
  };
#pragma unroll
  for (int st = 0; st < 8; ++st) {
    wait_vn(st <= 6 ? 4 : ZB ? 8 : 0);
    WL0();
    BAR();
    if (st == 0) STAMP(2);
    SCHED();
#pragma unroll
    for (int i = 0; i < 10; ++i) {
      if (st > 0) mm(2 * st - 1, i);
      SCHED();
      if (i < 6) rd(2 * st, i);
      if (st + 2 < 8) { if (i == 3) piece1(st + 2, 0); if (i == 8) piece1(st + 2, 1); }
      if (ZB && st == 6 && (i % 3) == 0) piece3(i / 3);
      if (st == 7 && i == 7) earlyP(0);
      SCHED();
    }
    WL0();
    SCHED();
#pragma unroll
    for (int i = 0; i < 12; ++i) {
      mm(2 * st, i);
      SCHED();
      if (i < 6) rd(2 * st + 1, i);
      if (st + 2 < 8) { if (i == 3) piece1(st + 2, 2); if (i == 9) piece1(st + 2, 3); }
      if (ZB && st == 6 && (i % 3) == 2) piece3(4 + i / 3);
      if (st == 7 && i == 8) earlyP(1);
      SCHED();
    }
    if (st == 2 && wid == 7) {
      nonce = __builtin_amdgcn_readfirstlane(*(LAS unsigned*)(size_t)(lane * 4)) + 1;
      st4_sc1(nonce, rG, 0, G_REQ + blockIdx.x * 4);
    }
    SCHED();
  }
  LAS float* const s_r = (LAS float*)(size_t)(P1_SCR);
  LAS float* const s_c = (LAS float*)(size_t)(P1_SCR + 512);
  LAS float* const s_u1 = (LAS float*)(size_t)(P1_SCR + 1024);
  LAS float* const s_u2 = (LAS float*)(size_t)(P1_SCR + 1536);
  LAS float* const s_z = (LAS float*)(size_t)(P1_SCR + 2048);
  LAS float* const s_n = (LAS float*)(size_t)(P1_SCR + 4096);
  LAS float* const s_u3 = (LAS float*)(size_t)(P1_SCR + 5120);
  const unsigned tb0 = fr * 128 + fsw, tb1 = tb0 ^ 64;
  WAIT_V(4);
  {
    const unsigned cb0 = tb0 + wid * 8192, cb1 = cb0 ^ 64;
    bf16x8 c3[8];
    f32x4 cs3[4];
    if (ZB) {
#pragma unroll
      for (int t = 0; t < 4; ++t) { c3[2 * t] = ld_frag(cb0 + t * 2048); c3[2 * t + 1] = ld_frag(cb1 + t * 2048); }
    }
    mm(15, 0); mm(15, 1);
    SCHED();
    if (ZB) {
#pragma unroll
      for (int t = 0; t < 4; ++t) cs3[t] = MFMA16(ONES, c3[2 * t], Z4);
#pragma unroll
      for (int t = 0; t < 4; ++t) cs3[t] = MFMA16(ONES, c3[2 * t + 1], cs3[t]);
    }
    SCHED();
#pragma unroll
    for (int i = 2; i < 10; ++i) mm(15, i);
    STAMP(3);
    unsigned i01 = 32 * wm + 16 * wn, i23 = 64 * kh + 16 * (2 * wn + (wm & 1));
    asm volatile("" : "+s"(i01), "+s"(i23));
    const float s1 = sel4(rsum, fr & 3), s2 = sel4(gram, fr & 3);
    const float mean = s1 * (1.0f / D);
    const float rstd = __builtin_amdgcn_rsqf(fmaxf(s2 * (1.0f / D) - mean * mean, 0.0f) + 1e-5f);
    if ((fr >> 2) == fq) { s_r[i01 + fr] = rstd; s_c[i01 + fr] = -mean * rstd; }
    if (fq < 2) s_u1[128 * fq + i23 + fr] = fq ? cs2[0] : cs1[0];
    if (ZB) s_u3[lane * 8 + wid] = sel4(f32x4{cs3[0][0], cs3[1][0], cs3[2][0], cs3[3][0]}, fq);
  }
  const int s0 = pm * 128 + 32 * wm;
  const rsrc_t rT = RSRC(a.ws + WS_Q, (2 * D + FH) * S * 2);
  const unsigned vT = ((fr >> 2) & 1) * 65536 + fq * 256 + (4 * (fr >> 3) + (fr & 3)) * 16;
  WL0();
  BAR();
  STAMP(4);
  if (wid == 7) dma4_sc1(rG, P1_SCR + 7168, 0, G_ANS + blockIdx.x * 4);
  earlyP(2); earlyP(3);
  f32x4 rq[2], cq[2];
  auto ld_rc = [&]() {
#pragma unroll
    for (int m = 0; m < 2; ++m) {
      rq[m] = *(LAS f32x4*)(s_r + 32 * wm + 16 * (m ^ wn) + 4 * fq);
      cq[m] = *(LAS f32x4*)(s_c + 32 * wm + 16 * (m ^ wn) + 4 * fq);
    }
  };
  ld_rc();
  auto store_t = [&](int T, f32x4 v0, f32x4 v1) {
    const unsigned a0 = cvt_pk(v0[0], v0[1]), a1 = cvt_pk(v0[2], v0[3]), b0 = cvt_pk(v1[0], v1[1]), b1 = cvt_pk(v1[2], v1[3]);
    const u32x4 o = wn ? u32x4{b0, b1, a0, a1} : u32x4{a0, a1, b0, b1};
    st16_sc1(o, rT, vT, (T >> 1) * 131072 + (T & 1) * 128 + s0 * 32);
  };
  auto lin_store = [&](f32x4 c0, f32x4 c1, float u, int T) { store_t(T, rq[0] * c0 + cq[0] * u, rq[1] * c1 + cq[1] * u); };
  float u1[2];
#pragma unroll
  for (int n = 0; n < 2; ++n) { const int ui = 16 * (2 * wn + (n ^ (wm & 1))) + fr; u1[n] = s_u1[ui] + s_u1[64 + ui]; }
  if (!ZB) {
#pragma unroll
    for (int n = 0; n < 2; ++n) {
      const int tile = 2 * wn + (n ^ (wm & 1)), ui = 16 * tile + fr;
      lin_store(acc1[0][n], acc1[1][n], u1[n], 56 + tile);
      lin_store(acc2[0][n], acc2[1][n], s_u2[ui] + s_u2[64 + ui], 60 + tile);
    }
    if (wid == 7) { WAIT_V(0); wait_go(rG, nonce, __builtin_amdgcn_readfirstlane(*(LAS unsigned*)(size_t)(P1_SCR + 7168 + lane * 4))); }
    STAMP(5); STAMP(6); STAMP(7); STAMP(8); STAMP(9);
    STAMP_OUT(a.ws, 0, 10);
    return;
  }
  {
    f32x4 ps[2], pq[2];
#pragma unroll
    for (int n = 0; n < 2; ++n)
#pragma unroll
      for (int m = 0; m < 2; ++m) {
        f32x4 v = rq[m] * acc1[m][n] + cq[m] * u1[n];
        const f32x2 g0 = gelu2(f32x2{v[0], v[1]}), g1 = gelu2(f32x2{v[2], v[3]});
        v = f32x4{g0[0], g0[1], g1[0], g1[1]};
        acc1[m][n] = v;
        if (n == 0) { ps[m] = v; pq[m] = v * v; } else { ps[m] += v; pq[m] += v * v; }
      }
    float t[8], h[4];
#define DPP2(d, x, y, ctl, m0, m1) "v_add_f32_dpp " d ", " x ", " x " " ctl " row_mask:0xf bank_mask:" m0 "\n\tv_add_f32_dpp " d ", " y ", " y " " ctl " row_mask:0xf bank_mask:" m1 "\n\t"
#define DPPQ(d, ctl) "v_add_f32_dpp " d ", " d ", " d " " ctl " row_mask:0xf bank_mask:0xf\n\t"
    asm volatile("s_nop 1\n\t" DPP2("%0", "%8", "%16", "row_ror:8", "0x3", "0xc") DPP2("%1", "%9", "%17", "row_ror:8", "0x3", "0xc")
                 DPP2("%2", "%10", "%18", "row_ror:8", "0x3", "0xc") DPP2("%3", "%11", "%19", "row_ror:8", "0x3", "0xc")
                 DPP2("%4", "%12", "%20", "row_ror:8", "0x3", "0xc") DPP2("%5", "%13", "%21", "row_ror:8", "0x3", "0xc")
                 DPP2("%6", "%14", "%22", "row_ror:8", "0x3", "0xc") DPP2("%7", "%15", "%23", "row_ror:8", "0x3", "0xc") "s_nop 1"
                 : "=&v"(t[0]), "=&v"(t[1]), "=&v"(t[2]), "=&v"(t[3]), "=&v"(t[4]), "=&v"(t[5]), "=&v"(t[6]), "=&v"(t[7])
                 : "v"(ps[0][0]), "v"(pq[0][0]), "v"(ps[0][1]), "v"(pq[0][1]), "v"(ps[0][2]), "v"(pq[0][2]), "v"(ps[0][3]), "v"(pq[0][3]),
                   "v"(ps[1][0]), "v"(pq[1][0]), "v"(ps[1][1]), "v"(pq[1][1]), "v"(ps[1][2]), "v"(pq[1][2]), "v"(ps[1][3]), "v"(pq[1][3]));
    asm volatile("s_nop 1\n\t" DPP2("%0", "%4", "%8", "row_half_mirror", "0x5", "0xa") DPP2("%1", "%5", "%9", "row_half_mirror", "0x5", "0xa")
                 DPP2("%2", "%6", "%10", "row_half_mirror", "0x5", "0xa") DPP2("%3", "%7", "%11", "row_half_mirror", "0x5", "0xa") "s_nop 1\n\t"
                 DPPQ("%0", "quad_perm:[1,0,3,2]") DPPQ("%1", "quad_perm:[1,0,3,2]") DPPQ("%2", "quad_perm:[1,0,3,2]") DPPQ("%3", "quad_perm:[1,0,3,2]")
                 DPPQ("%0", "quad_perm:[2,3,0,1]") DPPQ("%1", "quad_perm:[2,3,0,1]") DPPQ("%2", "quad_perm:[2,3,0,1]") DPPQ("%3", "quad_perm:[2,3,0,1]") "s_nop 0"
                 : "=&v"(h[0]), "=&v"(h[1]), "=&v"(h[2]), "=&v"(h[3])
                 : "v"(t[0]), "v"(t[1]), "v"(t[2]), "v"(t[3]), "v"(t[4]), "v"(t[5]), "v"(t[6]), "v"(t[7]));
    if ((fr & 3) == 0) *(LAS f32x4*)(s_z + (wn * 128 + 32 * wm + 8 * fq + 4 * ((fr >> 3) ^ wn) + 2 * ((fr >> 2) & 1)) * 2) = f32x4{h[0], h[1], h[2], h[3]};
  }
  WL0();
  BAR();
  STAMP(5);
  if (wid == 7) {
    __builtin_amdgcn_s_setprio(2);
    const f32x4 p = *(LAS f32x4*)(s_z + lane * 4) + *(LAS f32x4*)(s_z + 256 + lane * 4);
    WAIT_V(4);
    wait_go(rG, nonce, __builtin_amdgcn_readfirstlane(*(LAS unsigned*)(size_t)(P1_SCR + 7168 + lane * 4)));
    st16_sc1(__builtin_bit_cast(u32x4, p) | 1u, rG, lane * 16, G_PAY + (pm * 14 + cb) * 1024);
  }
  STAMP(6);
  {
    float u3[2];
    f32x4 acct[2][2] = {{Z4, Z4}, {Z4, Z4}};
    const unsigned t30 = tb0 + wn * 4096, t31 = tb1 + wn * 4096;
    bf16x8 w[8];
    auto rd3 = [&](int f) { const int kk = f >> 1; w[f & 7] = ld_frag(((kk & 1) ? t31 : t30) + (kk >> 1) * 8192 + (f & 1) * 2048); };
#pragma unroll
    for (int f = 0; f < 4; ++f) rd3(f);
    SCHED();
#pragma unroll
    for (int f = 0; f < 32; ++f) {
      const int kk = f >> 1, n = f & 1;
      acct[0][n] = MFMA16(xf[kk][0], w[f & 7], acct[0][n]);
      SCHED();
      if (f + 4 < 32) rd3(f + 4);
      if (f == 8 || f == 12) {
        const int m = (f - 8) >> 2;
        const float r = s_r[32 * wm + 16 * (m ^ wn) + fr], c = s_c[32 * wm + 16 * (m ^ wn) + fr];
#pragma unroll
        for (int q = 0; q < 2; ++q) {
          const int ui = 16 * (2 * wn + (q ^ (wm & 1))) + 4 * fq;
          z1v[m][q] = acc2[m][q] * r + (*(LAS f32x4*)(s_u2 + ui) + *(LAS f32x4*)(s_u2 + 64 + ui)) * c;
        }
      }
      if (f == GATHER_AT && wid == 7) {
#pragma unroll
        for (int i = 0; i < 14; ++i) dma16_sc1(rG, 0, P2_PB + i * 1024, lane * 16, G_PAY + pm * 14336 + i * 1024);
      }
      if (f == 20 || f == 24) {
        const int q = (f - 20) >> 2;
        const f32x4 uu = *(LAS f32x4*)(s_u3 + (32 * wn + 16 * q + fr) * 8) + *(LAS f32x4*)(s_u3 + (32 * wn + 16 * q + fr) * 8 + 4);
        u3[q] = (uu[0] + uu[1]) + (uu[2] + uu[3]);
      }
      SCHED();
      acct[1][n] = MFMA16(xf[kk][1], w[f & 7], acct[1][n]);
      SCHED();
    }
    if (wid != 7) {
#pragma unroll
      for (int n = 0; n < 2; ++n) lin_store(acct[0][n], acct[1][n], u3[n], 4 * cb + 2 * wn + n);
    }
    STAMP(7);
  if (wid == 7) {
    f32x4 t;
    for (unsigned spin = 0; spin < SPIN; ++spin) {
      WAIT_V(0);
      u32x4 al = *(LAS u32x4*)(size_t)(P2_PB + lane * 16);
      t = __builtin_bit_cast(f32x4, al);
#pragma unroll
      for (int i = 1; i < 14; ++i) { const u32x4 pv = *(LAS u32x4*)(size_t)(P2_PB + i * 1024 + lane * 16); al &= pv; t += __builtin_bit_cast(f32x4, pv); }
      if (__all((al[0] & al[1] & al[2] & al[3] & 1u) != 0)) break;
      WL0();
#pragma unroll
      for (int i = 0; i < 14; ++i) dma16_sc1(rG, 0, P2_PB + i * 1024, lane * 16, G_PAY + pm * 14336 + i * 1024);
    }
    const float m0 = t[0] * (1.0f / FH), m1 = t[2] * (1.0f / FH);
    const float r0 = __builtin_amdgcn_rsqf(fmaxf(t[1] * (1.0f / FH) - m0 * m0, 0.0f) + 1e-5f);
    const float r1 = __builtin_amdgcn_rsqf(fmaxf(t[3] * (1.0f / FH) - m1 * m1, 0.0f) + 1e-5f);
    *(LAS f32x2*)(s_n + 2 * lane) = f32x2{r0, r1};
    *(LAS f32x2*)(s_n + 128 + 2 * lane) = f32x2{-m0 * r0, -m1 * r1};
    __builtin_amdgcn_s_setprio(0);
  }
  WL0();
  BAR();
  if (wid == 7) {
#pragma unroll
    for (int n = 0; n < 2; ++n) lin_store(acct[0][n], acct[1][n], u3[n], 4 * cb + 2 * wn + n);
  }
  }
  STAMP(8);
  {
    f32x4 r2[2], c2[2];
#pragma unroll
    for (int m = 0; m < 2; ++m) {
      r2[m] = *(LAS f32x4*)(s_n + 32 * wm + 8 * fq + 4 * (m ^ wn));
      c2[m] = *(LAS f32x4*)(s_n + 128 + 32 * wm + 8 * fq + 4 * (m ^ wn));
    }
#pragma unroll
    for (int n = 0; n < 2; ++n) store_t(64 + 4 * cb + 2 * wn + (n ^ (wm & 1)), acc1[0][n] * r2[0] + c2[0], acc1[1][n] * r2[1] + c2[1]);
  }
  STAMP(9);
  STAMP_OUT(a.ws, 0, 10);
}

__device__ __forceinline__ void warm_window(const Args& a, int junk, int rb, int w, int wv, int lane) {
  if (w < 8) {
    const unsigned vo = (rb * 128 + wv * 32 + (lane >> 2)) * (S * 2) + (lane & 3) * 128;
#pragma unroll
    for (int m = 0; m < 3; ++m) {
      const rsrc_t r = RSRC(m == 0 ? a.wsr : m == 1 ? a.wsi : a.wsp, S * S * 2);
      touch(r, junk, vo, w * 512);
      touch(r, junk, vo, w * 512 + 16 * (S * 2));
    }
  } else {
    const rsrc_t r = RSRC(a.wout + (size_t)rb * 56 * D, 56 * D * 2);
    touch(r, junk, lane * 128, wv * 16384);
    touch(r, junk, lane * 128, wv * 16384 + 8192);
  }
}

__global__ __launch_bounds__(512, 2) void fused(Args a) {
  extern __shared__ __attribute__((aligned(1024))) char smem[];
  asm volatile("" ::"v"(smem));
  constexpr int KID = 1;
  unsigned long long pstart = __builtin_amdgcn_s_memrealtime(), ptime = pstart;
  const int bx = blockIdx.x & 7, bi = blockIdx.x >> 3;
  if (bi < CODEWARM && threadIdx.x < 64) {
    unsigned long long pc;
    asm volatile("s_getpc_b64 %0" : "=s"(pc));
    touch(RSRC(pc & ~127ull, CODEWARM * 8192), 1024, threadIdx.x * 128, bi * 8192);
  }
  const int pm = 4 * (bx >> 1) + (bi >> 3), cb = 2 * (bi & 7) + (bx & 1);
  int mat, tm, tn;
  if (bx & 1) { mat = 2; tm = pm; tn = bi & 7; }
  else { mat = bx >> 2; tm = ((bx >> 1) & 1) * 8 + (bi >> 2); tn = bi & 3; }
  const int kx = (tm ^ tn) & K2_KXMASK;
  const u16* Pp = (mat == 0 ? a.wsr : mat == 1 ? a.wsi : a.wsp) + (size_t)tm * 128 * S;
  f32x4 z1v[2][2];
  {
    const int kx1 = ((bi & 7) ^ (2 * (bi >> 3))) & K1_KXMASK;
    if (__builtin_expect(cb < 14, 1)) phase1<true>(a, pm, cb, kx1, Pp, kx, z1v, ptime);
    else if (cb == 14) phase1<false>(a, pm, cb, kx1, Pp, kx, z1v, ptime);
  }
  int tid = threadIdx.x;
  asm volatile("" : "+v"(tid));
  const int lane = tid & 63;
  const int wid = __builtin_amdgcn_readfirstlane(tid >> 6);
  STAMP_DECL STAMP(0);
  const int tm3 = bx * 4 + (bi >> 3), tn3 = bi & 7;
  const int fr = lane & 15, fq = lane >> 4;
  const unsigned vl = lane * 16;
  const rsrc_t rG = RSRC(a.ws + WS_G, G_END);
  const bool work2 = cb != 15;
  auto z1_finish = [&]() {
    if (cb < 14) {
      const int wm = wid & 3, wn = wid >> 2;
      const rsrc_t rZ = RSRC(a.ws + WS_Z1, FH * S * 2);
      const unsigned vZ = (fr >> 3) * (112 * 256) + (2 * ((fq & 1) ^ (wm & 1)) + (fq >> 1)) * 256 + (fr & 7) * 16;
      const unsigned sZ = ((pm * 8 + 2 * wm) * 112 + 8 * cb + 4 * wn) * 256;
#pragma unroll
      for (int m = 0; m < 2; ++m) {
        unsigned pk[2][2];
#pragma unroll
        for (int n = 0; n < 2; ++n) {
          const f32x2 g0 = gelu2(f32x2{z1v[m][n][0], z1v[m][n][1]}), g1 = gelu2(f32x2{z1v[m][n][2], z1v[m][n][3]});
          pk[n][0] = cvt_pk(g0[0], g0[1]); pk[n][1] = cvt_pk(g1[0], g1[1]);
        }
        auto w0 = __builtin_amdgcn_permlane16_swap(pk[0][0], pk[1][0], false, false);
        auto w1 = __builtin_amdgcn_permlane16_swap(pk[0][1], pk[1][1], false, false);
        st16_sc1(u32x4{w0[0], w1[0], w0[1], w1[1]}, rZ, vZ, sZ + 128 * (m ^ wn));
      }
    }
  };
  if (blockIdx.x == 255) {
    for (int i = 0; i < 36; ++i) st16_sc1(u32x4{0, 0, 0, 0}, rG, vl, (wid * 36 + i) * 1024);
    st16_sc1(u32x4{0, 0, 0, 0}, rG, vl, G_ALL);
  }
  unsigned bv[4];
#pragma unroll
  for (int ni = 0; ni < 4; ++ni) bv[ni] = a.bsp[(8 * tm + 4 * ((wid >> 1) & 1) + ni) * 16 + fr];
  asm volatile("" : "+v"(bv[0]), "+v"(bv[1]), "+v"(bv[2]), "+v"(bv[3]));
  if (work2 || blockIdx.x == 255) {
    WAIT_V(0);
    WL0();
    BAR();
  }
  if (wid >= 4) {
    const int lw = wid - 4;
    const int r8 = lane >> 3, p = lane & 7;
    if (wid == 7) {
      if (!work2) {
        if (blockIdx.x == 255) {
          for (unsigned spin = 0; spin < SPIN; ++spin) {
            const u32x4 rq = ld16_sc1(rG, vl, G_REQ);
            st16_sc1(rq, rG, vl, G_ANS);
            const u32x4 fl = ld16_sc1(rG, vl, G_ALL);
            if (__all((fl[0] & fl[1] & fl[2] & (lane == 63 ? 1u : fl[3])) == 1u)) break;
          }
        } else {
          const unsigned nonce = __builtin_amdgcn_readfirstlane(ld4_sc1(rG, 0, G_ANS + blockIdx.x * 4)) + 1;
          st4_sc1(nonce, rG, 0, G_REQ + blockIdx.x * 4);
          wait_go(rG, nonce, nonce - 1);
        }
      }
      const unsigned box = (fq & 1) * 128 + (cb >> 1) * 16 + fr;
      st4_sc1(1u, rG, fq < 2 ? box * 128 + (pm * 2 + (cb & 1)) * 4 : G_ALL + blockIdx.x * 4, 0);
    }
    const rsrc_t rP = RSRC(Pp, 128 * S * 2);
    const unsigned vo_e = (lw * 32 + r8) * (S * 2) + ((p ^ (r8 >> 1)) << 4);
    const unsigned vo_o = (vo_e ^ 64) + 8 * (S * 2);
    auto stageP = [&](int kt, unsigned poff) {
      const unsigned mb = poff + lw * 4096, so = (kt ^ kx) * 128;
#pragma unroll
      for (int j = 0; j < 4; ++j) dma16(rP, mb, P2_PB + j * 1024, (j & 1) ? vo_o : vo_e, so + (j >> 1) * 16 * (S * 2));
    };
    if (work2) {
      BAR();
      if (!BULKLATE) { stageP(4, 0); stageP(5, 16384); }
    }
    const rsrc_t rWo = RSRC(a.wout + 64 * tn3, (FH * D - 64 * tn3) * 2);
    const unsigned vw_e = (32 * lw + r8) * (D * 2) + ((p ^ (((r8 >> 1) & 1) << 1)) << 4), vw_o = (vw_e ^ 64) + 8 * (D * 2);
    u32x4 wreg[12];
    auto wpark = [&]() {
#pragma unroll
      for (int i = 0; i < 12; ++i) {
        const int rg = i < 8 ? 2 + (i >> 2) : 6;
        ld16_async(wreg[i], rWo, (i & 1) ? vw_o : vw_e, (rg * 128 + ((i >> 1) & 1) * 16) * (D * 2));
      }
    };
    if (!BULKLATE && work2) wpark();
    auto wput = [&](int rg) {
#pragma unroll
      for (int j = 0; j < 4 && !NOPARK; ++j) *(LAS u32x4*)(size_t)(rg * 16384 + lw * 4096 + j * 1024 + vl) = wreg[(rg == 6 ? 8 : (rg - 2) * 4) + j];
    };
    auto wdma = [&](int rg) {
#pragma unroll
      for (int j = 0; j < 4; ++j) dma16(rWo, lw * 4096, rg * 16384 + j * 1024, (j & 1) ? vw_o : vw_e, (rg * 128 + (j >> 1) * 16) * (D * 2));
    };
    if (work2) {
      const unsigned box = (mat * 4 + tn) * 16 + tm;
      bool allq = true;
      if (wid == 7) {
        for (unsigned spin = 0; spin < SPIN; ++spin) {
          const unsigned fl = ld4_sc1(rG, (lane & 31) * 4, G_F1 + box * 128);
          const bool ok = fl == 1u || (box >= 112 && box < 128 && (lane & 1));
          allq = __all(ok);
          if (__all(ok || (WINSTART && (lane & 31) > 3))) break;
        }
      }
      BAR();
      STAMP(1);
      const rsrc_t rQ = RSRC(a.ws + WS_Q + (size_t)(mat * 4 + tn) * 128 * S * 2, 128 * S * 2);
      const unsigned vq = vl + lw * 131072;
      auto stageQ = [&](int kt) {
        const unsigned mb = (kt & 3) * 16384 + lw * 4096, so = (kt ^ kx) * 2048;
#pragma unroll
        for (int j = 0; j < 4; ++j) dma16_sc1(rQ, mb, j * 1024, vq, so + (j >> 1) * 65536 + (j & 1) * 1024);
      };
      stageQ(0); stageQ(1);
      z1_finish();
      WAIT_V(4); BAR();
      stageQ(2); stageQ(3);
      for (unsigned spin = 0; !allq && spin < SPIN; ++spin)
        allq = __all(ld4_sc1(rG, (lane & 31) * 4, G_F1 + box * 128) == 1u || (box >= 112 && box < 128 && (lane & 1)));
      if (BULKLATE) {
        stageP(4, 0); stageP(5, 16384);
        WAIT_V(16); BAR(); stageQ(4); stageP(6, 2 * 16384);
        WAIT_V(20); BAR(); stageQ(5); stageP(7, 3 * 16384);
        WAIT_V(24); BAR(); stageQ(6); stageP(8, 4 * 16384);
        wpark();
      } else {
        WAIT_V(8); BAR(); stageQ(4); stageP(6, 2 * 16384);
        WAIT_V(12); BAR(); stageQ(5); stageP(7, 3 * 16384);
        WAIT_V(16); BAR(); stageQ(6); stageP(8, 4 * 16384);
      }
      unsigned poff = 5 * 16384;
      for (int kt = 3; kt < 26; ++kt) {
        WAIT_V(20);
        BAR();
        if (kt == 3 && wid == 7 && cb < 14) st4_sc1(1u, rG, 0, G_FZ + (pm * 8 + (cb >> 1)) * 128 + (cb & 1) * 4);
        stageQ(kt + 4); stageP(kt + 6, poff);
        poff = poff == 5 * 16384 ? 0 : poff + 16384;
      }
      if (mat == 2) {
        const rsrc_t rZ = RSRC(a.ws + WS_Z1 + (size_t)((8 * tm + lw) * 112 + 16 * tn) * 256, 4 * 112 * 256 + 4096);
        auto stageZ = [&](int h) {
#pragma unroll
          for (int j = 0; j < 4; ++j) dma16_sc1(rZ, lw * 4096, P2_Z + h * 16384 + j * 1024, vl, h * (4 * 112 * 256) + j * 1024);
        };
        if (wid == 7) {
          for (unsigned spin = 0; spin < SPIN; ++spin)
            if (__all(ld4_sc1(rG, (lane & 1) * 4, G_FZ + (tm * 8 + tn) * 128) == 1u)) break;
        }
        WAIT_V(20); BAR(); stageQ(30); stageZ(0);
        WAIT_V(20); BAR(); stageQ(31); stageZ(1);
        WAIT_V(20); BAR(); wdma(0); wdma(4);
        WAIT_V(20); BAR(); wdma(1); wdma(5);
        WAIT_V(20); BAR(); wput(2); wput(6);
        WAIT_V(16); BAR(); wput(3);
      } else {
        WAIT_V(20); BAR(); stageQ(30);
        WAIT_V(16); BAR(); stageQ(31);
        WAIT_V(12); BAR(); wdma(0); wdma(4);
        WAIT_V(12); BAR(); wdma(1); wdma(5);
        WAIT_V(16); BAR(); wput(2); wput(6);
        BAR(); wput(3);
      }
    } else {
      STAMP(1);
      wdma(0); wdma(4); wdma(1); wdma(5); wdma(2); wdma(3); wdma(6);
    }
    STAMP(2); STAMP(3);
    WAIT_V(0);
  } else if (work2) {
    const int wp = wid >> 1, wq = wid & 1;
    f32x4 acc[4][4];
#pragma unroll
    for (int ni = 0; ni < 4; ++ni) {
      const float bias = mat == 2 ? __uint_as_float(bv[ni] << 16) : 0.0f;
#pragma unroll
      for (int n = 0; n < 4; ++n) acc[ni][n] = f32x4{bias, bias, bias, bias};
    }
    BAR();
    if (DWARM && cb == 14) warm_window(a, 0, pm, 2, wid, lane);
    const int sw = (fr >> 1) & 7;
    const unsigned ra = P2_PB + (wp * 64 + fr) * 128 + ((fq ^ sw) << 4);
    const unsigned rb = wq * 8192 + vl;
    bf16x8 a0[4], b0[4], a1[4], b1[4];
    unsigned poff = 2 * 16384;
    unsigned pa0 = ra + poff, pa1 = pa0 ^ 64;
#define K2_RD(AF, BF, pa, sl, ks, i) do { \
    if ((i) == 0) AF[0] = ld_frag(pa); \
    else if ((i) <= 4) BF[(i) - 1] = ld_frag(rb + (sl) * 16384 + ((i) - 1) * 2048 + (ks) * 1024); \
    else AF[(i) - 4] = ld_frag(pa + ((i) - 4) * 2048); } while (0)
#define K2_MM(AF, BF, i) acc[(i) >> 2][(i) & 3] = MFMA16(BF[(i) & 3], AF[(i) >> 2], acc[(i) >> 2][(i) & 3])
#define K2_STEP(s, LAST) do { \
    SCHED(); WL0(); SCHED(); \
    _Pragma("unroll") for (int i = 0; i < 16; ++i) { K2_MM(a0, b0, i); SCHED(); if (i < 8) K2_RD(a1, b1, pa1, s, 1, i); \
      if (i == 10) { poff = poff == 5 * 16384 ? 0 : poff + 16384; pa0 = ra + poff; } SCHED(); } \
    WL0(); \
    if ((s) == 3) WAIT_V(0); \
    BAR(); \
    SCHED(); \
    _Pragma("unroll") for (int i = 0; i < 16; ++i) { K2_MM(a1, b1, i); SCHED(); if (!(LAST) && i < 8) K2_RD(a0, b0, pa0, ((s) + 1) & 3, 0, i); \
      if (i == 10) pa1 = pa0 ^ 64; SCHED(); } \
  } while (0)
    if (DWARM && cb == 14) WAIT_V(0);
    BAR();
    z1_finish();
    BAR();
    STAMP(1);
#pragma unroll
    for (int i = 0; i < 8; ++i) K2_RD(a0, b0, pa0, 0, 0, i);
    for (int kt = 0; kt < 28; kt += 4) { K2_STEP(0, 0); K2_STEP(1, 0); K2_STEP(2, 0); K2_STEP(3, 0); }
    K2_STEP(0, 0); K2_STEP(1, 0); K2_STEP(2, 0); K2_STEP(3, 1);
    SCHED();
    STAMP(2);
    if (mat < 2) {
      const rsrc_t rY = RSRC(a.ws + WS_Y + (size_t)mat * S * D * 2, S * D * 2);
      const unsigned so = ((8 * tm + 4 * wp) * 64 + 16 * tn + 8 * wq) * 256;
#pragma unroll
      for (int ni = 0; ni < 4; ++ni)
#pragma unroll
        for (int P = 0; P < 2; ++P) {
          const f32x4 c0 = acc[ni][2 * P], c1 = acc[ni][2 * P + 1];
          const u32x4 v = {cvt_pk(c0[0], c0[1]), cvt_pk(c0[2], c0[3]), cvt_pk(c1[0], c1[1]), cvt_pk(c1[2], c1[3])};
          st16_sc1(v, rY, vl, so + (ni * 64 + 4 * P) * 256);
        }
    } else {
      const rsrc_t rH = RSRC(a.ws + WS_H, S * FH * 2);
      const unsigned so = ((8 * tm + 4 * wp) * 112 + 16 * tn + 8 * wq) * 256;
      const unsigned za = P2_Z + wp * 16384 + wq * 2048 + (fq * 16 + ((fr & 4) << 1) + ((fr & 8) >> 1) + (fr & 3)) * 16;
#pragma unroll
      for (int ni = 0; ni < 4; ++ni) {
#pragma unroll
        for (int P = 0; P < 2; ++P) {
          const f32x4 c0 = acc[ni][2 * P], c1 = acc[ni][2 * P + 1];
          const u32x4 z = *(const LAS u32x4*)(size_t)(za + ni * 4096 + P * 1024);
          const u32x4 v = {cvt_pk(bf_lo(z[0]) * c0[0], bf_hi(z[0]) * c0[1]), cvt_pk(bf_lo(z[1]) * c0[2], bf_hi(z[1]) * c0[3]),
                           cvt_pk(bf_lo(z[2]) * c1[0], bf_hi(z[2]) * c1[1]), cvt_pk(bf_lo(z[3]) * c1[2], bf_hi(z[3]) * c1[3])};
          st16_sc1(v, rH, vl, so + (ni * 112 + 4 * P) * 256);
        }
      }
    }
    STAMP(3);
    WAIT_V(0);
  } else {
    STAMP(1);
    for (int i = 0; i < PF_N && blockIdx.x != 255; ++i) {
      warm_window(a, P2_Z, pm, (PF_LIST >> (4 * i)) & 15, wid, lane);
      WAIT_V(6);
    }
    STAMP(2);
    WAIT_V(0);
    STAMP(3);
  }
  WL0();
  BAR();
  STAMP(4);
  {
    const int wr = (wid + tn3) & 7, tp = wr >> 2, kq = wr & 3;
    const int tt3 = 4 * tm3 + 2 * tp + (kq & 1);
    const int cq = 2 * (fq & 1) + (fq >> 1);
    const int c3 = 8 * tn3 + 2 * (kq & 2);
    if (wid == 4 && work2) {
      const unsigned box = mat == 2 ? fr : (fr & 8) + 2 * tn + (fr & 1);
      st4_sc1(1u, rG, box * 128, G_FP + tm * 2048 + (mat == 2 ? tn : 8 + mat) * 4);
    }
    const int q4 = fr >> 2, pp = lane & 3;
    const int sp = ((q4 >> 1) & 1) | ((fq & 1) << 1);
    const unsigned wa = kq * 4096 + 128 * (8 * fq + q4) + 16 * ((2 * (kq & 2) + (pp >> 1)) ^ (sp << 1)) + 8 * (pp & 1);
    bf16x8 wf[7][4];
    auto ld_w = [&]() {
#pragma unroll
      for (int mi = 0; mi < 4; ++mi) {
        unsigned lo = wa ^ (mi * 32), hi = lo + 65536;
        OPAQUE(hi);
#pragma unroll
        for (int g = 0; g < 7; ++g) wf[g][mi] = ld_tr((g < 4 ? lo : hi) + (g & 3) * 16384);
      }
    };
    if (wid == 4) {
      for (unsigned spin = 0; spin < SPIN; ++spin) {
        const unsigned fl = ld4_sc1(rG, fr * 4, G_FP + (tm3 * 8 + tn3) * 128);
        if (__all(fl == 1u || fr == 7 || fr > 9)) break;
      }
    } else {
      ld_w();
    }
    BAR();
    STAMP(5);
    const rsrc_t rH = RSRC(a.ws + WS_H + (size_t)((4 * tm3 + 2 * tp) * 112 + 4 * kq) * 256, 2 * 112 * 256);
    const rsrc_t rY = RSRC(a.ws + WS_Y + (size_t)(tt3 * 64 + c3) * 256, S * D * 2 + 1024);
    const unsigned vh0 = vl + (kq & 1) * (112 * 256), vh1 = vl + ((kq & 1) ^ 1) * (112 * 256);
    u32x4 hf[7][2];
#pragma unroll
    for (int g = 0; g < 7; ++g) {
      hf[g][0] = __builtin_amdgcn_raw_buffer_load_b128(rH, vh0, g * 4096, 16);
      hf[g][1] = __builtin_amdgcn_raw_buffer_load_b128(rH, vh1, g * 4096, 16);
    }
    const unsigned vy = cq * 256 + fr * 16;
    const u32x4 e1 = __builtin_amdgcn_raw_buffer_load_b128(rY, vy, 0, 16), e2 = __builtin_amdgcn_raw_buffer_load_b128(rY, vy, S * D * 2, 16);
    const u32x4 ex = *(const u32x4*)(a.x + (size_t)(tt3 * 16 + fr) * D + (c3 + cq) * 8);
    SCHED();
    if (wid == 4) ld_w();
    f32x4 acc[2][4];
#pragma unroll
    for (int g = 0; g < 7; ++g)
#pragma unroll
      for (int tl = 0; tl < 2; ++tl)
#pragma unroll
        for (int mi = 0; mi < 4; ++mi) acc[tl][mi] = MFMA16(wf[g][mi], __builtin_bit_cast(bf16x8, hf[g][tl]), (g ? acc[tl][mi] : f32x4{0, 0, 0, 0}));
    STAMP(6);
    LAS f32x4* xb = (LAS f32x4*)(size_t)(P3_X) + lane;
#pragma unroll
    for (int d = 1; d < 4; ++d) {
      xb[((wr * 3 + d - 1) * 2) * 64] = acc[d & 1][d & 2];
      xb[((wr * 3 + d - 1) * 2 + 1) * 64] = acc[d & 1][(d & 2) + 1];
    }
    WL0();
    BAR();
    f32x4 s0 = acc[0][0], s1 = acc[0][1];
#pragma unroll
    for (int d = 1; d < 4; ++d) {
      s0 += xb[(((wr ^ d) * 3 + d - 1) * 2) * 64];
      s1 += xb[(((wr ^ d) * 3 + d - 1) * 2 + 1) * 64];
    }
    f32x4 c0, c1;
#pragma unroll
    for (int j = 0; j < 4; ++j) {
      auto w = __builtin_amdgcn_permlane16_swap(__float_as_uint(s0[j]), __float_as_uint(s1[j]), false, false);
      c0[j] = __uint_as_float(w[0]); c1[j] = __uint_as_float(w[1]);
    }
    u32x4 o;
#pragma unroll
    for (int k = 0; k < 4; ++k) {
      const f32x4 c = (k >> 1) ? c1 : c0;
      const float lo = bf_lo(ex[k]) + bf_lo(e1[k]) - bf_lo(e2[k]) + c[(k & 1) * 2];
      const float hi = bf_hi(ex[k]) + bf_hi(e1[k]) - bf_hi(e2[k]) + c[(k & 1) * 2 + 1];
      o[k] = cvt_pk(lo, hi);
    }
    st16_sc1(o, RSRC(a.out, S * D * 2), ((tt3 * 16 + fr) * D + (c3 + cq) * 8) * 2, 0);
  }
  STAMP(7);
  STAMP_OUT(a.ws, 1, 8);
  {
    asm volatile("s_waitcnt vmcnt(0)" ::: "memory");
    const unsigned long long pend = __builtin_amdgcn_s_memrealtime();
    const rsrc_t rS = RSRC(a.ws + (100u << 20), 65536);
    st16_sc1(u32x4{(unsigned)pstart, (unsigned)pend, 0, 0}, rS, 0, (blockIdx.x * 8 + wid) * 16);
    if (blockIdx.x == PBLK && wid == 0) {
      while (__builtin_amdgcn_s_memrealtime() < pstart + 10000) __builtin_amdgcn_s_sleep(1);
      int mns = 1 << 30, mxs = -(1 << 30), mxe = -(1 << 30);
      for (int i = 0; i < 32; ++i) {
        const u32x4 v = ld16_sc1(rS, lane * 16, i * 1024);
        const int st = (int)(v[0] - (unsigned)pstart), en = (int)(v[1] - (unsigned)pstart);
        mns = min(mns, st); mxs = max(mxs, st); mxe = max(mxe, en);
      }
      for (int o = 32; o > 0; o >>= 1) { mns = min(mns, __shfl_xor(mns, o)); mxs = max(mxs, __shfl_xor(mxs, o)); mxe = max(mxe, __shfl_xor(mxe, o)); }
      const int q = __builtin_amdgcn_readfirstlane(SKM == 0 ? mxs - mns : SKM == 1 ? mxe - mns : -mns);
      const unsigned long long tend = pstart + __builtin_amdgcn_readfirstlane(mns) + 20000 + SKEWK * q;
      while (__builtin_amdgcn_s_memrealtime() < tend) ;
    }
  }
}

extern "C" void kernel_launch(void* const* d_in, const int* in_sizes, int n_in, void* d_out, int out_size, void* d_ws, size_t ws_size,
                              hipStream_t stream) {
  (void)in_sizes; (void)n_in; (void)out_size; (void)ws_size;
  (void)hipFuncSetAttribute((const void*)fused, hipFuncAttributeMaxDynamicSharedMemorySize, LDS_BYTES);
  Args a{(const u16*)d_in[0], (const u16*)d_in[3], (const u16*)d_in[4], (const u16*)d_in[6], (const u16*)d_in[9],
         (const u16*)d_in[1], (const u16*)d_in[2], (const u16*)d_in[7], (const u16*)d_in[8], (char*)d_ws, (u16*)d_out};
  fused<<<256, 512, LDS_BYTES, stream>>>(a);
}
```

```cpp
#include <hip/hip_runtime.h>
#include <hip/hip_bf16.h>
#include <stdint.h>

typedef unsigned short u16;
typedef short bf16x8 __attribute__((ext_vector_type(8)));
typedef short s16x4 __attribute__((ext_vector_type(4)));
typedef float f32x4 __attribute__((ext_vector_type(4)));
typedef float f32x2 __attribute__((ext_vector_type(2)));
typedef unsigned u32x4 __attribute__((ext_vector_type(4)));
typedef __bf16 bf16x2v __attribute__((ext_vector_type(2)));
typedef __amdgpu_buffer_rsrc_t rsrc_t;
#define LAS __attribute__((address_space(3)))
#define WAIT_V(n) asm volatile("s_waitcnt vmcnt(%0)" ::"n"(n) : "memory")
#define WL0() __builtin_amdgcn_s_waitcnt(0xC07F)
#define BAR() do { asm volatile("" ::: "memory"); __builtin_amdgcn_s_barrier(); asm volatile("" ::: "memory"); } while (0)
#define SCHED() __builtin_amdgcn_sched_barrier(0)
#define MFMA16(a, b, c) __builtin_amdgcn_mfma_f32_16x16x32_bf16(a, b, c, 0, 0, 0)
#define RSRC(p, n) __builtin_amdgcn_make_buffer_rsrc((void*)(p), 0, (unsigned)(n), 0x00020000)
#define OPAQUE(x) asm volatile("" : "+v"(x))

#define STAMP_DECL
#define STAMP(i)
#define STAMP_OUT(ws, kid, n)
#define K2_KXMASK 3
#define K1_KXMASK 3
#define DWARM 0
#define CODEWARM 5
#define NOPARK 0
#define PF_LIST 0x753810u
#define PF_N 6

constexpr int S = 2048, D = 512, F = 1792, FH = 896;
constexpr size_t WS_Q = 0;
constexpr size_t WS_Z1 = WS_Q + (size_t)(2 * D + FH) * S * 2;
constexpr size_t WS_H = WS_Z1 + (size_t)FH * S * 2;
constexpr size_t WS_Y = WS_H + (size_t)FH * S * 2;
constexpr size_t WS_G = WS_Y + (size_t)2 * D * S * 2;
constexpr unsigned G_F1 = 0;
constexpr unsigned G_FP = 32768;
constexpr unsigned G_FZ = 32768 + 64;
constexpr unsigned G_PAY = 65536;
constexpr unsigned G_ALL = G_PAY + 16 * 14336;
constexpr unsigned G_REQ = G_ALL + 1024;
constexpr unsigned G_ANS = G_REQ + 1024;
constexpr unsigned G_END = G_ANS + 1024;

constexpr int LDS_BYTES = 163840;
constexpr int P1_RING = 32768, P1_SCR = 81920;
constexpr int P2_PB = 65536, P2_Z = 131072, P3_X = 114688;
constexpr unsigned SPIN = 1u << 20;

struct Args {
  const u16* x; const u16* wdr; const u16* wdi; const u16* win; const u16* wout;
  const u16* wsr; const u16* wsi; const u16* wsp; const u16* bsp;
  char* ws; u16* out;
};

__device__ __forceinline__ unsigned cvt_pk(float lo, float hi) {
  f32x2 v = {lo, hi};
  return __builtin_bit_cast(unsigned, __builtin_convertvector(v, bf16x2v));
}
__device__ __forceinline__ float bf_lo(unsigned u) { return __uint_as_float(u << 16); }
__device__ __forceinline__ float bf_hi(unsigned u) { return __uint_as_float(u & 0xffff0000u); }
constexpr float GC1 = -2.0f * 0.7978845608028654f * 1.4426950408889634f, GC2 = GC1 * 0.044715f;
__device__ __forceinline__ f32x2 gelu2(f32x2 v) {
  const f32x2 t = v * ((v * v) * GC2 + GC1);
  const f32x2 e = f32x2{__builtin_amdgcn_exp2f(t[0]), __builtin_amdgcn_exp2f(t[1])} + 1.0f;
  return v * f32x2{__builtin_amdgcn_rcpf(e[0]), __builtin_amdgcn_rcpf(e[1])};
}
__device__ __forceinline__ void dot_sq(float& c, unsigned a) { asm volatile("v_dot2c_f32_bf16 %0, %1, %1" : "+v"(c) : "v"(a)); }
__device__ __forceinline__ void dot_sum(float& c, unsigned a) { asm volatile("v_dot2c_f32_bf16 %0, 0x3f803f80, %1" : "+v"(c) : "v"(a)); }
__device__ __forceinline__ float sum_rows(float v) {
  auto a = __builtin_amdgcn_permlane32_swap(__float_as_uint(v), __float_as_uint(v), false, false);
  v = __uint_as_float(a[0]) + __uint_as_float(a[1]);
  auto b = __builtin_amdgcn_permlane16_swap(__float_as_uint(v), __float_as_uint(v), false, false);
  return __uint_as_float(b[0]) + __uint_as_float(b[1]);
}
__device__ __forceinline__ float sel4(f32x4 v, int i) { const float a = (i & 1) ? v[1] : v[0], b = (i & 1) ? v[3] : v[2]; return (i & 2) ? b : a; }

__device__ __forceinline__ void dma16(rsrc_t r, unsigned mbase, int mimm, unsigned voff, unsigned soff) {
  asm volatile("s_add_i32 m0, %0, %1\n\ts_nop 0\n\tbuffer_load_dwordx4 %2, %3, %4 offen lds" ::"s"(mbase), "n"(mimm), "v"(voff), "s"(r), "s"(soff) : "scc");
}
__device__ __forceinline__ void dma16_sc1(rsrc_t r, unsigned mbase, int mimm, unsigned voff, unsigned soff) {
  asm volatile("s_add_i32 m0, %0, %1\n\ts_nop 0\n\tbuffer_load_dwordx4 %2, %3, %4 offen sc1 lds" ::"s"(mbase), "n"(mimm), "v"(voff), "s"(r), "s"(soff) : "scc");
}
__device__ __forceinline__ void dma4_sc1(rsrc_t r, int ldsaddr, unsigned voff, unsigned soff) {
  asm volatile("s_mov_b32 m0, %0\n\ts_nop 0\n\tbuffer_load_dword %1, %2, %3 offen sc1 lds" ::"n"(ldsaddr), "v"(voff), "s"(r), "s"(soff) : "memory");
}
__device__ __forceinline__ void touch(rsrc_t r, int junk, unsigned voff, unsigned soff) {
  asm volatile("s_mov_b32 m0, %0\n\ts_nop 0\n\tbuffer_load_dword %1, %2, %3 offen lds" ::"n"(junk), "v"(voff), "s"(r), "s"(soff) : "memory");
}
__device__ __forceinline__ void st16_sc1(u32x4 v, rsrc_t r, unsigned voff, unsigned soff) {
  asm volatile("buffer_store_dwordx4 %0, %1, %2, %3 offen sc1\n\ts_nop 1" ::"v"(v), "v"(voff), "s"(r), "s"(soff) : "memory");
}
__device__ __forceinline__ void st4_sc1(unsigned v, rsrc_t r, unsigned voff, unsigned soff) {
  asm volatile("buffer_store_dword %0, %1, %2, %3 offen sc1" ::"v"(v), "v"(voff), "s"(r), "s"(soff) : "memory");
}
__device__ __forceinline__ unsigned ld4_sc1(rsrc_t r, unsigned voff, unsigned soff) {
  unsigned v;
  asm volatile("buffer_load_dword %0, %1, %2, %3 offen sc1\n\ts_waitcnt vmcnt(0)" : "=&v"(v) : "v"(voff), "s"(r), "s"(soff) : "memory");
  return v;
}
__device__ __forceinline__ u32x4 ld16_sc1(rsrc_t r, unsigned voff, unsigned soff) {
  u32x4 v;
  asm volatile("buffer_load_dwordx4 %0, %1, %2, %3 offen sc1\n\ts_waitcnt vmcnt(0)" : "=&v"(v) : "v"(voff), "s"(r), "s"(soff) : "memory");
  return v;
}
__device__ __forceinline__ void wait_vn(int n) { WAIT_V(n); }
__device__ __forceinline__ bf16x8 ld_tr(unsigned addr) {
  s16x4 lo = __builtin_amdgcn_ds_read_tr16_b64_v4i16((LAS s16x4*)(size_t)(addr));
  s16x4 hi = __builtin_amdgcn_ds_read_tr16_b64_v4i16((LAS s16x4*)(size_t)(addr + 512));
  return __builtin_shufflevector(lo, hi, 0, 1, 2, 3, 4, 5, 6, 7);
}
__device__ __forceinline__ bf16x8 ld_frag(unsigned addr) { return *(const LAS bf16x8*)(size_t)(addr); }
__device__ __forceinline__ void wait_go(rsrc_t rG, unsigned nonce, unsigned seen) {
  for (unsigned spin = 0; seen != nonce && spin < SPIN; ++spin) {
    seen = __builtin_amdgcn_readfirstlane(ld4_sc1(rG, 0, G_ANS + blockIdx.x * 4));
    __builtin_amdgcn_s_sleep(1);
  }
}

template <bool ZB>
__device__ __forceinline__ void phase1(const Args& a, int pm, int cb, int kx, const u16* Pp, int kx2, f32x4 (&z1v)[2][2]) {
  const int tid = threadIdx.x, lane = tid & 63;
  const int wid = __builtin_amdgcn_readfirstlane(tid >> 6);
  const int wm = wid & 3, wn = wid >> 2;
  STAMP_DECL STAMP(0);
  const rsrc_t rX = RSRC(a.x + (size_t)pm * 128 * D, 128 * D * 2);
  const rsrc_t rW = ZB ? RSRC(a.win + 64 * cb, (D * F - 64 * cb) * 2) : RSRC(a.wdi + (size_t)384 * D, 128 * D * 2);
  const int r8 = lane >> 3, p8 = lane & 7;
  const unsigned sw_e = (p8 ^ (r8 >> 1)) << 4;
  const unsigned vx_e = (32 * (wid >> 1) + 4 * (wid & 1) + 8 * (r8 >> 2) + (r8 & 3)) * (D * 2) + sw_e;
  const unsigned vx_o = (vx_e ^ 64) + 16 * (D * 2);
  const unsigned vw2 = ZB ? (8 * wid + r8) * (F * 2) + ((p8 ^ ((((r8 >> 1) & 1) | ((wid & 1) << 1)) << 1)) << 4)
                          : (64 + 8 * wid + r8) * (D * 2) + (sw_e ^ ((wid & 1) << 6));
  const unsigned vw1 = ZB ? vw2 + FH * 2 : vw2 - 64 * (D * 2);
  constexpr unsigned WKB = ZB ? 64 * F * 2 : 128;
  const unsigned mb1 = wid * 1024, mb2 = wid * 2048;
  auto piece1 = [&](int st, int p) {
    const unsigned kb = st ^ kx;
    const int base = P1_RING + (st & 3) * 32768;
    if (p == 0) dma16(rX, mb2, base, vx_e, kb * 128);
    else if (p == 1) dma16(rW, mb1, base + 16384, vw1, kb * WKB);
    else if (p == 2) dma16(rX, mb2, base + 1024, vx_o, kb * 128);
    else dma16(rW, mb1, base + 24576, vw2, kb * WKB);
  };
  piece1(0, 0); piece1(0, 1); piece1(0, 2); piece1(0, 3);
  SCHED();

  const int fr = lane & 15, fq = lane >> 4;
  const int kh = wm >> 1;
  const unsigned fsw = ((fq ^ ((fr >> 1) & 7)) << 4) ^ (kh << 6);
  unsigned xa[2][2][2], wb[2][2][2];
#pragma unroll
  for (int m = 0; m < 2; ++m) { xa[0][m][0] = P1_RING + (32 * wm + 16 * (m ^ wn) + fr) * 128 + fsw; xa[0][m][1] = xa[0][m][0] ^ 64; }
#pragma unroll
  for (int n = 0; n < 2; ++n) {
    const int tile = 2 * wn + (n ^ (wm & 1));
    if (ZB) {
      const int q4 = fr >> 2, pp = lane & 3;
      const int sp = ((q4 >> 1) & 1) | ((fq & 1) << 1);
      const int ch = (2 * tile + (pp >> 1)) ^ (sp << 1);
      wb[0][n][0] = P1_RING + 16384 + 128 * (8 * fq + q4) + 16 * ch + 8 * (pp & 1) + kh * 4096;
      wb[0][n][1] = wb[0][n][0] ^ 4096;
    } else {
      wb[0][n][0] = P1_RING + 16384 + (16 * tile + fr) * 128 + fsw;
      wb[0][n][1] = wb[0][n][0] ^ 64;
    }
  }
#pragma unroll
  for (int i = 0; i < 2; ++i)
#pragma unroll
    for (int ks = 0; ks < 2; ++ks) {
      xa[1][i][ks] = xa[0][i][ks] + 65536; wb[1][i][ks] = wb[0][i][ks] + 65536;
      OPAQUE(xa[0][i][ks]); OPAQUE(wb[0][i][ks]); OPAQUE(xa[1][i][ks]); OPAQUE(wb[1][i][ks]);
    }
  auto ldw = [&](unsigned addr) -> bf16x8 { return ZB ? ld_tr(addr) : ld_frag(addr); };
  SCHED();
  piece1(1, 0); piece1(1, 1); piece1(1, 2); piece1(1, 3);
  const rsrc_t rG = RSRC(a.ws + WS_G, G_END);
  if (wid == 7) dma4_sc1(rG, 0, 0, G_ANS + blockIdx.x * 4);
  unsigned nonce = 0;
  STAMP(1);

  bf16x8 xf[16][2], wf[2][4];
  const f32x4 Z4 = {0, 0, 0, 0};
  f32x4 acc1[2][2] = {{Z4, Z4}, {Z4, Z4}}, acc2[2][2] = {{Z4, Z4}, {Z4, Z4}};
  f32x4 gram = Z4, rsum = Z4, cs1 = Z4, cs2 = Z4;
  const bf16x8 ONES = {0x3f80, 0x3f80, 0x3f80, 0x3f80, 0x3f80, 0x3f80, 0x3f80, 0x3f80};

  auto mm = [&](int kk, int i) {
    const int ks = kk & 1;
    if (i == 0) gram = MFMA16(xf[kk][0], xf[kk][0], gram);
    else if (i == 1) rsum = MFMA16(xf[kk][0], ONES, rsum);
    else if (i < 6) { const int m = (i - 2) >> 1, n = i & 1; acc1[m][n] = MFMA16(xf[kk][m], wf[ks][n], acc1[m][n]); }
    else if (i < 10) { const int m = (i - 6) >> 1, n = i & 1; acc2[m][n] = ZB ? MFMA16(wf[ks][2 + n], xf[kk][m], acc2[m][n]) : MFMA16(xf[kk][m], wf[ks][2 + n], acc2[m][n]); }
    else if (i == 10) cs1 = MFMA16(ONES, wf[0][0], cs1);
    else cs2 = MFMA16(ONES, wf[0][2], cs2);
  };
  auto rd = [&](int kk, int i) {
    const int st = kk >> 1, ks = kk & 1, wi = (st >> 1) & 1, so = (st & 1) * 32768;
    if (i == 0 || i == 3) xf[kk][i & 1] = ld_frag(xa[wi][i & 1][ks] + so);
    else if (i < 3) wf[ks][i - 1] = ldw(wb[wi][i - 1][ks] + so);
    else wf[ks][i - 2] = ldw(wb[wi][i - 4][ks] + so + 8192);
  };
  auto piece3 = [&](int p) {
    const rsrc_t rD = RSRC((cb < 8 ? a.wdr : a.wdi) + (size_t)(cb & 7) * 64 * D, 64 * D * 2);
    const unsigned ve = r8 * (D * 2) + sw_e, vo = (ve ^ 64) + 8 * (D * 2);
    dma16(rD, wid * 8192, p * 1024, (p & 1) ? vo : ve, (wid ^ kx) * 128 + (p >> 1) * 16 * (D * 2));
  };
  auto earlyP = [&](int k) {
    const rsrc_t rP = RSRC(Pp, 128 * S * 2);
    const unsigned ve = (16 * wid + r8) * (S * 2) + sw_e, vo = (ve ^ 64) + 8 * (S * 2);
    dma16(rP, mb2, P2_PB + (k + 2) * 16384, ve, (k ^ kx2) * 128);
    dma16(rP, mb2, P2_PB + (k + 2) * 16384 + 1024, vo, (k ^ kx2) * 128);
  };
#pragma unroll
  for (int st = 0; st < 8; ++st) {
    wait_vn(st <= 6 ? 4 : ZB ? 8 : 0);
    WL0();
    BAR();
    if (st == 0) STAMP(2);
    SCHED();
#pragma unroll
    for (int i = 0; i < 10; ++i) {
      if (st > 0) mm(2 * st - 1, i);
      SCHED();
      if (i < 6) rd(2 * st, i);
      if (st + 2 < 8) { if (i == 3) piece1(st + 2, 0); if (i == 8) piece1(st + 2, 1); }
      if (ZB && st == 6 && (i % 3) == 0) piece3(i / 3);
      if (st == 7 && i == 7) earlyP(0);
      SCHED();
    }
    WL0();
    SCHED();
#pragma unroll
    for (int i = 0; i < 12; ++i) {
      mm(2 * st, i);
      SCHED();
      if (i < 6) rd(2 * st + 1, i);
      if (st + 2 < 8) { if (i == 3) piece1(st + 2, 2); if (i == 9) piece1(st + 2, 3); }
      if (ZB && st == 6 && (i % 3) == 2) piece3(4 + i / 3);
      if (st == 7 && i == 8) earlyP(1);
      SCHED();
    }
    if (st == 2 && wid == 7) {
      nonce = __builtin_amdgcn_readfirstlane(*(LAS unsigned*)(size_t)(lane * 4)) + 1;
      st4_sc1(nonce, rG, 0, G_REQ + blockIdx.x * 4);
    }
    if (st == 6 && wid == 7) dma4_sc1(rG, P1_SCR + 7168, 0, G_ANS + blockIdx.x * 4);
    SCHED();
  }
  LAS float* const s_r = (LAS float*)(size_t)(P1_SCR);
  LAS float* const s_c = (LAS float*)(size_t)(P1_SCR + 512);
  LAS float* const s_u1 = (LAS float*)(size_t)(P1_SCR + 1024);
  LAS float* const s_u2 = (LAS float*)(size_t)(P1_SCR + 1536);
  LAS float* const s_z = (LAS float*)(size_t)(P1_SCR + 2048);
  LAS float* const s_n = (LAS float*)(size_t)(P1_SCR + 4096);
  LAS float* const s_u3 = (LAS float*)(size_t)(P1_SCR + 5120);
  const unsigned tb0 = fr * 128 + fsw, tb1 = tb0 ^ 64;
  WAIT_V(4);
  {
    const unsigned cb0 = tb0 + wid * 8192, cb1 = cb0 ^ 64;
    bf16x8 c3[8];
    f32x4 cs3[4];
    if (ZB) {
#pragma unroll
      for (int t = 0; t < 4; ++t) { c3[2 * t] = ld_frag(cb0 + t * 2048); c3[2 * t + 1] = ld_frag(cb1 + t * 2048); }
    }
    mm(15, 0); mm(15, 1);
    SCHED();
    if (ZB) {
#pragma unroll
      for (int t = 0; t < 4; ++t) cs3[t] = MFMA16(ONES, c3[2 * t], Z4);
#pragma unroll
      for (int t = 0; t < 4; ++t) cs3[t] = MFMA16(ONES, c3[2 * t + 1], cs3[t]);
    }
    SCHED();
#pragma unroll
    for (int i = 2; i < 10; ++i) mm(15, i);
    STAMP(3);
    unsigned i01 = 32 * wm + 16 * wn, i23 = 64 * kh + 16 * (2 * wn + (wm & 1));
    asm volatile("" : "+s"(i01), "+s"(i23));
    const float s1 = sel4(rsum, fr & 3), s2 = sel4(gram, fr & 3);
    const float mean = s1 * (1.0f / D);
    const float rstd = __builtin_amdgcn_rsqf(fmaxf(s2 * (1.0f / D) - mean * mean, 0.0f) + 1e-5f);
    if ((fr >> 2) == fq) { s_r[i01 + fr] = rstd; s_c[i01 + fr] = -mean * rstd; }
    if (fq < 2) s_u1[128 * fq + i23 + fr] = fq ? cs2[0] : cs1[0];
    if (ZB) s_u3[lane * 8 + wid] = sel4(f32x4{cs3[0][0], cs3[1][0], cs3[2][0], cs3[3][0]}, fq);
  }
  const int s0 = pm * 128 + 32 * wm;
  const rsrc_t rT = RSRC(a.ws + WS_Q, (2 * D + FH) * S * 2);
  const unsigned vT = ((fr >> 2) & 1) * 65536 + fq * 256 + (4 * (fr >> 3) + (fr & 3)) * 16;
  WL0();
  BAR();
  STAMP(4);
  earlyP(2); earlyP(3);
  f32x4 rq[2], cq[2];
  auto ld_rc = [&]() {
#pragma unroll
    for (int m = 0; m < 2; ++m) {
      rq[m] = *(LAS f32x4*)(s_r + 32 * wm + 16 * (m ^ wn) + 4 * fq);
      cq[m] = *(LAS f32x4*)(s_c + 32 * wm + 16 * (m ^ wn) + 4 * fq);
    }
  };
  ld_rc();
  auto store_t = [&](int T, f32x4 v0, f32x4 v1) {
    const unsigned a0 = cvt_pk(v0[0], v0[1]), a1 = cvt_pk(v0[2], v0[3]), b0 = cvt_pk(v1[0], v1[1]), b1 = cvt_pk(v1[2], v1[3]);
    const u32x4 o = wn ? u32x4{b0, b1, a0, a1} : u32x4{a0, a1, b0, b1};
    st16_sc1(o, rT, vT, (T >> 1) * 131072 + (T & 1) * 128 + s0 * 32);
  };
  auto lin_store = [&](f32x4 c0, f32x4 c1, float u, int T) { store_t(T, rq[0] * c0 + cq[0] * u, rq[1] * c1 + cq[1] * u); };
  float u1[2];
#pragma unroll
  for (int n = 0; n < 2; ++n) { const int ui = 16 * (2 * wn + (n ^ (wm & 1))) + fr; u1[n] = s_u1[ui] + s_u1[64 + ui]; }
  if (!ZB) {
#pragma unroll
    for (int n = 0; n < 2; ++n) {
      const int tile = 2 * wn + (n ^ (wm & 1)), ui = 16 * tile + fr;
      lin_store(acc1[0][n], acc1[1][n], u1[n], 56 + tile);
      lin_store(acc2[0][n], acc2[1][n], s_u2[ui] + s_u2[64 + ui], 60 + tile);
    }
    if (wid == 7) wait_go(rG, nonce, __builtin_amdgcn_readfirstlane(*(LAS unsigned*)(size_t)(P1_SCR + 7168 + lane * 4)));
    STAMP(5); STAMP(6); STAMP(7); STAMP(8); STAMP(9);
    STAMP_OUT(a.ws, 0, 10);
    return;
  }
  {
    f32x4 ps[2], pq[2];
#pragma unroll
    for (int n = 0; n < 2; ++n)
#pragma unroll
      for (int m = 0; m < 2; ++m) {
        f32x4 v = rq[m] * acc1[m][n] + cq[m] * u1[n];
        const f32x2 g0 = gelu2(f32x2{v[0], v[1]}), g1 = gelu2(f32x2{v[2], v[3]});
        v = f32x4{g0[0], g0[1], g1[0], g1[1]};
        acc1[m][n] = v;
        if (n == 0) { ps[m] = v; pq[m] = v * v; } else { ps[m] += v; pq[m] += v * v; }
      }
    float t[8], h[4];
#define DPP2(d, x, y, ctl, m0, m1) "v_add_f32_dpp " d ", " x ", " x " " ctl " row_mask:0xf bank_mask:" m0 "\n\tv_add_f32_dpp " d ", " y ", " y " " ctl " row_mask:0xf bank_mask:" m1 "\n\t"
#define DPPQ(d, ctl) "v_add_f32_dpp " d ", " d ", " d " " ctl " row_mask:0xf bank_mask:0xf\n\t"
    asm volatile("s_nop 1\n\t" DPP2("%0", "%8", "%16", "row_ror:8", "0x3", "0xc") DPP2("%1", "%9", "%17", "row_ror:8", "0x3", "0xc")
                 DPP2("%2", "%10", "%18", "row_ror:8", "0x3", "0xc") DPP2("%3", "%11", "%19", "row_ror:8", "0x3", "0xc")
                 DPP2("%4", "%12", "%20", "row_ror:8", "0x3", "0xc") DPP2("%5", "%13", "%21", "row_ror:8", "0x3", "0xc")
                 DPP2("%6", "%14", "%22", "row_ror:8", "0x3", "0xc") DPP2("%7", "%15", "%23", "row_ror:8", "0x3", "0xc") "s_nop 1"
                 : "=&v"(t[0]), "=&v"(t[1]), "=&v"(t[2]), "=&v"(t[3]), "=&v"(t[4]), "=&v"(t[5]), "=&v"(t[6]), "=&v"(t[7])
                 : "v"(ps[0][0]), "v"(pq[0][0]), "v"(ps[0][1]), "v"(pq[0][1]), "v"(ps[0][2]), "v"(pq[0][2]), "v"(ps[0][3]), "v"(pq[0][3]),
                   "v"(ps[1][0]), "v"(pq[1][0]), "v"(ps[1][1]), "v"(pq[1][1]), "v"(ps[1][2]), "v"(pq[1][2]), "v"(ps[1][3]), "v"(pq[1][3]));
    asm volatile("s_nop 1\n\t" DPP2("%0", "%4", "%8", "row_half_mirror", "0x5", "0xa") DPP2("%1", "%5", "%9", "row_half_mirror", "0x5", "0xa")
                 DPP2("%2", "%6", "%10", "row_half_mirror", "0x5", "0xa") DPP2("%3", "%7", "%11", "row_half_mirror", "0x5", "0xa") "s_nop 1\n\t"
                 DPPQ("%0", "quad_perm:[1,0,3,2]") DPPQ("%1", "quad_perm:[1,0,3,2]") DPPQ("%2", "quad_perm:[1,0,3,2]") DPPQ("%3", "quad_perm:[1,0,3,2]")
                 DPPQ("%0", "quad_perm:[2,3,0,1]") DPPQ("%1", "quad_perm:[2,3,0,1]") DPPQ("%2", "quad_perm:[2,3,0,1]") DPPQ("%3", "quad_perm:[2,3,0,1]") "s_nop 0"
                 : "=&v"(h[0]), "=&v"(h[1]), "=&v"(h[2]), "=&v"(h[3])
                 : "v"(t[0]), "v"(t[1]), "v"(t[2]), "v"(t[3]), "v"(t[4]), "v"(t[5]), "v"(t[6]), "v"(t[7]));
    if ((fr & 3) == 0) *(LAS f32x4*)(s_z + (wn * 128 + 32 * wm + 8 * fq + 4 * ((fr >> 3) ^ wn) + 2 * ((fr >> 2) & 1)) * 2) = f32x4{h[0], h[1], h[2], h[3]};
  }
  WL0();
  BAR();
  STAMP(5);
  if (wid == 7) {
    __builtin_amdgcn_s_setprio(2);
    const f32x4 p = *(LAS f32x4*)(s_z + lane * 4) + *(LAS f32x4*)(s_z + 256 + lane * 4);
    wait_go(rG, nonce, __builtin_amdgcn_readfirstlane(*(LAS unsigned*)(size_t)(P1_SCR + 7168 + lane * 4)));
    st16_sc1(__builtin_bit_cast(u32x4, p) | 1u, rG, lane * 16, G_PAY + (pm * 14 + cb) * 1024);
  }
  STAMP(6);
  {
    float u3[2];
    f32x4 acct[2][2] = {{Z4, Z4}, {Z4, Z4}};
    const unsigned t30 = tb0 + wn * 4096, t31 = tb1 + wn * 4096;
    bf16x8 w[8];
    auto rd3 = [&](int f) { const int kk = f >> 1; w[f & 7] = ld_frag(((kk & 1) ? t31 : t30) + (kk >> 1) * 8192 + (f & 1) * 2048); };
#pragma unroll
    for (int f = 0; f < 4; ++f) rd3(f);
    SCHED();
#pragma unroll
    for (int f = 0; f < 32; ++f) {
      const int kk = f >> 1, n = f & 1;
      acct[0][n] = MFMA16(xf[kk][0], w[f & 7], acct[0][n]);
      SCHED();
      if (f + 4 < 32) rd3(f + 4);
      if (f == 8 || f == 12) {
        const int m = (f - 8) >> 2;
        const float r = s_r[32 * wm + 16 * (m ^ wn) + fr], c = s_c[32 * wm + 16 * (m ^ wn) + fr];
#pragma unroll
        for (int q = 0; q < 2; ++q) {
          const int ui = 16 * (2 * wn + (q ^ (wm & 1))) + 4 * fq;
          z1v[m][q] = acc2[m][q] * r + (*(LAS f32x4*)(s_u2 + ui) + *(LAS f32x4*)(s_u2 + 64 + ui)) * c;
        }
      }
      if (f == 20 || f == 24) {
        const int q = (f - 20) >> 2;
        const f32x4 uu = *(LAS f32x4*)(s_u3 + (32 * wn + 16 * q + fr) * 8) + *(LAS f32x4*)(s_u3 + (32 * wn + 16 * q + fr) * 8 + 4);
        u3[q] = (uu[0] + uu[1]) + (uu[2] + uu[3]);
      }
      SCHED();
      acct[1][n] = MFMA16(xf[kk][1], w[f & 7], acct[1][n]);
      SCHED();
    }
    if (wid != 7) {
#pragma unroll
      for (int n = 0; n < 2; ++n) lin_store(acct[0][n], acct[1][n], u3[n], 4 * cb + 2 * wn + n);
    }
    STAMP(7);
  if (wid == 7) {
    f32x4 t;
    for (unsigned spin = 0; spin < SPIN; ++spin) {
      u32x4 pv[14];
#pragma unroll
      for (int i = 0; i < 14; ++i) pv[i] = __builtin_amdgcn_raw_buffer_load_b128(rG, lane * 16 + (i & 3) * 1024, G_PAY + pm * 14336 + (i >> 2) * 4096, 16);
      if (spin == 0) {
#pragma unroll
        for (int n = 0; n < 2; ++n) lin_store(acct[0][n], acct[1][n], u3[n], 4 * cb + 2 * wn + n);
      }
      u32x4 al = pv[0];
      t = __builtin_bit_cast(f32x4, pv[0]);
#pragma unroll
      for (int i = 1; i < 14; ++i) { al &= pv[i]; t += __builtin_bit_cast(f32x4, pv[i]); }
      if (__all((al[0] & al[1] & al[2] & al[3] & 1u) != 0)) break;
      asm volatile("" ::: "memory");
    }
    const float m0 = t[0] * (1.0f / FH), m1 = t[2] * (1.0f / FH);
    const float r0 = __builtin_amdgcn_rsqf(fmaxf(t[1] * (1.0f / FH) - m0 * m0, 0.0f) + 1e-5f);
    const float r1 = __builtin_amdgcn_rsqf(fmaxf(t[3] * (1.0f / FH) - m1 * m1, 0.0f) + 1e-5f);
    *(LAS f32x2*)(s_n + 2 * lane) = f32x2{r0, r1};
    *(LAS f32x2*)(s_n + 128 + 2 * lane) = f32x2{-m0 * r0, -m1 * r1};
    __builtin_amdgcn_s_setprio(0);
  }
  }
  WL0();
  BAR();
  STAMP(8);
  {
    f32x4 r2[2], c2[2];
#pragma unroll
    for (int m = 0; m < 2; ++m) {
      r2[m] = *(LAS f32x4*)(s_n + 32 * wm + 8 * fq + 4 * (m ^ wn));
      c2[m] = *(LAS f32x4*)(s_n + 128 + 32 * wm + 8 * fq + 4 * (m ^ wn));
    }
#pragma unroll
    for (int n = 0; n < 2; ++n) store_t(64 + 4 * cb + 2 * wn + (n ^ (wm & 1)), acc1[0][n] * r2[0] + c2[0], acc1[1][n] * r2[1] + c2[1]);
  }
  STAMP(9);
  STAMP_OUT(a.ws, 0, 10);
}

__device__ __forceinline__ void warm_window(const Args& a, int junk, int rb, int w, int wv, int lane) {
  if (w < 8) {
    const unsigned vo = (rb * 128 + wv * 32 + (lane >> 2)) * (S * 2) + (lane & 3) * 128;
#pragma unroll
    for (int m = 0; m < 3; ++m) {
      const rsrc_t r = RSRC(m == 0 ? a.wsr : m == 1 ? a.wsi : a.wsp, S * S * 2);
      touch(r, junk, vo, w * 512);
      touch(r, junk, vo, w * 512 + 16 * (S * 2));
    }
  } else {
    const rsrc_t r = RSRC(a.wout + (size_t)rb * 56 * D, 56 * D * 2);
    touch(r, junk, lane * 128, wv * 16384);
    touch(r, junk, lane * 128, wv * 16384 + 8192);
  }
}

__global__ __launch_bounds__(512, 2) void fused(Args a) {
  extern __shared__ __attribute__((aligned(1024))) char smem[];
  asm volatile("" ::"v"(smem));
  const unsigned long long pstart = __builtin_amdgcn_s_memrealtime();
  const int bx = blockIdx.x & 7, bi = blockIdx.x >> 3;
  if (bi < CODEWARM && threadIdx.x < 64) {
    unsigned long long pc;
    asm volatile("s_getpc_b64 %0" : "=s"(pc));
    touch(RSRC(pc & ~127ull, CODEWARM * 8192), 1024, threadIdx.x * 128, bi * 8192);
  }
  const int pm = 4 * (bx >> 1) + (bi >> 3), cb = 2 * (bi & 7) + (bx & 1);
  int mat, tm, tn;
  if (bx & 1) { mat = 2; tm = pm; tn = bi & 7; }
  else { mat = bx >> 2; tm = ((bx >> 1) & 1) * 8 + (bi >> 2); tn = bi & 3; }
  const int kx = (tm ^ tn) & K2_KXMASK;
  const u16* Pp = (mat == 0 ? a.wsr : mat == 1 ? a.wsi : a.wsp) + (size_t)tm * 128 * S;
  f32x4 z1v[2][2];
  {
    const int kx1 = ((bi & 7) ^ (2 * (bi >> 3))) & K1_KXMASK;
    if (cb < 14) phase1<true>(a, pm, cb, kx1, Pp, kx, z1v);
    else if (cb == 14) phase1<false>(a, pm, cb, kx1, Pp, kx, z1v);
  }
  int tid = threadIdx.x;
  asm volatile("" : "+v"(tid));
  const int lane = tid & 63;
  const int wid = __builtin_amdgcn_readfirstlane(tid >> 6);
  STAMP_DECL STAMP(0);
  const int tm3 = bx * 4 + (bi >> 3), tn3 = bi & 7;
  const int fr = lane & 15, fq = lane >> 4;
  const unsigned vl = lane * 16;
  const rsrc_t rG = RSRC(a.ws + WS_G, G_END);
  const bool work2 = cb != 15;
  auto z1_finish = [&]() {
    if (cb < 14) {
      const int wm = wid & 3, wn = wid >> 2;
      const rsrc_t rZ = RSRC(a.ws + WS_Z1, FH * S * 2);
      const unsigned vZ = (fr >> 3) * (112 * 256) + (2 * ((fq & 1) ^ (wm & 1)) + (fq >> 1)) * 256 + (fr & 7) * 16;
      const unsigned sZ = ((pm * 8 + 2 * wm) * 112 + 8 * cb + 4 * wn) * 256;
#pragma unroll
      for (int m = 0; m < 2; ++m) {
        unsigned pk[2][2];
#pragma unroll
        for (int n = 0; n < 2; ++n) {
          const f32x2 g0 = gelu2(f32x2{z1v[m][n][0], z1v[m][n][1]}), g1 = gelu2(f32x2{z1v[m][n][2], z1v[m][n][3]});
          pk[n][0] = cvt_pk(g0[0], g0[1]); pk[n][1] = cvt_pk(g1[0], g1[1]);
        }
        auto w0 = __builtin_amdgcn_permlane16_swap(pk[0][0], pk[1][0], false, false);
        auto w1 = __builtin_amdgcn_permlane16_swap(pk[0][1], pk[1][1], false, false);
        st16_sc1(u32x4{w0[0], w1[0], w0[1], w1[1]}, rZ, vZ, sZ + 128 * (m ^ wn));
      }
    }
  };
  if (blockIdx.x == 255) {
    for (int i = 0; i < 36; ++i) st16_sc1(u32x4{0, 0, 0, 0}, rG, vl, (wid * 36 + i) * 1024);
    st16_sc1(u32x4{0, 0, 0, 0}, rG, vl, G_ALL);
  }
  unsigned bv[4];
#pragma unroll
  for (int ni = 0; ni < 4; ++ni) bv[ni] = a.bsp[(8 * tm + 4 * ((wid >> 1) & 1) + ni) * 16 + fr];
  asm volatile("" : "+v"(bv[0]), "+v"(bv[1]), "+v"(bv[2]), "+v"(bv[3]));
  if (work2 || blockIdx.x == 255) {
    WAIT_V(0);
    WL0();
    BAR();
  }
  if (wid >= 4) {
    const int lw = wid - 4;
    const int r8 = lane >> 3, p = lane & 7;
    if (wid == 7) {
      if (!work2) {
        if (blockIdx.x == 255) {
          for (unsigned spin = 0; spin < SPIN; ++spin) {
            const u32x4 rq = ld16_sc1(rG, vl, G_REQ);
            st16_sc1(rq, rG, vl, G_ANS);
            const u32x4 fl = ld16_sc1(rG, vl, G_ALL);
            if (__all((fl[0] & fl[1] & fl[2] & (lane == 63 ? 1u : fl[3])) == 1u)) break;
          }
        } else {
          const unsigned nonce = __builtin_amdgcn_readfirstlane(ld4_sc1(rG, 0, G_ANS + blockIdx.x * 4)) + 1;
          st4_sc1(nonce, rG, 0, G_REQ + blockIdx.x * 4);
          wait_go(rG, nonce, nonce - 1);
        }
      }
      const unsigned box = (fq & 1) * 128 + (cb >> 1) * 16 + fr;
      st4_sc1(1u, rG, fq < 2 ? box * 128 + (pm * 2 + (cb & 1)) * 4 : G_ALL + blockIdx.x * 4, 0);
    }
    const rsrc_t rP = RSRC(Pp, 128 * S * 2);
    const unsigned vo_e = (lw * 32 + r8) * (S * 2) + ((p ^ (r8 >> 1)) << 4);
    const unsigned vo_o = (vo_e ^ 64) + 8 * (S * 2);
    auto stageP = [&](int kt, unsigned poff) {
      const unsigned mb = poff + lw * 4096, so = (kt ^ kx) * 128;
#pragma unroll
      for (int j = 0; j < 4; ++j) dma16(rP, mb, P2_PB + j * 1024, (j & 1) ? vo_o : vo_e, so + (j >> 1) * 16 * (S * 2));
    };
    if (work2) {
      BAR();
      stageP(4, 0); stageP(5, 16384);
    }
    const rsrc_t rWo = RSRC(a.wout + 64 * tn3, (FH * D - 64 * tn3) * 2);
    const unsigned vw_e = (32 * lw + r8) * (D * 2) + ((p ^ (((r8 >> 1) & 1) << 1)) << 4), vw_o = (vw_e ^ 64) + 8 * (D * 2);
    u32x4 wreg[12];
#pragma unroll
    for (int i = 0; i < 12 && !NOPARK; ++i) {
      const int rg = i < 8 ? 2 + (i >> 2) : 6;
      wreg[i] = __builtin_amdgcn_raw_buffer_load_b128(rWo, (i & 1) ? vw_o : vw_e, (rg * 128 + ((i >> 1) & 1) * 16) * (D * 2), 0);
    }
    auto wput = [&](int rg) {
#pragma unroll
      for (int j = 0; j < 4 && !NOPARK; ++j) *(LAS u32x4*)(size_t)(rg * 16384 + lw * 4096 + j * 1024 + vl) = wreg[(rg == 6 ? 8 : (rg - 2) * 4) + j];
    };
    auto wdma = [&](int rg) {
#pragma unroll
      for (int j = 0; j < 4; ++j) dma16(rWo, lw * 4096, rg * 16384 + j * 1024, (j & 1) ? vw_o : vw_e, (rg * 128 + (j >> 1) * 16) * (D * 2));
    };
    if (work2) {
      if (wid == 7) {
        const unsigned box = (mat * 4 + tn) * 16 + tm;
        for (unsigned spin = 0; spin < SPIN; ++spin) {
          const unsigned fl = ld4_sc1(rG, (lane & 31) * 4, G_F1 + box * 128);
          if (__all(fl == 1u || (box >= 112 && box < 128 && (lane & 1)))) break;
        }
      }
      BAR();
      STAMP(1);
      const rsrc_t rQ = RSRC(a.ws + WS_Q + (size_t)(mat * 4 + tn) * 128 * S * 2, 128 * S * 2);
      const unsigned vq = vl + lw * 131072;
      auto stageQ = [&](int kt) {
        const unsigned mb = (kt & 3) * 16384 + lw * 4096, so = (kt ^ kx) * 2048;
#pragma unroll
        for (int j = 0; j < 4; ++j) dma16_sc1(rQ, mb, j * 1024, vq, so + (j >> 1) * 65536 + (j & 1) * 1024);
      };
#pragma unroll
      for (int i = 0; i < 12 && !NOPARK; ++i) OPAQUE(wreg[i]);
      stageQ(0); stageQ(1);
      z1_finish();
      WAIT_V(4); BAR();
      stageQ(2); stageQ(3);
      WAIT_V(8); BAR(); stageQ(4); stageP(6, 2 * 16384);
      WAIT_V(12); BAR(); stageQ(5); stageP(7, 3 * 16384);
      WAIT_V(16); BAR(); stageQ(6); stageP(8, 4 * 16384);
      unsigned poff = 5 * 16384;
      for (int kt = 3; kt < 26; ++kt) {
        WAIT_V(20);
        BAR();
        if (kt == 3 && wid == 7 && cb < 14) st4_sc1(1u, rG, 0, G_FZ + (pm * 8 + (cb >> 1)) * 128 + (cb & 1) * 4);
        stageQ(kt + 4); stageP(kt + 6, poff);
        poff = poff == 5 * 16384 ? 0 : poff + 16384;
      }
      if (mat == 2) {
        const rsrc_t rZ = RSRC(a.ws + WS_Z1 + (size_t)((8 * tm + lw) * 112 + 16 * tn) * 256, 4 * 112 * 256 + 4096);
        auto stageZ = [&](int h) {
#pragma unroll
          for (int j = 0; j < 4; ++j) dma16_sc1(rZ, lw * 4096, P2_Z + h * 16384 + j * 1024, vl, h * (4 * 112 * 256) + j * 1024);
        };
        if (wid == 7) {
          for (unsigned spin = 0; spin < SPIN; ++spin)
            if (__all(ld4_sc1(rG, (lane & 1) * 4, G_FZ + (tm * 8 + tn) * 128) == 1u)) break;
        }
        WAIT_V(20); BAR(); stageQ(30); stageZ(0);
        WAIT_V(20); BAR(); stageQ(31); stageZ(1);
        WAIT_V(20); BAR(); wdma(0); wdma(4);
        WAIT_V(20); BAR(); wdma(1); wdma(5);
        WAIT_V(20); BAR(); wput(2); wput(6);
        WAIT_V(16); BAR(); wput(3);
      } else {
        WAIT_V(20); BAR(); stageQ(30);
        WAIT_V(16); BAR(); stageQ(31);
        WAIT_V(12); BAR(); wdma(0); wdma(4);
        WAIT_V(12); BAR(); wdma(1); wdma(5);
        WAIT_V(16); BAR(); wput(2); wput(6);
        BAR(); wput(3);
      }
    } else {
      STAMP(1);
      wdma(0); wdma(4); wdma(1); wdma(5); wput(2); wput(3); wput(6);
    }
    STAMP(2); STAMP(3);
    WAIT_V(0);
  } else if (work2) {
    const int wp = wid >> 1, wq = wid & 1;
    f32x4 acc[4][4];
#pragma unroll
    for (int ni = 0; ni < 4; ++ni) {
      const float bias = mat == 2 ? __uint_as_float(bv[ni] << 16) : 0.0f;
#pragma unroll
      for (int n = 0; n < 4; ++n) acc[ni][n] = f32x4{bias, bias, bias, bias};
    }
    BAR();
    if (DWARM && cb == 14) warm_window(a, 0, pm, 2, wid, lane);
    const int sw = (fr >> 1) & 7;
    const unsigned ra = P2_PB + (wp * 64 + fr) * 128 + ((fq ^ sw) << 4);
    const unsigned rb = wq * 8192 + vl;
    bf16x8 a0[4], b0[4], a1[4], b1[4];
    unsigned poff = 2 * 16384;
    unsigned pa0 = ra + poff, pa1 = pa0 ^ 64;
#define K2_RD(AF, BF, pa, sl, ks, i) do { \
    if ((i) == 0) AF[0] = ld_frag(pa); \
    else if ((i) <= 4) BF[(i) - 1] = ld_frag(rb + (sl) * 16384 + ((i) - 1) * 2048 + (ks) * 1024); \
    else AF[(i) - 4] = ld_frag(pa + ((i) - 4) * 2048); } while (0)
#define K2_MM(AF, BF, i) acc[(i) >> 2][(i) & 3] = MFMA16(BF[(i) & 3], AF[(i) >> 2], acc[(i) >> 2][(i) & 3])
#define K2_STEP(s, LAST) do { \
    SCHED(); WL0(); SCHED(); \
    _Pragma("unroll") for (int i = 0; i < 16; ++i) { K2_MM(a0, b0, i); SCHED(); if (i < 8) K2_RD(a1, b1, pa1, s, 1, i); \
      if (i == 10) { poff = poff == 5 * 16384 ? 0 : poff + 16384; pa0 = ra + poff; } SCHED(); } \
    WL0(); \
    if ((s) == 3) WAIT_V(0); \
    BAR(); \
    SCHED(); \
    _Pragma("unroll") for (int i = 0; i < 16; ++i) { K2_MM(a1, b1, i); SCHED(); if (!(LAST) && i < 8) K2_RD(a0, b0, pa0, ((s) + 1) & 3, 0, i); \
      if (i == 10) pa1 = pa0 ^ 64; SCHED(); } \
  } while (0)
    if (DWARM && cb == 14) WAIT_V(0);
    BAR();
    z1_finish();
    BAR();
    STAMP(1);
#pragma unroll
    for (int i = 0; i < 8; ++i) K2_RD(a0, b0, pa0, 0, 0, i);
    for (int kt = 0; kt < 28; kt += 4) { K2_STEP(0, 0); K2_STEP(1, 0); K2_STEP(2, 0); K2_STEP(3, 0); }
    K2_STEP(0, 0); K2_STEP(1, 0); K2_STEP(2, 0); K2_STEP(3, 1);
    SCHED();
    STAMP(2);
    if (mat < 2) {
      const rsrc_t rY = RSRC(a.ws + WS_Y + (size_t)mat * S * D * 2, S * D * 2);
      const unsigned so = ((8 * tm + 4 * wp) * 64 + 16 * tn + 8 * wq) * 256;
#pragma unroll
      for (int ni = 0; ni < 4; ++ni)
#pragma unroll
        for (int P = 0; P < 2; ++P) {
          const f32x4 c0 = acc[ni][2 * P], c1 = acc[ni][2 * P + 1];
          const u32x4 v = {cvt_pk(c0[0], c0[1]), cvt_pk(c0[2], c0[3]), cvt_pk(c1[0], c1[1]), cvt_pk(c1[2], c1[3])};
          st16_sc1(v, rY, vl, so + (ni * 64 + 4 * P) * 256);
        }
    } else {
      const rsrc_t rH = RSRC(a.ws + WS_H, S * FH * 2);
      const unsigned so = ((8 * tm + 4 * wp) * 112 + 16 * tn + 8 * wq) * 256;
      const unsigned za = P2_Z + wp * 16384 + wq * 2048 + (fq * 16 + ((fr & 4) << 1) + ((fr & 8) >> 1) + (fr & 3)) * 16;
#pragma unroll
      for (int ni = 0; ni < 4; ++ni) {
#pragma unroll
        for (int P = 0; P < 2; ++P) {
          const f32x4 c0 = acc[ni][2 * P], c1 = acc[ni][2 * P + 1];
          const u32x4 z = *(const LAS u32x4*)(size_t)(za + ni * 4096 + P * 1024);
          const u32x4 v = {cvt_pk(bf_lo(z[0]) * c0[0], bf_hi(z[0]) * c0[1]), cvt_pk(bf_lo(z[1]) * c0[2], bf_hi(z[1]) * c0[3]),
                           cvt_pk(bf_lo(z[2]) * c1[0], bf_hi(z[2]) * c1[1]), cvt_pk(bf_lo(z[3]) * c1[2], bf_hi(z[3]) * c1[3])};
          st16_sc1(v, rH, vl, so + (ni * 112 + 4 * P) * 256);
        }
      }
    }
    STAMP(3);
    WAIT_V(0);
  } else {
    STAMP(1);
    for (int i = 0; i < PF_N && blockIdx.x != 255; ++i) {
      warm_window(a, P2_Z, pm, (PF_LIST >> (4 * i)) & 15, wid, lane);
      WAIT_V(6);
    }
    STAMP(2);
    WAIT_V(0);
    STAMP(3);
  }
  WL0();
  BAR();
  STAMP(4);
  {
    const int wr = (wid + tn3) & 7, tp = wr >> 2, kq = wr & 3;
    const int tt3 = 4 * tm3 + 2 * tp + (kq & 1);
    const int cq = 2 * (fq & 1) + (fq >> 1);
    const int c3 = 8 * tn3 + 2 * (kq & 2);
    if (wid == 4 && work2) {
      const unsigned box = mat == 2 ? fr : (fr & 8) + 2 * tn + (fr & 1);
      st4_sc1(1u, rG, box * 128, G_FP + tm * 2048 + (mat == 2 ? tn : 8 + mat) * 4);
    }
    const int q4 = fr >> 2, pp = lane & 3;
    const int sp = ((q4 >> 1) & 1) | ((fq & 1) << 1);
    const unsigned wa = kq * 4096 + 128 * (8 * fq + q4) + 16 * ((2 * (kq & 2) + (pp >> 1)) ^ (sp << 1)) + 8 * (pp & 1);
    bf16x8 wf[7][4];
    auto ld_w = [&]() {
#pragma unroll
      for (int mi = 0; mi < 4; ++mi) {
        unsigned lo = wa ^ (mi * 32), hi = lo + 65536;
        OPAQUE(hi);
#pragma unroll
        for (int g = 0; g < 7; ++g) wf[g][mi] = ld_tr((g < 4 ? lo : hi) + (g & 3) * 16384);
      }
    };
    if (wid == 4) {
      for (unsigned spin = 0; spin < SPIN; ++spin) {
        const unsigned fl = ld4_sc1(rG, fr * 4, G_FP + (tm3 * 8 + tn3) * 128);
        if (__all(fl == 1u || fr == 7 || fr > 9)) break;
      }
    } else {
      ld_w();
    }
    BAR();
    STAMP(5);
    const rsrc_t rH = RSRC(a.ws + WS_H + (size_t)((4 * tm3 + 2 * tp) * 112 + 4 * kq) * 256, 2 * 112 * 256);
    const rsrc_t rY = RSRC(a.ws + WS_Y + (size_t)(tt3 * 64 + c3) * 256, S * D * 2 + 1024);
    const unsigned vh0 = vl + (kq & 1) * (112 * 256), vh1 = vl + ((kq & 1) ^ 1) * (112 * 256);
    u32x4 hf[7][2];
#pragma unroll
    for (int g = 0; g < 7; ++g) {
      hf[g][0] = __builtin_amdgcn_raw_buffer_load_b128(rH, vh0, g * 4096, 16);
      hf[g][1] = __builtin_amdgcn_raw_buffer_load_b128(rH, vh1, g * 4096, 16);
    }
    const unsigned vy = cq * 256 + fr * 16;
    const u32x4 e1 = __builtin_amdgcn_raw_buffer_load_b128(rY, vy, 0, 16), e2 = __builtin_amdgcn_raw_buffer_load_b128(rY, vy, S * D * 2, 16);
    const u32x4 ex = *(const u32x4*)(a.x + (size_t)(tt3 * 16 + fr) * D + (c3 + cq) * 8);
    SCHED();
    if (wid == 4) ld_w();
    f32x4 acc[2][4];
#pragma unroll
    for (int g = 0; g < 7; ++g)
#pragma unroll
      for (int tl = 0; tl < 2; ++tl)
#pragma unroll
        for (int mi = 0; mi < 4; ++mi) acc[tl][mi] = MFMA16(wf[g][mi], __builtin_bit_cast(bf16x8, hf[g][tl]), (g ? acc[tl][mi] : f32x4{0, 0, 0, 0}));
    STAMP(6);
    LAS f32x4* xb = (LAS f32x4*)(size_t)(P3_X) + lane;
#pragma unroll
    for (int d = 1; d < 4; ++d) {
      xb[((wr * 3 + d - 1) * 2) * 64] = acc[d & 1][d & 2];
      xb[((wr * 3 + d - 1) * 2 + 1) * 64] = acc[d & 1][(d & 2) + 1];
    }
    WL0();
    BAR();
    f32x4 s0 = acc[0][0], s1 = acc[0][1];
#pragma unroll
    for (int d = 1; d < 4; ++d) {
      s0 += xb[(((wr ^ d) * 3 + d - 1) * 2) * 64];
      s1 += xb[(((wr ^ d) * 3 + d - 1) * 2 + 1) * 64];
    }
    f32x4 c0, c1;
#pragma unroll
    for (int j = 0; j < 4; ++j) {
      auto w = __builtin_amdgcn_permlane16_swap(__float_as_uint(s0[j]), __float_as_uint(s1[j]), false, false);
      c0[j] = __uint_as_float(w[0]); c1[j] = __uint_as_float(w[1]);
    }
    u32x4 o;
#pragma unroll
    for (int k = 0; k < 4; ++k) {
      const f32x4 c = (k >> 1) ? c1 : c0;
      const float lo = bf_lo(ex[k]) + bf_lo(e1[k]) - bf_lo(e2[k]) + c[(k & 1) * 2];
      const float hi = bf_hi(ex[k]) + bf_hi(e1[k]) - bf_hi(e2[k]) + c[(k & 1) * 2 + 1];
      o[k] = cvt_pk(lo, hi);
    }
    __builtin_nontemporal_store(o, (u32x4*)(a.out + (size_t)(tt3 * 16 + fr) * D + (c3 + cq) * 8));
  }
  STAMP(7);
  STAMP_OUT(a.ws, 1, 8);
  {
    asm volatile("s_waitcnt vmcnt(0)" ::: "memory");
    const unsigned long long pend = __builtin_amdgcn_s_memrealtime();
    const rsrc_t rS = RSRC(a.ws + (100u << 20), 65536);
    st16_sc1(u32x4{(unsigned)pstart, (unsigned)pend, 0, 0}, rS, 0, (blockIdx.x * 8 + wid) * 16);
    if (blockIdx.x == 73 && wid == 0) {
      while (__builtin_amdgcn_s_memrealtime() < pstart + 6000) __builtin_amdgcn_s_sleep(1);
      int mns = 1 << 30, mxe = -(1 << 30);
      for (int i = 0; i < 32; ++i) {
        const u32x4 v = ld16_sc1(rS, lane * 16, i * 1024);
        const int st = (int)(v[0] - (unsigned)pstart), en = (int)(v[1] - (unsigned)pstart);
        mns = min(mns, st); mxe = max(mxe, en);
      }
      for (int o = 32; o > 0; o >>= 1) { mns = min(mns, __shfl_xor(mns, o)); mxe = max(mxe, __shfl_xor(mxe, o)); }
      const int q = __builtin_amdgcn_readfirstlane(mxe - mns);
      const unsigned long long tend = pstart + __builtin_amdgcn_readfirstlane(mns) + 10 * q;
      while (__builtin_amdgcn_s_memrealtime() < tend) ;
    }
  }
}

extern "C" void kernel_launch(void* const* d_in, const int* in_sizes, int n_in, void* d_out, int out_size, void* d_ws, size_t ws_size,
                              hipStream_t stream) {
  (void)in_sizes; (void)n_in; (void)out_size; (void)ws_size;
  (void)hipFuncSetAttribute((const void*)fused, hipFuncAttributeMaxDynamicSharedMemorySize, LDS_BYTES);
  Args a{(const u16*)d_in[0], (const u16*)d_in[3], (const u16*)d_in[4], (const u16*)d_in[6], (const u16*)d_in[9],
         (const u16*)d_in[1], (const u16*)d_in[2], (const u16*)d_in[7], (const u16*)d_in[8], (char*)d_ws, (u16*)d_out};
  fused<<<256, 512, LDS_BYTES, stream>>>(a);
}
```

```cpp
#include <hip/hip_runtime.h>
#include <hip/hip_bf16.h>
#include <stdint.h>

typedef unsigned short u16;
typedef short bf16x8 __attribute__((ext_vector_type(8)));
typedef short s16x4 __attribute__((ext_vector_type(4)));
typedef float f32x4 __attribute__((ext_vector_type(4)));
typedef float f32x2 __attribute__((ext_vector_type(2)));
typedef unsigned u32x4 __attribute__((ext_vector_type(4)));
typedef __bf16 bf16x2v __attribute__((ext_vector_type(2)));
typedef __amdgpu_buffer_rsrc_t rsrc_t;
#define LAS __attribute__((address_space(3)))
#define WAIT_V(n) asm volatile("s_waitcnt vmcnt(%0)" ::"n"(n) : "memory")
#define WL0() __builtin_amdgcn_s_waitcnt(0xC07F)
#define BAR() do { asm volatile("" ::: "memory"); __builtin_amdgcn_s_barrier(); asm volatile("" ::: "memory"); } while (0)
#define SCHED() __builtin_amdgcn_sched_barrier(0)
#define MFMA16(a, b, c) __builtin_amdgcn_mfma_f32_16x16x32_bf16(a, b, c, 0, 0, 0)
#define RSRC(p, n) __builtin_amdgcn_make_buffer_rsrc((void*)(p), 0, (unsigned)(n), 0x00020000)
#define OPAQUE(x) asm volatile("" : "+v"(x))

#define STAMP_DECL
#define STAMP(i)
#define STAMP_OUT(ws, kid, n)
#define K2_KXMASK 3
#define K1_KXMASK 3
#define DWARM 0
#define CODEWARM 6
#define NOPARK 0
#define BULKLATE 0
#define ZNB 0
#define WPARK_LATE 1
#define DW 2
#define PF_EARLY 0
#define B10_AT 3
#define B11_AT 10
#define PF_PASS 3
#define GATHER_AT 24
#define WINSTART 1
#define PF_LIST 0x75810u
#define PF_N 5

constexpr int S = 2048, D = 512, F = 1792, FH = 896;
constexpr size_t WS_Q = 0;
constexpr size_t WS_Z1 = WS_Q + (size_t)(2 * D + FH) * S * 2;
constexpr size_t WS_H = WS_Z1 + (size_t)FH * S * 2;
constexpr size_t WS_Y = WS_H + (size_t)FH * S * 2;
constexpr size_t WS_G = WS_Y + (size_t)2 * D * S * 2;
constexpr unsigned G_F1 = 0;
constexpr unsigned G_FP = 32768;
constexpr unsigned G_FZ = 32768 + 64;
constexpr unsigned G_ALL = 65536;
constexpr unsigned G_REQ = G_ALL + 1024;
constexpr unsigned G_AN1 = G_REQ + 1024;
constexpr unsigned G_AN2 = G_AN1 + 1024;
constexpr unsigned G_PAY = G_AN2 + 1024;
constexpr unsigned G_END = G_PAY + 16 * 14336;

constexpr int LDS_BYTES = 163840;
constexpr int P1_RING = 65536, P1_SCR = 65536, P1_GAT = 77824, P1_W2 = 98304;
constexpr int P2_QB = 98304, P2_Z = 32768, P3_X3 = 147456;
__device__ constexpr int wbase(int rg) { return rg < 2 ? 65536 + rg * 16384 : rg < 4 ? (rg - 2) * 16384 : 98304 + (rg - 4) * 16384; }
constexpr unsigned SPIN = 1u << 20;

struct Args {
  const u16* x; const u16* wdr; const u16* wdi; const u16* win; const u16* wout;
  const u16* wsr; const u16* wsi; const u16* wsp; const u16* bsp;
  char* ws; u16* out;
};

__device__ __forceinline__ unsigned cvt_pk(float lo, float hi) {
  f32x2 v = {lo, hi};
  return __builtin_bit_cast(unsigned, __builtin_convertvector(v, bf16x2v));
}
__device__ __forceinline__ float bf_lo(unsigned u) { return __uint_as_float(u << 16); }
__device__ __forceinline__ float bf_hi(unsigned u) { return __uint_as_float(u & 0xffff0000u); }
constexpr float GC1 = -2.0f * 0.7978845608028654f * 1.4426950408889634f, GC2 = GC1 * 0.044715f;
__device__ __forceinline__ f32x2 gelu2(f32x2 v) {
  const f32x2 t = v * ((v * v) * GC2 + GC1);
  const f32x2 e = f32x2{__builtin_amdgcn_exp2f(t[0]), __builtin_amdgcn_exp2f(t[1])} + 1.0f;
  return v * f32x2{__builtin_amdgcn_rcpf(e[0]), __builtin_amdgcn_rcpf(e[1])};
}
__device__ __forceinline__ void dot_sq(float& c, unsigned a) { asm volatile("v_dot2c_f32_bf16 %0, %1, %1" : "+v"(c) : "v"(a)); }
__device__ __forceinline__ void dot_sum(float& c, unsigned a) { asm volatile("v_dot2c_f32_bf16 %0, 0x3f803f80, %1" : "+v"(c) : "v"(a)); }
__device__ __forceinline__ float sum_rows(float v) {
  auto a = __builtin_amdgcn_permlane32_swap(__float_as_uint(v), __float_as_uint(v), false, false);
  v = __uint_as_float(a[0]) + __uint_as_float(a[1]);
  auto b = __builtin_amdgcn_permlane16_swap(__float_as_uint(v), __float_as_uint(v), false, false);
  return __uint_as_float(b[0]) + __uint_as_float(b[1]);
}
__device__ __forceinline__ float sel4(f32x4 v, int i) { const float a = (i & 1) ? v[1] : v[0], b = (i & 1) ? v[3] : v[2]; return (i & 2) ? b : a; }

__device__ __forceinline__ void dma16(rsrc_t r, unsigned mbase, int mimm, unsigned voff, unsigned soff) {
  asm volatile("s_add_i32 m0, %0, %1\n\ts_nop 0\n\tbuffer_load_dwordx4 %2, %3, %4 offen lds" ::"s"(mbase), "n"(mimm), "v"(voff), "s"(r), "s"(soff) : "scc");
}
__device__ __forceinline__ void dma16_sc1(rsrc_t r, unsigned mbase, int mimm, unsigned voff, unsigned soff) {
  asm volatile("s_add_i32 m0, %0, %1\n\ts_nop 0\n\tbuffer_load_dwordx4 %2, %3, %4 offen sc1 lds" ::"s"(mbase), "n"(mimm), "v"(voff), "s"(r), "s"(soff) : "scc");
}
__device__ __forceinline__ void dma4_sc1(rsrc_t r, int ldsaddr, unsigned voff, unsigned soff) {
  asm volatile("s_mov_b32 m0, %0\n\ts_nop 0\n\tbuffer_load_dword %1, %2, %3 offen sc1 lds" ::"n"(ldsaddr), "v"(voff), "s"(r), "s"(soff) : "memory");
}
__device__ __forceinline__ void touch(rsrc_t r, int junk, unsigned voff, unsigned soff) {
  asm volatile("s_mov_b32 m0, %0\n\ts_nop 0\n\tbuffer_load_dword %1, %2, %3 offen lds" ::"n"(junk), "v"(voff), "s"(r), "s"(soff) : "memory");
}
__device__ __forceinline__ void st16_sc1(u32x4 v, rsrc_t r, unsigned voff, unsigned soff) {
  asm volatile("buffer_store_dwordx4 %0, %1, %2, %3 offen sc1\n\ts_nop 1" ::"v"(v), "v"(voff), "s"(r), "s"(soff) : "memory");
}
__device__ __forceinline__ void st4_sc1(unsigned v, rsrc_t r, unsigned voff, unsigned soff) {
  asm volatile("buffer_store_dword %0, %1, %2, %3 offen sc1" ::"v"(v), "v"(voff), "s"(r), "s"(soff) : "memory");
}
__device__ __forceinline__ unsigned ld4_sc1(rsrc_t r, unsigned voff, unsigned soff) {
  unsigned v;
  asm volatile("buffer_load_dword %0, %1, %2, %3 offen sc1\n\ts_waitcnt vmcnt(0)" : "=&v"(v) : "v"(voff), "s"(r), "s"(soff) : "memory");
  return v;
}
__device__ __forceinline__ u32x4 ld16_sc1(rsrc_t r, unsigned voff, unsigned soff) {
  u32x4 v;
  asm volatile("buffer_load_dwordx4 %0, %1, %2, %3 offen sc1\n\ts_waitcnt vmcnt(0)" : "=&v"(v) : "v"(voff), "s"(r), "s"(soff) : "memory");
  return v;
}
__device__ __forceinline__ void ld16_async(u32x4& v, rsrc_t r, unsigned voff, unsigned soff) {
  asm volatile("buffer_load_dwordx4 %0, %1, %2, %3 offen" : "=v"(v) : "v"(voff), "s"(r), "s"(soff) : "memory");
}
__device__ __forceinline__ void pin(unsigned& v) { OPAQUE(v); }
__device__ __forceinline__ void wait_vn(int n) { WAIT_V(n); }
__device__ __forceinline__ bf16x8 ld_tr(unsigned addr) {
  s16x4 lo = __builtin_amdgcn_ds_read_tr16_b64_v4i16((LAS s16x4*)(size_t)(addr));
  s16x4 hi = __builtin_amdgcn_ds_read_tr16_b64_v4i16((LAS s16x4*)(size_t)(addr + 512));
  return __builtin_shufflevector(lo, hi, 0, 1, 2, 3, 4, 5, 6, 7);
}
__device__ __forceinline__ bf16x8 ld_frag(unsigned addr) { return *(const LAS bf16x8*)(size_t)(addr); }
__device__ __forceinline__ void wait_go(rsrc_t rG, unsigned word, unsigned nonce, unsigned seen) {
  for (unsigned spin = 0; seen != nonce && spin < SPIN; ++spin) {
    seen = __builtin_amdgcn_readfirstlane(ld4_sc1(rG, 0, word));
    __builtin_amdgcn_s_sleep(1);
  }
}
__device__ __forceinline__ unsigned lds_word(unsigned addr) { return __builtin_amdgcn_readfirstlane(*(LAS unsigned*)(size_t)(addr)); }

__device__ __forceinline__ void warm_window(const Args& a, int junk, int rb, int w, int wv, int lane) {
  if (w < 8) {
    const unsigned vo = (rb * 128 + wv * 32 + (lane >> 2)) * (S * 2) + (lane & 3) * 128;
#pragma unroll
    for (int m = 0; m < 3; ++m) {
      const rsrc_t r = RSRC(m == 0 ? a.wsr : m == 1 ? a.wsi : a.wsp, S * S * 2);
      touch(r, junk, vo, w * 512);
      touch(r, junk, vo, w * 512 + 16 * (S * 2));
    }
  } else {
    const rsrc_t r = RSRC(a.wout + (size_t)rb * 56 * D, 56 * D * 2);
    touch(r, junk, lane * 128, wv * 16384);
    touch(r, junk, lane * 128, wv * 16384 + 8192);
  }
}

template <bool ZB>
__device__ __forceinline__ bool phase1(const Args& a, int pm, int cb, int kx, const u16* Pp, int kx2, unsigned qbox, f32x4 (&z1v)[2][2], const u16* bp, unsigned& bv) {
  const int tid = threadIdx.x, lane = tid & 63;
  const int wid = __builtin_amdgcn_readfirstlane(tid >> 6);
  const int wm = wid & 3, wn = wid >> 2;
  STAMP_DECL STAMP(0);
  const rsrc_t rX = RSRC(a.x + (size_t)pm * 128 * D, 128 * D * 2);
  const rsrc_t rW = ZB ? RSRC(a.win + 64 * cb, (D * F - 64 * cb) * 2) : RSRC(a.wdi + (size_t)384 * D, 64 * D * 2);
  const int r8 = lane >> 3, p8 = lane & 7;
  const unsigned sw_e = (p8 ^ (r8 >> 1)) << 4;
  const unsigned vx_e = (32 * (wid >> 1) + 4 * (wid & 1) + 8 * (r8 >> 2) + (r8 & 3)) * (D * 2) + sw_e;
  const unsigned vx_o = (vx_e ^ 64) + 16 * (D * 2);
  const unsigned vw1 = ZB ? (8 * wid + r8) * (F * 2) + ((p8 ^ ((((r8 >> 1) & 1) | ((wid & 1) << 1)) << 1)) << 4) + FH * 2
                          : (8 * wid + r8) * (D * 2) + (sw_e ^ ((wid & 1) << 6));
  constexpr unsigned WKB = ZB ? 64 * F * 2 : 128;
  const unsigned mb1 = wid * 1024, mb2 = wid * 2048;
  auto piece1 = [&](int st, int p) {
    const unsigned kb = st ^ kx;
    const int base = P1_RING + (st % 3) * 24576;
    if (p == 0) dma16(rX, mb2, base, vx_e, kb * 128);
    else if (p == 1) dma16(rW, mb1, base + 16384, vw1, kb * WKB);
    else dma16(rX, mb2, base + 1024, vx_o, kb * 128);
  };
  piece1(0, 0); piece1(0, 1); piece1(0, 2);
  const rsrc_t rG = RSRC(a.ws + WS_G, G_END);
  const unsigned role4 = (pm * 16 + cb) * 4;
  if (wid == 7) dma4_sc1(rG, 0, (lane & 1) * 1024, G_AN1 + role4);
  SCHED();

  const int fr = lane & 15, fq = lane >> 4;
  const int kh = wm >> 1;
  const unsigned fsw = ((fq ^ ((fr >> 1) & 7)) << 4) ^ (kh << 6);
  unsigned xa[2][2], wb[2][2];
#pragma unroll
  for (int m = 0; m < 2; ++m) { xa[m][0] = P1_RING + (32 * wm + 16 * (m ^ wn) + fr) * 128 + fsw; xa[m][1] = xa[m][0] ^ 64; }
  const int q4 = fr >> 2, pp = lane & 3;
  const int sp = ((q4 >> 1) & 1) | ((fq & 1) << 1);
  const unsigned trl = 128 * (8 * fq + q4) + 16 * ((pp >> 1) ^ (sp << 1)) + 8 * (pp & 1);
#pragma unroll
  for (int n = 0; n < 2; ++n) {
    const int tile = 2 * wn + (n ^ (wm & 1));
    if (ZB) { wb[n][0] = P1_RING + 16384 + (trl ^ (tile * 32)) + kh * 4096; wb[n][1] = wb[n][0] ^ 4096; }
    else { wb[n][0] = P1_RING + 16384 + (16 * tile + fr) * 128 + fsw; wb[n][1] = wb[n][0] ^ 64; }
  }
#pragma unroll
  for (int i = 0; i < 2; ++i)
#pragma unroll
    for (int ks = 0; ks < 2; ++ks) { OPAQUE(xa[i][ks]); OPAQUE(wb[i][ks]); }
  SCHED();
  piece1(1, 0); piece1(1, 1); piece1(1, 2);
  unsigned nonce = 0;
  STAMP(1);

  bf16x8 xf[16][2], wf[2][2];
  const f32x4 Z4 = {0, 0, 0, 0};
  f32x4 acc1[2][2] = {{Z4, Z4}, {Z4, Z4}};
  f32x4 gram = Z4, rsum = Z4, cs1 = Z4;
  const bf16x8 ONES = {0x3f80, 0x3f80, 0x3f80, 0x3f80, 0x3f80, 0x3f80, 0x3f80, 0x3f80};

  auto mm = [&](int kk, int i) {
    const int ks = kk & 1;
    if (i == 0) gram = MFMA16(xf[kk][0], xf[kk][0], gram);
    else if (i == 1) rsum = MFMA16(xf[kk][0], ONES, rsum);
    else if (i < 6) { const int m = (i - 2) >> 1, n = i & 1; acc1[m][n] = MFMA16(xf[kk][m], wf[ks][n], acc1[m][n]); }
    else cs1 = MFMA16(ONES, wf[0][0], cs1);
  };
  auto rd = [&](int kk, int i) {
    const int ks = kk & 1, so = ((kk >> 1) % 3) * 24576;
    if (i == 0 || i == 3) xf[kk][i & 1] = ld_frag(xa[i & 1][ks] + so);
    else wf[ks][i - 1] = ZB ? ld_tr(wb[i - 1][ks] + so) : ld_frag(wb[i - 1][ks] + so);
  };
  auto pieceT = [&](int p) {
    const rsrc_t rD = RSRC(ZB ? (cb < 8 ? a.wdr : a.wdi) + (size_t)(cb & 7) * 64 * D : a.wdi + (size_t)448 * D, 64 * D * 2);
    const unsigned ve = r8 * (D * 2) + sw_e, vo = (ve ^ 64) + 8 * (D * 2);
    dma16(rD, wid * 8192, p * 1024, (p & 1) ? vo : ve, (wid ^ kx) * 128 + (p >> 1) * 16 * (D * 2));
  };
  auto pieceW2 = [&](int p) {
    const unsigned ve = r8 * (F * 2) + ((p8 ^ (((r8 >> 1) & 1) << 1)) << 4), vo = (ve ^ 64) + 8 * (F * 2);
    dma16(rW, wid * 8192, P1_W2 + p * 1024, (p & 1) ? vo : ve, (wid ^ kx) * WKB + (p >> 1) * 16 * (F * 2));
  };
#define PIN_ACC() asm volatile("" : "+v"(acc1[0][0]), "+v"(acc1[0][1]), "+v"(acc1[1][0]), "+v"(acc1[1][1]), "+v"(gram), "+v"(rsum), "+v"(cs1))
#pragma unroll
  for (int st = 0; st < 8; ++st) {
    wait_vn(st <= 6 ? 3 : 4);
    WL0();
    BAR();
    if (st == 0) STAMP(2);
    SCHED();
#pragma unroll
    for (int i = 0; i < 6; ++i) {
      if (st > 0) mm(2 * st - 1, i);
      SCHED();
      if (i < 4) rd(2 * st, i);
      if (st + 2 < 8) { if (i == 1) piece1(st + 2, 0); if (i == 5) piece1(st + 2, 1); }
      else if (i == 1 || i == 4) pieceT((st - 6) * 4 + (i >> 2));
      SCHED();
    }
    PIN_ACC();
    WL0();
    SCHED();
#pragma unroll
    for (int i = 0; i < 7; ++i) {
      mm(2 * st, i);
      SCHED();
      if (i < 4) rd(2 * st + 1, i);
      if (st + 2 < 8) { if (i == 4) piece1(st + 2, 2); }
      else if (i == 1 || i == 5) pieceT((st - 6) * 4 + 2 + (i >> 2));
      SCHED();
    }
    PIN_ACC();
    if (st == 0 && wid == 7) {
      const unsigned a1 = lds_word(0), a2 = lds_word(4);
      nonce = a1 + 1 == a2 ? a1 + 2 : a1 + 1;
      st4_sc1(nonce, rG, 0, G_REQ + role4);
    }
    SCHED();
  }
  LAS float* const s_r = (LAS float*)(size_t)(P1_SCR);
  LAS float* const s_c = (LAS float*)(size_t)(P1_SCR + 512);
  LAS float* const s_u1 = (LAS float*)(size_t)(P1_SCR + 1024);
  LAS float* const s_z = (LAS float*)(size_t)(P1_SCR + 2048);
  LAS float* const s_n = (LAS float*)(size_t)(P1_SCR + 4096);
  LAS float* const s_u3 = (LAS float*)(size_t)(P1_SCR + 5120);
  LAS float* const s_u2 = (LAS float*)(size_t)(P1_SCR + 7168);
  constexpr int S_AN = P1_SCR + 9216, S_F1 = P1_SCR + 9472;
  {
#pragma unroll
    for (int i = 0; i < 6; ++i) mm(15, i);
    STAMP(3);
    unsigned i01 = 32 * wm + 16 * wn, i23 = 64 * kh + 16 * (2 * wn + (wm & 1));
    asm volatile("" : "+s"(i01), "+s"(i23));
    const float s1 = sel4(rsum, fr & 3), s2 = sel4(gram, fr & 3);
    const float mean = s1 * (1.0f / D);
    const float rstd = __builtin_amdgcn_rsqf(fmaxf(s2 * (1.0f / D) - mean * mean, 0.0f) + 1e-5f);
    if ((fr >> 2) == fq) { s_r[i01 + fr] = rstd; s_c[i01 + fr] = -mean * rstd; }
    if (fq == 0) s_u1[i23 + fr] = cs1[0];
  }
  const int s0 = pm * 128 + 32 * wm;
  const rsrc_t rT = RSRC(a.ws + WS_Q, (2 * D + FH) * S * 2);
  const unsigned vT = ((fr >> 2) & 1) * 65536 + fq * 256 + (4 * (fr >> 3) + (fr & 3)) * 16;
  const unsigned tb0 = fr * 128 + fsw, tb1 = tb0 ^ 64;
  if (wid == 7) dma4_sc1(rG, S_AN, (lane & 1) * 1024, G_AN1 + role4);
  WL0();
  BAR();
  STAMP(4);
  if (ZB) {
#pragma unroll
    for (int p = 0; p < 4; ++p) pieceW2(p);
  }
  f32x4 rq[2], cq[2];
#pragma unroll
  for (int m = 0; m < 2; ++m) {
    rq[m] = *(LAS f32x4*)(s_r + 32 * wm + 16 * (m ^ wn) + 4 * fq);
    cq[m] = *(LAS f32x4*)(s_c + 32 * wm + 16 * (m ^ wn) + 4 * fq);
  }
  auto store_t = [&](int T, f32x4 v0, f32x4 v1) {
    const unsigned a0 = cvt_pk(v0[0], v0[1]), a1 = cvt_pk(v0[2], v0[3]), b0 = cvt_pk(v1[0], v1[1]), b1 = cvt_pk(v1[2], v1[3]);
    const u32x4 o = wn ? u32x4{b0, b1, a0, a1} : u32x4{a0, a1, b0, b1};
    st16_sc1(o, rT, vT, (T >> 1) * 131072 + (T & 1) * 128 + s0 * 32);
  };
  auto lin_store = [&](f32x4 c0, f32x4 c1, float u, int T) { store_t(T, rq[0] * c0 + cq[0] * u, rq[1] * c1 + cq[1] * u); };
  float u1[2];
#pragma unroll
  for (int n = 0; n < 2; ++n) { const int ui = 16 * (2 * wn + (n ^ (wm & 1))) + fr; u1[n] = s_u1[ui] + s_u1[64 + ui]; }
  if (ZB) {
    f32x4 ps[2], pq[2];
#pragma unroll
    for (int n = 0; n < 2; ++n) {
#pragma unroll
      for (int m = 0; m < 2; ++m) {
        f32x4 v = rq[m] * acc1[m][n] + cq[m] * u1[n];
        const f32x2 g0 = gelu2(f32x2{v[0], v[1]}), g1 = gelu2(f32x2{v[2], v[3]});
        v = f32x4{g0[0], g0[1], g1[0], g1[1]};
        acc1[m][n] = v;
        if (n == 0) { ps[m] = v; pq[m] = v * v; } else { ps[m] += v; pq[m] += v * v; }
      }
      if (n == 0) {
        SCHED();
#pragma unroll
        for (int p = 4; p < 8; ++p) pieceW2(p);
        SCHED();
      }
    }
    float t[8], h[4];
#define DPP2(d, x, y, ctl, m0, m1) "v_add_f32_dpp " d ", " x ", " x " " ctl " row_mask:0xf bank_mask:" m0 "\n\tv_add_f32_dpp " d ", " y ", " y " " ctl " row_mask:0xf bank_mask:" m1 "\n\t"
#define DPPQ(d, ctl) "v_add_f32_dpp " d ", " d ", " d " " ctl " row_mask:0xf bank_mask:0xf\n\t"
    asm volatile("s_nop 1\n\t" DPP2("%0", "%8", "%16", "row_ror:8", "0x3", "0xc") DPP2("%1", "%9", "%17", "row_ror:8", "0x3", "0xc")
                 DPP2("%2", "%10", "%18", "row_ror:8", "0x3", "0xc") DPP2("%3", "%11", "%19", "row_ror:8", "0x3", "0xc")
                 DPP2("%4", "%12", "%20", "row_ror:8", "0x3", "0xc") DPP2("%5", "%13", "%21", "row_ror:8", "0x3", "0xc")
                 DPP2("%6", "%14", "%22", "row_ror:8", "0x3", "0xc") DPP2("%7", "%15", "%23", "row_ror:8", "0x3", "0xc") "s_nop 1"
                 : "=&v"(t[0]), "=&v"(t[1]), "=&v"(t[2]), "=&v"(t[3]), "=&v"(t[4]), "=&v"(t[5]), "=&v"(t[6]), "=&v"(t[7])
                 : "v"(ps[0][0]), "v"(pq[0][0]), "v"(ps[0][1]), "v"(pq[0][1]), "v"(ps[0][2]), "v"(pq[0][2]), "v"(ps[0][3]), "v"(pq[0][3]),
                   "v"(ps[1][0]), "v"(pq[1][0]), "v"(ps[1][1]), "v"(pq[1][1]), "v"(ps[1][2]), "v"(pq[1][2]), "v"(ps[1][3]), "v"(pq[1][3]));
    asm volatile("s_nop 1\n\t" DPP2("%0", "%4", "%8", "row_half_mirror", "0x5", "0xa") DPP2("%1", "%5", "%9", "row_half_mirror", "0x5", "0xa")
                 DPP2("%2", "%6", "%10", "row_half_mirror", "0x5", "0xa") DPP2("%3", "%7", "%11", "row_half_mirror", "0x5", "0xa") "s_nop 1\n\t"
                 DPPQ("%0", "quad_perm:[1,0,3,2]") DPPQ("%1", "quad_perm:[1,0,3,2]") DPPQ("%2", "quad_perm:[1,0,3,2]") DPPQ("%3", "quad_perm:[1,0,3,2]")
                 DPPQ("%0", "quad_perm:[2,3,0,1]") DPPQ("%1", "quad_perm:[2,3,0,1]") DPPQ("%2", "quad_perm:[2,3,0,1]") DPPQ("%3", "quad_perm:[2,3,0,1]") "s_nop 0"
                 : "=&v"(h[0]), "=&v"(h[1]), "=&v"(h[2]), "=&v"(h[3])
                 : "v"(t[0]), "v"(t[1]), "v"(t[2]), "v"(t[3]), "v"(t[4]), "v"(t[5]), "v"(t[6]), "v"(t[7]));
    if ((fr & 3) == 0) *(LAS f32x4*)(s_z + (wn * 128 + 32 * wm + 8 * fq + 4 * ((fr >> 3) ^ wn) + 2 * ((fr >> 2) & 1)) * 2) = f32x4{h[0], h[1], h[2], h[3]};
    WAIT_V(8);
  } else {
#pragma unroll
    for (int n = 0; n < 2; ++n) lin_store(acc1[0][n], acc1[1][n], u1[n], 56 + 2 * wn + (n ^ (wm & 1)));
    WAIT_V(2);
  }
  WL0();
  BAR();
  STAMP(5);
  if (ZB && wid == 7) {
    __builtin_amdgcn_s_setprio(2);
    const f32x4 p = *(LAS f32x4*)(s_z + lane * 4) + *(LAS f32x4*)(s_z + 256 + lane * 4);
    wait_go(rG, G_AN1 + role4, nonce, lds_word(S_AN));
    st16_sc1(__builtin_bit_cast(u32x4, p) | 1u, rG, lane * 16, G_PAY + (pm * 14 + cb) * 1024);
  }
  STAMP(6);
  unsigned w2[2][2];
  bf16x8 zw[4][2];
  auto rd2 = [&](int q, int n) { zw[q & 3][n] = ld_tr(w2[n][q & 1] + (q >> 1) * 8192); };
  {
    f32x4 acct[2][2] = {{Z4, Z4}, {Z4, Z4}};
    const unsigned t30 = tb0 + wn * 4096, t31 = tb1 + wn * 4096;
    const unsigned cb0 = tb0 + wid * 8192, cb1 = cb0 ^ 64, c2b = P1_W2 + wid * 8192 + trl;
    bf16x8 w[8], cf[2];
    f32x4 cs[4];
    float u3[2];
    auto rd3 = [&](int f) { const int kk = f >> 1; w[f & 7] = ld_frag(((kk & 1) ? t31 : t30) + (kk >> 1) * 8192 + (f & 1) * 2048); };
#pragma unroll
    for (int f = 0; f < 4; ++f) rd3(f);
    SCHED();
#pragma unroll
    for (int f = 0; f < 32; ++f) {
      const int kk = f >> 1, n = f & 1;
      acct[0][n] = MFMA16(xf[kk][0], w[f & 7], acct[0][n]);
      SCHED();
      if (f == 13) { if (ZB) wait_vn(0); WL0(); BAR(); }
      if (f + 4 < 32) rd3(f + 4);
      if (f < 8) cf[f & 1] = ld_frag(((f & 1) ? cb1 : cb0) + (f >> 1) * 2048);
      if (f == 11) s_u3[lane * 8 + wid] = sel4(f32x4{cs[0][0], cs[1][0], cs[2][0], cs[3][0]}, fq);
      if (ZB && f >= 14 && f < 22) cf[f & 1] = ld_tr((c2b ^ (((f - 14) >> 1) * 32)) + (f & 1) * 4096);
      if (ZB && f == GATHER_AT && wid == 7) {
        dma4_sc1(rG, S_AN, (lane & 1) * 1024, G_AN1 + role4);
#pragma unroll
        for (int i = 0; i < 14; ++i) dma16_sc1(rG, 0, P1_GAT + i * 1024, lane * 16, G_PAY + pm * 14336 + i * 1024);
      }
      if (f == 23 || f == 25) {
        const int q = (f - 23) >> 1;
        const f32x4 uu = *(LAS f32x4*)(s_u3 + (32 * wn + 16 * q + fr) * 8) + *(LAS f32x4*)(s_u3 + (32 * wn + 16 * q + fr) * 8 + 4);
        u3[q] = (uu[0] + uu[1]) + (uu[2] + uu[3]);
      }
      if (ZB && f == 26) s_u2[wid * 64 + lane] = sel4(f32x4{cs[0][0], cs[1][0], cs[2][0], cs[3][0]}, fq);
      if (ZB && f == 27) {
#pragma unroll
        for (int q = 0; q < 2; ++q)
#pragma unroll
          for (int ks = 0; ks < 2; ++ks) { w2[q][ks] = wb[q][ks] + (P1_W2 - P1_RING - 16384); OPAQUE(w2[q][ks]); }
      }
      if (ZB && f >= 28) rd2((f - 28) >> 1, f & 1);
      SCHED();
      acct[1][n] = MFMA16(xf[kk][1], w[f & 7], acct[1][n]);
      SCHED();
      if (f >= 1 && f < 9) { const int g = f - 1; cs[g >> 1] = MFMA16(ONES, cf[g & 1], (g & 1) ? cs[g >> 1] : Z4); SCHED(); }
      if (ZB && f >= 15 && f < 23) { const int g = f - 15; cs[g >> 1] = MFMA16(ONES, cf[(f - 1) & 1], (g & 1) ? cs[g >> 1] : Z4); SCHED(); }
    }
#pragma unroll
    for (int n = 0; n < 2; ++n) lin_store(acct[0][n], acct[1][n], u3[n], (ZB ? 4 * cb : 60) + 2 * wn + n);
  }
  STAMP(7);
  bv = bp[(wid & 2) * 32 + lane];
  auto publish = [&]() {
    if (ZB) WAIT_V(8); else { pin(bv); WAIT_V(0); }
    WL0();
    BAR();
    if (wid == 7) {
      wait_go(rG, G_AN2 + role4, nonce, lds_word(S_AN + 4));
      const unsigned box = (fq & 1) * 128 + (cb >> 1) * 16 + fr;
      st4_sc1(1u, rG, fq < 2 ? box * 128 + (pm * 2 + (cb & 1)) * 4 : G_ALL + role4, 0);
    }
  };
  auto earlyP = [&](int k) {
    const rsrc_t rP = RSRC(Pp, 128 * S * 2);
    const unsigned ve = (16 * wid + r8) * (S * 2) + sw_e, vo = (ve ^ 64) + 8 * (S * 2);
    dma16(rP, mb2, k * 16384, ve, (k ^ kx2) * 128);
    dma16(rP, mb2, k * 16384 + 1024, vo, (k ^ kx2) * 128);
  };
  if (ZB) {
    f32x4 acc2[2][2] = {{Z4, Z4}, {Z4, Z4}};
    SCHED();
#pragma unroll
    for (int q = 0; q < 16; ++q) {
      if (q == B10_AT) {
        if (wid == 7) {
          f32x4 t;
          for (unsigned spin = 0; spin < SPIN; ++spin) {
            if (spin) WAIT_V(0); else WAIT_V(2);
            u32x4 al = *(LAS u32x4*)(size_t)(P1_GAT + lane * 16);
            t = __builtin_bit_cast(f32x4, al);
#pragma unroll
            for (int i = 1; i < 14; ++i) { const u32x4 pv = *(LAS u32x4*)(size_t)(P1_GAT + i * 1024 + lane * 16); al &= pv; t += __builtin_bit_cast(f32x4, pv); }
            if (__all((al[0] & al[1] & al[2] & al[3] & 1u) != 0)) break;
            WL0();
#pragma unroll
            for (int i = 0; i < 14; ++i) dma16_sc1(rG, 0, P1_GAT + i * 1024, lane * 16, G_PAY + pm * 14336 + i * 1024);
          }
          const float m0 = t[0] * (1.0f / FH), m1 = t[2] * (1.0f / FH);
          const float r0 = __builtin_amdgcn_rsqf(fmaxf(t[1] * (1.0f / FH) - m0 * m0, 0.0f) + 1e-5f);
          const float r1 = __builtin_amdgcn_rsqf(fmaxf(t[3] * (1.0f / FH) - m1 * m1, 0.0f) + 1e-5f);
          *(LAS f32x2*)(s_n + 2 * lane) = f32x2{r0, r1};
          *(LAS f32x2*)(s_n + 128 + 2 * lane) = f32x2{-m0 * r0, -m1 * r1};
          __builtin_amdgcn_s_setprio(0);
        }
        pin(bv);
        WL0();
        BAR();
        STAMP(8);
        f32x4 r2[2], c2[2];
#pragma unroll
        for (int m = 0; m < 2; ++m) {
          r2[m] = *(LAS f32x4*)(s_n + 32 * wm + 8 * fq + 4 * (m ^ wn));
          c2[m] = *(LAS f32x4*)(s_n + 128 + 32 * wm + 8 * fq + 4 * (m ^ wn));
        }
#pragma unroll
        for (int n = 0; n < 2; ++n) store_t(64 + 4 * cb + 2 * wn + (n ^ (wm & 1)), acc1[0][n] * r2[0] + c2[0], acc1[1][n] * r2[1] + c2[1]);
        SCHED();
      }
      if (q == B11_AT) { publish(); STAMP(9); SCHED(); }
#pragma unroll
      for (int i = 0; i < 4; ++i) {
        const int m = i >> 1, n = i & 1;
        acc2[m][n] = MFMA16(zw[q & 3][n], xf[q][m], acc2[m][n]);
        SCHED();
        if (i < 2 && q + 2 < 16) rd2(q + 2, i);
        if (i == 3 && q >= B10_AT && q < B10_AT + 4) earlyP(q - B10_AT);
        SCHED();
      }
    }
#pragma unroll
    for (int n = 0; n < 2; ++n) {
      const int ui = 16 * (2 * wn + (n ^ (wm & 1))) + 4 * fq;
      f32x4 u = *(LAS f32x4*)(s_u2 + ui);
#pragma unroll
      for (int j = 1; j < 8; ++j) u += *(LAS f32x4*)(s_u2 + j * 64 + ui);
#pragma unroll
      for (int m = 0; m < 2; ++m) z1v[m][n] = acc2[m][n] * s_r[32 * wm + 16 * (m ^ wn) + fr] + u * s_c[32 * wm + 16 * (m ^ wn) + fr];
    }
  } else {
    STAMP(8);
    publish();
    STAMP(9);
    if (DW >= 0 && wid < 4) warm_window(a, P1_SCR + 9728, pm, DW, wid, lane);
    earlyP(0); earlyP(1); earlyP(2); earlyP(3);
    if (DW >= 0 && wid < 4) WAIT_V(0);
  }
  const bool allq = true;
  STAMP_OUT(a.ws, 0, 10);
  return allq;
}

__global__ __launch_bounds__(512, 2) void fused(Args a) {
  extern __shared__ __attribute__((aligned(1024))) char smem[];
  asm volatile("" ::"v"(smem));
  const unsigned long long pstart = __builtin_amdgcn_s_memrealtime();
  const int bx = blockIdx.x & 7, bi = blockIdx.x >> 3;
  if (bi < CODEWARM && threadIdx.x < 64) {
    unsigned long long pc;
    asm volatile("s_getpc_b64 %0" : "=s"(pc));
    touch(RSRC(pc & ~127ull, CODEWARM * 8192), 1024, threadIdx.x * 128, bi * 8192);
  }
  const int pm = 4 * (bx >> 1) + (bi >> 3), cb = 2 * (bi & 7) + (bx & 1);
  int mat, tm, tn;
  if (bx & 1) { mat = 2; tm = pm; tn = bi & 7; }
  else { mat = bx >> 2; tm = ((bx >> 1) & 1) * 8 + (bi >> 2); tn = bi & 3; }
  const int kx = (tm ^ tn) & K2_KXMASK;
  const u16* Pp = (mat == 0 ? a.wsr : mat == 1 ? a.wsi : a.wsp) + (size_t)tm * 128 * S;
  f32x4 z1v[2][2];
  const unsigned qbox = (mat * 4 + tn) * 16 + tm;
  bool allq = true;
  unsigned bv = 0;
  {
    const int kx1 = ((bi & 7) ^ (2 * (bi >> 3))) & K1_KXMASK;
    if (__builtin_expect(cb < 14, 1)) allq = phase1<true>(a, pm, cb, kx1, Pp, kx, qbox, z1v, a.bsp + 128 * tm, bv);
    else if (cb == 14) allq = phase1<false>(a, pm, cb, kx1, Pp, kx, qbox, z1v, a.bsp + 128 * tm, bv);
  }
  int tid = threadIdx.x;
  asm volatile("" : "+v"(tid));
  const int lane = tid & 63;
  const int wid = __builtin_amdgcn_readfirstlane(tid >> 6);
  STAMP_DECL STAMP(0);
  const int tm3 = bx * 4 + (bi >> 3), tn3 = bi & 7;
  const int fr = lane & 15, fq = lane >> 4;
  const unsigned vl = lane * 16;
  const rsrc_t rG = RSRC(a.ws + WS_G, G_END);
  const bool work2 = cb != 15;
  auto z1_finish = [&]() {
    if (cb < 14) {
      const int wm = wid & 3, wn = wid >> 2;
      const rsrc_t rZ = RSRC(a.ws + WS_Z1, FH * S * 2);
      const unsigned vZ = (fr >> 3) * (112 * 256) + (2 * ((fq & 1) ^ (wm & 1)) + (fq >> 1)) * 256 + (fr & 7) * 16;
      const unsigned sZ = ((pm * 8 + 2 * wm) * 112 + 8 * cb + 4 * wn) * 256;
#pragma unroll
      for (int m = 0; m < 2; ++m) {
        unsigned pk[2][2];
#pragma unroll
        for (int n = 0; n < 2; ++n) {
          const f32x2 g0 = gelu2(f32x2{z1v[m][n][0], z1v[m][n][1]}), g1 = gelu2(f32x2{z1v[m][n][2], z1v[m][n][3]});
          pk[n][0] = cvt_pk(g0[0], g0[1]); pk[n][1] = cvt_pk(g1[0], g1[1]);
        }
        auto w0 = __builtin_amdgcn_permlane16_swap(pk[0][0], pk[1][0], false, false);
        auto w1 = __builtin_amdgcn_permlane16_swap(pk[0][1], pk[1][1], false, false);
        st16_sc1(u32x4{w0[0], w1[0], w0[1], w1[1]}, rZ, vZ, sZ + 128 * (m ^ wn));
      }
    }
  };
  if (blockIdx.x == 255) {
    for (int i = 0; i < 8; ++i) st16_sc1(u32x4{0, 0, 0, 0}, rG, vl, (wid * 8 + i) * 1024);
    st16_sc1(u32x4{0, 0, 0, 0}, rG, vl, G_ALL);
    WAIT_V(0);
  }
  if (blockIdx.x == 255) BAR();
  if (wid >= 4) {
    const int lw = wid - 4;
    const int r8 = lane >> 3, p = lane & 7;
    if (!work2) {
      __builtin_amdgcn_s_setprio(3);
      if (wid == 7) {
        const unsigned role4 = (pm * 16 + 15) * 4;
        for (int i = 0; i < 14; ++i) st16_sc1(u32x4{0, 0, 0, 0}, rG, vl, G_PAY + pm * 14336 + i * 1024);
        const unsigned nonce = __builtin_amdgcn_readfirstlane(ld4_sc1(rG, 0, G_AN2 + role4)) + 1;
        st4_sc1(nonce, rG, 0, G_REQ + role4);
        for (unsigned spin = 0; spin < SPIN; ++spin) {
          st4_sc1(ld4_sc1(rG, fr * 4, G_REQ + pm * 64), rG, fr * 4, G_AN1 + pm * 64);
          if (spin == PF_PASS) BAR();
          if (spin >= PF_PASS && __all(fr > 13 || ld4_sc1(rG, fr * 1024, G_PAY + pm * 14336) != 0u)) break;
        }
        wait_go(rG, G_AN2 + role4, nonce, nonce - 1);
        st4_sc1(1u, rG, 0, G_ALL + role4);
      } else {
        BAR();
      }
      if (wid == 6 && blockIdx.x == 255) {
        for (unsigned spin = 0; spin < SPIN; ++spin) {
          const u32x4 rq = ld16_sc1(rG, vl, G_REQ);
          st16_sc1(rq, rG, vl, G_AN2);
          const u32x4 fl = ld16_sc1(rG, vl, G_ALL);
          if (__all((fl[0] & fl[1] & fl[2] & fl[3]) == 1u)) break;
        }
      }
    }
    const rsrc_t rP = RSRC(Pp, 128 * S * 2);
    const unsigned vo_e = (lw * 32 + r8) * (S * 2) + ((p ^ (r8 >> 1)) << 4);
    const unsigned vo_o = (vo_e ^ 64) + 8 * (S * 2);
    auto stageP = [&](int kt, unsigned poff) {
      const unsigned mb = poff + lw * 4096, so = (kt ^ kx) * 128;
#pragma unroll
      for (int j = 0; j < 4; ++j) dma16(rP, mb, j * 1024, (j & 1) ? vo_o : vo_e, so + (j >> 1) * 16 * (S * 2));
    };
    const rsrc_t rWo = RSRC(a.wout + 64 * tn3, (FH * D - 64 * tn3) * 2);
    const unsigned vw_e = (32 * lw + r8) * (D * 2) + ((p ^ (((r8 >> 1) & 1) << 1)) << 4), vw_o = (vw_e ^ 64) + 8 * (D * 2);
    u32x4 wreg[12];
    auto wpark = [&](int c) {
#pragma unroll
      for (int i = 4 * c; i < 4 * c + 4; ++i) {
        const int rg = i < 8 ? 2 + (i >> 2) : 6;
        ld16_async(wreg[i], rWo, (i & 1) ? vw_o : vw_e, (rg * 128 + ((i >> 1) & 1) * 16) * (D * 2));
      }
    };
    auto wput = [&](int rg) {
#pragma unroll
      for (int j = 0; j < 4; ++j) *(LAS u32x4*)(size_t)(wbase(rg) + lw * 4096 + j * 1024 + vl) = wreg[(rg == 6 ? 8 : (rg - 2) * 4) + j];
    };
    auto wdma = [&](int rg) {
#pragma unroll
      for (int j = 0; j < 4; ++j) dma16(rWo, lw * 4096, wbase(rg) + j * 1024, (j & 1) ? vw_o : vw_e, (rg * 128 + (j >> 1) * 16) * (D * 2));
    };
    if (work2) {
      u32x4 preg[8];
#pragma unroll
      for (int i = 0; i < 8; ++i) ld16_async(preg[i], rP, (i & 1) ? vo_o : vo_e, ((4 + (i >> 2)) ^ kx) * 128 + ((i >> 1) & 1) * 16 * (S * 2));
      wpark(0); wpark(1); wpark(2);
      if (wid != 7) z1_finish();
      else {
        for (unsigned spin = 0; spin < SPIN; ++spin) {
          const unsigned fl = ld4_sc1(rG, (lane & 31) * 4, G_F1 + qbox * 128);
          const bool ok = fl == 1u || (qbox >= 112 && qbox < 128 && (lane & 1));
          allq = __all(ok);
          if (__all(ok || (WINSTART && (lane & 31) > 3))) break;
        }
      }
      WL0();
      BAR();
      STAMP(1);
      const rsrc_t rQ = RSRC(a.ws + WS_Q + (size_t)(mat * 4 + tn) * 128 * S * 2, 128 * S * 2);
      const unsigned vq = vl + lw * 131072;
      auto stageQ = [&](int kt) {
        const unsigned mb = (kt & 3) * 16384 + lw * 4096, so = (kt ^ kx) * 2048;
#pragma unroll
        for (int j = 0; j < 4; ++j) dma16_sc1(rQ, mb, P2_QB + j * 1024, vq, so + (j >> 1) * 65536 + (j & 1) * 1024);
      };
      stageQ(0); stageQ(1);
      if (wid == 7) z1_finish();
      WAIT_V(4); BAR();
      stageQ(2); stageQ(3);
#pragma unroll
      for (int i = 0; i < 8; ++i) *(LAS u32x4*)(size_t)((4 + (i >> 2)) * 16384 + lw * 4096 + (i & 3) * 1024 + vl) = preg[i];
      WL0();
      for (unsigned spin = 0; !allq && spin < SPIN; ++spin)
        allq = __all(ld4_sc1(rG, (lane & 31) * 4, G_F1 + qbox * 128) == 1u || (qbox >= 112 && qbox < 128 && (lane & 1)));
      WAIT_V(8); BAR(); stageQ(4); stageP(6, 0);
      WAIT_V(12); BAR(); stageQ(5); stageP(7, 16384);
      WAIT_V(16); BAR(); stageQ(6); stageP(8, 2 * 16384);
      unsigned poff = 3 * 16384;
      unsigned zf;
      for (int kt = 3; kt < 23; ++kt) {
        WAIT_V(20);
        BAR();
        if (kt == 3 && wid == 7 && cb < 14) st4_sc1(1u, rG, 0, G_FZ + (pm * 8 + (cb >> 1)) * 128 + (cb & 1) * 4);
        stageQ(kt + 4); stageP(kt + 6, poff);
        poff = poff == 5 * 16384 ? 0 : poff + 16384;
      }
      asm volatile("buffer_load_dword %0, %1, %2, %3 offen sc1" : "=v"(zf) : "v"((lane & 1) * 4), "s"(rG), "s"(G_FZ + (tm * 8 + tn) * 128) : "memory");
#pragma unroll
      for (int kt = 23; kt < 26; ++kt) {
        WAIT_V(20);
        BAR();
        stageQ(kt + 4); stageP(kt + 6, (kt % 6) * 16384);
      }
      asm volatile("s_waitcnt vmcnt(20)" : "+v"(zf)::"memory");
      if (mat == 2) {
        const rsrc_t rZ = RSRC(a.ws + WS_Z1 + (size_t)((8 * tm + lw) * 112 + 16 * tn) * 256, 4 * 112 * 256 + 4096);
        auto stageZ = [&](int h) {
#pragma unroll
          for (int j = 0; j < 4; ++j) dma16_sc1(rZ, lw * 4096, P2_Z + h * 16384 + j * 1024, vl, h * (4 * 112 * 256) + j * 1024);
        };
        for (unsigned spin = 0; !__all(zf == 1u) && spin < SPIN; ++spin) zf = ld4_sc1(rG, (lane & 1) * 4, G_FZ + (tm * 8 + tn) * 128);
        WAIT_V(20); BAR(); stageQ(30); stageZ(0);
        WAIT_V(20); BAR(); stageQ(31); stageZ(1);
        WAIT_V(20); BAR(); wdma(0); wdma(4);
        WAIT_V(20); BAR(); wdma(1); wdma(5);
        WAIT_V(20); BAR(); wput(2); wput(6);
        WAIT_V(16); BAR(); wput(3);
      } else {
        WAIT_V(20); BAR(); stageQ(30);
        WAIT_V(16); BAR(); stageQ(31);
        WAIT_V(12); BAR(); wdma(0); wdma(4);
        WAIT_V(12); BAR(); wdma(1); wdma(5);
        WAIT_V(16); BAR(); wput(2); wput(6);
        BAR(); wput(3);
      }
    } else {
      STAMP(1);
      wdma(0); wdma(4); wdma(1); wdma(5); wdma(2); wdma(3); wdma(6);
      __builtin_amdgcn_s_setprio(0);
    }
    STAMP(2); STAMP(3);
    WAIT_V(0);
  } else if (work2) {
    const int wp = wid >> 1, wq = wid & 1;
    f32x4 acc[4][4];
    const int sw = (fr >> 1) & 7;
    const unsigned ra = (wp * 64 + fr) * 128 + ((fq ^ sw) << 4);
    const unsigned rb = P2_QB + wq * 8192 + vl;
    bf16x8 a0[4], b0[4], a1[4], b1[4];
    unsigned poff = 0;
    unsigned pa0 = ra, pa1 = pa0 ^ 64;
#define K2_RD(AF, BF, pa, sl, ks, i) do { \
    if ((i) == 0) AF[0] = ld_frag(pa); \
    else if ((i) <= 4) BF[(i) - 1] = ld_frag(rb + (sl) * 16384 + ((i) - 1) * 2048 + (ks) * 1024); \
    else AF[(i) - 4] = ld_frag(pa + ((i) - 4) * 2048); } while (0)
#define K2_MM(AF, BF, i) acc[(i) >> 2][(i) & 3] = MFMA16(BF[(i) & 3], AF[(i) >> 2], acc[(i) >> 2][(i) & 3])
#define K2_STEP(s, LAST) do { \
    SCHED(); WL0(); SCHED(); \
    _Pragma("unroll") for (int i = 0; i < 16; ++i) { K2_MM(a0, b0, i); SCHED(); if (i < 8) K2_RD(a1, b1, pa1, s, 1, i); \
      if (i == 10) { poff = poff == 5 * 16384 ? 0 : poff + 16384; pa0 = ra + poff; } SCHED(); } \
    WL0(); \
    if ((s) == 3) WAIT_V(0); \
    BAR(); \
    SCHED(); \
    _Pragma("unroll") for (int i = 0; i < 16; ++i) { K2_MM(a1, b1, i); SCHED(); if (!(LAST) && i < 8) K2_RD(a0, b0, pa0, ((s) + 1) & 3, 0, i); \
      if (i == 10) pa1 = pa0 ^ 64; SCHED(); } \
  } while (0)
    z1_finish();
    WL0();
    BAR();
    if (cb < 14) WAIT_V(2); else WAIT_V(0);
#pragma unroll
    for (int ni = 0; ni < 4; ++ni) {
      const float bias = mat == 2 ? __uint_as_float((unsigned)__shfl(bv, 16 * ni + fr) << 16) : 0.0f;
#pragma unroll
      for (int n = 0; n < 4; ++n) acc[ni][n] = f32x4{bias, bias, bias, bias};
    }
    BAR();
    STAMP(1);
#pragma unroll
    for (int i = 0; i < 8; ++i) K2_RD(a0, b0, pa0, 0, 0, i);
    for (int kt = 0; kt < 28; kt += 4) {
      K2_STEP(0, 0); K2_STEP(1, 0); K2_STEP(2, 0); K2_STEP(3, 0);
    }
    K2_STEP(0, 0); K2_STEP(1, 0); K2_STEP(2, 0); K2_STEP(3, 1);
    SCHED();
    STAMP(2);
    if (mat < 2) {
      const rsrc_t rY = RSRC(a.ws + WS_Y + (size_t)mat * S * D * 2, S * D * 2);
      const unsigned so = ((8 * tm + 4 * wp) * 64 + 16 * tn + 8 * wq) * 256;
#pragma unroll
      for (int ni = 0; ni < 4; ++ni)
#pragma unroll
        for (int P = 0; P < 2; ++P) {
          const f32x4 c0 = acc[ni][2 * P], c1 = acc[ni][2 * P + 1];
          const u32x4 v = {cvt_pk(c0[0], c0[1]), cvt_pk(c0[2], c0[3]), cvt_pk(c1[0], c1[1]), cvt_pk(c1[2], c1[3])};
          st16_sc1(v, rY, vl, so + (ni * 64 + 4 * P) * 256);
        }
    } else {
      const rsrc_t rH = RSRC(a.ws + WS_H, S * FH * 2);
      const unsigned so = ((8 * tm + 4 * wp) * 112 + 16 * tn + 8 * wq) * 256;
      const unsigned za = P2_Z + wp * 16384 + wq * 2048 + (fq * 16 + ((fr & 4) << 1) + ((fr & 8) >> 1) + (fr & 3)) * 16;
#pragma unroll
      for (int ni = 0; ni < 4; ++ni) {
#pragma unroll
        for (int P = 0; P < 2; ++P) {
          const f32x4 c0 = acc[ni][2 * P], c1 = acc[ni][2 * P + 1];
          const u32x4 z = *(const LAS u32x4*)(size_t)(za + ni * 4096 + P * 1024);
          const u32x4 v = {cvt_pk(bf_lo(z[0]) * c0[0], bf_hi(z[0]) * c0[1]), cvt_pk(bf_lo(z[1]) * c0[2], bf_hi(z[1]) * c0[3]),
                           cvt_pk(bf_lo(z[2]) * c1[0], bf_hi(z[2]) * c1[1]), cvt_pk(bf_lo(z[3]) * c1[2], bf_hi(z[3]) * c1[3])};
          st16_sc1(v, rH, vl, so + (ni * 112 + 4 * P) * 256);
        }
      }
    }
    STAMP(3);
    WAIT_V(0);
  } else {
    if (PF_EARLY && blockIdx.x != 255) { warm_window(a, P2_Z, pm, 8, wid, lane); }
    BAR();
    STAMP(1);
    for (int i = 0; i < PF_N && blockIdx.x != 255; ++i) {
      warm_window(a, P2_Z, pm, (PF_LIST >> (4 * i)) & 15, wid, lane);
      WAIT_V(6);
    }
    STAMP(2);
    WAIT_V(0);
    STAMP(3);
  }
  WL0();
  BAR();
  STAMP(4);
  {
    const int wr = (wid + tn3) & 7, tp = wr >> 2, kq = wr & 3;
    const int tt3 = 4 * tm3 + 2 * tp + (kq & 1);
    const int cq = 2 * (fq & 1) + (fq >> 1);
    const int c3 = 8 * tn3 + 2 * (kq & 2);
    if (wid == 4 && work2) {
      const unsigned box = mat == 2 ? fr : (fr & 8) + 2 * tn + (fr & 1);
      st4_sc1(1u, rG, box * 128, G_FP + tm * 2048 + (mat == 2 ? tn : 8 + mat) * 4);
    }
    const int q4 = fr >> 2, pp = lane & 3;
    const int sp = ((q4 >> 1) & 1) | ((fq & 1) << 1);
    const unsigned wa = kq * 4096 + 128 * (8 * fq + q4) + 16 * ((2 * (kq & 2) + (pp >> 1)) ^ (sp << 1)) + 8 * (pp & 1);
    bf16x8 wf[7][4];
    auto ld_w = [&]() {
#pragma unroll
      for (int mi = 0; mi < 4; ++mi) {
        unsigned lo = wa ^ (mi * 32), mid = lo + 65536, hi = lo + 131072;
        OPAQUE(mid); OPAQUE(hi);
#pragma unroll
        for (int g = 0; g < 7; ++g) wf[g][mi] = ld_tr((wbase(g) < 65536 ? lo : wbase(g) < 131072 ? mid : hi) + (wbase(g) & 65535));
      }
    };
    if (wid == 4) {
      for (unsigned spin = 0; spin < SPIN; ++spin) {
        const unsigned fl = ld4_sc1(rG, fr * 4, G_FP + (tm3 * 8 + tn3) * 128);
        if (__all(fl == 1u || fr == 7 || fr > 9)) break;
      }
    } else {
      ld_w();
    }
    BAR();
    STAMP(5);
    const rsrc_t rH = RSRC(a.ws + WS_H + (size_t)((4 * tm3 + 2 * tp) * 112 + 4 * kq) * 256, 2 * 112 * 256);
    const rsrc_t rY = RSRC(a.ws + WS_Y + (size_t)(tt3 * 64 + c3) * 256, S * D * 2 + 1024);
    const unsigned vh0 = vl + (kq & 1) * (112 * 256), vh1 = vl + ((kq & 1) ^ 1) * (112 * 256);
    u32x4 hf[7][2];
#pragma unroll
    for (int g = 0; g < 7; ++g) {
      hf[g][0] = __builtin_amdgcn_raw_buffer_load_b128(rH, vh0, g * 4096, 16);
      hf[g][1] = __builtin_amdgcn_raw_buffer_load_b128(rH, vh1, g * 4096, 16);
    }
    const unsigned vy = cq * 256 + fr * 16;
    const u32x4 e1 = __builtin_amdgcn_raw_buffer_load_b128(rY, vy, 0, 16), e2 = __builtin_amdgcn_raw_buffer_load_b128(rY, vy, S * D * 2, 16);
    const u32x4 ex = *(const u32x4*)(a.x + (size_t)(tt3 * 16 + fr) * D + (c3 + cq) * 8);
    SCHED();
    if (wid == 4) ld_w();
    f32x4 acc[2][4];
#pragma unroll
    for (int g = 0; g < 7; ++g)
#pragma unroll
      for (int tl = 0; tl < 2; ++tl)
#pragma unroll
        for (int mi = 0; mi < 4; ++mi) acc[tl][mi] = MFMA16(wf[g][mi], __builtin_bit_cast(bf16x8, hf[g][tl]), (g ? acc[tl][mi] : f32x4{0, 0, 0, 0}));
    STAMP(6);
    auto xb = [&](int role, int d, int j) { return (LAS f32x4*)(size_t)((d < 3 ? P2_Z + (d - 1) * 16384 : P3_X3) + (role * 2 + j) * 1024 + vl); };
#pragma unroll
    for (int d = 1; d < 4; ++d) {
      *xb(wr, d, 0) = acc[d & 1][d & 2];
      *xb(wr, d, 1) = acc[d & 1][(d & 2) + 1];
    }
    WL0();
    BAR();
    f32x4 s0 = acc[0][0], s1 = acc[0][1];
#pragma unroll
    for (int d = 1; d < 4; ++d) {
      s0 += *xb(wr ^ d, d, 0);
      s1 += *xb(wr ^ d, d, 1);
    }
    f32x4 c0, c1;
#pragma unroll
    for (int j = 0; j < 4; ++j) {
      auto w = __builtin_amdgcn_permlane16_swap(__float_as_uint(s0[j]), __float_as_uint(s1[j]), false, false);
      c0[j] = __uint_as_float(w[0]); c1[j] = __uint_as_float(w[1]);
    }
    u32x4 o;
#pragma unroll
    for (int k = 0; k < 4; ++k) {
      const f32x4 c = (k >> 1) ? c1 : c0;
      const float lo = bf_lo(ex[k]) + bf_lo(e1[k]) - bf_lo(e2[k]) + c[(k & 1) * 2];
      const float hi = bf_hi(ex[k]) + bf_hi(e1[k]) - bf_hi(e2[k]) + c[(k & 1) * 2 + 1];
      o[k] = cvt_pk(lo, hi);
    }
    st16_sc1(o, RSRC(a.out, S * D * 2), ((tt3 * 16 + fr) * D + (c3 + cq) * 8) * 2, 0);
  }
  STAMP(7);
  STAMP_OUT(a.ws, 1, 8);
  {
    asm volatile("s_waitcnt vmcnt(0)" ::: "memory");
    const unsigned long long pend = __builtin_amdgcn_s_memrealtime();
    const rsrc_t rS = RSRC(a.ws + (100u << 20), 65536);
    st16_sc1(u32x4{(unsigned)pstart, (unsigned)pend, 0, 0}, rS, 0, (blockIdx.x * 8 + wid) * 16);
    if (blockIdx.x == 73 && wid == 0) {
      while (__builtin_amdgcn_s_memrealtime() < pstart + 6000) __builtin_amdgcn_s_sleep(1);
      int mns = 1 << 30, mxe = -(1 << 30);
      for (int i = 0; i < 32; ++i) {
        const u32x4 v = ld16_sc1(rS, lane * 16, i * 1024);
        const int st = (int)(v[0] - (unsigned)pstart), en = (int)(v[1] - (unsigned)pstart);
        mns = min(mns, st); mxe = max(mxe, en);
      }
      for (int o = 32; o > 0; o >>= 1) { mns = min(mns, __shfl_xor(mns, o)); mxe = max(mxe, __shfl_xor(mxe, o)); }
      const int q = __builtin_amdgcn_readfirstlane(mxe - mns);
      const unsigned long long tend = pstart + __builtin_amdgcn_readfirstlane(mns) + 10 * q;
      while (__builtin_amdgcn_s_memrealtime() < tend) ;
    }
  }
}

extern "C" void kernel_launch(void* const* d_in, const int* in_sizes, int n_in, void* d_out, int out_size, void* d_ws, size_t ws_size,
                              hipStream_t stream) {
  (void)in_sizes; (void)n_in; (void)out_size; (void)ws_size;
  (void)hipFuncSetAttribute((const void*)fused, hipFuncAttributeMaxDynamicSharedMemorySize, LDS_BYTES);
  Args a{(const u16*)d_in[0], (const u16*)d_in[3], (const u16*)d_in[4], (const u16*)d_in[6], (const u16*)d_in[9],
         (const u16*)d_in[1], (const u16*)d_in[2], (const u16*)d_in[7], (const u16*)d_in[8], (char*)d_ws, (u16*)d_out};
  fused<<<256, 512, LDS_BYTES, stream>>>(a);
}
```

```cpp
#define PROBE 2
#define PBLK 73
#include <hip/hip_runtime.h>
#include <hip/hip_bf16.h>
#include <stdint.h>

typedef unsigned short u16;
typedef short bf16x8 __attribute__((ext_vector_type(8)));
typedef short s16x4 __attribute__((ext_vector_type(4)));
typedef float f32x4 __attribute__((ext_vector_type(4)));
typedef float f32x2 __attribute__((ext_vector_type(2)));
typedef unsigned u32x4 __attribute__((ext_vector_type(4)));
typedef __bf16 bf16x2v __attribute__((ext_vector_type(2)));
typedef __amdgpu_buffer_rsrc_t rsrc_t;
#define LAS __attribute__((address_space(3)))
#define WAIT_V(n) asm volatile("s_waitcnt vmcnt(%0)" ::"n"(n) : "memory")
#define WL0() __builtin_amdgcn_s_waitcnt(0xC07F)
#define BAR() do { asm volatile("" ::: "memory"); __builtin_amdgcn_s_barrier(); asm volatile("" ::: "memory"); } while (0)
#define SCHED() __builtin_amdgcn_sched_barrier(0)
#define MFMA16(a, b, c) __builtin_amdgcn_mfma_f32_16x16x32_bf16(a, b, c, 0, 0, 0)
#define RSRC(p, n) __builtin_amdgcn_make_buffer_rsrc((void*)(p), 0, (unsigned)(n), 0x00020000)
#define OPAQUE(x) asm volatile("" : "+v"(x))

#define STAMP_DECL
#define STAMP(i) do { if (KID * 16 + (i) == PROBE) asm volatile("s_memrealtime %0\n\ts_waitcnt lgkmcnt(0)" : "=s"(ptime) :: "memory"); } while (0)
#define STAMP_OUT(ws, kid, n)
#define K2_KXMASK 3
#define K1_KXMASK 3
#define DWARM 0
#define CODEWARM 6
#define NOPARK 0
#define BULKLATE 0
#define ZNB 0
#define WPARK_LATE 1
#define DW 2
#define PF_EARLY 0
#define B10_AT 3
#define B11_AT 10
#define PF_PASS 3
#define GATHER_AT 24
#define WINSTART 1
#define PF_LIST 0x75810u
#define PF_N 5

constexpr int S = 2048, D = 512, F = 1792, FH = 896;
constexpr size_t WS_Q = 0;
constexpr size_t WS_Z1 = WS_Q + (size_t)(2 * D + FH) * S * 2;
constexpr size_t WS_H = WS_Z1 + (size_t)FH * S * 2;
constexpr size_t WS_Y = WS_H + (size_t)FH * S * 2;
constexpr size_t WS_G = WS_Y + (size_t)2 * D * S * 2;
constexpr unsigned G_F1 = 0;
constexpr unsigned G_FP = 32768;
constexpr unsigned G_FZ = 32768 + 64;
constexpr unsigned G_ALL = 65536;
constexpr unsigned G_REQ = G_ALL + 1024;
constexpr unsigned G_AN1 = G_REQ + 1024;
constexpr unsigned G_AN2 = G_AN1 + 1024;
constexpr unsigned G_PAY = G_AN2 + 1024;
constexpr unsigned G_END = G_PAY + 16 * 14336;

constexpr int LDS_BYTES = 163840;
constexpr int P1_RING = 65536, P1_SCR = 65536, P1_GAT = 77824, P1_W2 = 98304;
constexpr int P2_QB = 98304, P2_Z = 32768, P3_X3 = 147456;
__device__ constexpr int wbase(int rg) { return rg < 2 ? 65536 + rg * 16384 : rg < 4 ? (rg - 2) * 16384 : 98304 + (rg - 4) * 16384; }
constexpr unsigned SPIN = 1u << 20;

struct Args {
  const u16* x; const u16* wdr; const u16* wdi; const u16* win; const u16* wout;
  const u16* wsr; const u16* wsi; const u16* wsp; const u16* bsp;
  char* ws; u16* out;
};

__device__ __forceinline__ unsigned cvt_pk(float lo, float hi) {
  f32x2 v = {lo, hi};
  return __builtin_bit_cast(unsigned, __builtin_convertvector(v, bf16x2v));
}
__device__ __forceinline__ float bf_lo(unsigned u) { return __uint_as_float(u << 16); }
__device__ __forceinline__ float bf_hi(unsigned u) { return __uint_as_float(u & 0xffff0000u); }
constexpr float GC1 = -2.0f * 0.7978845608028654f * 1.4426950408889634f, GC2 = GC1 * 0.044715f;
__device__ __forceinline__ f32x2 gelu2(f32x2 v) {
  const f32x2 t = v * ((v * v) * GC2 + GC1);
  const f32x2 e = f32x2{__builtin_amdgcn_exp2f(t[0]), __builtin_amdgcn_exp2f(t[1])} + 1.0f;
  return v * f32x2{__builtin_amdgcn_rcpf(e[0]), __builtin_amdgcn_rcpf(e[1])};
}
__device__ __forceinline__ void dot_sq(float& c, unsigned a) { asm volatile("v_dot2c_f32_bf16 %0, %1, %1" : "+v"(c) : "v"(a)); }
__device__ __forceinline__ void dot_sum(float& c, unsigned a) { asm volatile("v_dot2c_f32_bf16 %0, 0x3f803f80, %1" : "+v"(c) : "v"(a)); }
__device__ __forceinline__ float sum_rows(float v) {
  auto a = __builtin_amdgcn_permlane32_swap(__float_as_uint(v), __float_as_uint(v), false, false);
  v = __uint_as_float(a[0]) + __uint_as_float(a[1]);
  auto b = __builtin_amdgcn_permlane16_swap(__float_as_uint(v), __float_as_uint(v), false, false);
  return __uint_as_float(b[0]) + __uint_as_float(b[1]);
}
__device__ __forceinline__ float sel4(f32x4 v, int i) { const float a = (i & 1) ? v[1] : v[0], b = (i & 1) ? v[3] : v[2]; return (i & 2) ? b : a; }

__device__ __forceinline__ void dma16(rsrc_t r, unsigned mbase, int mimm, unsigned voff, unsigned soff) {
  asm volatile("s_add_i32 m0, %0, %1\n\ts_nop 0\n\tbuffer_load_dwordx4 %2, %3, %4 offen lds" ::"s"(mbase), "n"(mimm), "v"(voff), "s"(r), "s"(soff) : "scc");
}
__device__ __forceinline__ void dma16_sc1(rsrc_t r, unsigned mbase, int mimm, unsigned voff, unsigned soff) {
  asm volatile("s_add_i32 m0, %0, %1\n\ts_nop 0\n\tbuffer_load_dwordx4 %2, %3, %4 offen sc1 lds" ::"s"(mbase), "n"(mimm), "v"(voff), "s"(r), "s"(soff) : "scc");
}
__device__ __forceinline__ void dma4_sc1(rsrc_t r, int ldsaddr, unsigned voff, unsigned soff) {
  asm volatile("s_mov_b32 m0, %0\n\ts_nop 0\n\tbuffer_load_dword %1, %2, %3 offen sc1 lds" ::"n"(ldsaddr), "v"(voff), "s"(r), "s"(soff) : "memory");
}
__device__ __forceinline__ void touch(rsrc_t r, int junk, unsigned voff, unsigned soff) {
  asm volatile("s_mov_b32 m0, %0\n\ts_nop 0\n\tbuffer_load_dword %1, %2, %3 offen lds" ::"n"(junk), "v"(voff), "s"(r), "s"(soff) : "memory");
}
__device__ __forceinline__ void st16_sc1(u32x4 v, rsrc_t r, unsigned voff, unsigned soff) {
  asm volatile("buffer_store_dwordx4 %0, %1, %2, %3 offen sc1\n\ts_nop 1" ::"v"(v), "v"(voff), "s"(r), "s"(soff) : "memory");
}
__device__ __forceinline__ void st4_sc1(unsigned v, rsrc_t r, unsigned voff, unsigned soff) {
  asm volatile("buffer_store_dword %0, %1, %2, %3 offen sc1" ::"v"(v), "v"(voff), "s"(r), "s"(soff) : "memory");
}
__device__ __forceinline__ unsigned ld4_sc1(rsrc_t r, unsigned voff, unsigned soff) {
  unsigned v;
  asm volatile("buffer_load_dword %0, %1, %2, %3 offen sc1\n\ts_waitcnt vmcnt(0)" : "=&v"(v) : "v"(voff), "s"(r), "s"(soff) : "memory");
  return v;
}
__device__ __forceinline__ u32x4 ld16_sc1(rsrc_t r, unsigned voff, unsigned soff) {
  u32x4 v;
  asm volatile("buffer_load_dwordx4 %0, %1, %2, %3 offen sc1\n\ts_waitcnt vmcnt(0)" : "=&v"(v) : "v"(voff), "s"(r), "s"(soff) : "memory");
  return v;
}
__device__ __forceinline__ void ld16_async(u32x4& v, rsrc_t r, unsigned voff, unsigned soff) {
  asm volatile("buffer_load_dwordx4 %0, %1, %2, %3 offen" : "=v"(v) : "v"(voff), "s"(r), "s"(soff) : "memory");
}
__device__ __forceinline__ void pin(unsigned& v) { OPAQUE(v); }
__device__ __forceinline__ void wait_vn(int n) { WAIT_V(n); }
__device__ __forceinline__ bf16x8 ld_tr(unsigned addr) {
  s16x4 lo = __builtin_amdgcn_ds_read_tr16_b64_v4i16((LAS s16x4*)(size_t)(addr));
  s16x4 hi = __builtin_amdgcn_ds_read_tr16_b64_v4i16((LAS s16x4*)(size_t)(addr + 512));
  return __builtin_shufflevector(lo, hi, 0, 1, 2, 3, 4, 5, 6, 7);
}
__device__ __forceinline__ bf16x8 ld_frag(unsigned addr) { return *(const LAS bf16x8*)(size_t)(addr); }
__device__ __forceinline__ void wait_go(rsrc_t rG, unsigned word, unsigned nonce, unsigned seen) {
  for (unsigned spin = 0; seen != nonce && spin < SPIN; ++spin) {
    seen = __builtin_amdgcn_readfirstlane(ld4_sc1(rG, 0, word));
    __builtin_amdgcn_s_sleep(1);
  }
}
__device__ __forceinline__ unsigned lds_word(unsigned addr) { return __builtin_amdgcn_readfirstlane(*(LAS unsigned*)(size_t)(addr)); }

__device__ __forceinline__ void warm_window(const Args& a, int junk, int rb, int w, int wv, int lane) {
  if (w < 8) {
    const unsigned vo = (rb * 128 + wv * 32 + (lane >> 2)) * (S * 2) + (lane & 3) * 128;
#pragma unroll
    for (int m = 0; m < 3; ++m) {
      const rsrc_t r = RSRC(m == 0 ? a.wsr : m == 1 ? a.wsi : a.wsp, S * S * 2);
      touch(r, junk, vo, w * 512);
      touch(r, junk, vo, w * 512 + 16 * (S * 2));
    }
  } else {
    const rsrc_t r = RSRC(a.wout + (size_t)rb * 56 * D, 56 * D * 2);
    touch(r, junk, lane * 128, wv * 16384);
    touch(r, junk, lane * 128, wv * 16384 + 8192);
  }
}

template <bool ZB>
__device__ __forceinline__ bool phase1(const Args& a, int pm, int cb, int kx, const u16* Pp, int kx2, unsigned qbox, f32x4 (&z1v)[2][2], const u16* bp, unsigned& bv, unsigned long long& ptime) {
  constexpr int KID = 0;
  const int tid = threadIdx.x, lane = tid & 63;
  const int wid = __builtin_amdgcn_readfirstlane(tid >> 6);
  const int wm = wid & 3, wn = wid >> 2;
  STAMP_DECL STAMP(0);
  const rsrc_t rX = RSRC(a.x + (size_t)pm * 128 * D, 128 * D * 2);
  const rsrc_t rW = ZB ? RSRC(a.win + 64 * cb, (D * F - 64 * cb) * 2) : RSRC(a.wdi + (size_t)384 * D, 64 * D * 2);
  const int r8 = lane >> 3, p8 = lane & 7;
  const unsigned sw_e = (p8 ^ (r8 >> 1)) << 4;
  const unsigned vx_e = (32 * (wid >> 1) + 4 * (wid & 1) + 8 * (r8 >> 2) + (r8 & 3)) * (D * 2) + sw_e;
  const unsigned vx_o = (vx_e ^ 64) + 16 * (D * 2);
  const unsigned vw1 = ZB ? (8 * wid + r8) * (F * 2) + ((p8 ^ ((((r8 >> 1) & 1) | ((wid & 1) << 1)) << 1)) << 4) + FH * 2
                          : (8 * wid + r8) * (D * 2) + (sw_e ^ ((wid & 1) << 6));
  constexpr unsigned WKB = ZB ? 64 * F * 2 : 128;
  const unsigned mb1 = wid * 1024, mb2 = wid * 2048;
  auto piece1 = [&](int st, int p) {
    const unsigned kb = st ^ kx;
    const int base = P1_RING + (st % 3) * 24576;
    if (p == 0) dma16(rX, mb2, base, vx_e, kb * 128);
    else if (p == 1) dma16(rW, mb1, base + 16384, vw1, kb * WKB);
    else dma16(rX, mb2, base + 1024, vx_o, kb * 128);
  };
  piece1(0, 0); piece1(0, 1); piece1(0, 2);
  const rsrc_t rG = RSRC(a.ws + WS_G, G_END);
  const unsigned role4 = (pm * 16 + cb) * 4;
  if (wid == 7) dma4_sc1(rG, 0, (lane & 1) * 1024, G_AN1 + role4);
  SCHED();

  const int fr = lane & 15, fq = lane >> 4;
  const int kh = wm >> 1;
  const unsigned fsw = ((fq ^ ((fr >> 1) & 7)) << 4) ^ (kh << 6);
  unsigned xa[2][2], wb[2][2];
#pragma unroll
  for (int m = 0; m < 2; ++m) { xa[m][0] = P1_RING + (32 * wm + 16 * (m ^ wn) + fr) * 128 + fsw; xa[m][1] = xa[m][0] ^ 64; }
  const int q4 = fr >> 2, pp = lane & 3;
  const int sp = ((q4 >> 1) & 1) | ((fq & 1) << 1);
  const unsigned trl = 128 * (8 * fq + q4) + 16 * ((pp >> 1) ^ (sp << 1)) + 8 * (pp & 1);
#pragma unroll
  for (int n = 0; n < 2; ++n) {
    const int tile = 2 * wn + (n ^ (wm & 1));
    if (ZB) { wb[n][0] = P1_RING + 16384 + (trl ^ (tile * 32)) + kh * 4096; wb[n][1] = wb[n][0] ^ 4096; }
    else { wb[n][0] = P1_RING + 16384 + (16 * tile + fr) * 128 + fsw; wb[n][1] = wb[n][0] ^ 64; }
  }
#pragma unroll
  for (int i = 0; i < 2; ++i)
#pragma unroll
    for (int ks = 0; ks < 2; ++ks) { OPAQUE(xa[i][ks]); OPAQUE(wb[i][ks]); }
  SCHED();
  piece1(1, 0); piece1(1, 1); piece1(1, 2);
  unsigned nonce = 0;
  STAMP(1);

  bf16x8 xf[16][2], wf[2][2];
  const f32x4 Z4 = {0, 0, 0, 0};
  f32x4 acc1[2][2] = {{Z4, Z4}, {Z4, Z4}};
  f32x4 gram = Z4, rsum = Z4, cs1 = Z4;
  const bf16x8 ONES = {0x3f80, 0x3f80, 0x3f80, 0x3f80, 0x3f80, 0x3f80, 0x3f80, 0x3f80};

  auto mm = [&](int kk, int i) {
    const int ks = kk & 1;
    if (i == 0) gram = MFMA16(xf[kk][0], xf[kk][0], gram);
    else if (i == 1) rsum = MFMA16(xf[kk][0], ONES, rsum);
    else if (i < 6) { const int m = (i - 2) >> 1, n = i & 1; acc1[m][n] = MFMA16(xf[kk][m], wf[ks][n], acc1[m][n]); }
    else cs1 = MFMA16(ONES, wf[0][0], cs1);
  };
  auto rd = [&](int kk, int i) {
    const int ks = kk & 1, so = ((kk >> 1) % 3) * 24576;
    if (i == 0 || i == 3) xf[kk][i & 1] = ld_frag(xa[i & 1][ks] + so);
    else wf[ks][i - 1] = ZB ? ld_tr(wb[i - 1][ks] + so) : ld_frag(wb[i - 1][ks] + so);
  };
  auto pieceT = [&](int p) {
    const rsrc_t rD = RSRC(ZB ? (cb < 8 ? a.wdr : a.wdi) + (size_t)(cb & 7) * 64 * D : a.wdi + (size_t)448 * D, 64 * D * 2);
    const unsigned ve = r8 * (D * 2) + sw_e, vo = (ve ^ 64) + 8 * (D * 2);
    dma16(rD, wid * 8192, p * 1024, (p & 1) ? vo : ve, (wid ^ kx) * 128 + (p >> 1) * 16 * (D * 2));
  };
  auto pieceW2 = [&](int p) {
    const unsigned ve = r8 * (F * 2) + ((p8 ^ (((r8 >> 1) & 1) << 1)) << 4), vo = (ve ^ 64) + 8 * (F * 2);
    dma16(rW, wid * 8192, P1_W2 + p * 1024, (p & 1) ? vo : ve, (wid ^ kx) * WKB + (p >> 1) * 16 * (F * 2));
  };
#define PIN_ACC() asm volatile("" : "+v"(acc1[0][0]), "+v"(acc1[0][1]), "+v"(acc1[1][0]), "+v"(acc1[1][1]), "+v"(gram), "+v"(rsum), "+v"(cs1))
#pragma unroll
  for (int st = 0; st < 8; ++st) {
    wait_vn(st <= 6 ? 3 : 4);
    WL0();
    BAR();
    if (st == 0) STAMP(2);
    SCHED();
#pragma unroll
    for (int i = 0; i < 6; ++i) {
      if (st > 0) mm(2 * st - 1, i);
      SCHED();
      if (i < 4) rd(2 * st, i);
      if (st + 2 < 8) { if (i == 1) piece1(st + 2, 0); if (i == 5) piece1(st + 2, 1); }
      else if (i == 1 || i == 4) pieceT((st - 6) * 4 + (i >> 2));
      SCHED();
    }
    PIN_ACC();
    WL0();
    SCHED();
#pragma unroll
    for (int i = 0; i < 7; ++i) {
      mm(2 * st, i);
      SCHED();
      if (i < 4) rd(2 * st + 1, i);
      if (st + 2 < 8) { if (i == 4) piece1(st + 2, 2); }
      else if (i == 1 || i == 5) pieceT((st - 6) * 4 + 2 + (i >> 2));
      SCHED();
    }
    PIN_ACC();
    if (st == 0 && wid == 7) {
      const unsigned a1 = lds_word(0), a2 = lds_word(4);
      nonce = a1 + 1 == a2 ? a1 + 2 : a1 + 1;
      st4_sc1(nonce, rG, 0, G_REQ + role4);
    }
    SCHED();
  }
  LAS float* const s_r = (LAS float*)(size_t)(P1_SCR);
  LAS float* const s_c = (LAS float*)(size_t)(P1_SCR + 512);
  LAS float* const s_u1 = (LAS float*)(size_t)(P1_SCR + 1024);
  LAS float* const s_z = (LAS float*)(size_t)(P1_SCR + 2048);
  LAS float* const s_n = (LAS float*)(size_t)(P1_SCR + 4096);
  LAS float* const s_u3 = (LAS float*)(size_t)(P1_SCR + 5120);
  LAS float* const s_u2 = (LAS float*)(size_t)(P1_SCR + 7168);
  constexpr int S_AN = P1_SCR + 9216, S_F1 = P1_SCR + 9472;
  {
#pragma unroll
    for (int i = 0; i < 6; ++i) mm(15, i);
    STAMP(3);
    unsigned i01 = 32 * wm + 16 * wn, i23 = 64 * kh + 16 * (2 * wn + (wm & 1));
    asm volatile("" : "+s"(i01), "+s"(i23));
    const float s1 = sel4(rsum, fr & 3), s2 = sel4(gram, fr & 3);
    const float mean = s1 * (1.0f / D);
    const float rstd = __builtin_amdgcn_rsqf(fmaxf(s2 * (1.0f / D) - mean * mean, 0.0f) + 1e-5f);
    if ((fr >> 2) == fq) { s_r[i01 + fr] = rstd; s_c[i01 + fr] = -mean * rstd; }
    if (fq == 0) s_u1[i23 + fr] = cs1[0];
  }
  const int s0 = pm * 128 + 32 * wm;
  const rsrc_t rT = RSRC(a.ws + WS_Q, (2 * D + FH) * S * 2);
  const unsigned vT = ((fr >> 2) & 1) * 65536 + fq * 256 + (4 * (fr >> 3) + (fr & 3)) * 16;
  const unsigned tb0 = fr * 128 + fsw, tb1 = tb0 ^ 64;
  if (wid == 7) dma4_sc1(rG, S_AN, (lane & 1) * 1024, G_AN1 + role4);
  WL0();
  BAR();
  STAMP(4);
  if (ZB) {
#pragma unroll
    for (int p = 0; p < 4; ++p) pieceW2(p);
  }
  f32x4 rq[2], cq[2];
#pragma unroll
  for (int m = 0; m < 2; ++m) {
    rq[m] = *(LAS f32x4*)(s_r + 32 * wm + 16 * (m ^ wn) + 4 * fq);
    cq[m] = *(LAS f32x4*)(s_c + 32 * wm + 16 * (m ^ wn) + 4 * fq);
  }
  auto store_t = [&](int T, f32x4 v0, f32x4 v1) {
    const unsigned a0 = cvt_pk(v0[0], v0[1]), a1 = cvt_pk(v0[2], v0[3]), b0 = cvt_pk(v1[0], v1[1]), b1 = cvt_pk(v1[2], v1[3]);
    const u32x4 o = wn ? u32x4{b0, b1, a0, a1} : u32x4{a0, a1, b0, b1};
    st16_sc1(o, rT, vT, (T >> 1) * 131072 + (T & 1) * 128 + s0 * 32);
  };
  auto lin_store = [&](f32x4 c0, f32x4 c1, float u, int T) { store_t(T, rq[0] * c0 + cq[0] * u, rq[1] * c1 + cq[1] * u); };
  float u1[2];
#pragma unroll
  for (int n = 0; n < 2; ++n) { const int ui = 16 * (2 * wn + (n ^ (wm & 1))) + fr; u1[n] = s_u1[ui] + s_u1[64 + ui]; }
  if (ZB) {
    f32x4 ps[2], pq[2];
#pragma unroll
    for (int n = 0; n < 2; ++n) {
#pragma unroll
      for (int m = 0; m < 2; ++m) {
        f32x4 v = rq[m] * acc1[m][n] + cq[m] * u1[n];
        const f32x2 g0 = gelu2(f32x2{v[0], v[1]}), g1 = gelu2(f32x2{v[2], v[3]});
        v = f32x4{g0[0], g0[1], g1[0], g1[1]};
        acc1[m][n] = v;
        if (n == 0) { ps[m] = v; pq[m] = v * v; } else { ps[m] += v; pq[m] += v * v; }
      }
      if (n == 0) {
        SCHED();
#pragma unroll
        for (int p = 4; p < 8; ++p) pieceW2(p);
        SCHED();
      }
    }
    float t[8], h[4];
#define DPP2(d, x, y, ctl, m0, m1) "v_add_f32_dpp " d ", " x ", " x " " ctl " row_mask:0xf bank_mask:" m0 "\n\tv_add_f32_dpp " d ", " y ", " y " " ctl " row_mask:0xf bank_mask:" m1 "\n\t"
#define DPPQ(d, ctl) "v_add_f32_dpp " d ", " d ", " d " " ctl " row_mask:0xf bank_mask:0xf\n\t"
    asm volatile("s_nop 1\n\t" DPP2("%0", "%8", "%16", "row_ror:8", "0x3", "0xc") DPP2("%1", "%9", "%17", "row_ror:8", "0x3", "0xc")
                 DPP2("%2", "%10", "%18", "row_ror:8", "0x3", "0xc") DPP2("%3", "%11", "%19", "row_ror:8", "0x3", "0xc")
                 DPP2("%4", "%12", "%20", "row_ror:8", "0x3", "0xc") DPP2("%5", "%13", "%21", "row_ror:8", "0x3", "0xc")
                 DPP2("%6", "%14", "%22", "row_ror:8", "0x3", "0xc") DPP2("%7", "%15", "%23", "row_ror:8", "0x3", "0xc") "s_nop 1"
                 : "=&v"(t[0]), "=&v"(t[1]), "=&v"(t[2]), "=&v"(t[3]), "=&v"(t[4]), "=&v"(t[5]), "=&v"(t[6]), "=&v"(t[7])
                 : "v"(ps[0][0]), "v"(pq[0][0]), "v"(ps[0][1]), "v"(pq[0][1]), "v"(ps[0][2]), "v"(pq[0][2]), "v"(ps[0][3]), "v"(pq[0][3]),
                   "v"(ps[1][0]), "v"(pq[1][0]), "v"(ps[1][1]), "v"(pq[1][1]), "v"(ps[1][2]), "v"(pq[1][2]), "v"(ps[1][3]), "v"(pq[1][3]));
    asm volatile("s_nop 1\n\t" DPP2("%0", "%4", "%8", "row_half_mirror", "0x5", "0xa") DPP2("%1", "%5", "%9", "row_half_mirror", "0x5", "0xa")
                 DPP2("%2", "%6", "%10", "row_half_mirror", "0x5", "0xa") DPP2("%3", "%7", "%11", "row_half_mirror", "0x5", "0xa") "s_nop 1\n\t"
                 DPPQ("%0", "quad_perm:[1,0,3,2]") DPPQ("%1", "quad_perm:[1,0,3,2]") DPPQ("%2", "quad_perm:[1,0,3,2]") DPPQ("%3", "quad_perm:[1,0,3,2]")
                 DPPQ("%0", "quad_perm:[2,3,0,1]") DPPQ("%1", "quad_perm:[2,3,0,1]") DPPQ("%2", "quad_perm:[2,3,0,1]") DPPQ("%3", "quad_perm:[2,3,0,1]") "s_nop 0"
                 : "=&v"(h[0]), "=&v"(h[1]), "=&v"(h[2]), "=&v"(h[3])
                 : "v"(t[0]), "v"(t[1]), "v"(t[2]), "v"(t[3]), "v"(t[4]), "v"(t[5]), "v"(t[6]), "v"(t[7]));
    if ((fr & 3) == 0) *(LAS f32x4*)(s_z + (wn * 128 + 32 * wm + 8 * fq + 4 * ((fr >> 3) ^ wn) + 2 * ((fr >> 2) & 1)) * 2) = f32x4{h[0], h[1], h[2], h[3]};
    WAIT_V(8);
  } else {
#pragma unroll
    for (int n = 0; n < 2; ++n) lin_store(acc1[0][n], acc1[1][n], u1[n], 56 + 2 * wn + (n ^ (wm & 1)));
    WAIT_V(2);
  }
  WL0();
  BAR();
  STAMP(5);
  if (ZB && wid == 7) {
    __builtin_amdgcn_s_setprio(2);
    const f32x4 p = *(LAS f32x4*)(s_z + lane * 4) + *(LAS f32x4*)(s_z + 256 + lane * 4);
    wait_go(rG, G_AN1 + role4, nonce, lds_word(S_AN));
    st16_sc1(__builtin_bit_cast(u32x4, p) | 1u, rG, lane * 16, G_PAY + (pm * 14 + cb) * 1024);
  }
  STAMP(6);
  unsigned w2[2][2];
  bf16x8 zw[4][2];
  auto rd2 = [&](int q, int n) { zw[q & 3][n] = ld_tr(w2[n][q & 1] + (q >> 1) * 8192); };
  {
    f32x4 acct[2][2] = {{Z4, Z4}, {Z4, Z4}};
    const unsigned t30 = tb0 + wn * 4096, t31 = tb1 + wn * 4096;
    const unsigned cb0 = tb0 + wid * 8192, cb1 = cb0 ^ 64, c2b = P1_W2 + wid * 8192 + trl;
    bf16x8 w[8], cf[2];
    f32x4 cs[4];
    float u3[2];
    auto rd3 = [&](int f) { const int kk = f >> 1; w[f & 7] = ld_frag(((kk & 1) ? t31 : t30) + (kk >> 1) * 8192 + (f & 1) * 2048); };
#pragma unroll
    for (int f = 0; f < 4; ++f) rd3(f);
    SCHED();
#pragma unroll
    for (int f = 0; f < 32; ++f) {
      const int kk = f >> 1, n = f & 1;
      acct[0][n] = MFMA16(xf[kk][0], w[f & 7], acct[0][n]);
      SCHED();
      if (f == 13) { if (ZB) wait_vn(0); WL0(); BAR(); }
      if (f + 4 < 32) rd3(f + 4);
      if (f < 8) cf[f & 1] = ld_frag(((f & 1) ? cb1 : cb0) + (f >> 1) * 2048);
      if (f == 11) s_u3[lane * 8 + wid] = sel4(f32x4{cs[0][0], cs[1][0], cs[2][0], cs[3][0]}, fq);
      if (ZB && f >= 14 && f < 22) cf[f & 1] = ld_tr((c2b ^ (((f - 14) >> 1) * 32)) + (f & 1) * 4096);
      if (ZB && f == GATHER_AT && wid == 7) {
        dma4_sc1(rG, S_AN, (lane & 1) * 1024, G_AN1 + role4);
#pragma unroll
        for (int i = 0; i < 14; ++i) dma16_sc1(rG, 0, P1_GAT + i * 1024, lane * 16, G_PAY + pm * 14336 + i * 1024);
      }
      if (f == 23 || f == 25) {
        const int q = (f - 23) >> 1;
        const f32x4 uu = *(LAS f32x4*)(s_u3 + (32 * wn + 16 * q + fr) * 8) + *(LAS f32x4*)(s_u3 + (32 * wn + 16 * q + fr) * 8 + 4);
        u3[q] = (uu[0] + uu[1]) + (uu[2] + uu[3]);
      }
      if (ZB && f == 26) s_u2[wid * 64 + lane] = sel4(f32x4{cs[0][0], cs[1][0], cs[2][0], cs[3][0]}, fq);
      if (ZB && f == 27) {
#pragma unroll
        for (int q = 0; q < 2; ++q)
#pragma unroll
          for (int ks = 0; ks < 2; ++ks) { w2[q][ks] = wb[q][ks] + (P1_W2 - P1_RING - 16384); OPAQUE(w2[q][ks]); }
      }
      if (ZB && f >= 28) rd2((f - 28) >> 1, f & 1);
      SCHED();
      acct[1][n] = MFMA16(xf[kk][1], w[f & 7], acct[1][n]);
      SCHED();
      if (f >= 1 && f < 9) { const int g = f - 1; cs[g >> 1] = MFMA16(ONES, cf[g & 1], (g & 1) ? cs[g >> 1] : Z4); SCHED(); }
      if (ZB && f >= 15 && f < 23) { const int g = f - 15; cs[g >> 1] = MFMA16(ONES, cf[(f - 1) & 1], (g & 1) ? cs[g >> 1] : Z4); SCHED(); }
    }
#pragma unroll
    for (int n = 0; n < 2; ++n) lin_store(acct[0][n], acct[1][n], u3[n], (ZB ? 4 * cb : 60) + 2 * wn + n);
  }
  STAMP(7);
  bv = bp[(wid & 2) * 32 + lane];
  auto publish = [&]() {
    if (ZB) WAIT_V(8); else { pin(bv); WAIT_V(0); }
    WL0();
    BAR();
    if (wid == 7) {
      wait_go(rG, G_AN2 + role4, nonce, lds_word(S_AN + 4));
      const unsigned box = (fq & 1) * 128 + (cb >> 1) * 16 + fr;
      st4_sc1(1u, rG, fq < 2 ? box * 128 + (pm * 2 + (cb & 1)) * 4 : G_ALL + role4, 0);
    }
  };
  auto earlyP = [&](int k) {
    const rsrc_t rP = RSRC(Pp, 128 * S * 2);
    const unsigned ve = (16 * wid + r8) * (S * 2) + sw_e, vo = (ve ^ 64) + 8 * (S * 2);
    dma16(rP, mb2, k * 16384, ve, (k ^ kx2) * 128);
    dma16(rP, mb2, k * 16384 + 1024, vo, (k ^ kx2) * 128);
  };
  if (ZB) {
    f32x4 acc2[2][2] = {{Z4, Z4}, {Z4, Z4}};
    SCHED();
#pragma unroll
    for (int q = 0; q < 16; ++q) {
      if (q == B10_AT) {
        if (wid == 7) {
          f32x4 t;
          for (unsigned spin = 0; spin < SPIN; ++spin) {
            if (spin) WAIT_V(0); else WAIT_V(2);
            u32x4 al = *(LAS u32x4*)(size_t)(P1_GAT + lane * 16);
            t = __builtin_bit_cast(f32x4, al);
#pragma unroll
            for (int i = 1; i < 14; ++i) { const u32x4 pv = *(LAS u32x4*)(size_t)(P1_GAT + i * 1024 + lane * 16); al &= pv; t += __builtin_bit_cast(f32x4, pv); }
            if (__all((al[0] & al[1] & al[2] & al[3] & 1u) != 0)) break;
            WL0();
#pragma unroll
            for (int i = 0; i < 14; ++i) dma16_sc1(rG, 0, P1_GAT + i * 1024, lane * 16, G_PAY + pm * 14336 + i * 1024);
          }
          const float m0 = t[0] * (1.0f / FH), m1 = t[2] * (1.0f / FH);
          const float r0 = __builtin_amdgcn_rsqf(fmaxf(t[1] * (1.0f / FH) - m0 * m0, 0.0f) + 1e-5f);
          const float r1 = __builtin_amdgcn_rsqf(fmaxf(t[3] * (1.0f / FH) - m1 * m1, 0.0f) + 1e-5f);
          *(LAS f32x2*)(s_n + 2 * lane) = f32x2{r0, r1};
          *(LAS f32x2*)(s_n + 128 + 2 * lane) = f32x2{-m0 * r0, -m1 * r1};
          __builtin_amdgcn_s_setprio(0);
        }
        pin(bv);
        WL0();
        BAR();
        STAMP(8);
        f32x4 r2[2], c2[2];
#pragma unroll
        for (int m = 0; m < 2; ++m) {
          r2[m] = *(LAS f32x4*)(s_n + 32 * wm + 8 * fq + 4 * (m ^ wn));
          c2[m] = *(LAS f32x4*)(s_n + 128 + 32 * wm + 8 * fq + 4 * (m ^ wn));
        }
#pragma unroll
        for (int n = 0; n < 2; ++n) store_t(64 + 4 * cb + 2 * wn + (n ^ (wm & 1)), acc1[0][n] * r2[0] + c2[0], acc1[1][n] * r2[1] + c2[1]);
        SCHED();
      }
      if (q == B11_AT) { publish(); STAMP(9); SCHED(); }
#pragma unroll
      for (int i = 0; i < 4; ++i) {
        const int m = i >> 1, n = i & 1;
        acc2[m][n] = MFMA16(zw[q & 3][n], xf[q][m], acc2[m][n]);
        SCHED();
        if (i < 2 && q + 2 < 16) rd2(q + 2, i);
        if (i == 3 && q >= B10_AT && q < B10_AT + 4) earlyP(q - B10_AT);
        SCHED();
      }
    }
#pragma unroll
    for (int n = 0; n < 2; ++n) {
      const int ui = 16 * (2 * wn + (n ^ (wm & 1))) + 4 * fq;
      f32x4 u = *(LAS f32x4*)(s_u2 + ui);
#pragma unroll
      for (int j = 1; j < 8; ++j) u += *(LAS f32x4*)(s_u2 + j * 64 + ui);
#pragma unroll
      for (int m = 0; m < 2; ++m) z1v[m][n] = acc2[m][n] * s_r[32 * wm + 16 * (m ^ wn) + fr] + u * s_c[32 * wm + 16 * (m ^ wn) + fr];
    }
  } else {
    STAMP(8);
    publish();
    STAMP(9);
    if (DW >= 0 && wid < 4) warm_window(a, P1_SCR + 9728, pm, DW, wid, lane);
    earlyP(0); earlyP(1); earlyP(2); earlyP(3);
    if (DW >= 0 && wid < 4) WAIT_V(0);
  }
  const bool allq = true;
  STAMP_OUT(a.ws, 0, 10);
  return allq;
}

__global__ __launch_bounds__(512, 2) void fused(Args a) {
  extern __shared__ __attribute__((aligned(1024))) char smem[];
  asm volatile("" ::"v"(smem));
  constexpr int KID = 1;
  unsigned long long pstart = __builtin_amdgcn_s_memrealtime(), ptime = pstart;
  const int bx = blockIdx.x & 7, bi = blockIdx.x >> 3;
  if (bi < CODEWARM && threadIdx.x < 64) {
    unsigned long long pc;
    asm volatile("s_getpc_b64 %0" : "=s"(pc));
    touch(RSRC(pc & ~127ull, CODEWARM * 8192), 1024, threadIdx.x * 128, bi * 8192);
  }
  const int pm = 4 * (bx >> 1) + (bi >> 3), cb = 2 * (bi & 7) + (bx & 1);
  int mat, tm, tn;
  if (bx & 1) { mat = 2; tm = pm; tn = bi & 7; }
  else { mat = bx >> 2; tm = ((bx >> 1) & 1) * 8 + (bi >> 2); tn = bi & 3; }
  const int kx = (tm ^ tn) & K2_KXMASK;
  const u16* Pp = (mat == 0 ? a.wsr : mat == 1 ? a.wsi : a.wsp) + (size_t)tm * 128 * S;
  f32x4 z1v[2][2];
  const unsigned qbox = (mat * 4 + tn) * 16 + tm;
  bool allq = true;
  unsigned bv = 0;
  {
    const int kx1 = ((bi & 7) ^ (2 * (bi >> 3))) & K1_KXMASK;
    if (__builtin_expect(cb < 14, 1)) allq = phase1<true>(a, pm, cb, kx1, Pp, kx, qbox, z1v, a.bsp + 128 * tm, bv, ptime);
    else if (cb == 14) allq = phase1<false>(a, pm, cb, kx1, Pp, kx, qbox, z1v, a.bsp + 128 * tm, bv, ptime);
  }
  int tid = threadIdx.x;
  asm volatile("" : "+v"(tid));
  const int lane = tid & 63;
  const int wid = __builtin_amdgcn_readfirstlane(tid >> 6);
  STAMP_DECL STAMP(0);
  const int tm3 = bx * 4 + (bi >> 3), tn3 = bi & 7;
  const int fr = lane & 15, fq = lane >> 4;
  const unsigned vl = lane * 16;
  const rsrc_t rG = RSRC(a.ws + WS_G, G_END);
  const bool work2 = cb != 15;
  auto z1_finish = [&]() {
    if (cb < 14) {
      const int wm = wid & 3, wn = wid >> 2;
      const rsrc_t rZ = RSRC(a.ws + WS_Z1, FH * S * 2);
      const unsigned vZ = (fr >> 3) * (112 * 256) + (2 * ((fq & 1) ^ (wm & 1)) + (fq >> 1)) * 256 + (fr & 7) * 16;
      const unsigned sZ = ((pm * 8 + 2 * wm) * 112 + 8 * cb + 4 * wn) * 256;
#pragma unroll
      for (int m = 0; m < 2; ++m) {
        unsigned pk[2][2];
#pragma unroll
        for (int n = 0; n < 2; ++n) {
          const f32x2 g0 = gelu2(f32x2{z1v[m][n][0], z1v[m][n][1]}), g1 = gelu2(f32x2{z1v[m][n][2], z1v[m][n][3]});
          pk[n][0] = cvt_pk(g0[0], g0[1]); pk[n][1] = cvt_pk(g1[0], g1[1]);
        }
        auto w0 = __builtin_amdgcn_permlane16_swap(pk[0][0], pk[1][0], false, false);
        auto w1 = __builtin_amdgcn_permlane16_swap(pk[0][1], pk[1][1], false, false);
        st16_sc1(u32x4{w0[0], w1[0], w0[1], w1[1]}, rZ, vZ, sZ + 128 * (m ^ wn));
      }
    }
  };
  if (blockIdx.x == 255) {
    for (int i = 0; i < 8; ++i) st16_sc1(u32x4{0, 0, 0, 0}, rG, vl, (wid * 8 + i) * 1024);
    st16_sc1(u32x4{0, 0, 0, 0}, rG, vl, G_ALL);
    WAIT_V(0);
  }
  if (blockIdx.x == 255) BAR();
  if (wid >= 4) {
    const int lw = wid - 4;
    const int r8 = lane >> 3, p = lane & 7;
    if (!work2) {
      __builtin_amdgcn_s_setprio(3);
      if (wid == 7) {
        const unsigned role4 = (pm * 16 + 15) * 4;
        for (int i = 0; i < 14; ++i) st16_sc1(u32x4{0, 0, 0, 0}, rG, vl, G_PAY + pm * 14336 + i * 1024);
        const unsigned nonce = __builtin_amdgcn_readfirstlane(ld4_sc1(rG, 0, G_AN2 + role4)) + 1;
        st4_sc1(nonce, rG, 0, G_REQ + role4);
        for (unsigned spin = 0; spin < SPIN; ++spin) {
          st4_sc1(ld4_sc1(rG, fr * 4, G_REQ + pm * 64), rG, fr * 4, G_AN1 + pm * 64);
          if (spin == PF_PASS) BAR();
          if (spin >= PF_PASS && __all(fr > 13 || ld4_sc1(rG, fr * 1024, G_PAY + pm * 14336) != 0u)) break;
        }
        wait_go(rG, G_AN2 + role4, nonce, nonce - 1);
        st4_sc1(1u, rG, 0, G_ALL + role4);
      } else {
        BAR();
      }
      if (wid == 6 && blockIdx.x == 255) {
        for (unsigned spin = 0; spin < SPIN; ++spin) {
          const u32x4 rq = ld16_sc1(rG, vl, G_REQ);
          st16_sc1(rq, rG, vl, G_AN2);
          const u32x4 fl = ld16_sc1(rG, vl, G_ALL);
          if (__all((fl[0] & fl[1] & fl[2] & fl[3]) == 1u)) break;
        }
      }
    }
    const rsrc_t rP = RSRC(Pp, 128 * S * 2);
    const unsigned vo_e = (lw * 32 + r8) * (S * 2) + ((p ^ (r8 >> 1)) << 4);
    const unsigned vo_o = (vo_e ^ 64) + 8 * (S * 2);
    auto stageP = [&](int kt, unsigned poff) {
      const unsigned mb = poff + lw * 4096, so = (kt ^ kx) * 128;
#pragma unroll
      for (int j = 0; j < 4; ++j) dma16(rP, mb, j * 1024, (j & 1) ? vo_o : vo_e, so + (j >> 1) * 16 * (S * 2));
    };
    const rsrc_t rWo = RSRC(a.wout + 64 * tn3, (FH * D - 64 * tn3) * 2);
    const unsigned vw_e = (32 * lw + r8) * (D * 2) + ((p ^ (((r8 >> 1) & 1) << 1)) << 4), vw_o = (vw_e ^ 64) + 8 * (D * 2);
    u32x4 wreg[12];
    auto wpark = [&](int c) {
#pragma unroll
      for (int i = 4 * c; i < 4 * c + 4; ++i) {
        const int rg = i < 8 ? 2 + (i >> 2) : 6;
        ld16_async(wreg[i], rWo, (i & 1) ? vw_o : vw_e, (rg * 128 + ((i >> 1) & 1) * 16) * (D * 2));
      }
    };
    auto wput = [&](int rg) {
#pragma unroll
      for (int j = 0; j < 4; ++j) *(LAS u32x4*)(size_t)(wbase(rg) + lw * 4096 + j * 1024 + vl) = wreg[(rg == 6 ? 8 : (rg - 2) * 4) + j];
    };
    auto wdma = [&](int rg) {
#pragma unroll
      for (int j = 0; j < 4; ++j) dma16(rWo, lw * 4096, wbase(rg) + j * 1024, (j & 1) ? vw_o : vw_e, (rg * 128 + (j >> 1) * 16) * (D * 2));
    };
    if (work2) {
      u32x4 preg[8];
#pragma unroll
      for (int i = 0; i < 8; ++i) ld16_async(preg[i], rP, (i & 1) ? vo_o : vo_e, ((4 + (i >> 2)) ^ kx) * 128 + ((i >> 1) & 1) * 16 * (S * 2));
      wpark(0); wpark(1); wpark(2);
      if (wid != 7) z1_finish();
      else {
        for (unsigned spin = 0; spin < SPIN; ++spin) {
          const unsigned fl = ld4_sc1(rG, (lane & 31) * 4, G_F1 + qbox * 128);
          const bool ok = fl == 1u || (qbox >= 112 && qbox < 128 && (lane & 1));
          allq = __all(ok);
          if (__all(ok || (WINSTART && (lane & 31) > 3))) break;
        }
      }
      WL0();
      BAR();
      STAMP(1);
      const rsrc_t rQ = RSRC(a.ws + WS_Q + (size_t)(mat * 4 + tn) * 128 * S * 2, 128 * S * 2);
      const unsigned vq = vl + lw * 131072;
      auto stageQ = [&](int kt) {
        const unsigned mb = (kt & 3) * 16384 + lw * 4096, so = (kt ^ kx) * 2048;
#pragma unroll
        for (int j = 0; j < 4; ++j) dma16_sc1(rQ, mb, P2_QB + j * 1024, vq, so + (j >> 1) * 65536 + (j & 1) * 1024);
      };
      stageQ(0); stageQ(1);
      if (wid == 7) z1_finish();
      WAIT_V(4); BAR();
      stageQ(2); stageQ(3);
#pragma unroll
      for (int i = 0; i < 8; ++i) *(LAS u32x4*)(size_t)((4 + (i >> 2)) * 16384 + lw * 4096 + (i & 3) * 1024 + vl) = preg[i];
      WL0();
      for (unsigned spin = 0; !allq && spin < SPIN; ++spin)
        allq = __all(ld4_sc1(rG, (lane & 31) * 4, G_F1 + qbox * 128) == 1u || (qbox >= 112 && qbox < 128 && (lane & 1)));
      WAIT_V(8); BAR(); stageQ(4); stageP(6, 0);
      WAIT_V(12); BAR(); stageQ(5); stageP(7, 16384);
      WAIT_V(16); BAR(); stageQ(6); stageP(8, 2 * 16384);
      unsigned poff = 3 * 16384;
      unsigned zf;
      for (int kt = 3; kt < 23; ++kt) {
        WAIT_V(20);
        BAR();
        if (kt == 3 && wid == 7 && cb < 14) st4_sc1(1u, rG, 0, G_FZ + (pm * 8 + (cb >> 1)) * 128 + (cb & 1) * 4);
        stageQ(kt + 4); stageP(kt + 6, poff);
        poff = poff == 5 * 16384 ? 0 : poff + 16384;
      }
      asm volatile("buffer_load_dword %0, %1, %2, %3 offen sc1" : "=v"(zf) : "v"((lane & 1) * 4), "s"(rG), "s"(G_FZ + (tm * 8 + tn) * 128) : "memory");
#pragma unroll
      for (int kt = 23; kt < 26; ++kt) {
        WAIT_V(20);
        BAR();
        stageQ(kt + 4); stageP(kt + 6, (kt % 6) * 16384);
      }
      asm volatile("s_waitcnt vmcnt(20)" : "+v"(zf)::"memory");
      if (mat == 2) {
        const rsrc_t rZ = RSRC(a.ws + WS_Z1 + (size_t)((8 * tm + lw) * 112 + 16 * tn) * 256, 4 * 112 * 256 + 4096);
        auto stageZ = [&](int h) {
#pragma unroll
          for (int j = 0; j < 4; ++j) dma16_sc1(rZ, lw * 4096, P2_Z + h * 16384 + j * 1024, vl, h * (4 * 112 * 256) + j * 1024);
        };
        for (unsigned spin = 0; !__all(zf == 1u) && spin < SPIN; ++spin) zf = ld4_sc1(rG, (lane & 1) * 4, G_FZ + (tm * 8 + tn) * 128);
        WAIT_V(20); BAR(); stageQ(30); stageZ(0);
        WAIT_V(20); BAR(); stageQ(31); stageZ(1);
        WAIT_V(20); BAR(); wdma(0); wdma(4);
        WAIT_V(20); BAR(); wdma(1); wdma(5);
        WAIT_V(20); BAR(); wput(2); wput(6);
        WAIT_V(16); BAR(); wput(3);
      } else {
        WAIT_V(20); BAR(); stageQ(30);
        WAIT_V(16); BAR(); stageQ(31);
        WAIT_V(12); BAR(); wdma(0); wdma(4);
        WAIT_V(12); BAR(); wdma(1); wdma(5);
        WAIT_V(16); BAR(); wput(2); wput(6);
        BAR(); wput(3);
      }
    } else {
      STAMP(1);
      wdma(0); wdma(4); wdma(1); wdma(5); wdma(2); wdma(3); wdma(6);
      __builtin_amdgcn_s_setprio(0);
    }
    STAMP(2); STAMP(3);
    WAIT_V(0);
  } else if (work2) {
    const int wp = wid >> 1, wq = wid & 1;
    f32x4 acc[4][4];
    const int sw = (fr >> 1) & 7;
    const unsigned ra = (wp * 64 + fr) * 128 + ((fq ^ sw) << 4);
    const unsigned rb = P2_QB + wq * 8192 + vl;
    bf16x8 a0[4], b0[4], a1[4], b1[4];
    unsigned poff = 0;
    unsigned pa0 = ra, pa1 = pa0 ^ 64;
#define K2_RD(AF, BF, pa, sl, ks, i) do { \
    if ((i) == 0) AF[0] = ld_frag(pa); \
    else if ((i) <= 4) BF[(i) - 1] = ld_frag(rb + (sl) * 16384 + ((i) - 1) * 2048 + (ks) * 1024); \
    else AF[(i) - 4] = ld_frag(pa + ((i) - 4) * 2048); } while (0)
#define K2_MM(AF, BF, i) acc[(i) >> 2][(i) & 3] = MFMA16(BF[(i) & 3], AF[(i) >> 2], acc[(i) >> 2][(i) & 3])
#define K2_STEP(s, LAST) do { \
    SCHED(); WL0(); SCHED(); \
    _Pragma("unroll") for (int i = 0; i < 16; ++i) { K2_MM(a0, b0, i); SCHED(); if (i < 8) K2_RD(a1, b1, pa1, s, 1, i); \
      if (i == 10) { poff = poff == 5 * 16384 ? 0 : poff + 16384; pa0 = ra + poff; } SCHED(); } \
    WL0(); \
    if ((s) == 3) WAIT_V(0); \
    BAR(); \
    SCHED(); \
    _Pragma("unroll") for (int i = 0; i < 16; ++i) { K2_MM(a1, b1, i); SCHED(); if (!(LAST) && i < 8) K2_RD(a0, b0, pa0, ((s) + 1) & 3, 0, i); \
      if (i == 10) pa1 = pa0 ^ 64; SCHED(); } \
  } while (0)
    z1_finish();
    WL0();
    BAR();
    if (cb < 14) WAIT_V(2); else WAIT_V(0);
#pragma unroll
    for (int ni = 0; ni < 4; ++ni) {
      const float bias = mat == 2 ? __uint_as_float((unsigned)__shfl(bv, 16 * ni + fr) << 16) : 0.0f;
#pragma unroll
      for (int n = 0; n < 4; ++n) acc[ni][n] = f32x4{bias, bias, bias, bias};
    }
    BAR();
    STAMP(1);
#pragma unroll
    for (int i = 0; i < 8; ++i) K2_RD(a0, b0, pa0, 0, 0, i);
    for (int kt = 0; kt < 28; kt += 4) {
      K2_STEP(0, 0); K2_STEP(1, 0); K2_STEP(2, 0); K2_STEP(3, 0);
    }
    K2_STEP(0, 0); K2_STEP(1, 0); K2_STEP(2, 0); K2_STEP(3, 1);
    SCHED();
    STAMP(2);
    if (mat < 2) {
      const rsrc_t rY = RSRC(a.ws + WS_Y + (size_t)mat * S * D * 2, S * D * 2);
      const unsigned so = ((8 * tm + 4 * wp) * 64 + 16 * tn + 8 * wq) * 256;
#pragma unroll
      for (int ni = 0; ni < 4; ++ni)
#pragma unroll
        for (int P = 0; P < 2; ++P) {
          const f32x4 c0 = acc[ni][2 * P], c1 = acc[ni][2 * P + 1];
          const u32x4 v = {cvt_pk(c0[0], c0[1]), cvt_pk(c0[2], c0[3]), cvt_pk(c1[0], c1[1]), cvt_pk(c1[2], c1[3])};
          st16_sc1(v, rY, vl, so + (ni * 64 + 4 * P) * 256);
        }
    } else {
      const rsrc_t rH = RSRC(a.ws + WS_H, S * FH * 2);
      const unsigned so = ((8 * tm + 4 * wp) * 112 + 16 * tn + 8 * wq) * 256;
      const unsigned za = P2_Z + wp * 16384 + wq * 2048 + (fq * 16 + ((fr & 4) << 1) + ((fr & 8) >> 1) + (fr & 3)) * 16;
#pragma unroll
      for (int ni = 0; ni < 4; ++ni) {
#pragma unroll
        for (int P = 0; P < 2; ++P) {
          const f32x4 c0 = acc[ni][2 * P], c1 = acc[ni][2 * P + 1];
          const u32x4 z = *(const LAS u32x4*)(size_t)(za + ni * 4096 + P * 1024);
          const u32x4 v = {cvt_pk(bf_lo(z[0]) * c0[0], bf_hi(z[0]) * c0[1]), cvt_pk(bf_lo(z[1]) * c0[2], bf_hi(z[1]) * c0[3]),
                           cvt_pk(bf_lo(z[2]) * c1[0], bf_hi(z[2]) * c1[1]), cvt_pk(bf_lo(z[3]) * c1[2], bf_hi(z[3]) * c1[3])};
          st16_sc1(v, rH, vl, so + (ni * 112 + 4 * P) * 256);
        }
      }
    }
    STAMP(3);
    WAIT_V(0);
  } else {
    if (PF_EARLY && blockIdx.x != 255) { warm_window(a, P2_Z, pm, 8, wid, lane); }
    BAR();
    STAMP(1);
    for (int i = 0; i < PF_N && blockIdx.x != 255; ++i) {
      warm_window(a, P2_Z, pm, (PF_LIST >> (4 * i)) & 15, wid, lane);
      WAIT_V(6);
    }
    STAMP(2);
    WAIT_V(0);
    STAMP(3);
  }
  WL0();
  BAR();
  STAMP(4);
  {
    const int wr = (wid + tn3) & 7, tp = wr >> 2, kq = wr & 3;
    const int tt3 = 4 * tm3 + 2 * tp + (kq & 1);
    const int cq = 2 * (fq & 1) + (fq >> 1);
    const int c3 = 8 * tn3 + 2 * (kq & 2);
    if (wid == 4 && work2) {
      const unsigned box = mat == 2 ? fr : (fr & 8) + 2 * tn + (fr & 1);
      st4_sc1(1u, rG, box * 128, G_FP + tm * 2048 + (mat == 2 ? tn : 8 + mat) * 4);
    }
    const int q4 = fr >> 2, pp = lane & 3;
    const int sp = ((q4 >> 1) & 1) | ((fq & 1) << 1);
    const unsigned wa = kq * 4096 + 128 * (8 * fq + q4) + 16 * ((2 * (kq & 2) + (pp >> 1)) ^ (sp << 1)) + 8 * (pp & 1);
    bf16x8 wf[7][4];
    auto ld_w = [&]() {
#pragma unroll
      for (int mi = 0; mi < 4; ++mi) {
        unsigned lo = wa ^ (mi * 32), mid = lo + 65536, hi = lo + 131072;
        OPAQUE(mid); OPAQUE(hi);
#pragma unroll
        for (int g = 0; g < 7; ++g) wf[g][mi] = ld_tr((wbase(g) < 65536 ? lo : wbase(g) < 131072 ? mid : hi) + (wbase(g) & 65535));
      }
    };
    if (wid == 4) {
      for (unsigned spin = 0; spin < SPIN; ++spin) {
        const unsigned fl = ld4_sc1(rG, fr * 4, G_FP + (tm3 * 8 + tn3) * 128);
        if (__all(fl == 1u || fr == 7 || fr > 9)) break;
      }
    } else {
      ld_w();
    }
    BAR();
    STAMP(5);
    const rsrc_t rH = RSRC(a.ws + WS_H + (size_t)((4 * tm3 + 2 * tp) * 112 + 4 * kq) * 256, 2 * 112 * 256);
    const rsrc_t rY = RSRC(a.ws + WS_Y + (size_t)(tt3 * 64 + c3) * 256, S * D * 2 + 1024);
    const unsigned vh0 = vl + (kq & 1) * (112 * 256), vh1 = vl + ((kq & 1) ^ 1) * (112 * 256);
    u32x4 hf[7][2];
#pragma unroll
    for (int g = 0; g < 7; ++g) {
      hf[g][0] = __builtin_amdgcn_raw_buffer_load_b128(rH, vh0, g * 4096, 16);
      hf[g][1] = __builtin_amdgcn_raw_buffer_load_b128(rH, vh1, g * 4096, 16);
    }
    const unsigned vy = cq * 256 + fr * 16;
    const u32x4 e1 = __builtin_amdgcn_raw_buffer_load_b128(rY, vy, 0, 16), e2 = __builtin_amdgcn_raw_buffer_load_b128(rY, vy, S * D * 2, 16);
    const u32x4 ex = *(const u32x4*)(a.x + (size_t)(tt3 * 16 + fr) * D + (c3 + cq) * 8);
    SCHED();
    if (wid == 4) ld_w();
    f32x4 acc[2][4];
#pragma unroll
    for (int g = 0; g < 7; ++g)
#pragma unroll
      for (int tl = 0; tl < 2; ++tl)
#pragma unroll
        for (int mi = 0; mi < 4; ++mi) acc[tl][mi] = MFMA16(wf[g][mi], __builtin_bit_cast(bf16x8, hf[g][tl]), (g ? acc[tl][mi] : f32x4{0, 0, 0, 0}));
    STAMP(6);
    auto xb = [&](int role, int d, int j) { return (LAS f32x4*)(size_t)((d < 3 ? P2_Z + (d - 1) * 16384 : P3_X3) + (role * 2 + j) * 1024 + vl); };
#pragma unroll
    for (int d = 1; d < 4; ++d) {
      *xb(wr, d, 0) = acc[d & 1][d & 2];
      *xb(wr, d, 1) = acc[d & 1][(d & 2) + 1];
    }
    WL0();
    BAR();
    f32x4 s0 = acc[0][0], s1 = acc[0][1];
#pragma unroll
    for (int d = 1; d < 4; ++d) {
      s0 += *xb(wr ^ d, d, 0);
      s1 += *xb(wr ^ d, d, 1);
    }
    f32x4 c0, c1;
#pragma unroll
    for (int j = 0; j < 4; ++j) {
      auto w = __builtin_amdgcn_permlane16_swap(__float_as_uint(s0[j]), __float_as_uint(s1[j]), false, false);
      c0[j] = __uint_as_float(w[0]); c1[j] = __uint_as_float(w[1]);
    }
    u32x4 o;
#pragma unroll
    for (int k = 0; k < 4; ++k) {
      const f32x4 c = (k >> 1) ? c1 : c0;
      const float lo = bf_lo(ex[k]) + bf_lo(e1[k]) - bf_lo(e2[k]) + c[(k & 1) * 2];
      const float hi = bf_hi(ex[k]) + bf_hi(e1[k]) - bf_hi(e2[k]) + c[(k & 1) * 2 + 1];
      o[k] = cvt_pk(lo, hi);
    }
    st16_sc1(o, RSRC(a.out, S * D * 2), ((tt3 * 16 + fr) * D + (c3 + cq) * 8) * 2, 0);
  }
  STAMP(7);
  STAMP_OUT(a.ws, 1, 8);
  if (blockIdx.x == PBLK && threadIdx.x < 64) {
    const unsigned long long tend = pstart + 10000 + 10 * (ptime - pstart);
    while (__builtin_amdgcn_s_memrealtime() < tend) ;
  }
}

extern "C" void kernel_launch(void* const* d_in, const int* in_sizes, int n_in, void* d_out, int out_size, void* d_ws, size_t ws_size,
                              hipStream_t stream) {
  (void)in_sizes; (void)n_in; (void)out_size; (void)ws_size;
  (void)hipFuncSetAttribute((const void*)fused, hipFuncAttributeMaxDynamicSharedMemorySize, LDS_BYTES);
  Args a{(const u16*)d_in[0], (const u16*)d_in[3], (const u16*)d_in[4], (const u16*)d_in[6], (const u16*)d_in[9],
         (const u16*)d_in[1], (const u16*)d_in[2], (const u16*)d_in[7], (const u16*)d_in[8], (char*)d_ws, (u16*)d_out};
  fused<<<256, 512, LDS_BYTES, stream>>>(a);
}
```

```cpp
#include <hip/hip_runtime.h>
#include <hip/hip_bf16.h>
#include <stdint.h>

typedef unsigned short u16;
typedef short bf16x8 __attribute__((ext_vector_type(8)));
typedef short s16x4 __attribute__((ext_vector_type(4)));
typedef float f32x4 __attribute__((ext_vector_type(4)));
typedef float f32x2 __attribute__((ext_vector_type(2)));
typedef unsigned u32x4 __attribute__((ext_vector_type(4)));
typedef __bf16 bf16x2v __attribute__((ext_vector_type(2)));
typedef __amdgpu_buffer_rsrc_t rsrc_t;
#define LAS __attribute__((address_space(3)))
#define WAIT_V(n) asm volatile("s_waitcnt vmcnt(%0)" ::"n"(n) : "memory")
#define WL0() __builtin_amdgcn_s_waitcnt(0xC07F)
#define BAR() do { asm volatile("" ::: "memory"); __builtin_amdgcn_s_barrier(); asm volatile("" ::: "memory"); } while (0)
#define SCHED() __builtin_amdgcn_sched_barrier(0)
#define MFMA16(a, b, c) __builtin_amdgcn_mfma_f32_16x16x32_bf16(a, b, c, 0, 0, 0)
#define RSRC(p, n) __builtin_amdgcn_make_buffer_rsrc((void*)(p), 0, (unsigned)(n), 0x00020000)
#define OPAQUE(x) asm volatile("" : "+v"(x))

#define STAMP_DECL
#define STAMP(i)
#define STAMP_OUT(ws, kid, n)
#define K2_KXMASK 3
#define K1_KXMASK 3
#define DWARM 0
#define CODEWARM 6
#define NOPARK 0
#define BULKLATE 0
#define ZNB 0
#define WPARK_LATE 1
#define DW 2
#define PF_EARLY 0
#define COOP 1
#define B10_AT 3
#define B11_AT 10
#define PF_PASS 3
#define GATHER_AT 24
#define WINSTART 1
#define PF_LIST 0x75810u
#define PF_N 5

constexpr int S = 2048, D = 512, F = 1792, FH = 896;
constexpr size_t WS_Q = 0;
constexpr size_t WS_Z1 = WS_Q + (size_t)(2 * D + FH) * S * 2;
constexpr size_t WS_H = WS_Z1 + (size_t)FH * S * 2;
constexpr size_t WS_Y = WS_H + (size_t)FH * S * 2;
constexpr size_t WS_G = WS_Y + (size_t)2 * D * S * 2;
constexpr unsigned G_F1 = 0;
constexpr unsigned G_FP = 32768;
constexpr unsigned G_FZ = 32768 + 64;
constexpr unsigned G_ALL = 65536;
constexpr unsigned G_REQ = G_ALL + 1024;
constexpr unsigned G_AN1 = G_REQ + 1024;
constexpr unsigned G_AN2 = G_AN1 + 1024;
constexpr unsigned G_PAY = G_AN2 + 1024;
constexpr unsigned G_END = G_PAY + 16 * 14336;

constexpr int LDS_BYTES = 163840;
constexpr int P1_RING = 65536, P1_SCR = 65536, P1_GAT = 77824, P1_W2 = 98304;
constexpr int P2_QB = 98304, P2_Z = 32768, P3_X3 = 147456;
__device__ constexpr int wbase(int rg) { return rg < 2 ? 65536 + rg * 16384 : rg < 4 ? (rg - 2) * 16384 : 98304 + (rg - 4) * 16384; }
constexpr unsigned SPIN = 1u << 20;

struct Args {
  const u16* x; const u16* wdr; const u16* wdi; const u16* win; const u16* wout;
  const u16* wsr; const u16* wsi; const u16* wsp; const u16* bsp;
  char* ws; u16* out;
};

__device__ __forceinline__ unsigned cvt_pk(float lo, float hi) {
  f32x2 v = {lo, hi};
  return __builtin_bit_cast(unsigned, __builtin_convertvector(v, bf16x2v));
}
__device__ __forceinline__ float bf_lo(unsigned u) { return __uint_as_float(u << 16); }
__device__ __forceinline__ float bf_hi(unsigned u) { return __uint_as_float(u & 0xffff0000u); }
constexpr float GC1 = -2.0f * 0.7978845608028654f * 1.4426950408889634f, GC2 = GC1 * 0.044715f;
__device__ __forceinline__ f32x2 gelu2(f32x2 v) {
  const f32x2 t = v * ((v * v) * GC2 + GC1);
  const f32x2 e = f32x2{__builtin_amdgcn_exp2f(t[0]), __builtin_amdgcn_exp2f(t[1])} + 1.0f;
  return v * f32x2{__builtin_amdgcn_rcpf(e[0]), __builtin_amdgcn_rcpf(e[1])};
}
__device__ __forceinline__ void dot_sq(float& c, unsigned a) { asm volatile("v_dot2c_f32_bf16 %0, %1, %1" : "+v"(c) : "v"(a)); }
__device__ __forceinline__ void dot_sum(float& c, unsigned a) { asm volatile("v_dot2c_f32_bf16 %0, 0x3f803f80, %1" : "+v"(c) : "v"(a)); }
__device__ __forceinline__ float sum_rows(float v) {
  auto a = __builtin_amdgcn_permlane32_swap(__float_as_uint(v), __float_as_uint(v), false, false);
  v = __uint_as_float(a[0]) + __uint_as_float(a[1]);
  auto b = __builtin_amdgcn_permlane16_swap(__float_as_uint(v), __float_as_uint(v), false, false);
  return __uint_as_float(b[0]) + __uint_as_float(b[1]);
}
__device__ __forceinline__ float sel4(f32x4 v, int i) { const float a = (i & 1) ? v[1] : v[0], b = (i & 1) ? v[3] : v[2]; return (i & 2) ? b : a; }

__device__ __forceinline__ void dma16(rsrc_t r, unsigned mbase, int mimm, unsigned voff, unsigned soff) {
  asm volatile("s_add_i32 m0, %0, %1\n\ts_nop 0\n\tbuffer_load_dwordx4 %2, %3, %4 offen lds" ::"s"(mbase), "n"(mimm), "v"(voff), "s"(r), "s"(soff) : "scc");
}
__device__ __forceinline__ void dma16_sc1(rsrc_t r, unsigned mbase, int mimm, unsigned voff, unsigned soff) {
  asm volatile("s_add_i32 m0, %0, %1\n\ts_nop 0\n\tbuffer_load_dwordx4 %2, %3, %4 offen sc1 lds" ::"s"(mbase), "n"(mimm), "v"(voff), "s"(r), "s"(soff) : "scc");
}
__device__ __forceinline__ void dma4_sc1(rsrc_t r, int ldsaddr, unsigned voff, unsigned soff) {
  asm volatile("s_mov_b32 m0, %0\n\ts_nop 0\n\tbuffer_load_dword %1, %2, %3 offen sc1 lds" ::"n"(ldsaddr), "v"(voff), "s"(r), "s"(soff) : "memory");
}
__device__ __forceinline__ void touch(rsrc_t r, int junk, unsigned voff, unsigned soff) {
  asm volatile("s_mov_b32 m0, %0\n\ts_nop 0\n\tbuffer_load_dword %1, %2, %3 offen lds" ::"n"(junk), "v"(voff), "s"(r), "s"(soff) : "memory");
}
__device__ __forceinline__ void st16_sc1(u32x4 v, rsrc_t r, unsigned voff, unsigned soff) {
  asm volatile("buffer_store_dwordx4 %0, %1, %2, %3 offen sc1\n\ts_nop 1" ::"v"(v), "v"(voff), "s"(r), "s"(soff) : "memory");
}
__device__ __forceinline__ void st4_sc1(unsigned v, rsrc_t r, unsigned voff, unsigned soff) {
  asm volatile("buffer_store_dword %0, %1, %2, %3 offen sc1" ::"v"(v), "v"(voff), "s"(r), "s"(soff) : "memory");
}
__device__ __forceinline__ unsigned ld4_sc1(rsrc_t r, unsigned voff, unsigned soff) {
  unsigned v;
  asm volatile("buffer_load_dword %0, %1, %2, %3 offen sc1\n\ts_waitcnt vmcnt(0)" : "=&v"(v) : "v"(voff), "s"(r), "s"(soff) : "memory");
  return v;
}
__device__ __forceinline__ u32x4 ld16_sc1(rsrc_t r, unsigned voff, unsigned soff) {
  u32x4 v;
  asm volatile("buffer_load_dwordx4 %0, %1, %2, %3 offen sc1\n\ts_waitcnt vmcnt(0)" : "=&v"(v) : "v"(voff), "s"(r), "s"(soff) : "memory");
  return v;
}
__device__ __forceinline__ void ld16_async(u32x4& v, rsrc_t r, unsigned voff, unsigned soff) {
  asm volatile("buffer_load_dwordx4 %0, %1, %2, %3 offen" : "=v"(v) : "v"(voff), "s"(r), "s"(soff) : "memory");
}
__device__ __forceinline__ void pin(unsigned& v) { OPAQUE(v); }
__device__ __forceinline__ void wait_vn(int n) { WAIT_V(n); }
__device__ __forceinline__ bf16x8 ld_tr(unsigned addr) {
  s16x4 lo = __builtin_amdgcn_ds_read_tr16_b64_v4i16((LAS s16x4*)(size_t)(addr));
  s16x4 hi = __builtin_amdgcn_ds_read_tr16_b64_v4i16((LAS s16x4*)(size_t)(addr + 512));
  return __builtin_shufflevector(lo, hi, 0, 1, 2, 3, 4, 5, 6, 7);
}
__device__ __forceinline__ bf16x8 ld_frag(unsigned addr) { return *(const LAS bf16x8*)(size_t)(addr); }
__device__ __forceinline__ void wait_go(rsrc_t rG, unsigned word, unsigned nonce, unsigned seen) {
  for (unsigned spin = 0; seen != nonce && spin < SPIN; ++spin) {
    seen = __builtin_amdgcn_readfirstlane(ld4_sc1(rG, 0, word));
    __builtin_amdgcn_s_sleep(1);
  }
}
__device__ __forceinline__ unsigned lds_word(unsigned addr) { return __builtin_amdgcn_readfirstlane(*(LAS unsigned*)(size_t)(addr)); }

__device__ __forceinline__ void warm_window(const Args& a, int junk, int rb, int w, int wv, int lane) {
  if (w < 8) {
    const unsigned vo = (rb * 128 + wv * 32 + (lane >> 2)) * (S * 2) + (lane & 3) * 128;
#pragma unroll
    for (int m = 0; m < 3; ++m) {
      const rsrc_t r = RSRC(m == 0 ? a.wsr : m == 1 ? a.wsi : a.wsp, S * S * 2);
      touch(r, junk, vo, w * 512);
      touch(r, junk, vo, w * 512 + 16 * (S * 2));
    }
  } else {
    const rsrc_t r = RSRC(a.wout + (size_t)rb * 56 * D, 56 * D * 2);
    touch(r, junk, lane * 128, wv * 16384);
    touch(r, junk, lane * 128, wv * 16384 + 8192);
  }
}

template <bool ZB>
__device__ __forceinline__ bool phase1(const Args& a, int pm, int cb, int kx, const u16* Pp, int kx2, unsigned qbox, f32x4 (&z1v)[2][2], const u16* bp, unsigned& bv) {
  const int tid = threadIdx.x, lane = tid & 63;
  const int wid = __builtin_amdgcn_readfirstlane(tid >> 6);
  const int wm = wid & 3, wn = wid >> 2;
  STAMP_DECL STAMP(0);
  const rsrc_t rX = RSRC(a.x + (size_t)pm * 128 * D, 128 * D * 2);
  const rsrc_t rW = ZB ? RSRC(a.win + 64 * cb, (D * F - 64 * cb) * 2) : RSRC(a.wdi + (size_t)384 * D, 64 * D * 2);
  const int r8 = lane >> 3, p8 = lane & 7;
  const unsigned sw_e = (p8 ^ (r8 >> 1)) << 4;
  const unsigned vx_e = (32 * (wid >> 1) + 4 * (wid & 1) + 8 * (r8 >> 2) + (r8 & 3)) * (D * 2) + sw_e;
  const unsigned vx_o = (vx_e ^ 64) + 16 * (D * 2);
  const unsigned vw1 = ZB ? (8 * wid + r8) * (F * 2) + ((p8 ^ ((((r8 >> 1) & 1) | ((wid & 1) << 1)) << 1)) << 4) + FH * 2
                          : (8 * wid + r8) * (D * 2) + (sw_e ^ ((wid & 1) << 6));
  constexpr unsigned WKB = ZB ? 64 * F * 2 : 128;
  const unsigned mb1 = wid * 1024, mb2 = wid * 2048;
  auto piece1 = [&](int st, int p) {
    const unsigned kb = st ^ kx;
    const int base = P1_RING + (st % 3) * 24576;
    if (p == 0) dma16(rX, mb2, base, vx_e, kb * 128);
    else if (p == 1) dma16(rW, mb1, base + 16384, vw1, kb * WKB);
    else dma16(rX, mb2, base + 1024, vx_o, kb * 128);
  };
  piece1(0, 0); piece1(0, 1); piece1(0, 2);
  const rsrc_t rG = RSRC(a.ws + WS_G, G_END);
  const unsigned role4 = (pm * 16 + cb) * 4;
  if (wid == 7) dma4_sc1(rG, 0, (lane & 1) * 1024, G_AN1 + role4);
  if (wid == 1) touch(rX, 2048, lane * 128, cb * 8192);
  if (wid == 2 && !ZB) touch(rX, 2048, lane * 128, 15 * 8192);
  if (wid == 2 && ZB) touch(rW, 2048, (lane & 31) * (F * 2) + (lane >> 5) * (FH * 2), pm * 32 * (F * 2));
  if (wid == 3) touch(RSRC(ZB ? (cb < 8 ? a.wdr : a.wdi) + (size_t)(cb & 7) * 64 * D : a.wdi + (size_t)384 * D, (ZB ? 64 : 128) * D * 2), 2048, lane * 128 + (ZB ? (lane >> 5) << 20 : 0), pm * (ZB ? 4096 : 8192));
  SCHED();

  const int fr = lane & 15, fq = lane >> 4;
  const int kh = wm >> 1;
  const unsigned fsw = ((fq ^ ((fr >> 1) & 7)) << 4) ^ (kh << 6);
  unsigned xa[2][2], wb[2][2];
#pragma unroll
  for (int m = 0; m < 2; ++m) { xa[m][0] = P1_RING + (32 * wm + 16 * (m ^ wn) + fr) * 128 + fsw; xa[m][1] = xa[m][0] ^ 64; }
  const int q4 = fr >> 2, pp = lane & 3;
  const int sp = ((q4 >> 1) & 1) | ((fq & 1) << 1);
  const unsigned trl = 128 * (8 * fq + q4) + 16 * ((pp >> 1) ^ (sp << 1)) + 8 * (pp & 1);
#pragma unroll
  for (int n = 0; n < 2; ++n) {
    const int tile = 2 * wn + (n ^ (wm & 1));
    if (ZB) { wb[n][0] = P1_RING + 16384 + (trl ^ (tile * 32)) + kh * 4096; wb[n][1] = wb[n][0] ^ 4096; }
    else { wb[n][0] = P1_RING + 16384 + (16 * tile + fr) * 128 + fsw; wb[n][1] = wb[n][0] ^ 64; }
  }
#pragma unroll
  for (int i = 0; i < 2; ++i)
#pragma unroll
    for (int ks = 0; ks < 2; ++ks) { OPAQUE(xa[i][ks]); OPAQUE(wb[i][ks]); }
  SCHED();
  piece1(1, 0); piece1(1, 1); piece1(1, 2);
  unsigned nonce = 0;
  STAMP(1);

  bf16x8 xf[16][2], wf[2][2];
  const f32x4 Z4 = {0, 0, 0, 0};
  f32x4 acc1[2][2] = {{Z4, Z4}, {Z4, Z4}};
  f32x4 gram = Z4, rsum = Z4, cs1 = Z4;
  const bf16x8 ONES = {0x3f80, 0x3f80, 0x3f80, 0x3f80, 0x3f80, 0x3f80, 0x3f80, 0x3f80};

  auto mm = [&](int kk, int i) {
    const int ks = kk & 1;
    if (i == 0) gram = MFMA16(xf[kk][0], xf[kk][0], gram);
    else if (i == 1) rsum = MFMA16(xf[kk][0], ONES, rsum);
    else if (i < 6) { const int m = (i - 2) >> 1, n = i & 1; acc1[m][n] = MFMA16(xf[kk][m], wf[ks][n], acc1[m][n]); }
    else cs1 = MFMA16(ONES, wf[0][0], cs1);
  };
  auto rd = [&](int kk, int i) {
    const int ks = kk & 1, so = ((kk >> 1) % 3) * 24576;
    if (i == 0 || i == 3) xf[kk][i & 1] = ld_frag(xa[i & 1][ks] + so);
    else wf[ks][i - 1] = ZB ? ld_tr(wb[i - 1][ks] + so) : ld_frag(wb[i - 1][ks] + so);
  };
  auto pieceT = [&](int p) {
    const rsrc_t rD = RSRC(ZB ? (cb < 8 ? a.wdr : a.wdi) + (size_t)(cb & 7) * 64 * D : a.wdi + (size_t)448 * D, 64 * D * 2);
    const unsigned ve = r8 * (D * 2) + sw_e, vo = (ve ^ 64) + 8 * (D * 2);
    dma16(rD, wid * 8192, p * 1024, (p & 1) ? vo : ve, (wid ^ kx) * 128 + (p >> 1) * 16 * (D * 2));
  };
  auto pieceW2 = [&](int p) {
    const unsigned ve = r8 * (F * 2) + ((p8 ^ (((r8 >> 1) & 1) << 1)) << 4), vo = (ve ^ 64) + 8 * (F * 2);
    dma16(rW, wid * 8192, P1_W2 + p * 1024, (p & 1) ? vo : ve, (wid ^ kx) * WKB + (p >> 1) * 16 * (F * 2));
  };
#define PIN_ACC() asm volatile("" : "+v"(acc1[0][0]), "+v"(acc1[0][1]), "+v"(acc1[1][0]), "+v"(acc1[1][1]), "+v"(gram), "+v"(rsum), "+v"(cs1))
#pragma unroll
  for (int st = 0; st < 8; ++st) {
    wait_vn(st <= 6 ? 3 : 4);
    WL0();
    BAR();
    if (st == 0) STAMP(2);
    SCHED();
#pragma unroll
    for (int i = 0; i < 6; ++i) {
      if (st > 0) mm(2 * st - 1, i);
      SCHED();
      if (i < 4) rd(2 * st, i);
      if (st + 2 < 8) { if (i == 1) piece1(st + 2, 0); if (i == 5) piece1(st + 2, 1); }
      else if (i == 1 || i == 4) pieceT((st - 6) * 4 + (i >> 2));
      SCHED();
    }
    PIN_ACC();
    WL0();
    SCHED();
#pragma unroll
    for (int i = 0; i < 7; ++i) {
      mm(2 * st, i);
      SCHED();
      if (i < 4) rd(2 * st + 1, i);
      if (st + 2 < 8) { if (i == 4) piece1(st + 2, 2); }
      else if (i == 1 || i == 5) pieceT((st - 6) * 4 + 2 + (i >> 2));
      SCHED();
    }
    PIN_ACC();
    if (st == 0 && wid == 7) {
      const unsigned a1 = lds_word(0), a2 = lds_word(4);
      nonce = a1 + 1 == a2 ? a1 + 2 : a1 + 1;
      st4_sc1(nonce, rG, 0, G_REQ + role4);
    }
    SCHED();
  }
  LAS float* const s_r = (LAS float*)(size_t)(P1_SCR);
  LAS float* const s_c = (LAS float*)(size_t)(P1_SCR + 512);
  LAS float* const s_u1 = (LAS float*)(size_t)(P1_SCR + 1024);
  LAS float* const s_z = (LAS float*)(size_t)(P1_SCR + 2048);
  LAS float* const s_n = (LAS float*)(size_t)(P1_SCR + 4096);
  LAS float* const s_u3 = (LAS float*)(size_t)(P1_SCR + 5120);
  LAS float* const s_u2 = (LAS float*)(size_t)(P1_SCR + 7168);
  constexpr int S_AN = P1_SCR + 9216, S_F1 = P1_SCR + 9472;
  {
#pragma unroll
    for (int i = 0; i < 6; ++i) mm(15, i);
    STAMP(3);
    unsigned i01 = 32 * wm + 16 * wn, i23 = 64 * kh + 16 * (2 * wn + (wm & 1));
    asm volatile("" : "+s"(i01), "+s"(i23));
    const float s1 = sel4(rsum, fr & 3), s2 = sel4(gram, fr & 3);
    const float mean = s1 * (1.0f / D);
    const float rstd = __builtin_amdgcn_rsqf(fmaxf(s2 * (1.0f / D) - mean * mean, 0.0f) + 1e-5f);
    if ((fr >> 2) == fq) { s_r[i01 + fr] = rstd; s_c[i01 + fr] = -mean * rstd; }
    if (fq == 0) s_u1[i23 + fr] = cs1[0];
  }
  const int s0 = pm * 128 + 32 * wm;
  const rsrc_t rT = RSRC(a.ws + WS_Q, (2 * D + FH) * S * 2);
  const unsigned vT = ((fr >> 2) & 1) * 65536 + fq * 256 + (4 * (fr >> 3) + (fr & 3)) * 16;
  const unsigned tb0 = fr * 128 + fsw, tb1 = tb0 ^ 64;
  if (wid == 7) dma4_sc1(rG, S_AN, (lane & 1) * 1024, G_AN1 + role4);
  WL0();
  BAR();
  STAMP(4);
  if (ZB) {
#pragma unroll
    for (int p = 0; p < 4; ++p) pieceW2(p);
  }
  f32x4 rq[2], cq[2];
#pragma unroll
  for (int m = 0; m < 2; ++m) {
    rq[m] = *(LAS f32x4*)(s_r + 32 * wm + 16 * (m ^ wn) + 4 * fq);
    cq[m] = *(LAS f32x4*)(s_c + 32 * wm + 16 * (m ^ wn) + 4 * fq);
  }
  auto store_t = [&](int T, f32x4 v0, f32x4 v1) {
    const unsigned a0 = cvt_pk(v0[0], v0[1]), a1 = cvt_pk(v0[2], v0[3]), b0 = cvt_pk(v1[0], v1[1]), b1 = cvt_pk(v1[2], v1[3]);
    const u32x4 o = wn ? u32x4{b0, b1, a0, a1} : u32x4{a0, a1, b0, b1};
    st16_sc1(o, rT, vT, (T >> 1) * 131072 + (T & 1) * 128 + s0 * 32);
  };
  auto lin_store = [&](f32x4 c0, f32x4 c1, float u, int T) { store_t(T, rq[0] * c0 + cq[0] * u, rq[1] * c1 + cq[1] * u); };
  float u1[2];
#pragma unroll
  for (int n = 0; n < 2; ++n) { const int ui = 16 * (2 * wn + (n ^ (wm & 1))) + fr; u1[n] = s_u1[ui] + s_u1[64 + ui]; }
  if (ZB) {
    f32x4 ps[2], pq[2];
#pragma unroll
    for (int n = 0; n < 2; ++n) {
#pragma unroll
      for (int m = 0; m < 2; ++m) {
        f32x4 v = rq[m] * acc1[m][n] + cq[m] * u1[n];
        const f32x2 g0 = gelu2(f32x2{v[0], v[1]}), g1 = gelu2(f32x2{v[2], v[3]});
        v = f32x4{g0[0], g0[1], g1[0], g1[1]};
        acc1[m][n] = v;
        if (n == 0) { ps[m] = v; pq[m] = v * v; } else { ps[m] += v; pq[m] += v * v; }
      }
      if (n == 0) {
        SCHED();
#pragma unroll
        for (int p = 4; p < 8; ++p) pieceW2(p);
        SCHED();
      }
    }
    float t[8], h[4];
#define DPP2(d, x, y, ctl, m0, m1) "v_add_f32_dpp " d ", " x ", " x " " ctl " row_mask:0xf bank_mask:" m0 "\n\tv_add_f32_dpp " d ", " y ", " y " " ctl " row_mask:0xf bank_mask:" m1 "\n\t"
#define DPPQ(d, ctl) "v_add_f32_dpp " d ", " d ", " d " " ctl " row_mask:0xf bank_mask:0xf\n\t"
    asm volatile("s_nop 1\n\t" DPP2("%0", "%8", "%16", "row_ror:8", "0x3", "0xc") DPP2("%1", "%9", "%17", "row_ror:8", "0x3", "0xc")
                 DPP2("%2", "%10", "%18", "row_ror:8", "0x3", "0xc") DPP2("%3", "%11", "%19", "row_ror:8", "0x3", "0xc")
                 DPP2("%4", "%12", "%20", "row_ror:8", "0x3", "0xc") DPP2("%5", "%13", "%21", "row_ror:8", "0x3", "0xc")
                 DPP2("%6", "%14", "%22", "row_ror:8", "0x3", "0xc") DPP2("%7", "%15", "%23", "row_ror:8", "0x3", "0xc") "s_nop 1"
                 : "=&v"(t[0]), "=&v"(t[1]), "=&v"(t[2]), "=&v"(t[3]), "=&v"(t[4]), "=&v"(t[5]), "=&v"(t[6]), "=&v"(t[7])
                 : "v"(ps[0][0]), "v"(pq[0][0]), "v"(ps[0][1]), "v"(pq[0][1]), "v"(ps[0][2]), "v"(pq[0][2]), "v"(ps[0][3]), "v"(pq[0][3]),
                   "v"(ps[1][0]), "v"(pq[1][0]), "v"(ps[1][1]), "v"(pq[1][1]), "v"(ps[1][2]), "v"(pq[1][2]), "v"(ps[1][3]), "v"(pq[1][3]));
    asm volatile("s_nop 1\n\t" DPP2("%0", "%4", "%8", "row_half_mirror", "0x5", "0xa") DPP2("%1", "%5", "%9", "row_half_mirror", "0x5", "0xa")
                 DPP2("%2", "%6", "%10", "row_half_mirror", "0x5", "0xa") DPP2("%3", "%7", "%11", "row_half_mirror", "0x5", "0xa") "s_nop 1\n\t"
                 DPPQ("%0", "quad_perm:[1,0,3,2]") DPPQ("%1", "quad_perm:[1,0,3,2]") DPPQ("%2", "quad_perm:[1,0,3,2]") DPPQ("%3", "quad_perm:[1,0,3,2]")
                 DPPQ("%0", "quad_perm:[2,3,0,1]") DPPQ("%1", "quad_perm:[2,3,0,1]") DPPQ("%2", "quad_perm:[2,3,0,1]") DPPQ("%3", "quad_perm:[2,3,0,1]") "s_nop 0"
                 : "=&v"(h[0]), "=&v"(h[1]), "=&v"(h[2]), "=&v"(h[3])
                 : "v"(t[0]), "v"(t[1]), "v"(t[2]), "v"(t[3]), "v"(t[4]), "v"(t[5]), "v"(t[6]), "v"(t[7]));
    if ((fr & 3) == 0) *(LAS f32x4*)(s_z + (wn * 128 + 32 * wm + 8 * fq + 4 * ((fr >> 3) ^ wn) + 2 * ((fr >> 2) & 1)) * 2) = f32x4{h[0], h[1], h[2], h[3]};
    WAIT_V(8);
  } else {
#pragma unroll
    for (int n = 0; n < 2; ++n) lin_store(acc1[0][n], acc1[1][n], u1[n], 56 + 2 * wn + (n ^ (wm & 1)));
    WAIT_V(2);
  }
  WL0();
  BAR();
  STAMP(5);
  if (ZB && wid == 7) {
    __builtin_amdgcn_s_setprio(2);
    const f32x4 p = *(LAS f32x4*)(s_z + lane * 4) + *(LAS f32x4*)(s_z + 256 + lane * 4);
    wait_go(rG, G_AN1 + role4, nonce, lds_word(S_AN));
    st16_sc1(__builtin_bit_cast(u32x4, p) | 1u, rG, lane * 16, G_PAY + (pm * 14 + cb) * 1024);
  }
  STAMP(6);
  unsigned w2[2][2];
  bf16x8 zw[4][2];
  auto rd2 = [&](int q, int n) { zw[q & 3][n] = ld_tr(w2[n][q & 1] + (q >> 1) * 8192); };
  {
    f32x4 acct[2][2] = {{Z4, Z4}, {Z4, Z4}};
    const unsigned t30 = tb0 + wn * 4096, t31 = tb1 + wn * 4096;
    const unsigned cb0 = tb0 + wid * 8192, cb1 = cb0 ^ 64, c2b = P1_W2 + wid * 8192 + trl;
    bf16x8 w[8], cf[2];
    f32x4 cs[4];
    float u3[2];
    auto rd3 = [&](int f) { const int kk = f >> 1; w[f & 7] = ld_frag(((kk & 1) ? t31 : t30) + (kk >> 1) * 8192 + (f & 1) * 2048); };
#pragma unroll
    for (int f = 0; f < 4; ++f) rd3(f);
    SCHED();
#pragma unroll
    for (int f = 0; f < 32; ++f) {
      const int kk = f >> 1, n = f & 1;
      acct[0][n] = MFMA16(xf[kk][0], w[f & 7], acct[0][n]);
      SCHED();
      if (f == 13) { if (ZB) wait_vn(0); WL0(); BAR(); }
      if (f + 4 < 32) rd3(f + 4);
      if (f < 8) cf[f & 1] = ld_frag(((f & 1) ? cb1 : cb0) + (f >> 1) * 2048);
      if (f == 11) s_u3[lane * 8 + wid] = sel4(f32x4{cs[0][0], cs[1][0], cs[2][0], cs[3][0]}, fq);
      if (ZB && f >= 14 && f < 22) cf[f & 1] = ld_tr((c2b ^ (((f - 14) >> 1) * 32)) + (f & 1) * 4096);
      if (ZB && f == GATHER_AT && wid == 7) {
        dma4_sc1(rG, S_AN, (lane & 1) * 1024, G_AN1 + role4);
#pragma unroll
        for (int i = 0; i < 14; ++i) dma16_sc1(rG, 0, P1_GAT + i * 1024, lane * 16, G_PAY + pm * 14336 + i * 1024);
      }
      if (f == 23 || f == 25) {
        const int q = (f - 23) >> 1;
        const f32x4 uu = *(LAS f32x4*)(s_u3 + (32 * wn + 16 * q + fr) * 8) + *(LAS f32x4*)(s_u3 + (32 * wn + 16 * q + fr) * 8 + 4);
        u3[q] = (uu[0] + uu[1]) + (uu[2] + uu[3]);
      }
      if (ZB && f == 26) s_u2[wid * 64 + lane] = sel4(f32x4{cs[0][0], cs[1][0], cs[2][0], cs[3][0]}, fq);
      if (ZB && f == 27) {
#pragma unroll
        for (int q = 0; q < 2; ++q)
#pragma unroll
          for (int ks = 0; ks < 2; ++ks) { w2[q][ks] = wb[q][ks] + (P1_W2 - P1_RING - 16384); OPAQUE(w2[q][ks]); }
      }
      if (ZB && f >= 28) rd2((f - 28) >> 1, f & 1);
      SCHED();
      acct[1][n] = MFMA16(xf[kk][1], w[f & 7], acct[1][n]);
      SCHED();
      if (f >= 1 && f < 9) { const int g = f - 1; cs[g >> 1] = MFMA16(ONES, cf[g & 1], (g & 1) ? cs[g >> 1] : Z4); SCHED(); }
      if (ZB && f >= 15 && f < 23) { const int g = f - 15; cs[g >> 1] = MFMA16(ONES, cf[(f - 1) & 1], (g & 1) ? cs[g >> 1] : Z4); SCHED(); }
    }
#pragma unroll
    for (int n = 0; n < 2; ++n) lin_store(acct[0][n], acct[1][n], u3[n], (ZB ? 4 * cb : 60) + 2 * wn + n);
  }
  STAMP(7);
  bv = bp[(wid & 2) * 32 + lane];
  auto publish = [&]() {
    if (ZB) WAIT_V(8); else { pin(bv); WAIT_V(0); }
    WL0();
    BAR();
    if (wid == 7) {
      wait_go(rG, G_AN2 + role4, nonce, lds_word(S_AN + 4));
      const unsigned box = (fq & 1) * 128 + (cb >> 1) * 16 + fr;
      st4_sc1(1u, rG, fq < 2 ? box * 128 + (pm * 2 + (cb & 1)) * 4 : G_ALL + role4, 0);
    }
  };
  auto earlyP = [&](int k) {
    const rsrc_t rP = RSRC(Pp, 128 * S * 2);
    const unsigned ve = (16 * wid + r8) * (S * 2) + sw_e, vo = (ve ^ 64) + 8 * (S * 2);
    dma16(rP, mb2, k * 16384, ve, (k ^ kx2) * 128);
    dma16(rP, mb2, k * 16384 + 1024, vo, (k ^ kx2) * 128);
  };
  if (ZB) {
    f32x4 acc2[2][2] = {{Z4, Z4}, {Z4, Z4}};
    SCHED();
#pragma unroll
    for (int q = 0; q < 16; ++q) {
      if (q == B10_AT) {
        if (wid == 7) {
          f32x4 t;
          for (unsigned spin = 0; spin < SPIN; ++spin) {
            if (spin) WAIT_V(0); else WAIT_V(2);
            u32x4 al = *(LAS u32x4*)(size_t)(P1_GAT + lane * 16);
            t = __builtin_bit_cast(f32x4, al);
#pragma unroll
            for (int i = 1; i < 14; ++i) { const u32x4 pv = *(LAS u32x4*)(size_t)(P1_GAT + i * 1024 + lane * 16); al &= pv; t += __builtin_bit_cast(f32x4, pv); }
            if (__all((al[0] & al[1] & al[2] & al[3] & 1u) != 0)) break;
            WL0();
#pragma unroll
            for (int i = 0; i < 14; ++i) dma16_sc1(rG, 0, P1_GAT + i * 1024, lane * 16, G_PAY + pm * 14336 + i * 1024);
          }
          const float m0 = t[0] * (1.0f / FH), m1 = t[2] * (1.0f / FH);
          const float r0 = __builtin_amdgcn_rsqf(fmaxf(t[1] * (1.0f / FH) - m0 * m0, 0.0f) + 1e-5f);
          const float r1 = __builtin_amdgcn_rsqf(fmaxf(t[3] * (1.0f / FH) - m1 * m1, 0.0f) + 1e-5f);
          *(LAS f32x2*)(s_n + 2 * lane) = f32x2{r0, r1};
          *(LAS f32x2*)(s_n + 128 + 2 * lane) = f32x2{-m0 * r0, -m1 * r1};
          __builtin_amdgcn_s_setprio(0);
        }
        pin(bv);
        WL0();
        BAR();
        STAMP(8);
        f32x4 r2[2], c2[2];
#pragma unroll
        for (int m = 0; m < 2; ++m) {
          r2[m] = *(LAS f32x4*)(s_n + 32 * wm + 8 * fq + 4 * (m ^ wn));
          c2[m] = *(LAS f32x4*)(s_n + 128 + 32 * wm + 8 * fq + 4 * (m ^ wn));
        }
#pragma unroll
        for (int n = 0; n < 2; ++n) store_t(64 + 4 * cb + 2 * wn + (n ^ (wm & 1)), acc1[0][n] * r2[0] + c2[0], acc1[1][n] * r2[1] + c2[1]);
        SCHED();
      }
      if (q == B11_AT) { publish(); STAMP(9); SCHED(); }
#pragma unroll
      for (int i = 0; i < 4; ++i) {
        const int m = i >> 1, n = i & 1;
        acc2[m][n] = MFMA16(zw[q & 3][n], xf[q][m], acc2[m][n]);
        SCHED();
        if (i < 2 && q + 2 < 16) rd2(q + 2, i);
        if (i == 3 && q >= B10_AT && q < B10_AT + 4) earlyP(q - B10_AT);
        SCHED();
      }
    }
#pragma unroll
    for (int n = 0; n < 2; ++n) {
      const int ui = 16 * (2 * wn + (n ^ (wm & 1))) + 4 * fq;
      f32x4 u = *(LAS f32x4*)(s_u2 + ui);
#pragma unroll
      for (int j = 1; j < 8; ++j) u += *(LAS f32x4*)(s_u2 + j * 64 + ui);
#pragma unroll
      for (int m = 0; m < 2; ++m) z1v[m][n] = acc2[m][n] * s_r[32 * wm + 16 * (m ^ wn) + fr] + u * s_c[32 * wm + 16 * (m ^ wn) + fr];
    }
  } else {
    STAMP(8);
    publish();
    STAMP(9);
    if (DW >= 0 && wid < 4) warm_window(a, P1_SCR + 9728, pm, DW, wid, lane);
    earlyP(0); earlyP(1); earlyP(2); earlyP(3);
    if (DW >= 0 && wid < 4) WAIT_V(0);
  }
  const bool allq = true;
  STAMP_OUT(a.ws, 0, 10);
  return allq;
}

__global__ __launch_bounds__(512, 2) void fused(Args a) {
  extern __shared__ __attribute__((aligned(1024))) char smem[];
  asm volatile("" ::"v"(smem));
  const unsigned long long pstart = __builtin_amdgcn_s_memrealtime();
  const int bx = blockIdx.x & 7, bi = blockIdx.x >> 3;
  if (bi < CODEWARM && threadIdx.x < 64) {
    unsigned long long pc;
    asm volatile("s_getpc_b64 %0" : "=s"(pc));
    touch(RSRC(pc & ~127ull, CODEWARM * 8192), 1024, threadIdx.x * 128, bi * 8192);
  }
  const int pm = 4 * (bx >> 1) + (bi >> 3), cb = 2 * (bi & 7) + (bx & 1);
  int mat, tm, tn;
  if (bx & 1) { mat = 2; tm = pm; tn = bi & 7; }
  else { mat = bx >> 2; tm = ((bx >> 1) & 1) * 8 + (bi >> 2); tn = bi & 3; }
  const int kx = (tm ^ tn) & K2_KXMASK;
  const u16* Pp = (mat == 0 ? a.wsr : mat == 1 ? a.wsi : a.wsp) + (size_t)tm * 128 * S;
  f32x4 z1v[2][2];
  const unsigned qbox = (mat * 4 + tn) * 16 + tm;
  bool allq = true;
  unsigned bv = 0;
  {
    const int kx1 = ((bi & 7) ^ (2 * (bi >> 3))) & K1_KXMASK;
    if (__builtin_expect(cb < 14, 1)) allq = phase1<true>(a, pm, cb, kx1, Pp, kx, qbox, z1v, a.bsp + 128 * tm, bv);
    else if (cb == 14) allq = phase1<false>(a, pm, cb, kx1, Pp, kx, qbox, z1v, a.bsp + 128 * tm, bv);
  }
  int tid = threadIdx.x;
  asm volatile("" : "+v"(tid));
  const int lane = tid & 63;
  const int wid = __builtin_amdgcn_readfirstlane(tid >> 6);
  STAMP_DECL STAMP(0);
  const int tm3 = bx * 4 + (bi >> 3), tn3 = bi & 7;
  const int fr = lane & 15, fq = lane >> 4;
  const unsigned vl = lane * 16;
  const rsrc_t rG = RSRC(a.ws + WS_G, G_END);
  const bool work2 = cb != 15;
  auto z1_finish = [&]() {
    if (cb < 14) {
      const int wm = wid & 3, wn = wid >> 2;
      const rsrc_t rZ = RSRC(a.ws + WS_Z1, FH * S * 2);
      const unsigned vZ = (fr >> 3) * (112 * 256) + (2 * ((fq & 1) ^ (wm & 1)) + (fq >> 1)) * 256 + (fr & 7) * 16;
      const unsigned sZ = ((pm * 8 + 2 * wm) * 112 + 8 * cb + 4 * wn) * 256;
#pragma unroll
      for (int m = 0; m < 2; ++m) {
        unsigned pk[2][2];
#pragma unroll
        for (int n = 0; n < 2; ++n) {
          const f32x2 g0 = gelu2(f32x2{z1v[m][n][0], z1v[m][n][1]}), g1 = gelu2(f32x2{z1v[m][n][2], z1v[m][n][3]});
          pk[n][0] = cvt_pk(g0[0], g0[1]); pk[n][1] = cvt_pk(g1[0], g1[1]);
        }
        auto w0 = __builtin_amdgcn_permlane16_swap(pk[0][0], pk[1][0], false, false);
        auto w1 = __builtin_amdgcn_permlane16_swap(pk[0][1], pk[1][1], false, false);
        st16_sc1(u32x4{w0[0], w1[0], w0[1], w1[1]}, rZ, vZ, sZ + 128 * (m ^ wn));
      }
    }
  };
  if (blockIdx.x == 255) {
    for (int i = 0; i < 8; ++i) st16_sc1(u32x4{0, 0, 0, 0}, rG, vl, (wid * 8 + i) * 1024);
    st16_sc1(u32x4{0, 0, 0, 0}, rG, vl, G_ALL);
    WAIT_V(0);
  }
  if (blockIdx.x == 255) BAR();
  if (wid >= 4) {
    const int lw = wid - 4;
    const int r8 = lane >> 3, p = lane & 7;
    if (!work2) {
      __builtin_amdgcn_s_setprio(3);
      if (wid == 7) {
        const unsigned role4 = (pm * 16 + 15) * 4;
        for (int i = 0; i < 14; ++i) st16_sc1(u32x4{0, 0, 0, 0}, rG, vl, G_PAY + pm * 14336 + i * 1024);
        const unsigned nonce = __builtin_amdgcn_readfirstlane(ld4_sc1(rG, 0, G_AN2 + role4)) + 1;
        st4_sc1(nonce, rG, 0, G_REQ + role4);
        for (unsigned spin = 0; spin < SPIN; ++spin) {
          st4_sc1(ld4_sc1(rG, fr * 4, G_REQ + pm * 64), rG, fr * 4, G_AN1 + pm * 64);
          if (spin == PF_PASS) BAR();
          if (spin >= PF_PASS && __all(fr > 13 || ld4_sc1(rG, fr * 1024, G_PAY + pm * 14336) != 0u)) break;
        }
        wait_go(rG, G_AN2 + role4, nonce, nonce - 1);
        st4_sc1(1u, rG, 0, G_ALL + role4);
      } else {
        BAR();
      }
      if (wid == 6 && blockIdx.x == 255) {
        for (unsigned spin = 0; spin < SPIN; ++spin) {
          const u32x4 rq = ld16_sc1(rG, vl, G_REQ);
          st16_sc1(rq, rG, vl, G_AN2);
          const u32x4 fl = ld16_sc1(rG, vl, G_ALL);
          if (__all((fl[0] & fl[1] & fl[2] & fl[3]) == 1u)) break;
        }
      }
    }
    const rsrc_t rP = RSRC(Pp, 128 * S * 2);
    const unsigned vo_e = (lw * 32 + r8) * (S * 2) + ((p ^ (r8 >> 1)) << 4);
    const unsigned vo_o = (vo_e ^ 64) + 8 * (S * 2);
    auto stageP = [&](int kt, unsigned poff) {
      const unsigned mb = poff + lw * 4096, so = (kt ^ kx) * 128;
#pragma unroll
      for (int j = 0; j < 4; ++j) dma16(rP, mb, j * 1024, (j & 1) ? vo_o : vo_e, so + (j >> 1) * 16 * (S * 2));
    };
    const rsrc_t rWo = RSRC(a.wout + 64 * tn3, (FH * D - 64 * tn3) * 2);
    const unsigned vw_e = (32 * lw + r8) * (D * 2) + ((p ^ (((r8 >> 1) & 1) << 1)) << 4), vw_o = (vw_e ^ 64) + 8 * (D * 2);
    u32x4 wreg[12];
    auto wpark = [&](int c) {
#pragma unroll
      for (int i = 4 * c; i < 4 * c + 4; ++i) {
        const int rg = i < 8 ? 2 + (i >> 2) : 6;
        ld16_async(wreg[i], rWo, (i & 1) ? vw_o : vw_e, (rg * 128 + ((i >> 1) & 1) * 16) * (D * 2));
      }
    };
    auto wput = [&](int rg) {
#pragma unroll
      for (int j = 0; j < 4; ++j) *(LAS u32x4*)(size_t)(wbase(rg) + lw * 4096 + j * 1024 + vl) = wreg[(rg == 6 ? 8 : (rg - 2) * 4) + j];
    };
    auto wdma = [&](int rg) {
#pragma unroll
      for (int j = 0; j < 4; ++j) dma16(rWo, lw * 4096, wbase(rg) + j * 1024, (j & 1) ? vw_o : vw_e, (rg * 128 + (j >> 1) * 16) * (D * 2));
    };
    if (work2) {
      u32x4 preg[8];
#pragma unroll
      for (int i = 0; i < 8; ++i) ld16_async(preg[i], rP, (i & 1) ? vo_o : vo_e, ((4 + (i >> 2)) ^ kx) * 128 + ((i >> 1) & 1) * 16 * (S * 2));
      wpark(0); wpark(1); wpark(2);
      if (wid != 7) z1_finish();
      else {
        for (unsigned spin = 0; spin < SPIN; ++spin) {
          const unsigned fl = ld4_sc1(rG, (lane & 31) * 4, G_F1 + qbox * 128);
          const bool ok = fl == 1u || (qbox >= 112 && qbox < 128 && (lane & 1));
          allq = __all(ok);
          if (__all(ok || (WINSTART && (lane & 31) > 3))) break;
        }
      }
      WL0();
      BAR();
      STAMP(1);
      const rsrc_t rQ = RSRC(a.ws + WS_Q + (size_t)(mat * 4 + tn) * 128 * S * 2, 128 * S * 2);
      const unsigned vq = vl + lw * 131072;
      auto stageQ = [&](int kt) {
        const unsigned mb = (kt & 3) * 16384 + lw * 4096, so = (kt ^ kx) * 2048;
#pragma unroll
        for (int j = 0; j < 4; ++j) dma16_sc1(rQ, mb, P2_QB + j * 1024, vq, so + (j >> 1) * 65536 + (j & 1) * 1024);
      };
      stageQ(0); stageQ(1);
      if (wid == 7) z1_finish();
      WAIT_V(4); BAR();
      stageQ(2); stageQ(3);
#pragma unroll
      for (int i = 0; i < 8; ++i) *(LAS u32x4*)(size_t)((4 + (i >> 2)) * 16384 + lw * 4096 + (i & 3) * 1024 + vl) = preg[i];
      WL0();
      for (unsigned spin = 0; !allq && spin < SPIN; ++spin)
        allq = __all(ld4_sc1(rG, (lane & 31) * 4, G_F1 + qbox * 128) == 1u || (qbox >= 112 && qbox < 128 && (lane & 1)));
      WAIT_V(8); BAR(); stageQ(4); stageP(6, 0);
      WAIT_V(12); BAR(); stageQ(5); stageP(7, 16384);
      WAIT_V(16); BAR(); stageQ(6); stageP(8, 2 * 16384);
      unsigned poff = 3 * 16384;
      unsigned zf;
      for (int kt = 3; kt < 23; ++kt) {
        WAIT_V(20);
        BAR();
        if (kt == 3 && wid == 7 && cb < 14) st4_sc1(1u, rG, 0, G_FZ + (pm * 8 + (cb >> 1)) * 128 + (cb & 1) * 4);
        stageQ(kt + 4); stageP(kt + 6, poff);
        poff = poff == 5 * 16384 ? 0 : poff + 16384;
      }
      asm volatile("buffer_load_dword %0, %1, %2, %3 offen sc1" : "=v"(zf) : "v"((lane & 1) * 4), "s"(rG), "s"(G_FZ + (tm * 8 + tn) * 128) : "memory");
#pragma unroll
      for (int kt = 23; kt < 26; ++kt) {
        WAIT_V(20);
        BAR();
        stageQ(kt + 4); stageP(kt + 6, (kt % 6) * 16384);
      }
      asm volatile("s_waitcnt vmcnt(20)" : "+v"(zf)::"memory");
      if (mat == 2) {
        const rsrc_t rZ = RSRC(a.ws + WS_Z1 + (size_t)((8 * tm + lw) * 112 + 16 * tn) * 256, 4 * 112 * 256 + 4096);
        auto stageZ = [&](int h) {
#pragma unroll
          for (int j = 0; j < 4; ++j) dma16_sc1(rZ, lw * 4096, P2_Z + h * 16384 + j * 1024, vl, h * (4 * 112 * 256) + j * 1024);
        };
        for (unsigned spin = 0; !__all(zf == 1u) && spin < SPIN; ++spin) zf = ld4_sc1(rG, (lane & 1) * 4, G_FZ + (tm * 8 + tn) * 128);
        WAIT_V(20); BAR(); stageQ(30); stageZ(0);
        WAIT_V(20); BAR(); stageQ(31); stageZ(1);
        WAIT_V(20); BAR(); wdma(0); wdma(4);
        WAIT_V(20); BAR(); wdma(1); wdma(5);
        WAIT_V(20); BAR(); wput(2); wput(6);
        WAIT_V(16); BAR(); wput(3);
      } else {
        WAIT_V(20); BAR(); stageQ(30);
        WAIT_V(16); BAR(); stageQ(31);
        WAIT_V(12); BAR(); wdma(0); wdma(4);
        WAIT_V(12); BAR(); wdma(1); wdma(5);
        WAIT_V(16); BAR(); wput(2); wput(6);
        BAR(); wput(3);
      }
    } else {
      STAMP(1);
      wdma(0); wdma(4); wdma(1); wdma(5); wdma(2); wdma(3); wdma(6);
      __builtin_amdgcn_s_setprio(0);
    }
    STAMP(2); STAMP(3);
    WAIT_V(0);
  } else if (work2) {
    const int wp = wid >> 1, wq = wid & 1;
    f32x4 acc[4][4];
    const int sw = (fr >> 1) & 7;
    const unsigned ra = (wp * 64 + fr) * 128 + ((fq ^ sw) << 4);
    const unsigned rb = P2_QB + wq * 8192 + vl;
    bf16x8 a0[4], b0[4], a1[4], b1[4];
    unsigned poff = 0;
    unsigned pa0 = ra, pa1 = pa0 ^ 64;
#define K2_RD(AF, BF, pa, sl, ks, i) do { \
    if ((i) == 0) AF[0] = ld_frag(pa); \
    else if ((i) <= 4) BF[(i) - 1] = ld_frag(rb + (sl) * 16384 + ((i) - 1) * 2048 + (ks) * 1024); \
    else AF[(i) - 4] = ld_frag(pa + ((i) - 4) * 2048); } while (0)
#define K2_MM(AF, BF, i) acc[(i) >> 2][(i) & 3] = MFMA16(BF[(i) & 3], AF[(i) >> 2], acc[(i) >> 2][(i) & 3])
#define K2_STEP(s, LAST) do { \
    SCHED(); WL0(); SCHED(); \
    _Pragma("unroll") for (int i = 0; i < 16; ++i) { K2_MM(a0, b0, i); SCHED(); if (i < 8) K2_RD(a1, b1, pa1, s, 1, i); \
      if (i == 10) { poff = poff == 5 * 16384 ? 0 : poff + 16384; pa0 = ra + poff; } SCHED(); } \
    WL0(); \
    if ((s) == 3) WAIT_V(0); \
    BAR(); \
    SCHED(); \
    _Pragma("unroll") for (int i = 0; i < 16; ++i) { K2_MM(a1, b1, i); SCHED(); if (!(LAST) && i < 8) K2_RD(a0, b0, pa0, ((s) + 1) & 3, 0, i); \
      if (i == 10) pa1 = pa0 ^ 64; SCHED(); } \
  } while (0)
    z1_finish();
    WL0();
    BAR();
    if (cb < 14) WAIT_V(2); else WAIT_V(0);
#pragma unroll
    for (int ni = 0; ni < 4; ++ni) {
      const float bias = mat == 2 ? __uint_as_float((unsigned)__shfl(bv, 16 * ni + fr) << 16) : 0.0f;
#pragma unroll
      for (int n = 0; n < 4; ++n) acc[ni][n] = f32x4{bias, bias, bias, bias};
    }
    BAR();
    STAMP(1);
#pragma unroll
    for (int i = 0; i < 8; ++i) K2_RD(a0, b0, pa0, 0, 0, i);
    for (int kt = 0; kt < 28; kt += 4) {
      K2_STEP(0, 0); K2_STEP(1, 0); K2_STEP(2, 0); K2_STEP(3, 0);
    }
    K2_STEP(0, 0); K2_STEP(1, 0); K2_STEP(2, 0); K2_STEP(3, 1);
    SCHED();
    STAMP(2);
    if (mat < 2) {
      const rsrc_t rY = RSRC(a.ws + WS_Y + (size_t)mat * S * D * 2, S * D * 2);
      const unsigned so = ((8 * tm + 4 * wp) * 64 + 16 * tn + 8 * wq) * 256;
#pragma unroll
      for (int ni = 0; ni < 4; ++ni)
#pragma unroll
        for (int P = 0; P < 2; ++P) {
          const f32x4 c0 = acc[ni][2 * P], c1 = acc[ni][2 * P + 1];
          const u32x4 v = {cvt_pk(c0[0], c0[1]), cvt_pk(c0[2], c0[3]), cvt_pk(c1[0], c1[1]), cvt_pk(c1[2], c1[3])};
          st16_sc1(v, rY, vl, so + (ni * 64 + 4 * P) * 256);
        }
    } else {
      const rsrc_t rH = RSRC(a.ws + WS_H, S * FH * 2);
      const unsigned so = ((8 * tm + 4 * wp) * 112 + 16 * tn + 8 * wq) * 256;
      const unsigned za = P2_Z + wp * 16384 + wq * 2048 + (fq * 16 + ((fr & 4) << 1) + ((fr & 8) >> 1) + (fr & 3)) * 16;
#pragma unroll
      for (int ni = 0; ni < 4; ++ni) {
#pragma unroll
        for (int P = 0; P < 2; ++P) {
          const f32x4 c0 = acc[ni][2 * P], c1 = acc[ni][2 * P + 1];
          const u32x4 z = *(const LAS u32x4*)(size_t)(za + ni * 4096 + P * 1024);
          const u32x4 v = {cvt_pk(bf_lo(z[0]) * c0[0], bf_hi(z[0]) * c0[1]), cvt_pk(bf_lo(z[1]) * c0[2], bf_hi(z[1]) * c0[3]),
                           cvt_pk(bf_lo(z[2]) * c1[0], bf_hi(z[2]) * c1[1]), cvt_pk(bf_lo(z[3]) * c1[2], bf_hi(z[3]) * c1[3])};
          st16_sc1(v, rH, vl, so + (ni * 112 + 4 * P) * 256);
        }
      }
    }
    STAMP(3);
    WAIT_V(0);
  } else {
    if (PF_EARLY && blockIdx.x != 255) { warm_window(a, P2_Z, pm, 8, wid, lane); }
    BAR();
    STAMP(1);
    for (int i = 0; i < PF_N && blockIdx.x != 255; ++i) {
      warm_window(a, P2_Z, pm, (PF_LIST >> (4 * i)) & 15, wid, lane);
      WAIT_V(6);
    }
    STAMP(2);
    WAIT_V(0);
    STAMP(3);
  }
  WL0();
  BAR();
  STAMP(4);
  {
    const int wr = (wid + tn3) & 7, tp = wr >> 2, kq = wr & 3;
    const int tt3 = 4 * tm3 + 2 * tp + (kq & 1);
    const int cq = 2 * (fq & 1) + (fq >> 1);
    const int c3 = 8 * tn3 + 2 * (kq & 2);
    if (wid == 4 && work2) {
      const unsigned box = mat == 2 ? fr : (fr & 8) + 2 * tn + (fr & 1);
      st4_sc1(1u, rG, box * 128, G_FP + tm * 2048 + (mat == 2 ? tn : 8 + mat) * 4);
    }
    const int q4 = fr >> 2, pp = lane & 3;
    const int sp = ((q4 >> 1) & 1) | ((fq & 1) << 1);
    const unsigned wa = kq * 4096 + 128 * (8 * fq + q4) + 16 * ((2 * (kq & 2) + (pp >> 1)) ^ (sp << 1)) + 8 * (pp & 1);
    bf16x8 wf[7][4];
    auto ld_w = [&]() {
#pragma unroll
      for (int mi = 0; mi < 4; ++mi) {
        unsigned lo = wa ^ (mi * 32), mid = lo + 65536, hi = lo + 131072;
        OPAQUE(mid); OPAQUE(hi);
#pragma unroll
        for (int g = 0; g < 7; ++g) wf[g][mi] = ld_tr((wbase(g) < 65536 ? lo : wbase(g) < 131072 ? mid : hi) + (wbase(g) & 65535));
      }
    };
    if (wid == 4) {
      for (unsigned spin = 0; spin < SPIN; ++spin) {
        const unsigned fl = ld4_sc1(rG, fr * 4, G_FP + (tm3 * 8 + tn3) * 128);
        if (__all(fl == 1u || fr == 7 || fr > 9)) break;
      }
    } else {
      ld_w();
    }
    BAR();
    STAMP(5);
    const rsrc_t rH = RSRC(a.ws + WS_H + (size_t)((4 * tm3 + 2 * tp) * 112 + 4 * kq) * 256, 2 * 112 * 256);
    const rsrc_t rY = RSRC(a.ws + WS_Y + (size_t)(tt3 * 64 + c3) * 256, S * D * 2 + 1024);
    const unsigned vh0 = vl + (kq & 1) * (112 * 256), vh1 = vl + ((kq & 1) ^ 1) * (112 * 256);
    u32x4 hf[7][2];
#pragma unroll
    for (int g = 0; g < 7; ++g) {
      hf[g][0] = __builtin_amdgcn_raw_buffer_load_b128(rH, vh0, g * 4096, 16);
      hf[g][1] = __builtin_amdgcn_raw_buffer_load_b128(rH, vh1, g * 4096, 16);
    }
    const unsigned vy = cq * 256 + fr * 16;
    const u32x4 e1 = __builtin_amdgcn_raw_buffer_load_b128(rY, vy, 0, 16), e2 = __builtin_amdgcn_raw_buffer_load_b128(rY, vy, S * D * 2, 16);
    const u32x4 ex = *(const u32x4*)(a.x + (size_t)(tt3 * 16 + fr) * D + (c3 + cq) * 8);
    SCHED();
    if (wid == 4) ld_w();
    f32x4 acc[2][4];
#pragma unroll
    for (int g = 0; g < 7; ++g)
#pragma unroll
      for (int tl = 0; tl < 2; ++tl)
#pragma unroll
        for (int mi = 0; mi < 4; ++mi) acc[tl][mi] = MFMA16(wf[g][mi], __builtin_bit_cast(bf16x8, hf[g][tl]), (g ? acc[tl][mi] : f32x4{0, 0, 0, 0}));
    STAMP(6);
    auto xb = [&](int role, int d, int j) { return (LAS f32x4*)(size_t)((d < 3 ? P2_Z + (d - 1) * 16384 : P3_X3) + (role * 2 + j) * 1024 + vl); };
#pragma unroll
    for (int d = 1; d < 4; ++d) {
      *xb(wr, d, 0) = acc[d & 1][d & 2];
      *xb(wr, d, 1) = acc[d & 1][(d & 2) + 1];
    }
    WL0();
    BAR();
    f32x4 s0 = acc[0][0], s1 = acc[0][1];
#pragma unroll
    for (int d = 1; d < 4; ++d) {
      s0 += *xb(wr ^ d, d, 0);
      s1 += *xb(wr ^ d, d, 1);
    }
    f32x4 c0, c1;
#pragma unroll
    for (int j = 0; j < 4; ++j) {
      auto w = __builtin_amdgcn_permlane16_swap(__float_as_uint(s0[j]), __float_as_uint(s1[j]), false, false);
      c0[j] = __uint_as_float(w[0]); c1[j] = __uint_as_float(w[1]);
    }
    u32x4 o;
#pragma unroll
    for (int k = 0; k < 4; ++k) {
      const f32x4 c = (k >> 1) ? c1 : c0;
      const float lo = bf_lo(ex[k]) + bf_lo(e1[k]) - bf_lo(e2[k]) + c[(k & 1) * 2];
      const float hi = bf_hi(ex[k]) + bf_hi(e1[k]) - bf_hi(e2[k]) + c[(k & 1) * 2 + 1];
      o[k] = cvt_pk(lo, hi);
    }
    st16_sc1(o, RSRC(a.out, S * D * 2), ((tt3 * 16 + fr) * D + (c3 + cq) * 8) * 2, 0);
  }
  STAMP(7);
  STAMP_OUT(a.ws, 1, 8);
  {
    asm volatile("s_waitcnt vmcnt(0)" ::: "memory");
    const unsigned long long pend = __builtin_amdgcn_s_memrealtime();
    const rsrc_t rS = RSRC(a.ws + (100u << 20), 65536);
    st16_sc1(u32x4{(unsigned)pstart, (unsigned)pend, 0, 0}, rS, 0, (blockIdx.x * 8 + wid) * 16);
    if (blockIdx.x == 73 && wid == 0) {
      while (__builtin_amdgcn_s_memrealtime() < pstart + 6000) __builtin_amdgcn_s_sleep(1);
      int mns = 1 << 30, mxe = -(1 << 30);
      for (int i = 0; i < 32; ++i) {
        const u32x4 v = ld16_sc1(rS, lane * 16, i * 1024);
        const int st = (int)(v[0] - (unsigned)pstart), en = (int)(v[1] - (unsigned)pstart);
        mns = min(mns, st); mxe = max(mxe, en);
      }
      for (int o = 32; o > 0; o >>= 1) { mns = min(mns, __shfl_xor(mns, o)); mxe = max(mxe, __shfl_xor(mxe, o)); }
      const int q = __builtin_amdgcn_readfirstlane(mxe - mns);
      const unsigned long long tend = pstart + __builtin_amdgcn_readfirstlane(mns) + 10 * q;
      while (__builtin_amdgcn_s_memrealtime() < tend) ;
    }
  }
}

extern "C" void kernel_launch(void* const* d_in, const int* in_sizes, int n_in, void* d_out, int out_size, void* d_ws, size_t ws_size,
                              hipStream_t stream) {
  (void)in_sizes; (void)n_in; (void)out_size; (void)ws_size;
  (void)hipFuncSetAttribute((const void*)fused, hipFuncAttributeMaxDynamicSharedMemorySize, LDS_BYTES);
  Args a{(const u16*)d_in[0], (const u16*)d_in[3], (const u16*)d_in[4], (const u16*)d_in[6], (const u16*)d_in[9],
         (const u16*)d_in[1], (const u16*)d_in[2], (const u16*)d_in[7], (const u16*)d_in[8], (char*)d_ws, (u16*)d_out};
  fused<<<256, 512, LDS_BYTES, stream>>>(a);
}
```

```cpp
#include <hip/hip_runtime.h>
#include <hip/hip_bf16.h>
#include <stdint.h>

typedef unsigned short u16;
typedef short bf16x8 __attribute__((ext_vector_type(8)));
typedef short s16x4 __attribute__((ext_vector_type(4)));
typedef float f32x4 __attribute__((ext_vector_type(4)));
typedef float f32x2 __attribute__((ext_vector_type(2)));
typedef unsigned u32x4 __attribute__((ext_vector_type(4)));
typedef __bf16 bf16x2v __attribute__((ext_vector_type(2)));
typedef __amdgpu_buffer_rsrc_t rsrc_t;
#define LAS __attribute__((address_space(3)))
#define WAIT_V(n) asm volatile("s_waitcnt vmcnt(%0)" ::"n"(n) : "memory")
#define WL0() __builtin_amdgcn_s_waitcnt(0xC07F)
#define BAR() do { asm volatile("" ::: "memory"); __builtin_amdgcn_s_barrier(); asm volatile("" ::: "memory"); } while (0)
#define SCHED() __builtin_amdgcn_sched_barrier(0)
#define MFMA16(a, b, c) __builtin_amdgcn_mfma_f32_16x16x32_bf16(a, b, c, 0, 0, 0)
#define RSRC(p, n) __builtin_amdgcn_make_buffer_rsrc((void*)(p), 0, (unsigned)(n), 0x00020000)
#define OPAQUE(x) asm volatile("" : "+v"(x))

#define STAMP_DECL
#define STAMP(i)
#define STAMP_OUT(ws, kid, n)
#define K2_KXMASK 3
#define K1_KXMASK 3
#define DWARM 0
#define CODEWARM 5
#define NOPARK 0
#define Z1EARLY 1
#define PF_LIST 0x753810u
#define PF_N 6

constexpr int S = 2048, D = 512, F = 1792, FH = 896;
constexpr size_t WS_Q = 0;
constexpr size_t WS_Z1 = WS_Q + (size_t)(2 * D + FH) * S * 2;
constexpr size_t WS_H = WS_Z1 + (size_t)FH * S * 2;
constexpr size_t WS_Y = WS_H + (size_t)FH * S * 2;
constexpr size_t WS_G = WS_Y + (size_t)2 * D * S * 2;
constexpr unsigned G_F1 = 0;
constexpr unsigned G_FP = 32768;
constexpr unsigned G_FZ = 32768 + 64;
constexpr unsigned G_PAY = 65536;
constexpr unsigned G_ALL = G_PAY + 16 * 14336;
constexpr unsigned G_REQ = G_ALL + 1024;
constexpr unsigned G_ANS = G_REQ + 1024;
constexpr unsigned G_END = G_ANS + 1024;

constexpr int LDS_BYTES = 163840;
constexpr int P1_RING = 32768, P1_SCR = 81920;
constexpr int P2_PB = 65536, P2_Z = 131072, P3_X = 114688;
constexpr unsigned SPIN = 1u << 20;

struct Args {
  const u16* x; const u16* wdr; const u16* wdi; const u16* win; const u16* wout;
  const u16* wsr; const u16* wsi; const u16* wsp; const u16* bsp;
  char* ws; u16* out;
};

__device__ __forceinline__ unsigned cvt_pk(float lo, float hi) {
  f32x2 v = {lo, hi};
  return __builtin_bit_cast(unsigned, __builtin_convertvector(v, bf16x2v));
}
__device__ __forceinline__ float bf_lo(unsigned u) { return __uint_as_float(u << 16); }
__device__ __forceinline__ float bf_hi(unsigned u) { return __uint_as_float(u & 0xffff0000u); }
constexpr float GC1 = -2.0f * 0.7978845608028654f * 1.4426950408889634f, GC2 = GC1 * 0.044715f;
__device__ __forceinline__ f32x2 gelu2(f32x2 v) {
  const f32x2 t = v * ((v * v) * GC2 + GC1);
  const f32x2 e = f32x2{__builtin_amdgcn_exp2f(t[0]), __builtin_amdgcn_exp2f(t[1])} + 1.0f;
  return v * f32x2{__builtin_amdgcn_rcpf(e[0]), __builtin_amdgcn_rcpf(e[1])};
}
__device__ __forceinline__ void dot_sq(float& c, unsigned a) { asm volatile("v_dot2c_f32_bf16 %0, %1, %1" : "+v"(c) : "v"(a)); }
__device__ __forceinline__ void dot_sum(float& c, unsigned a) { asm volatile("v_dot2c_f32_bf16 %0, 0x3f803f80, %1" : "+v"(c) : "v"(a)); }
__device__ __forceinline__ float sum_rows(float v) {
  auto a = __builtin_amdgcn_permlane32_swap(__float_as_uint(v), __float_as_uint(v), false, false);
  v = __uint_as_float(a[0]) + __uint_as_float(a[1]);
  auto b = __builtin_amdgcn_permlane16_swap(__float_as_uint(v), __float_as_uint(v), false, false);
  return __uint_as_float(b[0]) + __uint_as_float(b[1]);
}
__device__ __forceinline__ float sel4(f32x4 v, int i) { const float a = (i & 1) ? v[1] : v[0], b = (i & 1) ? v[3] : v[2]; return (i & 2) ? b : a; }

__device__ __forceinline__ void dma16(rsrc_t r, unsigned mbase, int mimm, unsigned voff, unsigned soff) {
  asm volatile("s_add_i32 m0, %0, %1\n\ts_nop 0\n\tbuffer_load_dwordx4 %2, %3, %4 offen lds" ::"s"(mbase), "n"(mimm), "v"(voff), "s"(r), "s"(soff) : "scc");
}
__device__ __forceinline__ void dma16_sc1(rsrc_t r, unsigned mbase, int mimm, unsigned voff, unsigned soff) {
  asm volatile("s_add_i32 m0, %0, %1\n\ts_nop 0\n\tbuffer_load_dwordx4 %2, %3, %4 offen sc1 lds" ::"s"(mbase), "n"(mimm), "v"(voff), "s"(r), "s"(soff) : "scc");
}
__device__ __forceinline__ void dma4_sc1(rsrc_t r, int ldsaddr, unsigned voff, unsigned soff) {
  asm volatile("s_mov_b32 m0, %0\n\ts_nop 0\n\tbuffer_load_dword %1, %2, %3 offen sc1 lds" ::"n"(ldsaddr), "v"(voff), "s"(r), "s"(soff) : "memory");
}
__device__ __forceinline__ void touch(rsrc_t r, int junk, unsigned voff, unsigned soff) {
  asm volatile("s_mov_b32 m0, %0\n\ts_nop 0\n\tbuffer_load_dword %1, %2, %3 offen lds" ::"n"(junk), "v"(voff), "s"(r), "s"(soff) : "memory");
}
__device__ __forceinline__ void st16_sc1(u32x4 v, rsrc_t r, unsigned voff, unsigned soff) {
  asm volatile("buffer_store_dwordx4 %0, %1, %2, %3 offen sc1\n\ts_nop 1" ::"v"(v), "v"(voff), "s"(r), "s"(soff) : "memory");
}
__device__ __forceinline__ void st4_sc1(unsigned v, rsrc_t r, unsigned voff, unsigned soff) {
  asm volatile("buffer_store_dword %0, %1, %2, %3 offen sc1" ::"v"(v), "v"(voff), "s"(r), "s"(soff) : "memory");
}
__device__ __forceinline__ unsigned ld4_sc1(rsrc_t r, unsigned voff, unsigned soff) {
  unsigned v;
  asm volatile("buffer_load_dword %0, %1, %2, %3 offen sc1\n\ts_waitcnt vmcnt(0)" : "=&v"(v) : "v"(voff), "s"(r), "s"(soff) : "memory");
  return v;
}
__device__ __forceinline__ u32x4 ld16_sc1(rsrc_t r, unsigned voff, unsigned soff) {
  u32x4 v;
  asm volatile("buffer_load_dwordx4 %0, %1, %2, %3 offen sc1\n\ts_waitcnt vmcnt(0)" : "=&v"(v) : "v"(voff), "s"(r), "s"(soff) : "memory");
  return v;
}
__device__ __forceinline__ void ld16_async(u32x4& v, rsrc_t r, unsigned voff, unsigned soff) {
  asm volatile("buffer_load_dwordx4 %0, %1, %2, %3 offen" : "=v"(v) : "v"(voff), "s"(r), "s"(soff) : "memory");
}
__device__ __forceinline__ void wait_vn(int n) { WAIT_V(n); }
__device__ __forceinline__ bf16x8 ld_tr(unsigned addr) {
  s16x4 lo = __builtin_amdgcn_ds_read_tr16_b64_v4i16((LAS s16x4*)(size_t)(addr));
  s16x4 hi = __builtin_amdgcn_ds_read_tr16_b64_v4i16((LAS s16x4*)(size_t)(addr + 512));
  return __builtin_shufflevector(lo, hi, 0, 1, 2, 3, 4, 5, 6, 7);
}
__device__ __forceinline__ bf16x8 ld_frag(unsigned addr) { return *(const LAS bf16x8*)(size_t)(addr); }
__device__ __forceinline__ void wait_go(rsrc_t rG, unsigned nonce, unsigned seen) {
  for (unsigned spin = 0; seen != nonce && spin < SPIN; ++spin) {
    seen = __builtin_amdgcn_readfirstlane(ld4_sc1(rG, 0, G_ANS + blockIdx.x * 4));
    __builtin_amdgcn_s_sleep(1);
  }
}

template <bool ZB>
__device__ __forceinline__ void phase1(const Args& a, int pm, int cb, int kx, const u16* Pp, int kx2, f32x4 (&z1v)[2][2]) {
  const int tid = threadIdx.x, lane = tid & 63;
  const int wid = __builtin_amdgcn_readfirstlane(tid >> 6);
  const int wm = wid & 3, wn = wid >> 2;
  STAMP_DECL STAMP(0);
  const rsrc_t rX = RSRC(a.x + (size_t)pm * 128 * D, 128 * D * 2);
  const rsrc_t rW = ZB ? RSRC(a.win + 64 * cb, (D * F - 64 * cb) * 2) : RSRC(a.wdi + (size_t)384 * D, 128 * D * 2);
  const int r8 = lane >> 3, p8 = lane & 7;
  const unsigned sw_e = (p8 ^ (r8 >> 1)) << 4;
  const unsigned vx_e = (32 * (wid >> 1) + 4 * (wid & 1) + 8 * (r8 >> 2) + (r8 & 3)) * (D * 2) + sw_e;
  const unsigned vx_o = (vx_e ^ 64) + 16 * (D * 2);
  const unsigned vw2 = ZB ? (8 * wid + r8) * (F * 2) + ((p8 ^ ((((r8 >> 1) & 1) | ((wid & 1) << 1)) << 1)) << 4)
                          : (64 + 8 * wid + r8) * (D * 2) + (sw_e ^ ((wid & 1) << 6));
  const unsigned vw1 = ZB ? vw2 + FH * 2 : vw2 - 64 * (D * 2);
  constexpr unsigned WKB = ZB ? 64 * F * 2 : 128;
  const unsigned mb1 = wid * 1024, mb2 = wid * 2048;
  auto piece1 = [&](int st, int p) {
    const unsigned kb = st ^ kx;
    const int base = P1_RING + (st & 3) * 32768;
    if (p == 0) dma16(rX, mb2, base, vx_e, kb * 128);
    else if (p == 1) dma16(rW, mb1, base + 16384, vw1, kb * WKB);
    else if (p == 2) dma16(rX, mb2, base + 1024, vx_o, kb * 128);
    else dma16(rW, mb1, base + 24576, vw2, kb * WKB);
  };
  piece1(0, 0); piece1(0, 1); piece1(0, 2); piece1(0, 3);
  SCHED();

  const int fr = lane & 15, fq = lane >> 4;
  const int kh = wm >> 1;
  const unsigned fsw = ((fq ^ ((fr >> 1) & 7)) << 4) ^ (kh << 6);
  unsigned xa[2][2][2], wb[2][2][2];
#pragma unroll
  for (int m = 0; m < 2; ++m) { xa[0][m][0] = P1_RING + (32 * wm + 16 * (m ^ wn) + fr) * 128 + fsw; xa[0][m][1] = xa[0][m][0] ^ 64; }
#pragma unroll
  for (int n = 0; n < 2; ++n) {
    const int tile = 2 * wn + (n ^ (wm & 1));
    if (ZB) {
      const int q4 = fr >> 2, pp = lane & 3;
      const int sp = ((q4 >> 1) & 1) | ((fq & 1) << 1);
      const int ch = (2 * tile + (pp >> 1)) ^ (sp << 1);
      wb[0][n][0] = P1_RING + 16384 + 128 * (8 * fq + q4) + 16 * ch + 8 * (pp & 1) + kh * 4096;
      wb[0][n][1] = wb[0][n][0] ^ 4096;
    } else {
      wb[0][n][0] = P1_RING + 16384 + (16 * tile + fr) * 128 + fsw;
      wb[0][n][1] = wb[0][n][0] ^ 64;
    }
  }
#pragma unroll
  for (int i = 0; i < 2; ++i)
#pragma unroll
    for (int ks = 0; ks < 2; ++ks) {
      xa[1][i][ks] = xa[0][i][ks] + 65536; wb[1][i][ks] = wb[0][i][ks] + 65536;
      OPAQUE(xa[0][i][ks]); OPAQUE(wb[0][i][ks]); OPAQUE(xa[1][i][ks]); OPAQUE(wb[1][i][ks]);
    }
  auto ldw = [&](unsigned addr) -> bf16x8 { return ZB ? ld_tr(addr) : ld_frag(addr); };
  SCHED();
  piece1(1, 0); piece1(1, 1); piece1(1, 2); piece1(1, 3);
  const rsrc_t rG = RSRC(a.ws + WS_G, G_END);
  if (wid == 7) dma4_sc1(rG, 0, 0, G_ANS + blockIdx.x * 4);
  unsigned nonce = 0;
  STAMP(1);

  bf16x8 xf[16][2], wf[2][4];
  const f32x4 Z4 = {0, 0, 0, 0};
  f32x4 acc1[2][2] = {{Z4, Z4}, {Z4, Z4}}, acc2[2][2] = {{Z4, Z4}, {Z4, Z4}};
  f32x4 gram = Z4, rsum = Z4, cs1 = Z4, cs2 = Z4;
  const bf16x8 ONES = {0x3f80, 0x3f80, 0x3f80, 0x3f80, 0x3f80, 0x3f80, 0x3f80, 0x3f80};

  auto mm = [&](int kk, int i) {
    const int ks = kk & 1;
    if (i == 0) gram = MFMA16(xf[kk][0], xf[kk][0], gram);
    else if (i == 1) rsum = MFMA16(xf[kk][0], ONES, rsum);
    else if (i < 6) { const int m = (i - 2) >> 1, n = i & 1; acc1[m][n] = MFMA16(xf[kk][m], wf[ks][n], acc1[m][n]); }
    else if (i < 10) { const int m = (i - 6) >> 1, n = i & 1; acc2[m][n] = ZB ? MFMA16(wf[ks][2 + n], xf[kk][m], acc2[m][n]) : MFMA16(xf[kk][m], wf[ks][2 + n], acc2[m][n]); }
    else if (i == 10) cs1 = MFMA16(ONES, wf[0][0], cs1);
    else cs2 = MFMA16(ONES, wf[0][2], cs2);
  };
  auto rd = [&](int kk, int i) {
    const int st = kk >> 1, ks = kk & 1, wi = (st >> 1) & 1, so = (st & 1) * 32768;
    if (i == 0 || i == 3) xf[kk][i & 1] = ld_frag(xa[wi][i & 1][ks] + so);
    else if (i < 3) wf[ks][i - 1] = ldw(wb[wi][i - 1][ks] + so);
    else wf[ks][i - 2] = ldw(wb[wi][i - 4][ks] + so + 8192);
  };
  auto piece3 = [&](int p) {
    const rsrc_t rD = RSRC((cb < 8 ? a.wdr : a.wdi) + (size_t)(cb & 7) * 64 * D, 64 * D * 2);
    const unsigned ve = r8 * (D * 2) + sw_e, vo = (ve ^ 64) + 8 * (D * 2);
    dma16(rD, wid * 8192, p * 1024, (p & 1) ? vo : ve, (wid ^ kx) * 128 + (p >> 1) * 16 * (D * 2));
  };
  auto earlyP = [&](int k) {
    const rsrc_t rP = RSRC(Pp, 128 * S * 2);
    const unsigned ve = (16 * wid + r8) * (S * 2) + sw_e, vo = (ve ^ 64) + 8 * (S * 2);
    dma16(rP, mb2, P2_PB + (k + 2) * 16384, ve, (k ^ kx2) * 128);
    dma16(rP, mb2, P2_PB + (k + 2) * 16384 + 1024, vo, (k ^ kx2) * 128);
  };
#pragma unroll
  for (int st = 0; st < 8; ++st) {
    wait_vn(st <= 6 ? 4 : ZB ? 8 : 0);
    WL0();
    BAR();
    if (st == 0) STAMP(2);
    SCHED();
#pragma unroll
    for (int i = 0; i < 10; ++i) {
      if (st > 0) mm(2 * st - 1, i);
      SCHED();
      if (i < 6) rd(2 * st, i);
      if (st + 2 < 8) { if (i == 3) piece1(st + 2, 0); if (i == 8) piece1(st + 2, 1); }
      if (ZB && st == 6 && (i % 3) == 0) piece3(i / 3);
      if (st == 7 && i == 7) earlyP(0);
      SCHED();
    }
    WL0();
    SCHED();
#pragma unroll
    for (int i = 0; i < 12; ++i) {
      mm(2 * st, i);
      SCHED();
      if (i < 6) rd(2 * st + 1, i);
      if (st + 2 < 8) { if (i == 3) piece1(st + 2, 2); if (i == 9) piece1(st + 2, 3); }
      if (ZB && st == 6 && (i % 3) == 2) piece3(4 + i / 3);
      if (st == 7 && i == 8) earlyP(1);
      SCHED();
    }
    if (st == 2 && wid == 7) {
      nonce = __builtin_amdgcn_readfirstlane(*(LAS unsigned*)(size_t)(lane * 4)) + 1;
      st4_sc1(nonce, rG, 0, G_REQ + blockIdx.x * 4);
    }
    SCHED();
  }
  LAS float* const s_r = (LAS float*)(size_t)(P1_SCR);
  LAS float* const s_c = (LAS float*)(size_t)(P1_SCR + 512);
  LAS float* const s_u1 = (LAS float*)(size_t)(P1_SCR + 1024);
  LAS float* const s_u2 = (LAS float*)(size_t)(P1_SCR + 1536);
  LAS float* const s_z = (LAS float*)(size_t)(P1_SCR + 2048);
  LAS float* const s_n = (LAS float*)(size_t)(P1_SCR + 4096);
  LAS float* const s_u3 = (LAS float*)(size_t)(P1_SCR + 5120);
  const unsigned tb0 = fr * 128 + fsw, tb1 = tb0 ^ 64;
  WAIT_V(4);
  {
    const unsigned cb0 = tb0 + wid * 8192, cb1 = cb0 ^ 64;
    bf16x8 c3[8];
    f32x4 cs3[4];
    if (ZB) {
#pragma unroll
      for (int t = 0; t < 4; ++t) { c3[2 * t] = ld_frag(cb0 + t * 2048); c3[2 * t + 1] = ld_frag(cb1 + t * 2048); }
    }
    mm(15, 0); mm(15, 1);
    SCHED();
    if (ZB) {
#pragma unroll
      for (int t = 0; t < 4; ++t) cs3[t] = MFMA16(ONES, c3[2 * t], Z4);
#pragma unroll
      for (int t = 0; t < 4; ++t) cs3[t] = MFMA16(ONES, c3[2 * t + 1], cs3[t]);
    }
    SCHED();
#pragma unroll
    for (int i = 2; i < 10; ++i) mm(15, i);
    STAMP(3);
    unsigned i01 = 32 * wm + 16 * wn, i23 = 64 * kh + 16 * (2 * wn + (wm & 1));
    asm volatile("" : "+s"(i01), "+s"(i23));
    const float s1 = sel4(rsum, fr & 3), s2 = sel4(gram, fr & 3);
    const float mean = s1 * (1.0f / D);
    const float rstd = __builtin_amdgcn_rsqf(fmaxf(s2 * (1.0f / D) - mean * mean, 0.0f) + 1e-5f);
    if ((fr >> 2) == fq) { s_r[i01 + fr] = rstd; s_c[i01 + fr] = -mean * rstd; }
    if (fq < 2) s_u1[128 * fq + i23 + fr] = fq ? cs2[0] : cs1[0];
    if (ZB) s_u3[lane * 8 + wid] = sel4(f32x4{cs3[0][0], cs3[1][0], cs3[2][0], cs3[3][0]}, fq);
  }
  const int s0 = pm * 128 + 32 * wm;
  const rsrc_t rT = RSRC(a.ws + WS_Q, (2 * D + FH) * S * 2);
  const unsigned vT = ((fr >> 2) & 1) * 65536 + fq * 256 + (4 * (fr >> 3) + (fr & 3)) * 16;
  WL0();
  BAR();
  STAMP(4);
  if (wid == 7) dma4_sc1(rG, P1_SCR + 7168, 0, G_ANS + blockIdx.x * 4);
  earlyP(2); earlyP(3);
  f32x4 rq[2], cq[2];
  auto ld_rc = [&]() {
#pragma unroll
    for (int m = 0; m < 2; ++m) {
      rq[m] = *(LAS f32x4*)(s_r + 32 * wm + 16 * (m ^ wn) + 4 * fq);
      cq[m] = *(LAS f32x4*)(s_c + 32 * wm + 16 * (m ^ wn) + 4 * fq);
    }
  };
  ld_rc();
  auto store_t = [&](int T, f32x4 v0, f32x4 v1) {
    const unsigned a0 = cvt_pk(v0[0], v0[1]), a1 = cvt_pk(v0[2], v0[3]), b0 = cvt_pk(v1[0], v1[1]), b1 = cvt_pk(v1[2], v1[3]);
    const u32x4 o = wn ? u32x4{b0, b1, a0, a1} : u32x4{a0, a1, b0, b1};
    st16_sc1(o, rT, vT, (T >> 1) * 131072 + (T & 1) * 128 + s0 * 32);
  };
  auto lin_store = [&](f32x4 c0, f32x4 c1, float u, int T) { store_t(T, rq[0] * c0 + cq[0] * u, rq[1] * c1 + cq[1] * u); };
  float u1[2];
#pragma unroll
  for (int n = 0; n < 2; ++n) { const int ui = 16 * (2 * wn + (n ^ (wm & 1))) + fr; u1[n] = s_u1[ui] + s_u1[64 + ui]; }
  if (!ZB) {
#pragma unroll
    for (int n = 0; n < 2; ++n) {
      const int tile = 2 * wn + (n ^ (wm & 1)), ui = 16 * tile + fr;
      lin_store(acc1[0][n], acc1[1][n], u1[n], 56 + tile);
      lin_store(acc2[0][n], acc2[1][n], s_u2[ui] + s_u2[64 + ui], 60 + tile);
    }
    if (wid == 7) { WAIT_V(0); wait_go(rG, nonce, __builtin_amdgcn_readfirstlane(*(LAS unsigned*)(size_t)(P1_SCR + 7168 + lane * 4))); }
    STAMP(5); STAMP(6); STAMP(7); STAMP(8); STAMP(9);
    STAMP_OUT(a.ws, 0, 10);
    return;
  }
  {
    f32x4 ps[2], pq[2];
#pragma unroll
    for (int n = 0; n < 2; ++n)
#pragma unroll
      for (int m = 0; m < 2; ++m) {
        f32x4 v = rq[m] * acc1[m][n] + cq[m] * u1[n];
        const f32x2 g0 = gelu2(f32x2{v[0], v[1]}), g1 = gelu2(f32x2{v[2], v[3]});
        v = f32x4{g0[0], g0[1], g1[0], g1[1]};
        acc1[m][n] = v;
        if (n == 0) { ps[m] = v; pq[m] = v * v; } else { ps[m] += v; pq[m] += v * v; }
      }
    float t[8], h[4];
#define DPP2(d, x, y, ctl, m0, m1) "v_add_f32_dpp " d ", " x ", " x " " ctl " row_mask:0xf bank_mask:" m0 "\n\tv_add_f32_dpp " d ", " y ", " y " " ctl " row_mask:0xf bank_mask:" m1 "\n\t"
#define DPPQ(d, ctl) "v_add_f32_dpp " d ", " d ", " d " " ctl " row_mask:0xf bank_mask:0xf\n\t"
    asm volatile("s_nop 1\n\t" DPP2("%0", "%8", "%16", "row_ror:8", "0x3", "0xc") DPP2("%1", "%9", "%17", "row_ror:8", "0x3", "0xc")
                 DPP2("%2", "%10", "%18", "row_ror:8", "0x3", "0xc") DPP2("%3", "%11", "%19", "row_ror:8", "0x3", "0xc")
                 DPP2("%4", "%12", "%20", "row_ror:8", "0x3", "0xc") DPP2("%5", "%13", "%21", "row_ror:8", "0x3", "0xc")
                 DPP2("%6", "%14", "%22", "row_ror:8", "0x3", "0xc") DPP2("%7", "%15", "%23", "row_ror:8", "0x3", "0xc") "s_nop 1"
                 : "=&v"(t[0]), "=&v"(t[1]), "=&v"(t[2]), "=&v"(t[3]), "=&v"(t[4]), "=&v"(t[5]), "=&v"(t[6]), "=&v"(t[7])
                 : "v"(ps[0][0]), "v"(pq[0][0]), "v"(ps[0][1]), "v"(pq[0][1]), "v"(ps[0][2]), "v"(pq[0][2]), "v"(ps[0][3]), "v"(pq[0][3]),
                   "v"(ps[1][0]), "v"(pq[1][0]), "v"(ps[1][1]), "v"(pq[1][1]), "v"(ps[1][2]), "v"(pq[1][2]), "v"(ps[1][3]), "v"(pq[1][3]));
    asm volatile("s_nop 1\n\t" DPP2("%0", "%4", "%8", "row_half_mirror", "0x5", "0xa") DPP2("%1", "%5", "%9", "row_half_mirror", "0x5", "0xa")
                 DPP2("%2", "%6", "%10", "row_half_mirror", "0x5", "0xa") DPP2("%3", "%7", "%11", "row_half_mirror", "0x5", "0xa") "s_nop 1\n\t"
                 DPPQ("%0", "quad_perm:[1,0,3,2]") DPPQ("%1", "quad_perm:[1,0,3,2]") DPPQ("%2", "quad_perm:[1,0,3,2]") DPPQ("%3", "quad_perm:[1,0,3,2]")
                 DPPQ("%0", "quad_perm:[2,3,0,1]") DPPQ("%1", "quad_perm:[2,3,0,1]") DPPQ("%2", "quad_perm:[2,3,0,1]") DPPQ("%3", "quad_perm:[2,3,0,1]") "s_nop 0"
                 : "=&v"(h[0]), "=&v"(h[1]), "=&v"(h[2]), "=&v"(h[3])
                 : "v"(t[0]), "v"(t[1]), "v"(t[2]), "v"(t[3]), "v"(t[4]), "v"(t[5]), "v"(t[6]), "v"(t[7]));
    if ((fr & 3) == 0) *(LAS f32x4*)(s_z + (wn * 128 + 32 * wm + 8 * fq + 4 * ((fr >> 3) ^ wn) + 2 * ((fr >> 2) & 1)) * 2) = f32x4{h[0], h[1], h[2], h[3]};
  }
  WL0();
  BAR();
  STAMP(5);
  if (wid == 7) {
    __builtin_amdgcn_s_setprio(2);
    const f32x4 p = *(LAS f32x4*)(s_z + lane * 4) + *(LAS f32x4*)(s_z + 256 + lane * 4);
    WAIT_V(4);
    wait_go(rG, nonce, __builtin_amdgcn_readfirstlane(*(LAS unsigned*)(size_t)(P1_SCR + 7168 + lane * 4)));
    st16_sc1(__builtin_bit_cast(u32x4, p) | 1u, rG, lane * 16, G_PAY + (pm * 14 + cb) * 1024);
  }
  STAMP(6);
  {
    float u3[2];
    f32x4 acct[2][2] = {{Z4, Z4}, {Z4, Z4}};
    const unsigned t30 = tb0 + wn * 4096, t31 = tb1 + wn * 4096;
    bf16x8 w[8];
    auto rd3 = [&](int f) { const int kk = f >> 1; w[f & 7] = ld_frag(((kk & 1) ? t31 : t30) + (kk >> 1) * 8192 + (f & 1) * 2048); };
#pragma unroll
    for (int f = 0; f < 4; ++f) rd3(f);
    SCHED();
#pragma unroll
    for (int f = 0; f < 32; ++f) {
      const int kk = f >> 1, n = f & 1;
      acct[0][n] = MFMA16(xf[kk][0], w[f & 7], acct[0][n]);
      SCHED();
      if (f + 4 < 32) rd3(f + 4);
      if (f == 8 || f == 12) {
        const int m = (f - 8) >> 2;
        const float r = s_r[32 * wm + 16 * (m ^ wn) + fr], c = s_c[32 * wm + 16 * (m ^ wn) + fr];
#pragma unroll
        for (int q = 0; q < 2; ++q) {
          const int ui = 16 * (2 * wn + (q ^ (wm & 1))) + 4 * fq;
          z1v[m][q] = acc2[m][q] * r + (*(LAS f32x4*)(s_u2 + ui) + *(LAS f32x4*)(s_u2 + 64 + ui)) * c;
        }
      }
      if (f == 20 || f == 24) {
        const int q = (f - 20) >> 2;
        const f32x4 uu = *(LAS f32x4*)(s_u3 + (32 * wn + 16 * q + fr) * 8) + *(LAS f32x4*)(s_u3 + (32 * wn + 16 * q + fr) * 8 + 4);
        u3[q] = (uu[0] + uu[1]) + (uu[2] + uu[3]);
      }
      SCHED();
      acct[1][n] = MFMA16(xf[kk][1], w[f & 7], acct[1][n]);
      SCHED();
    }
    if (wid != 7) {
#pragma unroll
      for (int n = 0; n < 2; ++n) lin_store(acct[0][n], acct[1][n], u3[n], 4 * cb + 2 * wn + n);
    }
    STAMP(7);
  if (wid == 7) {
    f32x4 t;
    for (unsigned spin = 0; spin < SPIN; ++spin) {
      u32x4 pv[14];
#pragma unroll
      for (int i = 0; i < 14; ++i) pv[i] = __builtin_amdgcn_raw_buffer_load_b128(rG, lane * 16 + (i & 3) * 1024, G_PAY + pm * 14336 + (i >> 2) * 4096, 16);
      if (spin == 0) {
#pragma unroll
        for (int n = 0; n < 2; ++n) lin_store(acct[0][n], acct[1][n], u3[n], 4 * cb + 2 * wn + n);
      }
      u32x4 al = pv[0];
      t = __builtin_bit_cast(f32x4, pv[0]);
#pragma unroll
      for (int i = 1; i < 14; ++i) { al &= pv[i]; t += __builtin_bit_cast(f32x4, pv[i]); }
      if (__all((al[0] & al[1] & al[2] & al[3] & 1u) != 0)) break;
      asm volatile("" ::: "memory");
    }
    const float m0 = t[0] * (1.0f / FH), m1 = t[2] * (1.0f / FH);
    const float r0 = __builtin_amdgcn_rsqf(fmaxf(t[1] * (1.0f / FH) - m0 * m0, 0.0f) + 1e-5f);
    const float r1 = __builtin_amdgcn_rsqf(fmaxf(t[3] * (1.0f / FH) - m1 * m1, 0.0f) + 1e-5f);
    *(LAS f32x2*)(s_n + 2 * lane) = f32x2{r0, r1};
    *(LAS f32x2*)(s_n + 128 + 2 * lane) = f32x2{-m0 * r0, -m1 * r1};
    __builtin_amdgcn_s_setprio(0);
  }
  }
  WL0();
  BAR();
  STAMP(8);
  {
    f32x4 r2[2], c2[2];
#pragma unroll
    for (int m = 0; m < 2; ++m) {
      r2[m] = *(LAS f32x4*)(s_n + 32 * wm + 8 * fq + 4 * (m ^ wn));
      c2[m] = *(LAS f32x4*)(s_n + 128 + 32 * wm + 8 * fq + 4 * (m ^ wn));
    }
#pragma unroll
    for (int n = 0; n < 2; ++n) store_t(64 + 4 * cb + 2 * wn + (n ^ (wm & 1)), acc1[0][n] * r2[0] + c2[0], acc1[1][n] * r2[1] + c2[1]);
  }
  STAMP(9);
  STAMP_OUT(a.ws, 0, 10);
}

__device__ __forceinline__ void warm_window(const Args& a, int junk, int rb, int w, int wv, int lane) {
  if (w < 8) {
    const unsigned vo = (rb * 128 + wv * 32 + (lane >> 2)) * (S * 2) + (lane & 3) * 128;
#pragma unroll
    for (int m = 0; m < 3; ++m) {
      const rsrc_t r = RSRC(m == 0 ? a.wsr : m == 1 ? a.wsi : a.wsp, S * S * 2);
      touch(r, junk, vo, w * 512);
      touch(r, junk, vo, w * 512 + 16 * (S * 2));
    }
  } else {
    const rsrc_t r = RSRC(a.wout + (size_t)rb * 56 * D, 56 * D * 2);
    touch(r, junk, lane * 128, wv * 16384);
    touch(r, junk, lane * 128, wv * 16384 + 8192);
  }
}

__global__ __launch_bounds__(512, 2) void fused(Args a) {
  extern __shared__ __attribute__((aligned(1024))) char smem[];
  asm volatile("" ::"v"(smem));
  const unsigned long long pstart = __builtin_amdgcn_s_memrealtime();
  const int bx = blockIdx.x & 7, bi = blockIdx.x >> 3;
  if (bi < CODEWARM && threadIdx.x < 64) {
    unsigned long long pc;
    asm volatile("s_getpc_b64 %0" : "=s"(pc));
    touch(RSRC(pc & ~127ull, CODEWARM * 8192), 1024, threadIdx.x * 128, bi * 8192);
  }
  const int pm = 4 * (bx >> 1) + (bi >> 3), cb = 2 * (bi & 7) + (bx & 1);
  int mat, tm, tn;
  if (bx & 1) { mat = 2; tm = pm; tn = bi & 7; }
  else { mat = bx >> 2; tm = ((bx >> 1) & 1) * 8 + (bi >> 2); tn = bi & 3; }
  const int kx = (tm ^ tn) & K2_KXMASK;
  const u16* Pp = (mat == 0 ? a.wsr : mat == 1 ? a.wsi : a.wsp) + (size_t)tm * 128 * S;
  f32x4 z1v[2][2];
  {
    const int kx1 = ((bi & 7) ^ (2 * (bi >> 3))) & K1_KXMASK;
    if (__builtin_expect(cb < 14, 1)) phase1<true>(a, pm, cb, kx1, Pp, kx, z1v);
    else if (cb == 14) phase1<false>(a, pm, cb, kx1, Pp, kx, z1v);
  }
  int tid = threadIdx.x;
  asm volatile("" : "+v"(tid));
  const int lane = tid & 63;
  const int wid = __builtin_amdgcn_readfirstlane(tid >> 6);
  STAMP_DECL STAMP(0);
  const int tm3 = bx * 4 + (bi >> 3), tn3 = bi & 7;
  const int fr = lane & 15, fq = lane >> 4;
  const unsigned vl = lane * 16;
  const rsrc_t rG = RSRC(a.ws + WS_G, G_END);
  const bool work2 = cb != 15;
  auto z1_finish = [&]() {
    if (cb < 14) {
      const int wm = wid & 3, wn = wid >> 2;
      const rsrc_t rZ = RSRC(a.ws + WS_Z1, FH * S * 2);
      const unsigned vZ = (fr >> 3) * (112 * 256) + (2 * ((fq & 1) ^ (wm & 1)) + (fq >> 1)) * 256 + (fr & 7) * 16;
      const unsigned sZ = ((pm * 8 + 2 * wm) * 112 + 8 * cb + 4 * wn) * 256;
#pragma unroll
      for (int m = 0; m < 2; ++m) {
        unsigned pk[2][2];
#pragma unroll
        for (int n = 0; n < 2; ++n) {
          const f32x2 g0 = gelu2(f32x2{z1v[m][n][0], z1v[m][n][1]}), g1 = gelu2(f32x2{z1v[m][n][2], z1v[m][n][3]});
          pk[n][0] = cvt_pk(g0[0], g0[1]); pk[n][1] = cvt_pk(g1[0], g1[1]);
        }
        auto w0 = __builtin_amdgcn_permlane16_swap(pk[0][0], pk[1][0], false, false);
        auto w1 = __builtin_amdgcn_permlane16_swap(pk[0][1], pk[1][1], false, false);
        st16_sc1(u32x4{w0[0], w1[0], w0[1], w1[1]}, rZ, vZ, sZ + 128 * (m ^ wn));
      }
    }
  };
  if (blockIdx.x == 255) {
    for (int i = 0; i < 36; ++i) st16_sc1(u32x4{0, 0, 0, 0}, rG, vl, (wid * 36 + i) * 1024);
    st16_sc1(u32x4{0, 0, 0, 0}, rG, vl, G_ALL);
  }
  unsigned bv[4];
#pragma unroll
  for (int ni = 0; ni < 4; ++ni) bv[ni] = a.bsp[(8 * tm + 4 * ((wid >> 1) & 1) + ni) * 16 + fr];
  asm volatile("" : "+v"(bv[0]), "+v"(bv[1]), "+v"(bv[2]), "+v"(bv[3]));
  if (work2 || blockIdx.x == 255) {
    WAIT_V(0);
    WL0();
    BAR();
  }
  if (wid >= 4) {
    const int lw = wid - 4;
    const int r8 = lane >> 3, p = lane & 7;
    if (wid == 7) {
      if (!work2) {
        if (blockIdx.x == 255) {
          for (unsigned spin = 0; spin < SPIN; ++spin) {
            const u32x4 rq = ld16_sc1(rG, vl, G_REQ);
            st16_sc1(rq, rG, vl, G_ANS);
            const u32x4 fl = ld16_sc1(rG, vl, G_ALL);
            if (__all((fl[0] & fl[1] & fl[2] & (lane == 63 ? 1u : fl[3])) == 1u)) break;
          }
        } else {
          const unsigned nonce = __builtin_amdgcn_readfirstlane(ld4_sc1(rG, 0, G_ANS + blockIdx.x * 4)) + 1;
          st4_sc1(nonce, rG, 0, G_REQ + blockIdx.x * 4);
          wait_go(rG, nonce, nonce - 1);
        }
      }
      const unsigned box = (fq & 1) * 128 + (cb >> 1) * 16 + fr;
      st4_sc1(1u, rG, fq < 2 ? box * 128 + (pm * 2 + (cb & 1)) * 4 : G_ALL + blockIdx.x * 4, 0);
    }
    const rsrc_t rP = RSRC(Pp, 128 * S * 2);
    const unsigned vo_e = (lw * 32 + r8) * (S * 2) + ((p ^ (r8 >> 1)) << 4);
    const unsigned vo_o = (vo_e ^ 64) + 8 * (S * 2);
    auto stageP = [&](int kt, unsigned poff) {
      const unsigned mb = poff + lw * 4096, so = (kt ^ kx) * 128;
#pragma unroll
      for (int j = 0; j < 4; ++j) dma16(rP, mb, P2_PB + j * 1024, (j & 1) ? vo_o : vo_e, so + (j >> 1) * 16 * (S * 2));
    };
    if (work2) {
      BAR();
      stageP(4, 0); stageP(5, 16384);
    }
    const rsrc_t rWo = RSRC(a.wout + 64 * tn3, (FH * D - 64 * tn3) * 2);
    const unsigned vw_e = (32 * lw + r8) * (D * 2) + ((p ^ (((r8 >> 1) & 1) << 1)) << 4), vw_o = (vw_e ^ 64) + 8 * (D * 2);
    u32x4 wreg[12];
#pragma unroll
    for (int i = 0; i < 12; ++i) wreg[i] = u32x4{0, 0, 0, 0};
    auto wput = [&](int rg) {
#pragma unroll
      for (int j = 0; j < 4 && !NOPARK; ++j) *(LAS u32x4*)(size_t)(rg * 16384 + lw * 4096 + j * 1024 + vl) = wreg[(rg == 6 ? 8 : (rg - 2) * 4) + j];
    };
    auto wdma = [&](int rg) {
#pragma unroll
      for (int j = 0; j < 4; ++j) dma16(rWo, lw * 4096, rg * 16384 + j * 1024, (j & 1) ? vw_o : vw_e, (rg * 128 + (j >> 1) * 16) * (D * 2));
    };
    if (work2) {
#pragma unroll
      for (int i = 0; i < 12; ++i) {
        const int rg = i < 8 ? 2 + (i >> 2) : 6;
        ld16_async(wreg[i], rWo, (i & 1) ? vw_o : vw_e, (rg * 128 + ((i >> 1) & 1) * 16) * (D * 2));
      }
      if (Z1EARLY && wid != 7) z1_finish();
      if (wid == 7) {
        const unsigned box = (mat * 4 + tn) * 16 + tm;
        for (unsigned spin = 0; spin < SPIN; ++spin) {
          const unsigned fl = ld4_sc1(rG, (lane & 31) * 4, G_F1 + box * 128);
          if (__all(fl == 1u || (box >= 112 && box < 128 && (lane & 1)))) break;
        }
      }
      BAR();
      STAMP(1);
      const rsrc_t rQ = RSRC(a.ws + WS_Q + (size_t)(mat * 4 + tn) * 128 * S * 2, 128 * S * 2);
      const unsigned vq = vl + lw * 131072;
      auto stageQ = [&](int kt) {
        const unsigned mb = (kt & 3) * 16384 + lw * 4096, so = (kt ^ kx) * 2048;
#pragma unroll
        for (int j = 0; j < 4; ++j) dma16_sc1(rQ, mb, j * 1024, vq, so + (j >> 1) * 65536 + (j & 1) * 1024);
      };
      stageQ(0); stageQ(1); stageQ(2); stageQ(3);
      if (!Z1EARLY || wid == 7) z1_finish();
      WAIT_V(12); BAR();
      WAIT_V(8); BAR(); stageQ(4); stageP(6, 2 * 16384);
      WAIT_V(12); BAR(); stageQ(5); stageP(7, 3 * 16384);
      WAIT_V(16); BAR(); stageQ(6); stageP(8, 4 * 16384);
      unsigned poff = 5 * 16384;
      unsigned zf;
      for (int kt = 3; kt < 23; ++kt) {
        WAIT_V(20);
        BAR();
        if (kt == 3 && wid == 7 && cb < 14) st4_sc1(1u, rG, 0, G_FZ + (pm * 8 + (cb >> 1)) * 128 + (cb & 1) * 4);
        stageQ(kt + 4); stageP(kt + 6, poff);
        poff = poff == 5 * 16384 ? 0 : poff + 16384;
      }
      asm volatile("buffer_load_dword %0, %1, %2, %3 offen sc1" : "=v"(zf) : "v"((lane & 1) * 4), "s"(rG), "s"(G_FZ + (tm * 8 + tn) * 128) : "memory");
#pragma unroll
      for (int kt = 23; kt < 26; ++kt) {
        WAIT_V(20);
        BAR();
        stageQ(kt + 4); stageP(kt + 6, ((kt + 2) % 6) * 16384);
      }
      asm volatile("s_waitcnt vmcnt(20)" : "+v"(zf)::"memory");
      if (mat == 2) {
        const rsrc_t rZ = RSRC(a.ws + WS_Z1 + (size_t)((8 * tm + lw) * 112 + 16 * tn) * 256, 4 * 112 * 256 + 4096);
        auto stageZ = [&](int h) {
#pragma unroll
          for (int j = 0; j < 4; ++j) dma16_sc1(rZ, lw * 4096, P2_Z + h * 16384 + j * 1024, vl, h * (4 * 112 * 256) + j * 1024);
        };
        for (unsigned spin = 0; !__all(zf == 1u) && spin < SPIN; ++spin) zf = ld4_sc1(rG, (lane & 1) * 4, G_FZ + (tm * 8 + tn) * 128);
        WAIT_V(20); BAR(); stageQ(30); stageZ(0);
        WAIT_V(20); BAR(); stageQ(31); stageZ(1);
        WAIT_V(20); BAR(); wdma(0); wdma(4);
        WAIT_V(20); BAR(); wdma(1); wdma(5);
        WAIT_V(20); BAR(); wput(2); wput(6);
        WAIT_V(16); BAR(); wput(3);
      } else {
        WAIT_V(20); BAR(); stageQ(30);
        WAIT_V(16); BAR(); stageQ(31);
        WAIT_V(12); BAR(); wdma(0); wdma(4);
        WAIT_V(12); BAR(); wdma(1); wdma(5);
        WAIT_V(16); BAR(); wput(2); wput(6);
        BAR(); wput(3);
      }
    } else {
      STAMP(1);
      wdma(0); wdma(4); wdma(1); wdma(5); wdma(2); wdma(3); wdma(6);
    }
    STAMP(2); STAMP(3);
    WAIT_V(0);
  } else if (work2) {
    const int wp = wid >> 1, wq = wid & 1;
    f32x4 acc[4][4];
#pragma unroll
    for (int ni = 0; ni < 4; ++ni) {
      const float bias = mat == 2 ? __uint_as_float(bv[ni] << 16) : 0.0f;
#pragma unroll
      for (int n = 0; n < 4; ++n) acc[ni][n] = f32x4{bias, bias, bias, bias};
    }
    BAR();
    if (DWARM && cb == 14) warm_window(a, 0, pm, 2, wid, lane);
    const int sw = (fr >> 1) & 7;
    const unsigned ra = P2_PB + (wp * 64 + fr) * 128 + ((fq ^ sw) << 4);
    const unsigned rb = wq * 8192 + vl;
    bf16x8 a0[4], b0[4], a1[4], b1[4];
    unsigned poff = 2 * 16384;
    unsigned pa0 = ra + poff, pa1 = pa0 ^ 64;
#define K2_RD(AF, BF, pa, sl, ks, i) do { \
    if ((i) == 0) AF[0] = ld_frag(pa); \
    else if ((i) <= 4) BF[(i) - 1] = ld_frag(rb + (sl) * 16384 + ((i) - 1) * 2048 + (ks) * 1024); \
    else AF[(i) - 4] = ld_frag(pa + ((i) - 4) * 2048); } while (0)
#define K2_MM(AF, BF, i) acc[(i) >> 2][(i) & 3] = MFMA16(BF[(i) & 3], AF[(i) >> 2], acc[(i) >> 2][(i) & 3])
#define K2_STEP(s, LAST) do { \
    SCHED(); WL0(); SCHED(); \
    _Pragma("unroll") for (int i = 0; i < 16; ++i) { K2_MM(a0, b0, i); SCHED(); if (i < 8) K2_RD(a1, b1, pa1, s, 1, i); \
      if (i == 10) { poff = poff == 5 * 16384 ? 0 : poff + 16384; pa0 = ra + poff; } SCHED(); } \
    WL0(); \
    if ((s) == 3) WAIT_V(0); \
    BAR(); \
    SCHED(); \
    _Pragma("unroll") for (int i = 0; i < 16; ++i) { K2_MM(a1, b1, i); SCHED(); if (!(LAST) && i < 8) K2_RD(a0, b0, pa0, ((s) + 1) & 3, 0, i); \
      if (i == 10) pa1 = pa0 ^ 64; SCHED(); } \
  } while (0)
    if (DWARM && cb == 14) WAIT_V(0);
    if (Z1EARLY) z1_finish();
    BAR();
    if (!Z1EARLY) z1_finish();
    BAR();
    STAMP(1);
#pragma unroll
    for (int i = 0; i < 8; ++i) K2_RD(a0, b0, pa0, 0, 0, i);
    for (int kt = 0; kt < 28; kt += 4) { K2_STEP(0, 0); K2_STEP(1, 0); K2_STEP(2, 0); K2_STEP(3, 0); }
    K2_STEP(0, 0); K2_STEP(1, 0); K2_STEP(2, 0); K2_STEP(3, 1);
    SCHED();
    STAMP(2);
    if (mat < 2) {
      const rsrc_t rY = RSRC(a.ws + WS_Y + (size_t)mat * S * D * 2, S * D * 2);
      const unsigned so = ((8 * tm + 4 * wp) * 64 + 16 * tn + 8 * wq) * 256;
#pragma unroll
      for (int ni = 0; ni < 4; ++ni)
#pragma unroll
        for (int P = 0; P < 2; ++P) {
          const f32x4 c0 = acc[ni][2 * P], c1 = acc[ni][2 * P + 1];
          const u32x4 v = {cvt_pk(c0[0], c0[1]), cvt_pk(c0[2], c0[3]), cvt_pk(c1[0], c1[1]), cvt_pk(c1[2], c1[3])};
          st16_sc1(v, rY, vl, so + (ni * 64 + 4 * P) * 256);
        }
    } else {
      const rsrc_t rH = RSRC(a.ws + WS_H, S * FH * 2);
      const unsigned so = ((8 * tm + 4 * wp) * 112 + 16 * tn + 8 * wq) * 256;
      const unsigned za = P2_Z + wp * 16384 + wq * 2048 + (fq * 16 + ((fr & 4) << 1) + ((fr & 8) >> 1) + (fr & 3)) * 16;
#pragma unroll
      for (int ni = 0; ni < 4; ++ni) {
#pragma unroll
        for (int P = 0; P < 2; ++P) {
          const f32x4 c0 = acc[ni][2 * P], c1 = acc[ni][2 * P + 1];
          const u32x4 z = *(const LAS u32x4*)(size_t)(za + ni * 4096 + P * 1024);
          const u32x4 v = {cvt_pk(bf_lo(z[0]) * c0[0], bf_hi(z[0]) * c0[1]), cvt_pk(bf_lo(z[1]) * c0[2], bf_hi(z[1]) * c0[3]),
                           cvt_pk(bf_lo(z[2]) * c1[0], bf_hi(z[2]) * c1[1]), cvt_pk(bf_lo(z[3]) * c1[2], bf_hi(z[3]) * c1[3])};
          st16_sc1(v, rH, vl, so + (ni * 112 + 4 * P) * 256);
        }
      }
    }
    STAMP(3);
    WAIT_V(0);
  } else {
    STAMP(1);
    for (int i = 0; i < PF_N && blockIdx.x != 255; ++i) {
      warm_window(a, P2_Z, pm, (PF_LIST >> (4 * i)) & 15, wid, lane);
      WAIT_V(6);
    }
    STAMP(2);
    WAIT_V(0);
    STAMP(3);
  }
  WL0();
  BAR();
  STAMP(4);
  {
    const int wr = (wid + tn3) & 7, tp = wr >> 2, kq = wr & 3;
    const int tt3 = 4 * tm3 + 2 * tp + (kq & 1);
    const int cq = 2 * (fq & 1) + (fq >> 1);
    const int c3 = 8 * tn3 + 2 * (kq & 2);
    if (wid == 4 && work2) {
      const unsigned box = mat == 2 ? fr : (fr & 8) + 2 * tn + (fr & 1);
      st4_sc1(1u, rG, box * 128, G_FP + tm * 2048 + (mat == 2 ? tn : 8 + mat) * 4);
    }
    const int q4 = fr >> 2, pp = lane & 3;
    const int sp = ((q4 >> 1) & 1) | ((fq & 1) << 1);
    const unsigned wa = kq * 4096 + 128 * (8 * fq + q4) + 16 * ((2 * (kq & 2) + (pp >> 1)) ^ (sp << 1)) + 8 * (pp & 1);
    bf16x8 wf[7][4];
    auto ld_w = [&]() {
#pragma unroll
      for (int mi = 0; mi < 4; ++mi) {
        unsigned lo = wa ^ (mi * 32), hi = lo + 65536;
        OPAQUE(hi);
#pragma unroll
        for (int g = 0; g < 7; ++g) wf[g][mi] = ld_tr((g < 4 ? lo : hi) + (g & 3) * 16384);
      }
    };
    if (wid == 4) {
      for (unsigned spin = 0; spin < SPIN; ++spin) {
        const unsigned fl = ld4_sc1(rG, fr * 4, G_FP + (tm3 * 8 + tn3) * 128);
        if (__all(fl == 1u || fr == 7 || fr > 9)) break;
      }
    } else {
      ld_w();
    }
    BAR();
    STAMP(5);
    const rsrc_t rH = RSRC(a.ws + WS_H + (size_t)((4 * tm3 + 2 * tp) * 112 + 4 * kq) * 256, 2 * 112 * 256);
    const rsrc_t rY = RSRC(a.ws + WS_Y + (size_t)(tt3 * 64 + c3) * 256, S * D * 2 + 1024);
    const unsigned vh0 = vl + (kq & 1) * (112 * 256), vh1 = vl + ((kq & 1) ^ 1) * (112 * 256);
    u32x4 hf[7][2];
#pragma unroll
    for (int g = 0; g < 7; ++g) {
      hf[g][0] = __builtin_amdgcn_raw_buffer_load_b128(rH, vh0, g * 4096, 16);
      hf[g][1] = __builtin_amdgcn_raw_buffer_load_b128(rH, vh1, g * 4096, 16);
    }
    const unsigned vy = cq * 256 + fr * 16;
    const u32x4 e1 = __builtin_amdgcn_raw_buffer_load_b128(rY, vy, 0, 16), e2 = __builtin_amdgcn_raw_buffer_load_b128(rY, vy, S * D * 2, 16);
    const u32x4 ex = *(const u32x4*)(a.x + (size_t)(tt3 * 16 + fr) * D + (c3 + cq) * 8);
    SCHED();
    if (wid == 4) ld_w();
    f32x4 acc[2][4];
#pragma unroll
    for (int g = 0; g < 7; ++g)
#pragma unroll
      for (int tl = 0; tl < 2; ++tl)
#pragma unroll
        for (int mi = 0; mi < 4; ++mi) acc[tl][mi] = MFMA16(wf[g][mi], __builtin_bit_cast(bf16x8, hf[g][tl]), (g ? acc[tl][mi] : f32x4{0, 0, 0, 0}));
    STAMP(6);
    LAS f32x4* xb = (LAS f32x4*)(size_t)(P3_X) + lane;
#pragma unroll
    for (int d = 1; d < 4; ++d) {
      xb[((wr * 3 + d - 1) * 2) * 64] = acc[d & 1][d & 2];
      xb[((wr * 3 + d - 1) * 2 + 1) * 64] = acc[d & 1][(d & 2) + 1];
    }
    WL0();
    BAR();
    f32x4 s0 = acc[0][0], s1 = acc[0][1];
#pragma unroll
    for (int d = 1; d < 4; ++d) {
      s0 += xb[(((wr ^ d) * 3 + d - 1) * 2) * 64];
      s1 += xb[(((wr ^ d) * 3 + d - 1) * 2 + 1) * 64];
    }
    f32x4 c0, c1;
#pragma unroll
    for (int j = 0; j < 4; ++j) {
      auto w = __builtin_amdgcn_permlane16_swap(__float_as_uint(s0[j]), __float_as_uint(s1[j]), false, false);
      c0[j] = __uint_as_float(w[0]); c1[j] = __uint_as_float(w[1]);
    }
    u32x4 o;
#pragma unroll
    for (int k = 0; k < 4; ++k) {
      const f32x4 c = (k >> 1) ? c1 : c0;
      const float lo = bf_lo(ex[k]) + bf_lo(e1[k]) - bf_lo(e2[k]) + c[(k & 1) * 2];
      const float hi = bf_hi(ex[k]) + bf_hi(e1[k]) - bf_hi(e2[k]) + c[(k & 1) * 2 + 1];
      o[k] = cvt_pk(lo, hi);
    }
    st16_sc1(o, RSRC(a.out, S * D * 2), ((tt3 * 16 + fr) * D + (c3 + cq) * 8) * 2, 0);
  }
  STAMP(7);
  STAMP_OUT(a.ws, 1, 8);
  {
    asm volatile("s_waitcnt vmcnt(0)" ::: "memory");
    const unsigned long long pend = __builtin_amdgcn_s_memrealtime();
    const rsrc_t rS = RSRC(a.ws + (100u << 20), 65536);
    st16_sc1(u32x4{(unsigned)pstart, (unsigned)pend, 0, 0}, rS, 0, (blockIdx.x * 8 + wid) * 16);
    if (blockIdx.x == 73 && wid == 0) {
      while (__builtin_amdgcn_s_memrealtime() < pstart + 6000) __builtin_amdgcn_s_sleep(1);
      int mns = 1 << 30, mxe = -(1 << 30);
      for (int i = 0; i < 32; ++i) {
        const u32x4 v = ld16_sc1(rS, lane * 16, i * 1024);
        const int st = (int)(v[0] - (unsigned)pstart), en = (int)(v[1] - (unsigned)pstart);
        mns = min(mns, st); mxe = max(mxe, en);
      }
      for (int o = 32; o > 0; o >>= 1) { mns = min(mns, __shfl_xor(mns, o)); mxe = max(mxe, __shfl_xor(mxe, o)); }
      const int q = __builtin_amdgcn_readfirstlane(mxe - mns);
      const unsigned long long tend = pstart + __builtin_amdgcn_readfirstlane(mns) + 10 * q;
      while (__builtin_amdgcn_s_memrealtime() < tend) ;
    }
  }
}

extern "C" void kernel_launch(void* const* d_in, const int* in_sizes, int n_in, void* d_out, int out_size, void* d_ws, size_t ws_size,
                              hipStream_t stream) {
  (void)in_sizes; (void)n_in; (void)out_size; (void)ws_size;
  (void)hipFuncSetAttribute((const void*)fused, hipFuncAttributeMaxDynamicSharedMemorySize, LDS_BYTES);
  Args a{(const u16*)d_in[0], (const u16*)d_in[3], (const u16*)d_in[4], (const u16*)d_in[6], (const u16*)d_in[9],
         (const u16*)d_in[1], (const u16*)d_in[2], (const u16*)d_in[7], (const u16*)d_in[8], (char*)d_ws, (u16*)d_out};
  fused<<<256, 512, LDS_BYTES, stream>>>(a);
}
```

```cpp
#include <hip/hip_runtime.h>
#include <hip/hip_bf16.h>
#include <stdint.h>

typedef unsigned short u16;
typedef short bf16x8 __attribute__((ext_vector_type(8)));
typedef short s16x4 __attribute__((ext_vector_type(4)));
typedef float f32x4 __attribute__((ext_vector_type(4)));
typedef float f32x2 __attribute__((ext_vector_type(2)));
typedef unsigned u32x4 __attribute__((ext_vector_type(4)));
typedef __bf16 bf16x2v __attribute__((ext_vector_type(2)));
typedef __amdgpu_buffer_rsrc_t rsrc_t;
#define LAS __attribute__((address_space(3)))
#define WAIT_V(n) asm volatile("s_waitcnt vmcnt(%0)" ::"n"(n) : "memory")
#define WL0() __builtin_amdgcn_s_waitcnt(0xC07F)
#define BAR() do { asm volatile("" ::: "memory"); __builtin_amdgcn_s_barrier(); asm volatile("" ::: "memory"); } while (0)
#define SCHED() __builtin_amdgcn_sched_barrier(0)
#define MFMA16(a, b, c) __builtin_amdgcn_mfma_f32_16x16x32_bf16(a, b, c, 0, 0, 0)
#define RSRC(p, n) __builtin_amdgcn_make_buffer_rsrc((void*)(p), 0, (unsigned)(n), 0x00020000)
#define OPAQUE(x) asm volatile("" : "+v"(x))

#define STAMP_DECL
#define STAMP(i)
#define STAMP_OUT(ws, kid, n)
#define K2_KXMASK 3
#define K1_KXMASK 3
#define DWARM 0
#define CODEWARM 5
#define NOPARK 0
#define Z1EARLY 1
#define PF_LIST 0x753810u
#define PF_N 6

constexpr int S = 2048, D = 512, F = 1792, FH = 896;
constexpr size_t WS_Q = 0;
constexpr size_t WS_Z1 = WS_Q + (size_t)(2 * D + FH) * S * 2;
constexpr size_t WS_H = WS_Z1 + (size_t)FH * S * 2;
constexpr size_t WS_Y = WS_H + (size_t)FH * S * 2;
constexpr size_t WS_G = WS_Y + (size_t)2 * D * S * 2;
constexpr unsigned G_F1 = 0;
constexpr unsigned G_FP = 32768;
constexpr unsigned G_FZ = 32768 + 64;
constexpr unsigned G_PAY = 65536;
constexpr unsigned G_ALL = G_PAY + 16 * 14336;
constexpr unsigned G_REQ = G_ALL + 1024;
constexpr unsigned G_ANS = G_REQ + 1024;
constexpr unsigned G_END = G_ANS + 1024;

constexpr int LDS_BYTES = 163840;
constexpr int P1_RING = 32768, P1_SCR = 81920;
constexpr int P2_PB = 65536, P2_Z = 131072, P3_X = 114688;
constexpr unsigned SPIN = 1u << 20;

struct Args {
  const u16* x; const u16* wdr; const u16* wdi; const u16* win; const u16* wout;
  const u16* wsr; const u16* wsi; const u16* wsp; const u16* bsp;
  char* ws; u16* out;
};

__device__ __forceinline__ unsigned cvt_pk(float lo, float hi) {
  f32x2 v = {lo, hi};
  return __builtin_bit_cast(unsigned, __builtin_convertvector(v, bf16x2v));
}
__device__ __forceinline__ float bf_lo(unsigned u) { return __uint_as_float(u << 16); }
__device__ __forceinline__ float bf_hi(unsigned u) { return __uint_as_float(u & 0xffff0000u); }
constexpr float GC1 = -2.0f * 0.7978845608028654f * 1.4426950408889634f, GC2 = GC1 * 0.044715f;
__device__ __forceinline__ f32x2 gelu2(f32x2 v) {
  const f32x2 t = v * ((v * v) * GC2 + GC1);
  const f32x2 e = f32x2{__builtin_amdgcn_exp2f(t[0]), __builtin_amdgcn_exp2f(t[1])} + 1.0f;
  return v * f32x2{__builtin_amdgcn_rcpf(e[0]), __builtin_amdgcn_rcpf(e[1])};
}
__device__ __forceinline__ void dot_sq(float& c, unsigned a) { asm volatile("v_dot2c_f32_bf16 %0, %1, %1" : "+v"(c) : "v"(a)); }
__device__ __forceinline__ void dot_sum(float& c, unsigned a) { asm volatile("v_dot2c_f32_bf16 %0, 0x3f803f80, %1" : "+v"(c) : "v"(a)); }
__device__ __forceinline__ float sum_rows(float v) {
  auto a = __builtin_amdgcn_permlane32_swap(__float_as_uint(v), __float_as_uint(v), false, false);
  v = __uint_as_float(a[0]) + __uint_as_float(a[1]);
  auto b = __builtin_amdgcn_permlane16_swap(__float_as_uint(v), __float_as_uint(v), false, false);
  return __uint_as_float(b[0]) + __uint_as_float(b[1]);
}
__device__ __forceinline__ float sel4(f32x4 v, int i) { const float a = (i & 1) ? v[1] : v[0], b = (i & 1) ? v[3] : v[2]; return (i & 2) ? b : a; }

__device__ __forceinline__ void dma16(rsrc_t r, unsigned mbase, int mimm, unsigned voff, unsigned soff) {
  asm volatile("s_add_i32 m0, %0, %1\n\ts_nop 0\n\tbuffer_load_dwordx4 %2, %3, %4 offen lds" ::"s"(mbase), "n"(mimm), "v"(voff), "s"(r), "s"(soff) : "scc");
}
__device__ __forceinline__ void dma16_sc1(rsrc_t r, unsigned mbase, int mimm, unsigned voff, unsigned soff) {
  asm volatile("s_add_i32 m0, %0, %1\n\ts_nop 0\n\tbuffer_load_dwordx4 %2, %3, %4 offen sc1 lds" ::"s"(mbase), "n"(mimm), "v"(voff), "s"(r), "s"(soff) : "scc");
}
__device__ __forceinline__ void dma4_sc1(rsrc_t r, int ldsaddr, unsigned voff, unsigned soff) {
  asm volatile("s_mov_b32 m0, %0\n\ts_nop 0\n\tbuffer_load_dword %1, %2, %3 offen sc1 lds" ::"n"(ldsaddr), "v"(voff), "s"(r), "s"(soff) : "memory");
}
__device__ __forceinline__ void touch(rsrc_t r, int junk, unsigned voff, unsigned soff) {
  asm volatile("s_mov_b32 m0, %0\n\ts_nop 0\n\tbuffer_load_dword %1, %2, %3 offen lds" ::"n"(junk), "v"(voff), "s"(r), "s"(soff) : "memory");
}
__device__ __forceinline__ void st16_sc1(u32x4 v, rsrc_t r, unsigned voff, unsigned soff) {
  asm volatile("buffer_store_dwordx4 %0, %1, %2, %3 offen sc1\n\ts_nop 1" ::"v"(v), "v"(voff), "s"(r), "s"(soff) : "memory");
}
__device__ __forceinline__ void st4_sc1(unsigned v, rsrc_t r, unsigned voff, unsigned soff) {
  asm volatile("buffer_store_dword %0, %1, %2, %3 offen sc1" ::"v"(v), "v"(voff), "s"(r), "s"(soff) : "memory");
}
__device__ __forceinline__ unsigned ld4_sc1(rsrc_t r, unsigned voff, unsigned soff) {
  unsigned v;
  asm volatile("buffer_load_dword %0, %1, %2, %3 offen sc1\n\ts_waitcnt vmcnt(0)" : "=&v"(v) : "v"(voff), "s"(r), "s"(soff) : "memory");
  return v;
}
__device__ __forceinline__ u32x4 ld16_sc1(rsrc_t r, unsigned voff, unsigned soff) {
  u32x4 v;
  asm volatile("buffer_load_dwordx4 %0, %1, %2, %3 offen sc1\n\ts_waitcnt vmcnt(0)" : "=&v"(v) : "v"(voff), "s"(r), "s"(soff) : "memory");
  return v;
}
__device__ __forceinline__ void ld16_async(u32x4& v, rsrc_t r, unsigned voff, unsigned soff) {
  asm volatile("buffer_load_dwordx4 %0, %1, %2, %3 offen" : "=v"(v) : "v"(voff), "s"(r), "s"(soff) : "memory");
}
__device__ __forceinline__ void wait_vn(int n) { WAIT_V(n); }
__device__ __forceinline__ bf16x8 ld_tr(unsigned addr) {
  s16x4 lo = __builtin_amdgcn_ds_read_tr16_b64_v4i16((LAS s16x4*)(size_t)(addr));
  s16x4 hi = __builtin_amdgcn_ds_read_tr16_b64_v4i16((LAS s16x4*)(size_t)(addr + 512));
  return __builtin_shufflevector(lo, hi, 0, 1, 2, 3, 4, 5, 6, 7);
}
__device__ __forceinline__ bf16x8 ld_frag(unsigned addr) { return *(const LAS bf16x8*)(size_t)(addr); }
__device__ __forceinline__ void wait_go(rsrc_t rG, unsigned nonce, unsigned seen) {
  for (unsigned spin = 0; seen != nonce && spin < SPIN; ++spin) {
    seen = __builtin_amdgcn_readfirstlane(ld4_sc1(rG, 0, G_ANS + blockIdx.x * 4));
    __builtin_amdgcn_s_sleep(1);
  }
}

template <bool ZB>
__device__ __forceinline__ void phase1(const Args& a, int pm, int cb, int kx, const u16* Pp, int kx2, f32x4 (&z1v)[2][2]) {
  const int tid = threadIdx.x, lane = tid & 63;
  const int wid = __builtin_amdgcn_readfirstlane(tid >> 6);
  const int wm = wid & 3, wn = wid >> 2;
  STAMP_DECL STAMP(0);
  const rsrc_t rX = RSRC(a.x + (size_t)pm * 128 * D, 128 * D * 2);
  const rsrc_t rW = ZB ? RSRC(a.win + 64 * cb, (D * F - 64 * cb) * 2) : RSRC(a.wdi + (size_t)384 * D, 128 * D * 2);
  const int r8 = lane >> 3, p8 = lane & 7;
  const unsigned sw_e = (p8 ^ (r8 >> 1)) << 4;
  const unsigned vx_e = (32 * (wid >> 1) + 4 * (wid & 1) + 8 * (r8 >> 2) + (r8 & 3)) * (D * 2) + sw_e;
  const unsigned vx_o = (vx_e ^ 64) + 16 * (D * 2);
  const unsigned vw2 = ZB ? (8 * wid + r8) * (F * 2) + ((p8 ^ ((((r8 >> 1) & 1) | ((wid & 1) << 1)) << 1)) << 4)
                          : (64 + 8 * wid + r8) * (D * 2) + (sw_e ^ ((wid & 1) << 6));
  const unsigned vw1 = ZB ? vw2 + FH * 2 : vw2 - 64 * (D * 2);
  constexpr unsigned WKB = ZB ? 64 * F * 2 : 128;
  const unsigned mb1 = wid * 1024, mb2 = wid * 2048;
  auto piece1 = [&](int st, int p) {
    const unsigned kb = st ^ kx;
    const int base = P1_RING + (st & 3) * 32768;
    if (p == 0) dma16(rX, mb2, base, vx_e, kb * 128);
    else if (p == 1) dma16(rW, mb1, base + 16384, vw1, kb * WKB);
    else if (p == 2) dma16(rX, mb2, base + 1024, vx_o, kb * 128);
    else dma16(rW, mb1, base + 24576, vw2, kb * WKB);
  };
  piece1(0, 0); piece1(0, 1); piece1(0, 2); piece1(0, 3);
  SCHED();

  const int fr = lane & 15, fq = lane >> 4;
  const int kh = wm >> 1;
  const unsigned fsw = ((fq ^ ((fr >> 1) & 7)) << 4) ^ (kh << 6);
  unsigned xa[2][2][2], wb[2][2][2];
#pragma unroll
  for (int m = 0; m < 2; ++m) { xa[0][m][0] = P1_RING + (32 * wm + 16 * (m ^ wn) + fr) * 128 + fsw; xa[0][m][1] = xa[0][m][0] ^ 64; }
#pragma unroll
  for (int n = 0; n < 2; ++n) {
    const int tile = 2 * wn + (n ^ (wm & 1));
    if (ZB) {
      const int q4 = fr >> 2, pp = lane & 3;
      const int sp = ((q4 >> 1) & 1) | ((fq & 1) << 1);
      const int ch = (2 * tile + (pp >> 1)) ^ (sp << 1);
      wb[0][n][0] = P1_RING + 16384 + 128 * (8 * fq + q4) + 16 * ch + 8 * (pp & 1) + kh * 4096;
      wb[0][n][1] = wb[0][n][0] ^ 4096;
    } else {
      wb[0][n][0] = P1_RING + 16384 + (16 * tile + fr) * 128 + fsw;
      wb[0][n][1] = wb[0][n][0] ^ 64;
    }
  }
#pragma unroll
  for (int i = 0; i < 2; ++i)
#pragma unroll
    for (int ks = 0; ks < 2; ++ks) {
      xa[1][i][ks] = xa[0][i][ks] + 65536; wb[1][i][ks] = wb[0][i][ks] + 65536;
      OPAQUE(xa[0][i][ks]); OPAQUE(wb[0][i][ks]); OPAQUE(xa[1][i][ks]); OPAQUE(wb[1][i][ks]);
    }
  auto ldw = [&](unsigned addr) -> bf16x8 { return ZB ? ld_tr(addr) : ld_frag(addr); };
  SCHED();
  piece1(1, 0); piece1(1, 1); piece1(1, 2); piece1(1, 3);
  const rsrc_t rG = RSRC(a.ws + WS_G, G_END);
  if (wid == 7) dma4_sc1(rG, 0, 0, G_ANS + blockIdx.x * 4);
  unsigned nonce = 0;
  STAMP(1);

  bf16x8 xf[16][2], wf[2][4];
  const f32x4 Z4 = {0, 0, 0, 0};
  f32x4 acc1[2][2] = {{Z4, Z4}, {Z4, Z4}}, acc2[2][2] = {{Z4, Z4}, {Z4, Z4}};
  f32x4 gram = Z4, rsum = Z4, cs1 = Z4, cs2 = Z4;
  const bf16x8 ONES = {0x3f80, 0x3f80, 0x3f80, 0x3f80, 0x3f80, 0x3f80, 0x3f80, 0x3f80};

  auto mm = [&](int kk, int i) {
    const int ks = kk & 1;
    if (i == 0) gram = MFMA16(xf[kk][0], xf[kk][0], gram);
    else if (i == 1) rsum = MFMA16(xf[kk][0], ONES, rsum);
    else if (i < 6) { const int m = (i - 2) >> 1, n = i & 1; acc1[m][n] = MFMA16(xf[kk][m], wf[ks][n], acc1[m][n]); }
    else if (i < 10) { const int m = (i - 6) >> 1, n = i & 1; acc2[m][n] = ZB ? MFMA16(wf[ks][2 + n], xf[kk][m], acc2[m][n]) : MFMA16(xf[kk][m], wf[ks][2 + n], acc2[m][n]); }
    else if (i == 10) cs1 = MFMA16(ONES, wf[0][0], cs1);
    else cs2 = MFMA16(ONES, wf[0][2], cs2);
  };
  auto rd = [&](int kk, int i) {
    const int st = kk >> 1, ks = kk & 1, wi = (st >> 1) & 1, so = (st & 1) * 32768;
    if (i == 0 || i == 3) xf[kk][i & 1] = ld_frag(xa[wi][i & 1][ks] + so);
    else if (i < 3) wf[ks][i - 1] = ldw(wb[wi][i - 1][ks] + so);
    else wf[ks][i - 2] = ldw(wb[wi][i - 4][ks] + so + 8192);
  };
  auto piece3 = [&](int p) {
    const rsrc_t rD = RSRC((cb < 8 ? a.wdr : a.wdi) + (size_t)(cb & 7) * 64 * D, 64 * D * 2);
    const unsigned ve = r8 * (D * 2) + sw_e, vo = (ve ^ 64) + 8 * (D * 2);
    dma16(rD, wid * 8192, p * 1024, (p & 1) ? vo : ve, (wid ^ kx) * 128 + (p >> 1) * 16 * (D * 2));
  };
  auto earlyP = [&](int k) {
    const rsrc_t rP = RSRC(Pp, 128 * S * 2);
    const unsigned ve = (16 * wid + r8) * (S * 2) + sw_e, vo = (ve ^ 64) + 8 * (S * 2);
    dma16(rP, mb2, P2_PB + (k + 2) * 16384, ve, (k ^ kx2) * 128);
    dma16(rP, mb2, P2_PB + (k + 2) * 16384 + 1024, vo, (k ^ kx2) * 128);
  };
#pragma unroll
  for (int st = 0; st < 8; ++st) {
    wait_vn(st <= 6 ? 4 : ZB ? 8 : 0);
    WL0();
    BAR();
    if (st == 0) STAMP(2);
    SCHED();
#pragma unroll
    for (int i = 0; i < 10; ++i) {
      if (st > 0) mm(2 * st - 1, i);
      SCHED();
      if (i < 6) rd(2 * st, i);
      if (st + 2 < 8) { if (i == 3) piece1(st + 2, 0); if (i == 8) piece1(st + 2, 1); }
      if (ZB && st == 6 && (i % 3) == 0) piece3(i / 3);
      if (st == 7 && i == 7) earlyP(0);
      SCHED();
    }
    WL0();
    SCHED();
#pragma unroll
    for (int i = 0; i < 12; ++i) {
      mm(2 * st, i);
      SCHED();
      if (i < 6) rd(2 * st + 1, i);
      if (st + 2 < 8) { if (i == 3) piece1(st + 2, 2); if (i == 9) piece1(st + 2, 3); }
      if (ZB && st == 6 && (i % 3) == 2) piece3(4 + i / 3);
      if (st == 7 && i == 8) earlyP(1);
      SCHED();
    }
    if (st == 2 && wid == 7) {
      nonce = __builtin_amdgcn_readfirstlane(*(LAS unsigned*)(size_t)(lane * 4)) + 1;
      st4_sc1(nonce, rG, 0, G_REQ + blockIdx.x * 4);
    }
    SCHED();
  }
  LAS float* const s_r = (LAS float*)(size_t)(P1_SCR);
  LAS float* const s_c = (LAS float*)(size_t)(P1_SCR + 512);
  LAS float* const s_u1 = (LAS float*)(size_t)(P1_SCR + 1024);
  LAS float* const s_u2 = (LAS float*)(size_t)(P1_SCR + 1536);
  LAS float* const s_z = (LAS float*)(size_t)(P1_SCR + 2048);
  LAS float* const s_n = (LAS float*)(size_t)(P1_SCR + 4096);
  LAS float* const s_u3 = (LAS float*)(size_t)(P1_SCR + 5120);
  const unsigned tb0 = fr * 128 + fsw, tb1 = tb0 ^ 64;
  WAIT_V(4);
  {
    const unsigned cb0 = tb0 + wid * 8192, cb1 = cb0 ^ 64;
    bf16x8 c3[8];
    f32x4 cs3[4];
    if (ZB) {
#pragma unroll
      for (int t = 0; t < 4; ++t) { c3[2 * t] = ld_frag(cb0 + t * 2048); c3[2 * t + 1] = ld_frag(cb1 + t * 2048); }
    }
    mm(15, 0); mm(15, 1);
    SCHED();
    if (ZB) {
#pragma unroll
      for (int t = 0; t < 4; ++t) cs3[t] = MFMA16(ONES, c3[2 * t], Z4);
#pragma unroll
      for (int t = 0; t < 4; ++t) cs3[t] = MFMA16(ONES, c3[2 * t + 1], cs3[t]);
    }
    SCHED();
#pragma unroll
    for (int i = 2; i < 10; ++i) mm(15, i);
    STAMP(3);
    unsigned i01 = 32 * wm + 16 * wn, i23 = 64 * kh + 16 * (2 * wn + (wm & 1));
    asm volatile("" : "+s"(i01), "+s"(i23));
    const float s1 = sel4(rsum, fr & 3), s2 = sel4(gram, fr & 3);
    const float mean = s1 * (1.0f / D);
    const float rstd = __builtin_amdgcn_rsqf(fmaxf(s2 * (1.0f / D) - mean * mean, 0.0f) + 1e-5f);
    if ((fr >> 2) == fq) { s_r[i01 + fr] = rstd; s_c[i01 + fr] = -mean * rstd; }
    if (fq < 2) s_u1[128 * fq + i23 + fr] = fq ? cs2[0] : cs1[0];
    if (ZB) s_u3[lane * 8 + wid] = sel4(f32x4{cs3[0][0], cs3[1][0], cs3[2][0], cs3[3][0]}, fq);
  }
  const int s0 = pm * 128 + 32 * wm;
  const rsrc_t rT = RSRC(a.ws + WS_Q, (2 * D + FH) * S * 2);
  const unsigned vT = ((fr >> 2) & 1) * 65536 + fq * 256 + (4 * (fr >> 3) + (fr & 3)) * 16;
  WL0();
  BAR();
  STAMP(4);
  if (wid == 7) dma4_sc1(rG, P1_SCR + 7168, 0, G_ANS + blockIdx.x * 4);
  earlyP(2); earlyP(3);
  f32x4 rq[2], cq[2];
  auto ld_rc = [&]() {
#pragma unroll
    for (int m = 0; m < 2; ++m) {
      rq[m] = *(LAS f32x4*)(s_r + 32 * wm + 16 * (m ^ wn) + 4 * fq);
      cq[m] = *(LAS f32x4*)(s_c + 32 * wm + 16 * (m ^ wn) + 4 * fq);
    }
  };
  ld_rc();
  auto store_t = [&](int T, f32x4 v0, f32x4 v1) {
    const unsigned a0 = cvt_pk(v0[0], v0[1]), a1 = cvt_pk(v0[2], v0[3]), b0 = cvt_pk(v1[0], v1[1]), b1 = cvt_pk(v1[2], v1[3]);
    const u32x4 o = wn ? u32x4{b0, b1, a0, a1} : u32x4{a0, a1, b0, b1};
    st16_sc1(o, rT, vT, (T >> 1) * 131072 + (T & 1) * 128 + s0 * 32);
  };
  auto lin_store = [&](f32x4 c0, f32x4 c1, float u, int T) { store_t(T, rq[0] * c0 + cq[0] * u, rq[1] * c1 + cq[1] * u); };
  float u1[2];
#pragma unroll
  for (int n = 0; n < 2; ++n) { const int ui = 16 * (2 * wn + (n ^ (wm & 1))) + fr; u1[n] = s_u1[ui] + s_u1[64 + ui]; }
  if (!ZB) {
#pragma unroll
    for (int n = 0; n < 2; ++n) {
      const int tile = 2 * wn + (n ^ (wm & 1)), ui = 16 * tile + fr;
      lin_store(acc1[0][n], acc1[1][n], u1[n], 56 + tile);
      lin_store(acc2[0][n], acc2[1][n], s_u2[ui] + s_u2[64 + ui], 60 + tile);
    }
    if (wid == 7) { WAIT_V(0); wait_go(rG, nonce, __builtin_amdgcn_readfirstlane(*(LAS unsigned*)(size_t)(P1_SCR + 7168 + lane * 4))); }
    STAMP(5); STAMP(6); STAMP(7); STAMP(8); STAMP(9);
    STAMP_OUT(a.ws, 0, 10);
    return;
  }
  {
    f32x4 ps[2], pq[2];
#pragma unroll
    for (int n = 0; n < 2; ++n)
#pragma unroll
      for (int m = 0; m < 2; ++m) {
        f32x4 v = rq[m] * acc1[m][n] + cq[m] * u1[n];
        const f32x2 g0 = gelu2(f32x2{v[0], v[1]}), g1 = gelu2(f32x2{v[2], v[3]});
        v = f32x4{g0[0], g0[1], g1[0], g1[1]};
        acc1[m][n] = v;
        if (n == 0) { ps[m] = v; pq[m] = v * v; } else { ps[m] += v; pq[m] += v * v; }
      }
    float t[8], h[4];
#define DPP2(d, x, y, ctl, m0, m1) "v_add_f32_dpp " d ", " x ", " x " " ctl " row_mask:0xf bank_mask:" m0 "\n\tv_add_f32_dpp " d ", " y ", " y " " ctl " row_mask:0xf bank_mask:" m1 "\n\t"
#define DPPQ(d, ctl) "v_add_f32_dpp " d ", " d ", " d " " ctl " row_mask:0xf bank_mask:0xf\n\t"
    asm volatile("s_nop 1\n\t" DPP2("%0", "%8", "%16", "row_ror:8", "0x3", "0xc") DPP2("%1", "%9", "%17", "row_ror:8", "0x3", "0xc")
                 DPP2("%2", "%10", "%18", "row_ror:8", "0x3", "0xc") DPP2("%3", "%11", "%19", "row_ror:8", "0x3", "0xc")
                 DPP2("%4", "%12", "%20", "row_ror:8", "0x3", "0xc") DPP2("%5", "%13", "%21", "row_ror:8", "0x3", "0xc")
                 DPP2("%6", "%14", "%22", "row_ror:8", "0x3", "0xc") DPP2("%7", "%15", "%23", "row_ror:8", "0x3", "0xc") "s_nop 1"
                 : "=&v"(t[0]), "=&v"(t[1]), "=&v"(t[2]), "=&v"(t[3]), "=&v"(t[4]), "=&v"(t[5]), "=&v"(t[6]), "=&v"(t[7])
                 : "v"(ps[0][0]), "v"(pq[0][0]), "v"(ps[0][1]), "v"(pq[0][1]), "v"(ps[0][2]), "v"(pq[0][2]), "v"(ps[0][3]), "v"(pq[0][3]),
                   "v"(ps[1][0]), "v"(pq[1][0]), "v"(ps[1][1]), "v"(pq[1][1]), "v"(ps[1][2]), "v"(pq[1][2]), "v"(ps[1][3]), "v"(pq[1][3]));
    asm volatile("s_nop 1\n\t" DPP2("%0", "%4", "%8", "row_half_mirror", "0x5", "0xa") DPP2("%1", "%5", "%9", "row_half_mirror", "0x5", "0xa")
                 DPP2("%2", "%6", "%10", "row_half_mirror", "0x5", "0xa") DPP2("%3", "%7", "%11", "row_half_mirror", "0x5", "0xa") "s_nop 1\n\t"
                 DPPQ("%0", "quad_perm:[1,0,3,2]") DPPQ("%1", "quad_perm:[1,0,3,2]") DPPQ("%2", "quad_perm:[1,0,3,2]") DPPQ("%3", "quad_perm:[1,0,3,2]")
                 DPPQ("%0", "quad_perm:[2,3,0,1]") DPPQ("%1", "quad_perm:[2,3,0,1]") DPPQ("%2", "quad_perm:[2,3,0,1]") DPPQ("%3", "quad_perm:[2,3,0,1]") "s_nop 0"
                 : "=&v"(h[0]), "=&v"(h[1]), "=&v"(h[2]), "=&v"(h[3])
                 : "v"(t[0]), "v"(t[1]), "v"(t[2]), "v"(t[3]), "v"(t[4]), "v"(t[5]), "v"(t[6]), "v"(t[7]));
    if ((fr & 3) == 0) *(LAS f32x4*)(s_z + (wn * 128 + 32 * wm + 8 * fq + 4 * ((fr >> 3) ^ wn) + 2 * ((fr >> 2) & 1)) * 2) = f32x4{h[0], h[1], h[2], h[3]};
  }
  WL0();
  BAR();
  STAMP(5);
  if (wid == 7) {
    __builtin_amdgcn_s_setprio(2);
    const f32x4 p = *(LAS f32x4*)(s_z + lane * 4) + *(LAS f32x4*)(s_z + 256 + lane * 4);
    WAIT_V(4);
    wait_go(rG, nonce, __builtin_amdgcn_readfirstlane(*(LAS unsigned*)(size_t)(P1_SCR + 7168 + lane * 4)));
    st16_sc1(__builtin_bit_cast(u32x4, p) | 1u, rG, lane * 16, G_PAY + (pm * 14 + cb) * 1024);
  }
  STAMP(6);
  {
    float u3[2];
    f32x4 acct[2][2] = {{Z4, Z4}, {Z4, Z4}};
    const unsigned t30 = tb0 + wn * 4096, t31 = tb1 + wn * 4096;
    bf16x8 w[8];
    auto rd3 = [&](int f) { const int kk = f >> 1; w[f & 7] = ld_frag(((kk & 1) ? t31 : t30) + (kk >> 1) * 8192 + (f & 1) * 2048); };
#pragma unroll
    for (int f = 0; f < 4; ++f) rd3(f);
    SCHED();
#pragma unroll
    for (int f = 0; f < 32; ++f) {
      const int kk = f >> 1, n = f & 1;
      acct[0][n] = MFMA16(xf[kk][0], w[f & 7], acct[0][n]);
      SCHED();
      if (f + 4 < 32) rd3(f + 4);
      if (f == 8 || f == 12) {
        const int m = (f - 8) >> 2;
        const float r = s_r[32 * wm + 16 * (m ^ wn) + fr], c = s_c[32 * wm + 16 * (m ^ wn) + fr];
#pragma unroll
        for (int q = 0; q < 2; ++q) {
          const int ui = 16 * (2 * wn + (q ^ (wm & 1))) + 4 * fq;
          z1v[m][q] = acc2[m][q] * r + (*(LAS f32x4*)(s_u2 + ui) + *(LAS f32x4*)(s_u2 + 64 + ui)) * c;
        }
      }
      if (f == 20 || f == 24) {
        const int q = (f - 20) >> 2;
        const f32x4 uu = *(LAS f32x4*)(s_u3 + (32 * wn + 16 * q + fr) * 8) + *(LAS f32x4*)(s_u3 + (32 * wn + 16 * q + fr) * 8 + 4);
        u3[q] = (uu[0] + uu[1]) + (uu[2] + uu[3]);
      }
      SCHED();
      acct[1][n] = MFMA16(xf[kk][1], w[f & 7], acct[1][n]);
      SCHED();
    }
    if (wid != 7) {
#pragma unroll
      for (int n = 0; n < 2; ++n) lin_store(acct[0][n], acct[1][n], u3[n], 4 * cb + 2 * wn + n);
    }
    STAMP(7);
  if (wid == 7) {
    f32x4 t;
    for (unsigned spin = 0; spin < SPIN; ++spin) {
      u32x4 pv[14];
#pragma unroll
      for (int i = 0; i < 14; ++i) pv[i] = __builtin_amdgcn_raw_buffer_load_b128(rG, lane * 16 + (i & 3) * 1024, G_PAY + pm * 14336 + (i >> 2) * 4096, 16);
      if (spin == 0) {
#pragma unroll
        for (int n = 0; n < 2; ++n) lin_store(acct[0][n], acct[1][n], u3[n], 4 * cb + 2 * wn + n);
      }
      u32x4 al = pv[0];
      t = __builtin_bit_cast(f32x4, pv[0]);
#pragma unroll
      for (int i = 1; i < 14; ++i) { al &= pv[i]; t += __builtin_bit_cast(f32x4, pv[i]); }
      if (__all((al[0] & al[1] & al[2] & al[3] & 1u) != 0)) break;
      asm volatile("" ::: "memory");
    }
    const float m0 = t[0] * (1.0f / FH), m1 = t[2] * (1.0f / FH);
    const float r0 = __builtin_amdgcn_rsqf(fmaxf(t[1] * (1.0f / FH) - m0 * m0, 0.0f) + 1e-5f);
    const float r1 = __builtin_amdgcn_rsqf(fmaxf(t[3] * (1.0f / FH) - m1 * m1, 0.0f) + 1e-5f);
    *(LAS f32x2*)(s_n + 2 * lane) = f32x2{r0, r1};
    *(LAS f32x2*)(s_n + 128 + 2 * lane) = f32x2{-m0 * r0, -m1 * r1};
    __builtin_amdgcn_s_setprio(0);
  }
  }
  WL0();
  BAR();
  STAMP(8);
  {
    f32x4 r2[2], c2[2];
#pragma unroll
    for (int m = 0; m < 2; ++m) {
      r2[m] = *(LAS f32x4*)(s_n + 32 * wm + 8 * fq + 4 * (m ^ wn));
      c2[m] = *(LAS f32x4*)(s_n + 128 + 32 * wm + 8 * fq + 4 * (m ^ wn));
    }
#pragma unroll
    for (int n = 0; n < 2; ++n) store_t(64 + 4 * cb + 2 * wn + (n ^ (wm & 1)), acc1[0][n] * r2[0] + c2[0], acc1[1][n] * r2[1] + c2[1]);
  }
  STAMP(9);
  STAMP_OUT(a.ws, 0, 10);
}

__device__ __forceinline__ void warm_window(const Args& a, int junk, int rb, int w, int wv, int lane) {
  if (w < 8) {
    const unsigned vo = (rb * 128 + wv * 32 + (lane >> 2)) * (S * 2) + (lane & 3) * 128;
#pragma unroll
    for (int m = 0; m < 3; ++m) {
      const rsrc_t r = RSRC(m == 0 ? a.wsr : m == 1 ? a.wsi : a.wsp, S * S * 2);
      touch(r, junk, vo, w * 512);
      touch(r, junk, vo, w * 512 + 16 * (S * 2));
    }
  } else {
    const rsrc_t r = RSRC(a.wout + (size_t)rb * 56 * D, 56 * D * 2);
    touch(r, junk, lane * 128, wv * 16384);
    touch(r, junk, lane * 128, wv * 16384 + 8192);
  }
}

__global__ __launch_bounds__(512, 2) void fused(Args a) {
  extern __shared__ __attribute__((aligned(1024))) char smem[];
  asm volatile("" ::"v"(smem));
  const int bx = blockIdx.x & 7, bi = blockIdx.x >> 3;
  if (bi < CODEWARM && threadIdx.x < 64) {
    unsigned long long pc;
    asm volatile("s_getpc_b64 %0" : "=s"(pc));
    touch(RSRC(pc & ~127ull, CODEWARM * 8192), 1024, threadIdx.x * 128, bi * 8192);
  }
  const int pm = 4 * (bx >> 1) + (bi >> 3), cb = 2 * (bi & 7) + (bx & 1);
  int mat, tm, tn;
  if (bx & 1) { mat = 2; tm = pm; tn = bi & 7; }
  else { mat = bx >> 2; tm = ((bx >> 1) & 1) * 8 + (bi >> 2); tn = bi & 3; }
  const int kx = (tm ^ tn) & K2_KXMASK;
  const u16* Pp = (mat == 0 ? a.wsr : mat == 1 ? a.wsi : a.wsp) + (size_t)tm * 128 * S;
  f32x4 z1v[2][2];
  {
    const int kx1 = ((bi & 7) ^ (2 * (bi >> 3))) & K1_KXMASK;
    if (__builtin_expect(cb < 14, 1)) phase1<true>(a, pm, cb, kx1, Pp, kx, z1v);
    else if (cb == 14) phase1<false>(a, pm, cb, kx1, Pp, kx, z1v);
  }
  int tid = threadIdx.x;
  asm volatile("" : "+v"(tid));
  const int lane = tid & 63;
  const int wid = __builtin_amdgcn_readfirstlane(tid >> 6);
  STAMP_DECL STAMP(0);
  const int tm3 = bx * 4 + (bi >> 3), tn3 = bi & 7;
  const int fr = lane & 15, fq = lane >> 4;
  const unsigned vl = lane * 16;
  const rsrc_t rG = RSRC(a.ws + WS_G, G_END);
  const bool work2 = cb != 15;
  auto z1_finish = [&]() {
    if (cb < 14) {
      const int wm = wid & 3, wn = wid >> 2;
      const rsrc_t rZ = RSRC(a.ws + WS_Z1, FH * S * 2);
      const unsigned vZ = (fr >> 3) * (112 * 256) + (2 * ((fq & 1) ^ (wm & 1)) + (fq >> 1)) * 256 + (fr & 7) * 16;
      const unsigned sZ = ((pm * 8 + 2 * wm) * 112 + 8 * cb + 4 * wn) * 256;
#pragma unroll
      for (int m = 0; m < 2; ++m) {
        unsigned pk[2][2];
#pragma unroll
        for (int n = 0; n < 2; ++n) {
          const f32x2 g0 = gelu2(f32x2{z1v[m][n][0], z1v[m][n][1]}), g1 = gelu2(f32x2{z1v[m][n][2], z1v[m][n][3]});
          pk[n][0] = cvt_pk(g0[0], g0[1]); pk[n][1] = cvt_pk(g1[0], g1[1]);
        }
        auto w0 = __builtin_amdgcn_permlane16_swap(pk[0][0], pk[1][0], false, false);
        auto w1 = __builtin_amdgcn_permlane16_swap(pk[0][1], pk[1][1], false, false);
        st16_sc1(u32x4{w0[0], w1[0], w0[1], w1[1]}, rZ, vZ, sZ + 128 * (m ^ wn));
      }
    }
  };
  if (blockIdx.x == 255) {
    for (int i = 0; i < 36; ++i) st16_sc1(u32x4{0, 0, 0, 0}, rG, vl, (wid * 36 + i) * 1024);
    st16_sc1(u32x4{0, 0, 0, 0}, rG, vl, G_ALL);
  }
  unsigned bv[4];
#pragma unroll
  for (int ni = 0; ni < 4; ++ni) bv[ni] = a.bsp[(8 * tm + 4 * ((wid >> 1) & 1) + ni) * 16 + fr];
  asm volatile("" : "+v"(bv[0]), "+v"(bv[1]), "+v"(bv[2]), "+v"(bv[3]));
  if (work2 || blockIdx.x == 255) {
    WAIT_V(0);
    WL0();
    BAR();
  }
  if (wid >= 4) {
    const int lw = wid - 4;
    const int r8 = lane >> 3, p = lane & 7;
    if (wid == 7) {
      if (!work2) {
        if (blockIdx.x == 255) {
          for (unsigned spin = 0; spin < SPIN; ++spin) {
            const u32x4 rq = ld16_sc1(rG, vl, G_REQ);
            st16_sc1(rq, rG, vl, G_ANS);
            const u32x4 fl = ld16_sc1(rG, vl, G_ALL);
            if (__all((fl[0] & fl[1] & fl[2] & (lane == 63 ? 1u : fl[3])) == 1u)) break;
          }
        } else {
          const unsigned nonce = __builtin_amdgcn_readfirstlane(ld4_sc1(rG, 0, G_ANS + blockIdx.x * 4)) + 1;
          st4_sc1(nonce, rG, 0, G_REQ + blockIdx.x * 4);
          wait_go(rG, nonce, nonce - 1);
        }
      }
      const unsigned box = (fq & 1) * 128 + (cb >> 1) * 16 + fr;
      st4_sc1(1u, rG, fq < 2 ? box * 128 + (pm * 2 + (cb & 1)) * 4 : G_ALL + blockIdx.x * 4, 0);
    }
    const rsrc_t rP = RSRC(Pp, 128 * S * 2);
    const unsigned vo_e = (lw * 32 + r8) * (S * 2) + ((p ^ (r8 >> 1)) << 4);
    const unsigned vo_o = (vo_e ^ 64) + 8 * (S * 2);
    auto stageP = [&](int kt, unsigned poff) {
      const unsigned mb = poff + lw * 4096, so = (kt ^ kx) * 128;
#pragma unroll
      for (int j = 0; j < 4; ++j) dma16(rP, mb, P2_PB + j * 1024, (j & 1) ? vo_o : vo_e, so + (j >> 1) * 16 * (S * 2));
    };
    if (work2) {
      BAR();
      stageP(4, 0); stageP(5, 16384);
    }
    const rsrc_t rWo = RSRC(a.wout + 64 * tn3, (FH * D - 64 * tn3) * 2);
    const unsigned vw_e = (32 * lw + r8) * (D * 2) + ((p ^ (((r8 >> 1) & 1) << 1)) << 4), vw_o = (vw_e ^ 64) + 8 * (D * 2);
    u32x4 wreg[12];
#pragma unroll
    for (int i = 0; i < 12; ++i) wreg[i] = u32x4{0, 0, 0, 0};
    auto wput = [&](int rg) {
#pragma unroll
      for (int j = 0; j < 4 && !NOPARK; ++j) *(LAS u32x4*)(size_t)(rg * 16384 + lw * 4096 + j * 1024 + vl) = wreg[(rg == 6 ? 8 : (rg - 2) * 4) + j];
    };
    auto wdma = [&](int rg) {
#pragma unroll
      for (int j = 0; j < 4; ++j) dma16(rWo, lw * 4096, rg * 16384 + j * 1024, (j & 1) ? vw_o : vw_e, (rg * 128 + (j >> 1) * 16) * (D * 2));
    };
    if (work2) {
#pragma unroll
      for (int i = 0; i < 12; ++i) {
        const int rg = i < 8 ? 2 + (i >> 2) : 6;
        ld16_async(wreg[i], rWo, (i & 1) ? vw_o : vw_e, (rg * 128 + ((i >> 1) & 1) * 16) * (D * 2));
      }
      if (Z1EARLY && wid != 7) z1_finish();
      if (wid == 7) {
        const unsigned box = (mat * 4 + tn) * 16 + tm;
        for (unsigned spin = 0; spin < SPIN; ++spin) {
          const unsigned fl = ld4_sc1(rG, (lane & 31) * 4, G_F1 + box * 128);
          if (__all(fl == 1u || (box >= 112 && box < 128 && (lane & 1)))) break;
        }
      }
      BAR();
      STAMP(1);
      const rsrc_t rQ = RSRC(a.ws + WS_Q + (size_t)(mat * 4 + tn) * 128 * S * 2, 128 * S * 2);
      const unsigned vq = vl + lw * 131072;
      auto stageQ = [&](int kt) {
        const unsigned mb = (kt & 3) * 16384 + lw * 4096, so = (kt ^ kx) * 2048;
#pragma unroll
        for (int j = 0; j < 4; ++j) dma16_sc1(rQ, mb, j * 1024, vq, so + (j >> 1) * 65536 + (j & 1) * 1024);
      };
      stageQ(0); stageQ(1); stageQ(2); stageQ(3);
      if (!Z1EARLY || wid == 7) z1_finish();
      WAIT_V(12); BAR();
      WAIT_V(8); BAR(); stageQ(4); stageP(6, 2 * 16384);
      WAIT_V(12); BAR(); stageQ(5); stageP(7, 3 * 16384);
      WAIT_V(16); BAR(); stageQ(6); stageP(8, 4 * 16384);
      unsigned poff = 5 * 16384;
      unsigned zf;
      for (int kt = 3; kt < 23; ++kt) {
        WAIT_V(20);
        BAR();
        if (kt == 3 && wid == 7 && cb < 14) st4_sc1(1u, rG, 0, G_FZ + (pm * 8 + (cb >> 1)) * 128 + (cb & 1) * 4);
        stageQ(kt + 4); stageP(kt + 6, poff);
        poff = poff == 5 * 16384 ? 0 : poff + 16384;
      }
      asm volatile("buffer_load_dword %0, %1, %2, %3 offen sc1" : "=v"(zf) : "v"((lane & 1) * 4), "s"(rG), "s"(G_FZ + (tm * 8 + tn) * 128) : "memory");
#pragma unroll
      for (int kt = 23; kt < 26; ++kt) {
        WAIT_V(20);
        BAR();
        stageQ(kt + 4); stageP(kt + 6, ((kt + 2) % 6) * 16384);
      }
      asm volatile("s_waitcnt vmcnt(20)" : "+v"(zf)::"memory");
      if (mat == 2) {
        const rsrc_t rZ = RSRC(a.ws + WS_Z1 + (size_t)((8 * tm + lw) * 112 + 16 * tn) * 256, 4 * 112 * 256 + 4096);
        auto stageZ = [&](int h) {
#pragma unroll
          for (int j = 0; j < 4; ++j) dma16_sc1(rZ, lw * 4096, P2_Z + h * 16384 + j * 1024, vl, h * (4 * 112 * 256) + j * 1024);
        };
        for (unsigned spin = 0; !__all(zf == 1u) && spin < SPIN; ++spin) zf = ld4_sc1(rG, (lane & 1) * 4, G_FZ + (tm * 8 + tn) * 128);
        WAIT_V(20); BAR(); stageQ(30); stageZ(0);
        WAIT_V(20); BAR(); stageQ(31); stageZ(1);
        WAIT_V(20); BAR(); wdma(0); wdma(4);
        WAIT_V(20); BAR(); wdma(1); wdma(5);
        WAIT_V(20); BAR(); wput(2); wput(6);
        WAIT_V(16); BAR(); wput(3);
      } else {
        WAIT_V(20); BAR(); stageQ(30);
        WAIT_V(16); BAR(); stageQ(31);
        WAIT_V(12); BAR(); wdma(0); wdma(4);
        WAIT_V(12); BAR(); wdma(1); wdma(5);
        WAIT_V(16); BAR(); wput(2); wput(6);
        BAR(); wput(3);
      }
    } else {
      STAMP(1);
      wdma(0); wdma(4); wdma(1); wdma(5); wdma(2); wdma(3); wdma(6);
    }
    STAMP(2); STAMP(3);
    WAIT_V(0);
  } else if (work2) {
    const int wp = wid >> 1, wq = wid & 1;
    f32x4 acc[4][4];
#pragma unroll
    for (int ni = 0; ni < 4; ++ni) {
      const float bias = mat == 2 ? __uint_as_float(bv[ni] << 16) : 0.0f;
#pragma unroll
      for (int n = 0; n < 4; ++n) acc[ni][n] = f32x4{bias, bias, bias, bias};
    }
    BAR();
    if (DWARM && cb == 14) warm_window(a, 0, pm, 2, wid, lane);
    const int sw = (fr >> 1) & 7;
    const unsigned ra = P2_PB + (wp * 64 + fr) * 128 + ((fq ^ sw) << 4);
    const unsigned rb = wq * 8192 + vl;
    bf16x8 a0[4], b0[4], a1[4], b1[4];
    unsigned poff = 2 * 16384;
    unsigned pa0 = ra + poff, pa1 = pa0 ^ 64;
#define K2_RD(AF, BF, pa, sl, ks, i) do { \
    if ((i) == 0) AF[0] = ld_frag(pa); \
    else if ((i) <= 4) BF[(i) - 1] = ld_frag(rb + (sl) * 16384 + ((i) - 1) * 2048 + (ks) * 1024); \
    else AF[(i) - 4] = ld_frag(pa + ((i) - 4) * 2048); } while (0)
#define K2_MM(AF, BF, i) acc[(i) >> 2][(i) & 3] = MFMA16(BF[(i) & 3], AF[(i) >> 2], acc[(i) >> 2][(i) & 3])
#define K2_STEP(s, LAST) do { \
    SCHED(); WL0(); SCHED(); \
    _Pragma("unroll") for (int i = 0; i < 16; ++i) { K2_MM(a0, b0, i); SCHED(); if (i < 8) K2_RD(a1, b1, pa1, s, 1, i); \
      if (i == 10) { poff = poff == 5 * 16384 ? 0 : poff + 16384; pa0 = ra + poff; } SCHED(); } \
    WL0(); \
    if ((s) == 3) WAIT_V(0); \
    BAR(); \
    SCHED(); \
    _Pragma("unroll") for (int i = 0; i < 16; ++i) { K2_MM(a1, b1, i); SCHED(); if (!(LAST) && i < 8) K2_RD(a0, b0, pa0, ((s) + 1) & 3, 0, i); \
      if (i == 10) pa1 = pa0 ^ 64; SCHED(); } \
  } while (0)
    if (DWARM && cb == 14) WAIT_V(0);
    if (Z1EARLY) z1_finish();
    BAR();
    if (!Z1EARLY) z1_finish();
    BAR();
    STAMP(1);
#pragma unroll
    for (int i = 0; i < 8; ++i) K2_RD(a0, b0, pa0, 0, 0, i);
    for (int kt = 0; kt < 28; kt += 4) { K2_STEP(0, 0); K2_STEP(1, 0); K2_STEP(2, 0); K2_STEP(3, 0); }
    K2_STEP(0, 0); K2_STEP(1, 0); K2_STEP(2, 0); K2_STEP(3, 1);
    SCHED();
    STAMP(2);
    if (mat < 2) {
      const rsrc_t rY = RSRC(a.ws + WS_Y + (size_t)mat * S * D * 2, S * D * 2);
      const unsigned so = ((8 * tm + 4 * wp) * 64 + 16 * tn + 8 * wq) * 256;
#pragma unroll
      for (int ni = 0; ni < 4; ++ni)
#pragma unroll
        for (int P = 0; P < 2; ++P) {
          const f32x4 c0 = acc[ni][2 * P], c1 = acc[ni][2 * P + 1];
          const u32x4 v = {cvt_pk(c0[0], c0[1]), cvt_pk(c0[2], c0[3]), cvt_pk(c1[0], c1[1]), cvt_pk(c1[2], c1[3])};
          st16_sc1(v, rY, vl, so + (ni * 64 + 4 * P) * 256);
        }
    } else {
      const rsrc_t rH = RSRC(a.ws + WS_H, S * FH * 2);
      const unsigned so = ((8 * tm + 4 * wp) * 112 + 16 * tn + 8 * wq) * 256;
      const unsigned za = P2_Z + wp * 16384 + wq * 2048 + (fq * 16 + ((fr & 4) << 1) + ((fr & 8) >> 1) + (fr & 3)) * 16;
#pragma unroll
      for (int ni = 0; ni < 4; ++ni) {
#pragma unroll
        for (int P = 0; P < 2; ++P) {
          const f32x4 c0 = acc[ni][2 * P], c1 = acc[ni][2 * P + 1];
          const u32x4 z = *(const LAS u32x4*)(size_t)(za + ni * 4096 + P * 1024);
          const u32x4 v = {cvt_pk(bf_lo(z[0]) * c0[0], bf_hi(z[0]) * c0[1]), cvt_pk(bf_lo(z[1]) * c0[2], bf_hi(z[1]) * c0[3]),
                           cvt_pk(bf_lo(z[2]) * c1[0], bf_hi(z[2]) * c1[1]), cvt_pk(bf_lo(z[3]) * c1[2], bf_hi(z[3]) * c1[3])};
          st16_sc1(v, rH, vl, so + (ni * 112 + 4 * P) * 256);
        }
      }
    }
    STAMP(3);
    WAIT_V(0);
  } else {
    STAMP(1);
    for (int i = 0; i < PF_N && blockIdx.x != 255; ++i) {
      warm_window(a, P2_Z, pm, (PF_LIST >> (4 * i)) & 15, wid, lane);
      WAIT_V(6);
    }
    STAMP(2);
    WAIT_V(0);
    STAMP(3);
  }
  WL0();
  BAR();
  STAMP(4);
  {
    const int wr = (wid + tn3) & 7, tp = wr >> 2, kq = wr & 3;
    const int tt3 = 4 * tm3 + 2 * tp + (kq & 1);
    const int cq = 2 * (fq & 1) + (fq >> 1);
    const int c3 = 8 * tn3 + 2 * (kq & 2);
    if (wid == 4 && work2) {
      const unsigned box = mat == 2 ? fr : (fr & 8) + 2 * tn + (fr & 1);
      st4_sc1(1u, rG, box * 128, G_FP + tm * 2048 + (mat == 2 ? tn : 8 + mat) * 4);
    }
    const int q4 = fr >> 2, pp = lane & 3;
    const int sp = ((q4 >> 1) & 1) | ((fq & 1) << 1);
    const unsigned wa = kq * 4096 + 128 * (8 * fq + q4) + 16 * ((2 * (kq & 2) + (pp >> 1)) ^ (sp << 1)) + 8 * (pp & 1);
    bf16x8 wf[7][4];
    auto ld_w = [&]() {
#pragma unroll
      for (int mi = 0; mi < 4; ++mi) {
        unsigned lo = wa ^ (mi * 32), hi = lo + 65536;
        OPAQUE(hi);
#pragma unroll
        for (int g = 0; g < 7; ++g) wf[g][mi] = ld_tr((g < 4 ? lo : hi) + (g & 3) * 16384);
      }
    };
    if (wid == 4) {
      for (unsigned spin = 0; spin < SPIN; ++spin) {
        const unsigned fl = ld4_sc1(rG, fr * 4, G_FP + (tm3 * 8 + tn3) * 128);
        if (__all(fl == 1u || fr == 7 || fr > 9)) break;
      }
    } else {
      ld_w();
    }
    BAR();
    STAMP(5);
    const rsrc_t rH = RSRC(a.ws + WS_H + (size_t)((4 * tm3 + 2 * tp) * 112 + 4 * kq) * 256, 2 * 112 * 256);
    const rsrc_t rY = RSRC(a.ws + WS_Y + (size_t)(tt3 * 64 + c3) * 256, S * D * 2 + 1024);
    const unsigned vh0 = vl + (kq & 1) * (112 * 256), vh1 = vl + ((kq & 1) ^ 1) * (112 * 256);
    u32x4 hf[7][2];
#pragma unroll
    for (int g = 0; g < 7; ++g) {
      hf[g][0] = __builtin_amdgcn_raw_buffer_load_b128(rH, vh0, g * 4096, 16);
      hf[g][1] = __builtin_amdgcn_raw_buffer_load_b128(rH, vh1, g * 4096, 16);
    }
    const unsigned vy = cq * 256 + fr * 16;
    const u32x4 e1 = __builtin_amdgcn_raw_buffer_load_b128(rY, vy, 0, 16), e2 = __builtin_amdgcn_raw_buffer_load_b128(rY, vy, S * D * 2, 16);
    const u32x4 ex = *(const u32x4*)(a.x + (size_t)(tt3 * 16 + fr) * D + (c3 + cq) * 8);
    SCHED();
    if (wid == 4) ld_w();
    f32x4 acc[2][4];
#pragma unroll
    for (int g = 0; g < 7; ++g)
#pragma unroll
      for (int tl = 0; tl < 2; ++tl)
#pragma unroll
        for (int mi = 0; mi < 4; ++mi) acc[tl][mi] = MFMA16(wf[g][mi], __builtin_bit_cast(bf16x8, hf[g][tl]), (g ? acc[tl][mi] : f32x4{0, 0, 0, 0}));
    STAMP(6);
    LAS f32x4* xb = (LAS f32x4*)(size_t)(P3_X) + lane;
#pragma unroll
    for (int d = 1; d < 4; ++d) {
      xb[((wr * 3 + d - 1) * 2) * 64] = acc[d & 1][d & 2];
      xb[((wr * 3 + d - 1) * 2 + 1) * 64] = acc[d & 1][(d & 2) + 1];
    }
    WL0();
    BAR();
    f32x4 s0 = acc[0][0], s1 = acc[0][1];
#pragma unroll
    for (int d = 1; d < 4; ++d) {
      s0 += xb[(((wr ^ d) * 3 + d - 1) * 2) * 64];
      s1 += xb[(((wr ^ d) * 3 + d - 1) * 2 + 1) * 64];
    }
    f32x4 c0, c1;
#pragma unroll
    for (int j = 0; j < 4; ++j) {
      auto w = __builtin_amdgcn_permlane16_swap(__float_as_uint(s0[j]), __float_as_uint(s1[j]), false, false);
      c0[j] = __uint_as_float(w[0]); c1[j] = __uint_as_float(w[1]);
    }
    u32x4 o;
#pragma unroll
    for (int k = 0; k < 4; ++k) {
      const f32x4 c = (k >> 1) ? c1 : c0;
      const float lo = bf_lo(ex[k]) + bf_lo(e1[k]) - bf_lo(e2[k]) + c[(k & 1) * 2];
      const float hi = bf_hi(ex[k]) + bf_hi(e1[k]) - bf_hi(e2[k]) + c[(k & 1) * 2 + 1];
      o[k] = cvt_pk(lo, hi);
    }
    st16_sc1(o, RSRC(a.out, S * D * 2), ((tt3 * 16 + fr) * D + (c3 + cq) * 8) * 2, 0);
  }
  STAMP(7);
  STAMP_OUT(a.ws, 1, 8);
}

extern "C" void kernel_launch(void* const* d_in, const int* in_sizes, int n_in, void* d_out, int out_size, void* d_ws, size_t ws_size,
                              hipStream_t stream) {
  (void)in_sizes; (void)n_in; (void)out_size; (void)ws_size;
  (void)hipFuncSetAttribute((const void*)fused, hipFuncAttributeMaxDynamicSharedMemorySize, LDS_BYTES);
  Args a{(const u16*)d_in[0], (const u16*)d_in[3], (const u16*)d_in[4], (const u16*)d_in[6], (const u16*)d_in[9],
         (const u16*)d_in[1], (const u16*)d_in[2], (const u16*)d_in[7], (const u16*)d_in[8], (char*)d_ws, (u16*)d_out};
  fused<<<256, 512, LDS_BYTES, stream>>>(a);
}
```

```cpp
#include <hip/hip_runtime.h>
#include <hip/hip_bf16.h>
#include <stdint.h>

typedef unsigned short u16;
typedef short bf16x8 __attribute__((ext_vector_type(8)));
typedef short s16x4 __attribute__((ext_vector_type(4)));
typedef float f32x4 __attribute__((ext_vector_type(4)));
typedef float f32x2 __attribute__((ext_vector_type(2)));
typedef unsigned u32x4 __attribute__((ext_vector_type(4)));
typedef __bf16 bf16x2v __attribute__((ext_vector_type(2)));
typedef __amdgpu_buffer_rsrc_t rsrc_t;
#define LAS __attribute__((address_space(3)))
#define WAIT_V(n) asm volatile("s_waitcnt vmcnt(%0)" ::"n"(n) : "memory")
#define WL0() __builtin_amdgcn_s_waitcnt(0xC07F)
#define BAR() do { asm volatile("" ::: "memory"); __builtin_amdgcn_s_barrier(); asm volatile("" ::: "memory"); } while (0)
#define SCHED() __builtin_amdgcn_sched_barrier(0)
#define MFMA16(a, b, c) __builtin_amdgcn_mfma_f32_16x16x32_bf16(a, b, c, 0, 0, 0)
#define RSRC(p, n) __builtin_amdgcn_make_buffer_rsrc((void*)(p), 0, (unsigned)(n), 0x00020000)
#define OPAQUE(x) asm volatile("" : "+v"(x))

#define STAMP_DECL
#define STAMP(i)
#define STAMP_OUT(ws, kid, n)
#define K2_KXMASK 3
#define K1_KXMASK 3
#define DWARM 0
#define CODEWARM 5
#define NOPARK 0
#define PF_LIST 0x753810u
#define PF_N 6

constexpr int S = 2048, D = 512, F = 1792, FH = 896;
constexpr size_t WS_Q = 0;
constexpr size_t WS_Z1 = WS_Q + (size_t)(2 * D + FH) * S * 2;
constexpr size_t WS_H = WS_Z1 + (size_t)FH * S * 2;
constexpr size_t WS_Y = WS_H + (size_t)FH * S * 2;
constexpr size_t WS_G = WS_Y + (size_t)2 * D * S * 2;
constexpr unsigned G_F1 = 0;
constexpr unsigned G_FP = 32768;
constexpr unsigned G_FZ = 32768 + 64;
constexpr unsigned G_PAY = 65536;
constexpr unsigned G_ALL = G_PAY + 16 * 14336;
constexpr unsigned G_REQ = G_ALL + 1024;
constexpr unsigned G_ANS = G_REQ + 1024;
constexpr unsigned G_END = G_ANS + 1024;

constexpr int LDS_BYTES = 163840;
constexpr int P1_RING = 32768, P1_SCR = 81920;
constexpr int P2_PB = 65536, P2_Z = 131072, P3_X = 114688;
constexpr unsigned SPIN = 1u << 20;

struct Args {
  const u16* x; const u16* wdr; const u16* wdi; const u16* win; const u16* wout;
  const u16* wsr; const u16* wsi; const u16* wsp; const u16* bsp;
  char* ws; u16* out;
};

__device__ __forceinline__ unsigned cvt_pk(float lo, float hi) {
  f32x2 v = {lo, hi};
  return __builtin_bit_cast(unsigned, __builtin_convertvector(v, bf16x2v));
}
__device__ __forceinline__ float bf_lo(unsigned u) { return __uint_as_float(u << 16); }
__device__ __forceinline__ float bf_hi(unsigned u) { return __uint_as_float(u & 0xffff0000u); }
constexpr float GC1 = -2.0f * 0.7978845608028654f * 1.4426950408889634f, GC2 = GC1 * 0.044715f;
__device__ __forceinline__ f32x2 gelu2(f32x2 v) {
  const f32x2 t = v * ((v * v) * GC2 + GC1);
  const f32x2 e = f32x2{__builtin_amdgcn_exp2f(t[0]), __builtin_amdgcn_exp2f(t[1])} + 1.0f;
  return v * f32x2{__builtin_amdgcn_rcpf(e[0]), __builtin_amdgcn_rcpf(e[1])};
}
__device__ __forceinline__ void dot_sq(float& c, unsigned a) { asm volatile("v_dot2c_f32_bf16 %0, %1, %1" : "+v"(c) : "v"(a)); }
__device__ __forceinline__ void dot_sum(float& c, unsigned a) { asm volatile("v_dot2c_f32_bf16 %0, 0x3f803f80, %1" : "+v"(c) : "v"(a)); }
__device__ __forceinline__ float sum_rows(float v) {
  auto a = __builtin_amdgcn_permlane32_swap(__float_as_uint(v), __float_as_uint(v), false, false);
  v = __uint_as_float(a[0]) + __uint_as_float(a[1]);
  auto b = __builtin_amdgcn_permlane16_swap(__float_as_uint(v), __float_as_uint(v), false, false);
  return __uint_as_float(b[0]) + __uint_as_float(b[1]);
}
__device__ __forceinline__ float sel4(f32x4 v, int i) { const float a = (i & 1) ? v[1] : v[0], b = (i & 1) ? v[3] : v[2]; return (i & 2) ? b : a; }

__device__ __forceinline__ void dma16(rsrc_t r, unsigned mbase, int mimm, unsigned voff, unsigned soff) {
  asm volatile("s_add_i32 m0, %0, %1\n\ts_nop 0\n\tbuffer_load_dwordx4 %2, %3, %4 offen lds" ::"s"(mbase), "n"(mimm), "v"(voff), "s"(r), "s"(soff) : "scc");
}
__device__ __forceinline__ void dma16_sc1(rsrc_t r, unsigned mbase, int mimm, unsigned voff, unsigned soff) {
  asm volatile("s_add_i32 m0, %0, %1\n\ts_nop 0\n\tbuffer_load_dwordx4 %2, %3, %4 offen sc1 lds" ::"s"(mbase), "n"(mimm), "v"(voff), "s"(r), "s"(soff) : "scc");
}
__device__ __forceinline__ void dma4_sc1(rsrc_t r, int ldsaddr, unsigned voff, unsigned soff) {
  asm volatile("s_mov_b32 m0, %0\n\ts_nop 0\n\tbuffer_load_dword %1, %2, %3 offen sc1 lds" ::"n"(ldsaddr), "v"(voff), "s"(r), "s"(soff) : "memory");
}
__device__ __forceinline__ void touch(rsrc_t r, int junk, unsigned voff, unsigned soff) {
  asm volatile("s_mov_b32 m0, %0\n\ts_nop 0\n\tbuffer_load_dword %1, %2, %3 offen lds" ::"n"(junk), "v"(voff), "s"(r), "s"(soff) : "memory");
}
__device__ __forceinline__ void st16_sc1(u32x4 v, rsrc_t r, unsigned voff, unsigned soff) {
  asm volatile("buffer_store_dwordx4 %0, %1, %2, %3 offen sc1\n\ts_nop 1" ::"v"(v), "v"(voff), "s"(r), "s"(soff) : "memory");
}
__device__ __forceinline__ void st4_sc1(unsigned v, rsrc_t r, unsigned voff, unsigned soff) {
  asm volatile("buffer_store_dword %0, %1, %2, %3 offen sc1" ::"v"(v), "v"(voff), "s"(r), "s"(soff) : "memory");
}
__device__ __forceinline__ unsigned ld4_sc1(rsrc_t r, unsigned voff, unsigned soff) {
  unsigned v;
  asm volatile("buffer_load_dword %0, %1, %2, %3 offen sc1\n\ts_waitcnt vmcnt(0)" : "=&v"(v) : "v"(voff), "s"(r), "s"(soff) : "memory");
  return v;
}
__device__ __forceinline__ u32x4 ld16_sc1(rsrc_t r, unsigned voff, unsigned soff) {
  u32x4 v;
  asm volatile("buffer_load_dwordx4 %0, %1, %2, %3 offen sc1\n\ts_waitcnt vmcnt(0)" : "=&v"(v) : "v"(voff), "s"(r), "s"(soff) : "memory");
  return v;
}
__device__ __forceinline__ void wait_vn(int n) { WAIT_V(n); }
__device__ __forceinline__ bf16x8 ld_tr(unsigned addr) {
  s16x4 lo = __builtin_amdgcn_ds_read_tr16_b64_v4i16((LAS s16x4*)(size_t)(addr));
  s16x4 hi = __builtin_amdgcn_ds_read_tr16_b64_v4i16((LAS s16x4*)(size_t)(addr + 512));
  return __builtin_shufflevector(lo, hi, 0, 1, 2, 3, 4, 5, 6, 7);
}
__device__ __forceinline__ bf16x8 ld_frag(unsigned addr) { return *(const LAS bf16x8*)(size_t)(addr); }
__device__ __forceinline__ void wait_go(rsrc_t rG, unsigned nonce, unsigned seen) {
  for (unsigned spin = 0; seen != nonce && spin < SPIN; ++spin) {
    seen = __builtin_amdgcn_readfirstlane(ld4_sc1(rG, 0, G_ANS + blockIdx.x * 4));
    __builtin_amdgcn_s_sleep(1);
  }
}

template <bool ZB>
__device__ __forceinline__ void phase1(const Args& a, int pm, int cb, int kx, const u16* Pp, int kx2, f32x4 (&z1v)[2][2]) {
  const int tid = threadIdx.x, lane = tid & 63;
  const int wid = __builtin_amdgcn_readfirstlane(tid >> 6);
  const int wm = wid & 3, wn = wid >> 2;
  STAMP_DECL STAMP(0);
  const rsrc_t rX = RSRC(a.x + (size_t)pm * 128 * D, 128 * D * 2);
  const rsrc_t rW = ZB ? RSRC(a.win + 64 * cb, (D * F - 64 * cb) * 2) : RSRC(a.wdi + (size_t)384 * D, 128 * D * 2);
  const int r8 = lane >> 3, p8 = lane & 7;
  const unsigned sw_e = (p8 ^ (r8 >> 1)) << 4;
  const unsigned vx_e = (32 * (wid >> 1) + 4 * (wid & 1) + 8 * (r8 >> 2) + (r8 & 3)) * (D * 2) + sw_e;
  const unsigned vx_o = (vx_e ^ 64) + 16 * (D * 2);
  const unsigned vw2 = ZB ? (8 * wid + r8) * (F * 2) + ((p8 ^ ((((r8 >> 1) & 1) | ((wid & 1) << 1)) << 1)) << 4)
                          : (64 + 8 * wid + r8) * (D * 2) + (sw_e ^ ((wid & 1) << 6));
  const unsigned vw1 = ZB ? vw2 + FH * 2 : vw2 - 64 * (D * 2);
  constexpr unsigned WKB = ZB ? 64 * F * 2 : 128;
  const unsigned mb1 = wid * 1024, mb2 = wid * 2048;
  auto piece1 = [&](int st, int p) {
    const unsigned kb = st ^ kx;
    const int base = P1_RING + (st & 3) * 32768;
    if (p == 0) dma16(rX, mb2, base, vx_e, kb * 128);
    else if (p == 1) dma16(rW, mb1, base + 16384, vw1, kb * WKB);
    else if (p == 2) dma16(rX, mb2, base + 1024, vx_o, kb * 128);
    else dma16(rW, mb1, base + 24576, vw2, kb * WKB);
  };
  piece1(0, 0); piece1(0, 1); piece1(0, 2); piece1(0, 3);
  SCHED();

  const int fr = lane & 15, fq = lane >> 4;
  const int kh = wm >> 1;
  const unsigned fsw = ((fq ^ ((fr >> 1) & 7)) << 4) ^ (kh << 6);
  unsigned xa[2][2][2], wb[2][2][2];
#pragma unroll
  for (int m = 0; m < 2; ++m) { xa[0][m][0] = P1_RING + (32 * wm + 16 * (m ^ wn) + fr) * 128 + fsw; xa[0][m][1] = xa[0][m][0] ^ 64; }
#pragma unroll
  for (int n = 0; n < 2; ++n) {
    const int tile = 2 * wn + (n ^ (wm & 1));
    if (ZB) {
      const int q4 = fr >> 2, pp = lane & 3;
      const int sp = ((q4 >> 1) & 1) | ((fq & 1) << 1);
      const int ch = (2 * tile + (pp >> 1)) ^ (sp << 1);
      wb[0][n][0] = P1_RING + 16384 + 128 * (8 * fq + q4) + 16 * ch + 8 * (pp & 1) + kh * 4096;
      wb[0][n][1] = wb[0][n][0] ^ 4096;
    } else {
      wb[0][n][0] = P1_RING + 16384 + (16 * tile + fr) * 128 + fsw;
      wb[0][n][1] = wb[0][n][0] ^ 64;
    }
  }
#pragma unroll
  for (int i = 0; i < 2; ++i)
#pragma unroll
    for (int ks = 0; ks < 2; ++ks) {
      xa[1][i][ks] = xa[0][i][ks] + 65536; wb[1][i][ks] = wb[0][i][ks] + 65536;
      OPAQUE(xa[0][i][ks]); OPAQUE(wb[0][i][ks]); OPAQUE(xa[1][i][ks]); OPAQUE(wb[1][i][ks]);
    }
  auto ldw = [&](unsigned addr) -> bf16x8 { return ZB ? ld_tr(addr) : ld_frag(addr); };
  SCHED();
  piece1(1, 0); piece1(1, 1); piece1(1, 2); piece1(1, 3);
  const rsrc_t rG = RSRC(a.ws + WS_G, G_END);
  if (wid == 7) dma4_sc1(rG, 0, 0, G_ANS + blockIdx.x * 4);
  unsigned nonce = 0;
  STAMP(1);

  bf16x8 xf[16][2], wf[2][4];
  const f32x4 Z4 = {0, 0, 0, 0};
  f32x4 acc1[2][2] = {{Z4, Z4}, {Z4, Z4}}, acc2[2][2] = {{Z4, Z4}, {Z4, Z4}};
  f32x4 gram = Z4, rsum = Z4, cs1 = Z4, cs2 = Z4;
  const bf16x8 ONES = {0x3f80, 0x3f80, 0x3f80, 0x3f80, 0x3f80, 0x3f80, 0x3f80, 0x3f80};

  auto mm = [&](int kk, int i) {
    const int ks = kk & 1;
    if (i == 0) gram = MFMA16(xf[kk][0], xf[kk][0], gram);
    else if (i == 1) rsum = MFMA16(xf[kk][0], ONES, rsum);
    else if (i < 6) { const int m = (i - 2) >> 1, n = i & 1; acc1[m][n] = MFMA16(xf[kk][m], wf[ks][n], acc1[m][n]); }
    else if (i < 10) { const int m = (i - 6) >> 1, n = i & 1; acc2[m][n] = ZB ? MFMA16(wf[ks][2 + n], xf[kk][m], acc2[m][n]) : MFMA16(xf[kk][m], wf[ks][2 + n], acc2[m][n]); }
    else if (i == 10) cs1 = MFMA16(ONES, wf[0][0], cs1);
    else cs2 = MFMA16(ONES, wf[0][2], cs2);
  };
  auto rd = [&](int kk, int i) {
    const int st = kk >> 1, ks = kk & 1, wi = (st >> 1) & 1, so = (st & 1) * 32768;
    if (i == 0 || i == 3) xf[kk][i & 1] = ld_frag(xa[wi][i & 1][ks] + so);
    else if (i < 3) wf[ks][i - 1] = ldw(wb[wi][i - 1][ks] + so);
    else wf[ks][i - 2] = ldw(wb[wi][i - 4][ks] + so + 8192);
  };
  auto piece3 = [&](int p) {
    const rsrc_t rD = RSRC((cb < 8 ? a.wdr : a.wdi) + (size_t)(cb & 7) * 64 * D, 64 * D * 2);
    const unsigned ve = r8 * (D * 2) + sw_e, vo = (ve ^ 64) + 8 * (D * 2);
    dma16(rD, wid * 8192, p * 1024, (p & 1) ? vo : ve, (wid ^ kx) * 128 + (p >> 1) * 16 * (D * 2));
  };
  auto earlyP = [&](int k) {
    const rsrc_t rP = RSRC(Pp, 128 * S * 2);
    const unsigned ve = (16 * wid + r8) * (S * 2) + sw_e, vo = (ve ^ 64) + 8 * (S * 2);
    dma16(rP, mb2, P2_PB + (k + 2) * 16384, ve, (k ^ kx2) * 128);
    dma16(rP, mb2, P2_PB + (k + 2) * 16384 + 1024, vo, (k ^ kx2) * 128);
  };
#pragma unroll
  for (int st = 0; st < 8; ++st) {
    wait_vn(st <= 6 ? 4 : ZB ? 8 : 0);
    WL0();
    BAR();
    if (st == 0) STAMP(2);
    SCHED();
#pragma unroll
    for (int i = 0; i < 10; ++i) {
      if (st > 0) mm(2 * st - 1, i);
      SCHED();
      if (i < 6) rd(2 * st, i);
      if (st + 2 < 8) { if (i == 3) piece1(st + 2, 0); if (i == 8) piece1(st + 2, 1); }
      if (ZB && st == 6 && (i % 3) == 0) piece3(i / 3);
      if (st == 7 && i == 7) earlyP(0);
      SCHED();
    }
    WL0();
    SCHED();
#pragma unroll
    for (int i = 0; i < 12; ++i) {
      mm(2 * st, i);
      SCHED();
      if (i < 6) rd(2 * st + 1, i);
      if (st + 2 < 8) { if (i == 3) piece1(st + 2, 2); if (i == 9) piece1(st + 2, 3); }
      if (ZB && st == 6 && (i % 3) == 2) piece3(4 + i / 3);
      if (st == 7 && i == 8) earlyP(1);
      SCHED();
    }
    if (st == 2 && wid == 7) {
      nonce = __builtin_amdgcn_readfirstlane(*(LAS unsigned*)(size_t)(lane * 4)) + 1;
      st4_sc1(nonce, rG, 0, G_REQ + blockIdx.x * 4);
    }
    if (st == 6 && wid == 7) dma4_sc1(rG, P1_SCR + 7168, 0, G_ANS + blockIdx.x * 4);
    SCHED();
  }
  LAS float* const s_r = (LAS float*)(size_t)(P1_SCR);
  LAS float* const s_c = (LAS float*)(size_t)(P1_SCR + 512);
  LAS float* const s_u1 = (LAS float*)(size_t)(P1_SCR + 1024);
  LAS float* const s_u2 = (LAS float*)(size_t)(P1_SCR + 1536);
  LAS float* const s_z = (LAS float*)(size_t)(P1_SCR + 2048);
  LAS float* const s_n = (LAS float*)(size_t)(P1_SCR + 4096);
  LAS float* const s_u3 = (LAS float*)(size_t)(P1_SCR + 5120);
  const unsigned tb0 = fr * 128 + fsw, tb1 = tb0 ^ 64;
  WAIT_V(4);
  {
    const unsigned cb0 = tb0 + wid * 8192, cb1 = cb0 ^ 64;
    bf16x8 c3[8];
    f32x4 cs3[4];
    if (ZB) {
#pragma unroll
      for (int t = 0; t < 4; ++t) { c3[2 * t] = ld_frag(cb0 + t * 2048); c3[2 * t + 1] = ld_frag(cb1 + t * 2048); }
    }
    mm(15, 0); mm(15, 1);
    SCHED();
    if (ZB) {
#pragma unroll
      for (int t = 0; t < 4; ++t) cs3[t] = MFMA16(ONES, c3[2 * t], Z4);
#pragma unroll
      for (int t = 0; t < 4; ++t) cs3[t] = MFMA16(ONES, c3[2 * t + 1], cs3[t]);
    }
    SCHED();
#pragma unroll
    for (int i = 2; i < 10; ++i) mm(15, i);
    STAMP(3);
    unsigned i01 = 32 * wm + 16 * wn, i23 = 64 * kh + 16 * (2 * wn + (wm & 1));
    asm volatile("" : "+s"(i01), "+s"(i23));
    const float s1 = sel4(rsum, fr & 3), s2 = sel4(gram, fr & 3);
    const float mean = s1 * (1.0f / D);
    const float rstd = __builtin_amdgcn_rsqf(fmaxf(s2 * (1.0f / D) - mean * mean, 0.0f) + 1e-5f);
    if ((fr >> 2) == fq) { s_r[i01 + fr] = rstd; s_c[i01 + fr] = -mean * rstd; }
    if (fq < 2) s_u1[128 * fq + i23 + fr] = fq ? cs2[0] : cs1[0];
    if (ZB) s_u3[lane * 8 + wid] = sel4(f32x4{cs3[0][0], cs3[1][0], cs3[2][0], cs3[3][0]}, fq);
  }
  const int s0 = pm * 128 + 32 * wm;
  const rsrc_t rT = RSRC(a.ws + WS_Q, (2 * D + FH) * S * 2);
  const unsigned vT = ((fr >> 2) & 1) * 65536 + fq * 256 + (4 * (fr >> 3) + (fr & 3)) * 16;
  WL0();
  BAR();
  STAMP(4);
  earlyP(2); earlyP(3);
  f32x4 rq[2], cq[2];
  auto ld_rc = [&]() {
#pragma unroll
    for (int m = 0; m < 2; ++m) {
      rq[m] = *(LAS f32x4*)(s_r + 32 * wm + 16 * (m ^ wn) + 4 * fq);
      cq[m] = *(LAS f32x4*)(s_c + 32 * wm + 16 * (m ^ wn) + 4 * fq);
    }
  };
  ld_rc();
  auto store_t = [&](int T, f32x4 v0, f32x4 v1) {
    const unsigned a0 = cvt_pk(v0[0], v0[1]), a1 = cvt_pk(v0[2], v0[3]), b0 = cvt_pk(v1[0], v1[1]), b1 = cvt_pk(v1[2], v1[3]);
    const u32x4 o = wn ? u32x4{b0, b1, a0, a1} : u32x4{a0, a1, b0, b1};
    st16_sc1(o, rT, vT, (T >> 1) * 131072 + (T & 1) * 128 + s0 * 32);
  };
  auto lin_store = [&](f32x4 c0, f32x4 c1, float u, int T) { store_t(T, rq[0] * c0 + cq[0] * u, rq[1] * c1 + cq[1] * u); };
  float u1[2];
#pragma unroll
  for (int n = 0; n < 2; ++n) { const int ui = 16 * (2 * wn + (n ^ (wm & 1))) + fr; u1[n] = s_u1[ui] + s_u1[64 + ui]; }
  if (!ZB) {
#pragma unroll
    for (int n = 0; n < 2; ++n) {
      const int tile = 2 * wn + (n ^ (wm & 1)), ui = 16 * tile + fr;
      lin_store(acc1[0][n], acc1[1][n], u1[n], 56 + tile);
      lin_store(acc2[0][n], acc2[1][n], s_u2[ui] + s_u2[64 + ui], 60 + tile);
    }
    if (wid == 7) wait_go(rG, nonce, __builtin_amdgcn_readfirstlane(*(LAS unsigned*)(size_t)(P1_SCR + 7168 + lane * 4)));
    STAMP(5); STAMP(6); STAMP(7); STAMP(8); STAMP(9);
    STAMP_OUT(a.ws, 0, 10);
    return;
  }
  {
    f32x4 ps[2], pq[2];
#pragma unroll
    for (int n = 0; n < 2; ++n)
#pragma unroll
      for (int m = 0; m < 2; ++m) {
        f32x4 v = rq[m] * acc1[m][n] + cq[m] * u1[n];
        const f32x2 g0 = gelu2(f32x2{v[0], v[1]}), g1 = gelu2(f32x2{v[2], v[3]});
        v = f32x4{g0[0], g0[1], g1[0], g1[1]};
        acc1[m][n] = v;
        if (n == 0) { ps[m] = v; pq[m] = v * v; } else { ps[m] += v; pq[m] += v * v; }
      }
    float t[8], h[4];
#define DPP2(d, x, y, ctl, m0, m1) "v_add_f32_dpp " d ", " x ", " x " " ctl " row_mask:0xf bank_mask:" m0 "\n\tv_add_f32_dpp " d ", " y ", " y " " ctl " row_mask:0xf bank_mask:" m1 "\n\t"
#define DPPQ(d, ctl) "v_add_f32_dpp " d ", " d ", " d " " ctl " row_mask:0xf bank_mask:0xf\n\t"
    asm volatile("s_nop 1\n\t" DPP2("%0", "%8", "%16", "row_ror:8", "0x3", "0xc") DPP2("%1", "%9", "%17", "row_ror:8", "0x3", "0xc")
                 DPP2("%2", "%10", "%18", "row_ror:8", "0x3", "0xc") DPP2("%3", "%11", "%19", "row_ror:8", "0x3", "0xc")
                 DPP2("%4", "%12", "%20", "row_ror:8", "0x3", "0xc") DPP2("%5", "%13", "%21", "row_ror:8", "0x3", "0xc")
                 DPP2("%6", "%14", "%22", "row_ror:8", "0x3", "0xc") DPP2("%7", "%15", "%23", "row_ror:8", "0x3", "0xc") "s_nop 1"
                 : "=&v"(t[0]), "=&v"(t[1]), "=&v"(t[2]), "=&v"(t[3]), "=&v"(t[4]), "=&v"(t[5]), "=&v"(t[6]), "=&v"(t[7])
                 : "v"(ps[0][0]), "v"(pq[0][0]), "v"(ps[0][1]), "v"(pq[0][1]), "v"(ps[0][2]), "v"(pq[0][2]), "v"(ps[0][3]), "v"(pq[0][3]),
                   "v"(ps[1][0]), "v"(pq[1][0]), "v"(ps[1][1]), "v"(pq[1][1]), "v"(ps[1][2]), "v"(pq[1][2]), "v"(ps[1][3]), "v"(pq[1][3]));
    asm volatile("s_nop 1\n\t" DPP2("%0", "%4", "%8", "row_half_mirror", "0x5", "0xa") DPP2("%1", "%5", "%9", "row_half_mirror", "0x5", "0xa")
                 DPP2("%2", "%6", "%10", "row_half_mirror", "0x5", "0xa") DPP2("%3", "%7", "%11", "row_half_mirror", "0x5", "0xa") "s_nop 1\n\t"
                 DPPQ("%0", "quad_perm:[1,0,3,2]") DPPQ("%1", "quad_perm:[1,0,3,2]") DPPQ("%2", "quad_perm:[1,0,3,2]") DPPQ("%3", "quad_perm:[1,0,3,2]")
                 DPPQ("%0", "quad_perm:[2,3,0,1]") DPPQ("%1", "quad_perm:[2,3,0,1]") DPPQ("%2", "quad_perm:[2,3,0,1]") DPPQ("%3", "quad_perm:[2,3,0,1]") "s_nop 0"
                 : "=&v"(h[0]), "=&v"(h[1]), "=&v"(h[2]), "=&v"(h[3])
                 : "v"(t[0]), "v"(t[1]), "v"(t[2]), "v"(t[3]), "v"(t[4]), "v"(t[5]), "v"(t[6]), "v"(t[7]));
    if ((fr & 3) == 0) *(LAS f32x4*)(s_z + (wn * 128 + 32 * wm + 8 * fq + 4 * ((fr >> 3) ^ wn) + 2 * ((fr >> 2) & 1)) * 2) = f32x4{h[0], h[1], h[2], h[3]};
  }
  WL0();
  BAR();
  STAMP(5);
  if (wid == 7) {
    __builtin_amdgcn_s_setprio(2);
    const f32x4 p = *(LAS f32x4*)(s_z + lane * 4) + *(LAS f32x4*)(s_z + 256 + lane * 4);
    wait_go(rG, nonce, __builtin_amdgcn_readfirstlane(*(LAS unsigned*)(size_t)(P1_SCR + 7168 + lane * 4)));
    st16_sc1(__builtin_bit_cast(u32x4, p) | 1u, rG, lane * 16, G_PAY + (pm * 14 + cb) * 1024);
  }
  STAMP(6);
  {
    float u3[2];
    f32x4 acct[2][2] = {{Z4, Z4}, {Z4, Z4}};
    const unsigned t30 = tb0 + wn * 4096, t31 = tb1 + wn * 4096;
    bf16x8 w[8];
    auto rd3 = [&](int f) { const int kk = f >> 1; w[f & 7] = ld_frag(((kk & 1) ? t31 : t30) + (kk >> 1) * 8192 + (f & 1) * 2048); };
#pragma unroll
    for (int f = 0; f < 4; ++f) rd3(f);
    SCHED();
#pragma unroll
    for (int f = 0; f < 32; ++f) {
      const int kk = f >> 1, n = f & 1;
      acct[0][n] = MFMA16(xf[kk][0], w[f & 7], acct[0][n]);
      SCHED();
      if (f + 4 < 32) rd3(f + 4);
      if (f == 8 || f == 12) {
        const int m = (f - 8) >> 2;
        const float r = s_r[32 * wm + 16 * (m ^ wn) + fr], c = s_c[32 * wm + 16 * (m ^ wn) + fr];
#pragma unroll
        for (int q = 0; q < 2; ++q) {
          const int ui = 16 * (2 * wn + (q ^ (wm & 1))) + 4 * fq;
          z1v[m][q] = acc2[m][q] * r + (*(LAS f32x4*)(s_u2 + ui) + *(LAS f32x4*)(s_u2 + 64 + ui)) * c;
        }
      }
      if (f == 20 || f == 24) {
        const int q = (f - 20) >> 2;
        const f32x4 uu = *(LAS f32x4*)(s_u3 + (32 * wn + 16 * q + fr) * 8) + *(LAS f32x4*)(s_u3 + (32 * wn + 16 * q + fr) * 8 + 4);
        u3[q] = (uu[0] + uu[1]) + (uu[2] + uu[3]);
      }
      SCHED();
      acct[1][n] = MFMA16(xf[kk][1], w[f & 7], acct[1][n]);
      SCHED();
    }
    if (wid != 7) {
#pragma unroll
      for (int n = 0; n < 2; ++n) lin_store(acct[0][n], acct[1][n], u3[n], 4 * cb + 2 * wn + n);
    }
    STAMP(7);
  if (wid == 7) {
    f32x4 t;
    for (unsigned spin = 0; spin < SPIN; ++spin) {
      u32x4 pv[14];
#pragma unroll
      for (int i = 0; i < 14; ++i) pv[i] = __builtin_amdgcn_raw_buffer_load_b128(rG, lane * 16 + (i & 3) * 1024, G_PAY + pm * 14336 + (i >> 2) * 4096, 16);
      if (spin == 0) {
#pragma unroll
        for (int n = 0; n < 2; ++n) lin_store(acct[0][n], acct[1][n], u3[n], 4 * cb + 2 * wn + n);
      }
      u32x4 al = pv[0];
      t = __builtin_bit_cast(f32x4, pv[0]);
#pragma unroll
      for (int i = 1; i < 14; ++i) { al &= pv[i]; t += __builtin_bit_cast(f32x4, pv[i]); }
      if (__all((al[0] & al[1] & al[2] & al[3] & 1u) != 0)) break;
      asm volatile("" ::: "memory");
    }
    const float m0 = t[0] * (1.0f / FH), m1 = t[2] * (1.0f / FH);
    const float r0 = __builtin_amdgcn_rsqf(fmaxf(t[1] * (1.0f / FH) - m0 * m0, 0.0f) + 1e-5f);
    const float r1 = __builtin_amdgcn_rsqf(fmaxf(t[3] * (1.0f / FH) - m1 * m1, 0.0f) + 1e-5f);
    *(LAS f32x2*)(s_n + 2 * lane) = f32x2{r0, r1};
    *(LAS f32x2*)(s_n + 128 + 2 * lane) = f32x2{-m0 * r0, -m1 * r1};
    __builtin_amdgcn_s_setprio(0);
  }
  }
  WL0();
  BAR();
  STAMP(8);
  {
    f32x4 r2[2], c2[2];
#pragma unroll
    for (int m = 0; m < 2; ++m) {
      r2[m] = *(LAS f32x4*)(s_n + 32 * wm + 8 * fq + 4 * (m ^ wn));
      c2[m] = *(LAS f32x4*)(s_n + 128 + 32 * wm + 8 * fq + 4 * (m ^ wn));
    }
#pragma unroll
    for (int n = 0; n < 2; ++n) store_t(64 + 4 * cb + 2 * wn + (n ^ (wm & 1)), acc1[0][n] * r2[0] + c2[0], acc1[1][n] * r2[1] + c2[1]);
  }
  STAMP(9);
  STAMP_OUT(a.ws, 0, 10);
}

__device__ __forceinline__ void warm_window(const Args& a, int junk, int rb, int w, int wv, int lane) {
  if (w < 8) {
    const unsigned vo = (rb * 128 + wv * 32 + (lane >> 2)) * (S * 2) + (lane & 3) * 128;
#pragma unroll
    for (int m = 0; m < 3; ++m) {
      const rsrc_t r = RSRC(m == 0 ? a.wsr : m == 1 ? a.wsi : a.wsp, S * S * 2);
      touch(r, junk, vo, w * 512);
      touch(r, junk, vo, w * 512 + 16 * (S * 2));
    }
  } else {
    const rsrc_t r = RSRC(a.wout + (size_t)rb * 56 * D, 56 * D * 2);
    touch(r, junk, lane * 128, wv * 16384);
    touch(r, junk, lane * 128, wv * 16384 + 8192);
  }
}

__global__ __launch_bounds__(512, 2) void fused(Args a) {
  extern __shared__ __attribute__((aligned(1024))) char smem[];
  asm volatile("" ::"v"(smem));
  const int bx = blockIdx.x & 7, bi = blockIdx.x >> 3;
  if (bi < CODEWARM && threadIdx.x < 64) {
    unsigned long long pc;
    asm volatile("s_getpc_b64 %0" : "=s"(pc));
    touch(RSRC(pc & ~127ull, CODEWARM * 8192), 1024, threadIdx.x * 128, bi * 8192);
  }
  const int pm = 4 * (bx >> 1) + (bi >> 3), cb = 2 * (bi & 7) + (bx & 1);
  int mat, tm, tn;
  if (bx & 1) { mat = 2; tm = pm; tn = bi & 7; }
  else { mat = bx >> 2; tm = ((bx >> 1) & 1) * 8 + (bi >> 2); tn = bi & 3; }
  const int kx = (tm ^ tn) & K2_KXMASK;
  const u16* Pp = (mat == 0 ? a.wsr : mat == 1 ? a.wsi : a.wsp) + (size_t)tm * 128 * S;
  f32x4 z1v[2][2];
  {
    const int kx1 = ((bi & 7) ^ (2 * (bi >> 3))) & K1_KXMASK;
    if (cb < 14) phase1<true>(a, pm, cb, kx1, Pp, kx, z1v);
    else if (cb == 14) phase1<false>(a, pm, cb, kx1, Pp, kx, z1v);
  }
  int tid = threadIdx.x;
  asm volatile("" : "+v"(tid));
  const int lane = tid & 63;
  const int wid = __builtin_amdgcn_readfirstlane(tid >> 6);
  STAMP_DECL STAMP(0);
  const int tm3 = bx * 4 + (bi >> 3), tn3 = bi & 7;
  const int fr = lane & 15, fq = lane >> 4;
  const unsigned vl = lane * 16;
  const rsrc_t rG = RSRC(a.ws + WS_G, G_END);
  const bool work2 = cb != 15;
  auto z1_finish = [&]() {
    if (cb < 14) {
      const int wm = wid & 3, wn = wid >> 2;
      const rsrc_t rZ = RSRC(a.ws + WS_Z1, FH * S * 2);
      const unsigned vZ = (fr >> 3) * (112 * 256) + (2 * ((fq & 1) ^ (wm & 1)) + (fq >> 1)) * 256 + (fr & 7) * 16;
      const unsigned sZ = ((pm * 8 + 2 * wm) * 112 + 8 * cb + 4 * wn) * 256;
#pragma unroll
      for (int m = 0; m < 2; ++m) {
        unsigned pk[2][2];
#pragma unroll
        for (int n = 0; n < 2; ++n) {
          const f32x2 g0 = gelu2(f32x2{z1v[m][n][0], z1v[m][n][1]}), g1 = gelu2(f32x2{z1v[m][n][2], z1v[m][n][3]});
          pk[n][0] = cvt_pk(g0[0], g0[1]); pk[n][1] = cvt_pk(g1[0], g1[1]);
        }
        auto w0 = __builtin_amdgcn_permlane16_swap(pk[0][0], pk[1][0], false, false);
        auto w1 = __builtin_amdgcn_permlane16_swap(pk[0][1], pk[1][1], false, false);
        st16_sc1(u32x4{w0[0], w1[0], w0[1], w1[1]}, rZ, vZ, sZ + 128 * (m ^ wn));
      }
    }
  };
  if (blockIdx.x == 255) {
    for (int i = 0; i < 36; ++i) st16_sc1(u32x4{0, 0, 0, 0}, rG, vl, (wid * 36 + i) * 1024);
    st16_sc1(u32x4{0, 0, 0, 0}, rG, vl, G_ALL);
  }
  unsigned bv[4];
#pragma unroll
  for (int ni = 0; ni < 4; ++ni) bv[ni] = a.bsp[(8 * tm + 4 * ((wid >> 1) & 1) + ni) * 16 + fr];
  asm volatile("" : "+v"(bv[0]), "+v"(bv[1]), "+v"(bv[2]), "+v"(bv[3]));
  if (work2 || blockIdx.x == 255) {
    WAIT_V(0);
    WL0();
    BAR();
  }
  if (wid >= 4) {
    const int lw = wid - 4;
    const int r8 = lane >> 3, p = lane & 7;
    if (wid == 7) {
      if (!work2) {
        if (blockIdx.x == 255) {
          for (unsigned spin = 0; spin < SPIN; ++spin) {
            const u32x4 rq = ld16_sc1(rG, vl, G_REQ);
            st16_sc1(rq, rG, vl, G_ANS);
            const u32x4 fl = ld16_sc1(rG, vl, G_ALL);
            if (__all((fl[0] & fl[1] & fl[2] & (lane == 63 ? 1u : fl[3])) == 1u)) break;
          }
        } else {
          const unsigned nonce = __builtin_amdgcn_readfirstlane(ld4_sc1(rG, 0, G_ANS + blockIdx.x * 4)) + 1;
          st4_sc1(nonce, rG, 0, G_REQ + blockIdx.x * 4);
          wait_go(rG, nonce, nonce - 1);
        }
      }
      const unsigned box = (fq & 1) * 128 + (cb >> 1) * 16 + fr;
      st4_sc1(1u, rG, fq < 2 ? box * 128 + (pm * 2 + (cb & 1)) * 4 : G_ALL + blockIdx.x * 4, 0);
    }
    const rsrc_t rP = RSRC(Pp, 128 * S * 2);
    const unsigned vo_e = (lw * 32 + r8) * (S * 2) + ((p ^ (r8 >> 1)) << 4);
    const unsigned vo_o = (vo_e ^ 64) + 8 * (S * 2);
    auto stageP = [&](int kt, unsigned poff) {
      const unsigned mb = poff + lw * 4096, so = (kt ^ kx) * 128;
#pragma unroll
      for (int j = 0; j < 4; ++j) dma16(rP, mb, P2_PB + j * 1024, (j & 1) ? vo_o : vo_e, so + (j >> 1) * 16 * (S * 2));
    };
    if (work2) {
      BAR();
      stageP(4, 0); stageP(5, 16384);
    }
    const rsrc_t rWo = RSRC(a.wout + 64 * tn3, (FH * D - 64 * tn3) * 2);
    const unsigned vw_e = (32 * lw + r8) * (D * 2) + ((p ^ (((r8 >> 1) & 1) << 1)) << 4), vw_o = (vw_e ^ 64) + 8 * (D * 2);
    u32x4 wreg[12];
#pragma unroll
    for (int i = 0; i < 12 && !NOPARK; ++i) {
      const int rg = i < 8 ? 2 + (i >> 2) : 6;
      wreg[i] = __builtin_amdgcn_raw_buffer_load_b128(rWo, (i & 1) ? vw_o : vw_e, (rg * 128 + ((i >> 1) & 1) * 16) * (D * 2), 0);
    }
    auto wput = [&](int rg) {
#pragma unroll
      for (int j = 0; j < 4 && !NOPARK; ++j) *(LAS u32x4*)(size_t)(rg * 16384 + lw * 4096 + j * 1024 + vl) = wreg[(rg == 6 ? 8 : (rg - 2) * 4) + j];
    };
    auto wdma = [&](int rg) {
#pragma unroll
      for (int j = 0; j < 4; ++j) dma16(rWo, lw * 4096, rg * 16384 + j * 1024, (j & 1) ? vw_o : vw_e, (rg * 128 + (j >> 1) * 16) * (D * 2));
    };
    if (work2) {
      if (wid == 7) {
        const unsigned box = (mat * 4 + tn) * 16 + tm;
        for (unsigned spin = 0; spin < SPIN; ++spin) {
          const unsigned fl = ld4_sc1(rG, (lane & 31) * 4, G_F1 + box * 128);
          if (__all(fl == 1u || (box >= 112 && box < 128 && (lane & 1)))) break;
        }
      }
      BAR();
      STAMP(1);
      const rsrc_t rQ = RSRC(a.ws + WS_Q + (size_t)(mat * 4 + tn) * 128 * S * 2, 128 * S * 2);
      const unsigned vq = vl + lw * 131072;
      auto stageQ = [&](int kt) {
        const unsigned mb = (kt & 3) * 16384 + lw * 4096, so = (kt ^ kx) * 2048;
#pragma unroll
        for (int j = 0; j < 4; ++j) dma16_sc1(rQ, mb, j * 1024, vq, so + (j >> 1) * 65536 + (j & 1) * 1024);
      };
#pragma unroll
      for (int i = 0; i < 12 && !NOPARK; ++i) OPAQUE(wreg[i]);
      stageQ(0); stageQ(1);
      z1_finish();
      WAIT_V(4); BAR();
      stageQ(2); stageQ(3);
      WAIT_V(8); BAR(); stageQ(4); stageP(6, 2 * 16384);
      WAIT_V(12); BAR(); stageQ(5); stageP(7, 3 * 16384);
      WAIT_V(16); BAR(); stageQ(6); stageP(8, 4 * 16384);
      unsigned poff = 5 * 16384;
      for (int kt = 3; kt < 26; ++kt) {
        WAIT_V(20);
        BAR();
        if (kt == 3 && wid == 7 && cb < 14) st4_sc1(1u, rG, 0, G_FZ + (pm * 8 + (cb >> 1)) * 128 + (cb & 1) * 4);
        stageQ(kt + 4); stageP(kt + 6, poff);
        poff = poff == 5 * 16384 ? 0 : poff + 16384;
      }
      if (mat == 2) {
        const rsrc_t rZ = RSRC(a.ws + WS_Z1 + (size_t)((8 * tm + lw) * 112 + 16 * tn) * 256, 4 * 112 * 256 + 4096);
        auto stageZ = [&](int h) {
#pragma unroll
          for (int j = 0; j < 4; ++j) dma16_sc1(rZ, lw * 4096, P2_Z + h * 16384 + j * 1024, vl, h * (4 * 112 * 256) + j * 1024);
        };
        if (wid == 7) {
          for (unsigned spin = 0; spin < SPIN; ++spin)
            if (__all(ld4_sc1(rG, (lane & 1) * 4, G_FZ + (tm * 8 + tn) * 128) == 1u)) break;
        }
        WAIT_V(20); BAR(); stageQ(30); stageZ(0);
        WAIT_V(20); BAR(); stageQ(31); stageZ(1);
        WAIT_V(20); BAR(); wdma(0); wdma(4);
        WAIT_V(20); BAR(); wdma(1); wdma(5);
        WAIT_V(20); BAR(); wput(2); wput(6);
        WAIT_V(16); BAR(); wput(3);
      } else {
        WAIT_V(20); BAR(); stageQ(30);
        WAIT_V(16); BAR(); stageQ(31);
        WAIT_V(12); BAR(); wdma(0); wdma(4);
        WAIT_V(12); BAR(); wdma(1); wdma(5);
        WAIT_V(16); BAR(); wput(2); wput(6);
        BAR(); wput(3);
      }
    } else {
      STAMP(1);
      wdma(0); wdma(4); wdma(1); wdma(5); wput(2); wput(3); wput(6);
    }
    STAMP(2); STAMP(3);
    WAIT_V(0);
  } else if (work2) {
    const int wp = wid >> 1, wq = wid & 1;
    f32x4 acc[4][4];
#pragma unroll
    for (int ni = 0; ni < 4; ++ni) {
      const float bias = mat == 2 ? __uint_as_float(bv[ni] << 16) : 0.0f;
#pragma unroll
      for (int n = 0; n < 4; ++n) acc[ni][n] = f32x4{bias, bias, bias, bias};
    }
    BAR();
    if (DWARM && cb == 14) warm_window(a, 0, pm, 2, wid, lane);
    const int sw = (fr >> 1) & 7;
    const unsigned ra = P2_PB + (wp * 64 + fr) * 128 + ((fq ^ sw) << 4);
    const unsigned rb = wq * 8192 + vl;
    bf16x8 a0[4], b0[4], a1[4], b1[4];
    unsigned poff = 2 * 16384;
    unsigned pa0 = ra + poff, pa1 = pa0 ^ 64;
#define K2_RD(AF, BF, pa, sl, ks, i) do { \
    if ((i) == 0) AF[0] = ld_frag(pa); \
    else if ((i) <= 4) BF[(i) - 1] = ld_frag(rb + (sl) * 16384 + ((i) - 1) * 2048 + (ks) * 1024); \
    else AF[(i) - 4] = ld_frag(pa + ((i) - 4) * 2048); } while (0)
#define K2_MM(AF, BF, i) acc[(i) >> 2][(i) & 3] = MFMA16(BF[(i) & 3], AF[(i) >> 2], acc[(i) >> 2][(i) & 3])
#define K2_STEP(s, LAST) do { \
    SCHED(); WL0(); SCHED(); \
    _Pragma("unroll") for (int i = 0; i < 16; ++i) { K2_MM(a0, b0, i); SCHED(); if (i < 8) K2_RD(a1, b1, pa1, s, 1, i); \
      if (i == 10) { poff = poff == 5 * 16384 ? 0 : poff + 16384; pa0 = ra + poff; } SCHED(); } \
    WL0(); \
    if ((s) == 3) WAIT_V(0); \
    BAR(); \
    SCHED(); \
    _Pragma("unroll") for (int i = 0; i < 16; ++i) { K2_MM(a1, b1, i); SCHED(); if (!(LAST) && i < 8) K2_RD(a0, b0, pa0, ((s) + 1) & 3, 0, i); \
      if (i == 10) pa1 = pa0 ^ 64; SCHED(); } \
  } while (0)
    if (DWARM && cb == 14) WAIT_V(0);
    BAR();
    z1_finish();
    BAR();
    STAMP(1);
#pragma unroll
    for (int i = 0; i < 8; ++i) K2_RD(a0, b0, pa0, 0, 0, i);
    for (int kt = 0; kt < 28; kt += 4) { K2_STEP(0, 0); K2_STEP(1, 0); K2_STEP(2, 0); K2_STEP(3, 0); }
    K2_STEP(0, 0); K2_STEP(1, 0); K2_STEP(2, 0); K2_STEP(3, 1);
    SCHED();
    STAMP(2);
    if (mat < 2) {
      const rsrc_t rY = RSRC(a.ws + WS_Y + (size_t)mat * S * D * 2, S * D * 2);
      const unsigned so = ((8 * tm + 4 * wp) * 64 + 16 * tn + 8 * wq) * 256;
#pragma unroll
      for (int ni = 0; ni < 4; ++ni)
#pragma unroll
        for (int P = 0; P < 2; ++P) {
          const f32x4 c0 = acc[ni][2 * P], c1 = acc[ni][2 * P + 1];
          const u32x4 v = {cvt_pk(c0[0], c0[1]), cvt_pk(c0[2], c0[3]), cvt_pk(c1[0], c1[1]), cvt_pk(c1[2], c1[3])};
          st16_sc1(v, rY, vl, so + (ni * 64 + 4 * P) * 256);
        }
    } else {
      const rsrc_t rH = RSRC(a.ws + WS_H, S * FH * 2);
      const unsigned so = ((8 * tm + 4 * wp) * 112 + 16 * tn + 8 * wq) * 256;
      const unsigned za = P2_Z + wp * 16384 + wq * 2048 + (fq * 16 + ((fr & 4) << 1) + ((fr & 8) >> 1) + (fr & 3)) * 16;
#pragma unroll
      for (int ni = 0; ni < 4; ++ni) {
#pragma unroll
        for (int P = 0; P < 2; ++P) {
          const f32x4 c0 = acc[ni][2 * P], c1 = acc[ni][2 * P + 1];
          const u32x4 z = *(const LAS u32x4*)(size_t)(za + ni * 4096 + P * 1024);
          const u32x4 v = {cvt_pk(bf_lo(z[0]) * c0[0], bf_hi(z[0]) * c0[1]), cvt_pk(bf_lo(z[1]) * c0[2], bf_hi(z[1]) * c0[3]),
                           cvt_pk(bf_lo(z[2]) * c1[0], bf_hi(z[2]) * c1[1]), cvt_pk(bf_lo(z[3]) * c1[2], bf_hi(z[3]) * c1[3])};
          st16_sc1(v, rH, vl, so + (ni * 112 + 4 * P) * 256);
        }
      }
    }
    STAMP(3);
    WAIT_V(0);
  } else {
    STAMP(1);
    for (int i = 0; i < PF_N && blockIdx.x != 255; ++i) {
      warm_window(a, P2_Z, pm, (PF_LIST >> (4 * i)) & 15, wid, lane);
      WAIT_V(6);
    }
    STAMP(2);
    WAIT_V(0);
    STAMP(3);
  }
  WL0();
  BAR();
  STAMP(4);
  {
    const int wr = (wid + tn3) & 7, tp = wr >> 2, kq = wr & 3;
    const int tt3 = 4 * tm3 + 2 * tp + (kq & 1);
    const int cq = 2 * (fq & 1) + (fq >> 1);
    const int c3 = 8 * tn3 + 2 * (kq & 2);
    if (wid == 4 && work2) {
      const unsigned box = mat == 2 ? fr : (fr & 8) + 2 * tn + (fr & 1);
      st4_sc1(1u, rG, box * 128, G_FP + tm * 2048 + (mat == 2 ? tn : 8 + mat) * 4);
    }
    const int q4 = fr >> 2, pp = lane & 3;
    const int sp = ((q4 >> 1) & 1) | ((fq & 1) << 1);
    const unsigned wa = kq * 4096 + 128 * (8 * fq + q4) + 16 * ((2 * (kq & 2) + (pp >> 1)) ^ (sp << 1)) + 8 * (pp & 1);
    bf16x8 wf[7][4];
    auto ld_w = [&]() {
#pragma unroll
      for (int mi = 0; mi < 4; ++mi) {
        unsigned lo = wa ^ (mi * 32), hi = lo + 65536;
        OPAQUE(hi);
#pragma unroll
        for (int g = 0; g < 7; ++g) wf[g][mi] = ld_tr((g < 4 ? lo : hi) + (g & 3) * 16384);
      }
    };
    if (wid == 4) {
      for (unsigned spin = 0; spin < SPIN; ++spin) {
        const unsigned fl = ld4_sc1(rG, fr * 4, G_FP + (tm3 * 8 + tn3) * 128);
        if (__all(fl == 1u || fr == 7 || fr > 9)) break;
      }
    } else {
      ld_w();
    }
    BAR();
    STAMP(5);
    const rsrc_t rH = RSRC(a.ws + WS_H + (size_t)((4 * tm3 + 2 * tp) * 112 + 4 * kq) * 256, 2 * 112 * 256);
    const rsrc_t rY = RSRC(a.ws + WS_Y + (size_t)(tt3 * 64 + c3) * 256, S * D * 2 + 1024);
    const unsigned vh0 = vl + (kq & 1) * (112 * 256), vh1 = vl + ((kq & 1) ^ 1) * (112 * 256);
    u32x4 hf[7][2];
#pragma unroll
    for (int g = 0; g < 7; ++g) {
      hf[g][0] = __builtin_amdgcn_raw_buffer_load_b128(rH, vh0, g * 4096, 16);
      hf[g][1] = __builtin_amdgcn_raw_buffer_load_b128(rH, vh1, g * 4096, 16);
    }
    const unsigned vy = cq * 256 + fr * 16;
    const u32x4 e1 = __builtin_amdgcn_raw_buffer_load_b128(rY, vy, 0, 16), e2 = __builtin_amdgcn_raw_buffer_load_b128(rY, vy, S * D * 2, 16);
    const u32x4 ex = *(const u32x4*)(a.x + (size_t)(tt3 * 16 + fr) * D + (c3 + cq) * 8);
    SCHED();
    if (wid == 4) ld_w();
    f32x4 acc[2][4];
#pragma unroll
    for (int g = 0; g < 7; ++g)
#pragma unroll
      for (int tl = 0; tl < 2; ++tl)
#pragma unroll
        for (int mi = 0; mi < 4; ++mi) acc[tl][mi] = MFMA16(wf[g][mi], __builtin_bit_cast(bf16x8, hf[g][tl]), (g ? acc[tl][mi] : f32x4{0, 0, 0, 0}));
    STAMP(6);
    LAS f32x4* xb = (LAS f32x4*)(size_t)(P3_X) + lane;
#pragma unroll
    for (int d = 1; d < 4; ++d) {
      xb[((wr * 3 + d - 1) * 2) * 64] = acc[d & 1][d & 2];
      xb[((wr * 3 + d - 1) * 2 + 1) * 64] = acc[d & 1][(d & 2) + 1];
    }
    WL0();
    BAR();
    f32x4 s0 = acc[0][0], s1 = acc[0][1];
#pragma unroll
    for (int d = 1; d < 4; ++d) {
      s0 += xb[(((wr ^ d) * 3 + d - 1) * 2) * 64];
      s1 += xb[(((wr ^ d) * 3 + d - 1) * 2 + 1) * 64];
    }
    f32x4 c0, c1;
#pragma unroll
    for (int j = 0; j < 4; ++j) {
      auto w = __builtin_amdgcn_permlane16_swap(__float_as_uint(s0[j]), __float_as_uint(s1[j]), false, false);
      c0[j] = __uint_as_float(w[0]); c1[j] = __uint_as_float(w[1]);
    }
    u32x4 o;
#pragma unroll
    for (int k = 0; k < 4; ++k) {
      const f32x4 c = (k >> 1) ? c1 : c0;
      const float lo = bf_lo(ex[k]) + bf_lo(e1[k]) - bf_lo(e2[k]) + c[(k & 1) * 2];
      const float hi = bf_hi(ex[k]) + bf_hi(e1[k]) - bf_hi(e2[k]) + c[(k & 1) * 2 + 1];
      o[k] = cvt_pk(lo, hi);
    }
    __builtin_nontemporal_store(o, (u32x4*)(a.out + (size_t)(tt3 * 16 + fr) * D + (c3 + cq) * 8));
  }
  STAMP(7);
  STAMP_OUT(a.ws, 1, 8);
}

extern "C" void kernel_launch(void* const* d_in, const int* in_sizes, int n_in, void* d_out, int out_size, void* d_ws, size_t ws_size,
                              hipStream_t stream) {
  (void)in_sizes; (void)n_in; (void)out_size; (void)ws_size;
  (void)hipFuncSetAttribute((const void*)fused, hipFuncAttributeMaxDynamicSharedMemorySize, LDS_BYTES);
  Args a{(const u16*)d_in[0], (const u16*)d_in[3], (const u16*)d_in[4], (const u16*)d_in[6], (const u16*)d_in[9],
         (const u16*)d_in[1], (const u16*)d_in[2], (const u16*)d_in[7], (const u16*)d_in[8], (char*)d_ws, (u16*)d_out};
  fused<<<256, 512, LDS_BYTES, stream>>>(a);
}
```

```cpp
#include <hip/hip_runtime.h>
#include <hip/hip_bf16.h>
#include <stdint.h>

typedef unsigned short u16;
typedef short bf16x8 __attribute__((ext_vector_type(8)));
typedef short s16x4 __attribute__((ext_vector_type(4)));
typedef float f32x4 __attribute__((ext_vector_type(4)));
typedef float f32x2 __attribute__((ext_vector_type(2)));
typedef unsigned u32x4 __attribute__((ext_vector_type(4)));
typedef __bf16 bf16x2v __attribute__((ext_vector_type(2)));
typedef __amdgpu_buffer_rsrc_t rsrc_t;
#define LAS __attribute__((address_space(3)))
#define WAIT_V(n) asm volatile("s_waitcnt vmcnt(%0)" ::"n"(n) : "memory")
#define WL0() __builtin_amdgcn_s_waitcnt(0xC07F)
#define BAR() do { asm volatile("" ::: "memory"); __builtin_amdgcn_s_barrier(); asm volatile("" ::: "memory"); } while (0)
#define SCHED() __builtin_amdgcn_sched_barrier(0)
#define MFMA16(a, b, c) __builtin_amdgcn_mfma_f32_16x16x32_bf16(a, b, c, 0, 0, 0)
#define RSRC(p, n) __builtin_amdgcn_make_buffer_rsrc((void*)(p), 0, (unsigned)(n), 0x00020000)
#define OPAQUE(x) asm volatile("" : "+v"(x))

#define STAMP_DECL
#define STAMP(i)
#define STAMP_OUT(ws, kid, n)
#define K2_KXMASK 7
#define K1_KXMASK 3
#define DWARM 0
#define CODEWARM 5
#define NOPARK 0
#define Z1EARLY 0
#define PF_LIST 0x753810u
#define PF_N 6

constexpr int S = 2048, D = 512, F = 1792, FH = 896;
constexpr size_t WS_Q = 0;
constexpr size_t WS_Z1 = WS_Q + (size_t)(2 * D + FH) * S * 2;
constexpr size_t WS_H = WS_Z1 + (size_t)FH * S * 2;
constexpr size_t WS_Y = WS_H + (size_t)FH * S * 2;
constexpr size_t WS_G = WS_Y + (size_t)2 * D * S * 2;
constexpr unsigned G_F1 = 0;
constexpr unsigned G_FP = 32768;
constexpr unsigned G_FZ = 32768 + 64;
constexpr unsigned G_PAY = 65536;
constexpr unsigned G_ALL = G_PAY + 16 * 14336;
constexpr unsigned G_REQ = G_ALL + 1024;
constexpr unsigned G_ANS = G_REQ + 1024;
constexpr unsigned G_END = G_ANS + 1024;

constexpr int LDS_BYTES = 163840;
constexpr int P1_RING = 32768, P1_SCR = 81920;
constexpr int P2_PB = 65536, P2_Z = 131072, P3_X = 114688;
constexpr unsigned SPIN = 1u << 20;

struct Args {
  const u16* x; const u16* wdr; const u16* wdi; const u16* win; const u16* wout;
  const u16* wsr; const u16* wsi; const u16* wsp; const u16* bsp;
  char* ws; u16* out;
};

__device__ __forceinline__ unsigned cvt_pk(float lo, float hi) {
  f32x2 v = {lo, hi};
  return __builtin_bit_cast(unsigned, __builtin_convertvector(v, bf16x2v));
}
__device__ __forceinline__ float bf_lo(unsigned u) { return __uint_as_float(u << 16); }
__device__ __forceinline__ float bf_hi(unsigned u) { return __uint_as_float(u & 0xffff0000u); }
constexpr float GC1 = -2.0f * 0.7978845608028654f * 1.4426950408889634f, GC2 = GC1 * 0.044715f;
__device__ __forceinline__ f32x2 gelu2(f32x2 v) {
  const f32x2 t = v * ((v * v) * GC2 + GC1);
  const f32x2 e = f32x2{__builtin_amdgcn_exp2f(t[0]), __builtin_amdgcn_exp2f(t[1])} + 1.0f;
  return v * f32x2{__builtin_amdgcn_rcpf(e[0]), __builtin_amdgcn_rcpf(e[1])};
}
__device__ __forceinline__ void dot_sq(float& c, unsigned a) { asm volatile("v_dot2c_f32_bf16 %0, %1, %1" : "+v"(c) : "v"(a)); }
__device__ __forceinline__ void dot_sum(float& c, unsigned a) { asm volatile("v_dot2c_f32_bf16 %0, 0x3f803f80, %1" : "+v"(c) : "v"(a)); }
__device__ __forceinline__ float sum_rows(float v) {
  auto a = __builtin_amdgcn_permlane32_swap(__float_as_uint(v), __float_as_uint(v), false, false);
  v = __uint_as_float(a[0]) + __uint_as_float(a[1]);
  auto b = __builtin_amdgcn_permlane16_swap(__float_as_uint(v), __float_as_uint(v), false, false);
  return __uint_as_float(b[0]) + __uint_as_float(b[1]);
}
__device__ __forceinline__ float sel4(f32x4 v, int i) { const float a = (i & 1) ? v[1] : v[0], b = (i & 1) ? v[3] : v[2]; return (i & 2) ? b : a; }

__device__ __forceinline__ void dma16(rsrc_t r, unsigned mbase, int mimm, unsigned voff, unsigned soff) {
  asm volatile("s_add_i32 m0, %0, %1\n\ts_nop 0\n\tbuffer_load_dwordx4 %2, %3, %4 offen lds" ::"s"(mbase), "n"(mimm), "v"(voff), "s"(r), "s"(soff) : "scc");
}
__device__ __forceinline__ void dma16_sc1(rsrc_t r, unsigned mbase, int mimm, unsigned voff, unsigned soff) {
  asm volatile("s_add_i32 m0, %0, %1\n\ts_nop 0\n\tbuffer_load_dwordx4 %2, %3, %4 offen sc1 lds" ::"s"(mbase), "n"(mimm), "v"(voff), "s"(r), "s"(soff) : "scc");
}
__device__ __forceinline__ void dma4_sc1(rsrc_t r, int ldsaddr, unsigned voff, unsigned soff) {
  asm volatile("s_mov_b32 m0, %0\n\ts_nop 0\n\tbuffer_load_dword %1, %2, %3 offen sc1 lds" ::"n"(ldsaddr), "v"(voff), "s"(r), "s"(soff) : "memory");
}
__device__ __forceinline__ void touch(rsrc_t r, int junk, unsigned voff, unsigned soff) {
  asm volatile("s_mov_b32 m0, %0\n\ts_nop 0\n\tbuffer_load_dword %1, %2, %3 offen lds" ::"n"(junk), "v"(voff), "s"(r), "s"(soff) : "memory");
}
__device__ __forceinline__ void st16_sc1(u32x4 v, rsrc_t r, unsigned voff, unsigned soff) {
  asm volatile("buffer_store_dwordx4 %0, %1, %2, %3 offen sc1\n\ts_nop 1" ::"v"(v), "v"(voff), "s"(r), "s"(soff) : "memory");
}
__device__ __forceinline__ void st4_sc1(unsigned v, rsrc_t r, unsigned voff, unsigned soff) {
  asm volatile("buffer_store_dword %0, %1, %2, %3 offen sc1" ::"v"(v), "v"(voff), "s"(r), "s"(soff) : "memory");
}
__device__ __forceinline__ unsigned ld4_sc1(rsrc_t r, unsigned voff, unsigned soff) {
  unsigned v;
  asm volatile("buffer_load_dword %0, %1, %2, %3 offen sc1\n\ts_waitcnt vmcnt(0)" : "=&v"(v) : "v"(voff), "s"(r), "s"(soff) : "memory");
  return v;
}
__device__ __forceinline__ u32x4 ld16_sc1(rsrc_t r, unsigned voff, unsigned soff) {
  u32x4 v;
  asm volatile("buffer_load_dwordx4 %0, %1, %2, %3 offen sc1\n\ts_waitcnt vmcnt(0)" : "=&v"(v) : "v"(voff), "s"(r), "s"(soff) : "memory");
  return v;
}
__device__ __forceinline__ void ld16_async(u32x4& v, rsrc_t r, unsigned voff, unsigned soff) {
  asm volatile("buffer_load_dwordx4 %0, %1, %2, %3 offen" : "=v"(v) : "v"(voff), "s"(r), "s"(soff) : "memory");
}
__device__ __forceinline__ void wait_vn(int n) { WAIT_V(n); }
__device__ __forceinline__ bf16x8 ld_tr(unsigned addr) {
  s16x4 lo = __builtin_amdgcn_ds_read_tr16_b64_v4i16((LAS s16x4*)(size_t)(addr));
  s16x4 hi = __builtin_amdgcn_ds_read_tr16_b64_v4i16((LAS s16x4*)(size_t)(addr + 512));
  return __builtin_shufflevector(lo, hi, 0, 1, 2, 3, 4, 5, 6, 7);
}
__device__ __forceinline__ bf16x8 ld_frag(unsigned addr) { return *(const LAS bf16x8*)(size_t)(addr); }
__device__ __forceinline__ void wait_go(rsrc_t rG, unsigned nonce, unsigned seen) {
  for (unsigned spin = 0; seen != nonce && spin < SPIN; ++spin) {
    seen = __builtin_amdgcn_readfirstlane(ld4_sc1(rG, 0, G_ANS + blockIdx.x * 4));
    __builtin_amdgcn_s_sleep(1);
  }
}

template <bool ZB>
__device__ __forceinline__ void phase1(const Args& a, int pm, int cb, int kx, const u16* Pp, int kx2, f32x4 (&z1v)[2][2]) {
  const int tid = threadIdx.x, lane = tid & 63;
  const int wid = __builtin_amdgcn_readfirstlane(tid >> 6);
  const int wm = wid & 3, wn = wid >> 2;
  STAMP_DECL STAMP(0);
  const rsrc_t rX = RSRC(a.x + (size_t)pm * 128 * D, 128 * D * 2);
  const rsrc_t rW = ZB ? RSRC(a.win + 64 * cb, (D * F - 64 * cb) * 2) : RSRC(a.wdi + (size_t)384 * D, 128 * D * 2);
  const int r8 = lane >> 3, p8 = lane & 7;
  const unsigned sw_e = (p8 ^ (r8 >> 1)) << 4;
  const unsigned vx_e = (32 * (wid >> 1) + 4 * (wid & 1) + 8 * (r8 >> 2) + (r8 & 3)) * (D * 2) + sw_e;
  const unsigned vx_o = (vx_e ^ 64) + 16 * (D * 2);
  const unsigned vw2 = ZB ? (8 * wid + r8) * (F * 2) + ((p8 ^ ((((r8 >> 1) & 1) | ((wid & 1) << 1)) << 1)) << 4)
                          : (64 + 8 * wid + r8) * (D * 2) + (sw_e ^ ((wid & 1) << 6));
  const unsigned vw1 = ZB ? vw2 + FH * 2 : vw2 - 64 * (D * 2);
  constexpr unsigned WKB = ZB ? 64 * F * 2 : 128;
  const unsigned mb1 = wid * 1024, mb2 = wid * 2048;
  auto piece1 = [&](int st, int p) {
    const unsigned kb = st ^ kx;
    const int base = P1_RING + (st & 3) * 32768;
    if (p == 0) dma16(rX, mb2, base, vx_e, kb * 128);
    else if (p == 1) dma16(rW, mb1, base + 16384, vw1, kb * WKB);
    else if (p == 2) dma16(rX, mb2, base + 1024, vx_o, kb * 128);
    else dma16(rW, mb1, base + 24576, vw2, kb * WKB);
  };
  piece1(0, 0); piece1(0, 1); piece1(0, 2); piece1(0, 3);
  SCHED();

  const int fr = lane & 15, fq = lane >> 4;
  const int kh = wm >> 1;
  const unsigned fsw = ((fq ^ ((fr >> 1) & 7)) << 4) ^ (kh << 6);
  unsigned xa[2][2][2], wb[2][2][2];
#pragma unroll
  for (int m = 0; m < 2; ++m) { xa[0][m][0] = P1_RING + (32 * wm + 16 * (m ^ wn) + fr) * 128 + fsw; xa[0][m][1] = xa[0][m][0] ^ 64; }
#pragma unroll
  for (int n = 0; n < 2; ++n) {
    const int tile = 2 * wn + (n ^ (wm & 1));
    if (ZB) {
      const int q4 = fr >> 2, pp = lane & 3;
      const int sp = ((q4 >> 1) & 1) | ((fq & 1) << 1);
      const int ch = (2 * tile + (pp >> 1)) ^ (sp << 1);
      wb[0][n][0] = P1_RING + 16384 + 128 * (8 * fq + q4) + 16 * ch + 8 * (pp & 1) + kh * 4096;
      wb[0][n][1] = wb[0][n][0] ^ 4096;
    } else {
      wb[0][n][0] = P1_RING + 16384 + (16 * tile + fr) * 128 + fsw;
      wb[0][n][1] = wb[0][n][0] ^ 64;
    }
  }
#pragma unroll
  for (int i = 0; i < 2; ++i)
#pragma unroll
    for (int ks = 0; ks < 2; ++ks) {
      xa[1][i][ks] = xa[0][i][ks] + 65536; wb[1][i][ks] = wb[0][i][ks] + 65536;
      OPAQUE(xa[0][i][ks]); OPAQUE(wb[0][i][ks]); OPAQUE(xa[1][i][ks]); OPAQUE(wb[1][i][ks]);
    }
  auto ldw = [&](unsigned addr) -> bf16x8 { return ZB ? ld_tr(addr) : ld_frag(addr); };
  SCHED();
  piece1(1, 0); piece1(1, 1); piece1(1, 2); piece1(1, 3);
  const rsrc_t rG = RSRC(a.ws + WS_G, G_END);
  if (wid == 7) dma4_sc1(rG, 0, 0, G_ANS + blockIdx.x * 4);
  unsigned nonce = 0;
  STAMP(1);

  bf16x8 xf[16][2], wf[2][4];
  const f32x4 Z4 = {0, 0, 0, 0};
  f32x4 acc1[2][2] = {{Z4, Z4}, {Z4, Z4}}, acc2[2][2] = {{Z4, Z4}, {Z4, Z4}};
  f32x4 gram = Z4, rsum = Z4, cs1 = Z4, cs2 = Z4;
  const bf16x8 ONES = {0x3f80, 0x3f80, 0x3f80, 0x3f80, 0x3f80, 0x3f80, 0x3f80, 0x3f80};

  auto mm = [&](int kk, int i) {
    const int ks = kk & 1;
    if (i == 0) gram = MFMA16(xf[kk][0], xf[kk][0], gram);
    else if (i == 1) rsum = MFMA16(xf[kk][0], ONES, rsum);
    else if (i < 6) { const int m = (i - 2) >> 1, n = i & 1; acc1[m][n] = MFMA16(xf[kk][m], wf[ks][n], acc1[m][n]); }
    else if (i < 10) { const int m = (i - 6) >> 1, n = i & 1; acc2[m][n] = ZB ? MFMA16(wf[ks][2 + n], xf[kk][m], acc2[m][n]) : MFMA16(xf[kk][m], wf[ks][2 + n], acc2[m][n]); }
    else if (i == 10) cs1 = MFMA16(ONES, wf[0][0], cs1);
    else cs2 = MFMA16(ONES, wf[0][2], cs2);
  };
  auto rd = [&](int kk, int i) {
    const int st = kk >> 1, ks = kk & 1, wi = (st >> 1) & 1, so = (st & 1) * 32768;
    if (i == 0 || i == 3) xf[kk][i & 1] = ld_frag(xa[wi][i & 1][ks] + so);
    else if (i < 3) wf[ks][i - 1] = ldw(wb[wi][i - 1][ks] + so);
    else wf[ks][i - 2] = ldw(wb[wi][i - 4][ks] + so + 8192);
  };
  auto piece3 = [&](int p) {
    const rsrc_t rD = RSRC((cb < 8 ? a.wdr : a.wdi) + (size_t)(cb & 7) * 64 * D, 64 * D * 2);
    const unsigned ve = r8 * (D * 2) + sw_e, vo = (ve ^ 64) + 8 * (D * 2);
    dma16(rD, wid * 8192, p * 1024, (p & 1) ? vo : ve, (wid ^ kx) * 128 + (p >> 1) * 16 * (D * 2));
  };
  auto earlyP = [&](int k) {
    const rsrc_t rP = RSRC(Pp, 128 * S * 2);
    const unsigned ve = (16 * wid + r8) * (S * 2) + sw_e, vo = (ve ^ 64) + 8 * (S * 2);
    dma16(rP, mb2, P2_PB + (k + 2) * 16384, ve, (k ^ kx2) * 128);
    dma16(rP, mb2, P2_PB + (k + 2) * 16384 + 1024, vo, (k ^ kx2) * 128);
  };
#pragma unroll
  for (int st = 0; st < 8; ++st) {
    wait_vn(st <= 6 ? 4 : ZB ? 8 : 0);
    WL0();
    BAR();
    if (st == 0) STAMP(2);
    SCHED();
#pragma unroll
    for (int i = 0; i < 10; ++i) {
      if (st > 0) mm(2 * st - 1, i);
      SCHED();
      if (i < 6) rd(2 * st, i);
      if (st + 2 < 8) { if (i == 3) piece1(st + 2, 0); if (i == 8) piece1(st + 2, 1); }
      if (ZB && st == 6 && (i % 3) == 0) piece3(i / 3);
      if (st == 7 && i == 7) earlyP(0);
      SCHED();
    }
    WL0();
    SCHED();
#pragma unroll
    for (int i = 0; i < 12; ++i) {
      mm(2 * st, i);
      SCHED();
      if (i < 6) rd(2 * st + 1, i);
      if (st + 2 < 8) { if (i == 3) piece1(st + 2, 2); if (i == 9) piece1(st + 2, 3); }
      if (ZB && st == 6 && (i % 3) == 2) piece3(4 + i / 3);
      if (st == 7 && i == 8) earlyP(1);
      SCHED();
    }
    if (st == 2 && wid == 7) {
      nonce = __builtin_amdgcn_readfirstlane(*(LAS unsigned*)(size_t)(lane * 4)) + 1;
      st4_sc1(nonce, rG, 0, G_REQ + blockIdx.x * 4);
    }
    SCHED();
  }
  LAS float* const s_r = (LAS float*)(size_t)(P1_SCR);
  LAS float* const s_c = (LAS float*)(size_t)(P1_SCR + 512);
  LAS float* const s_u1 = (LAS float*)(size_t)(P1_SCR + 1024);
  LAS float* const s_u2 = (LAS float*)(size_t)(P1_SCR + 1536);
  LAS float* const s_z = (LAS float*)(size_t)(P1_SCR + 2048);
  LAS float* const s_n = (LAS float*)(size_t)(P1_SCR + 4096);
  LAS float* const s_u3 = (LAS float*)(size_t)(P1_SCR + 5120);
  const unsigned tb0 = fr * 128 + fsw, tb1 = tb0 ^ 64;
  WAIT_V(4);
  {
    const unsigned cb0 = tb0 + wid * 8192, cb1 = cb0 ^ 64;
    bf16x8 c3[8];
    f32x4 cs3[4];
    if (ZB) {
#pragma unroll
      for (int t = 0; t < 4; ++t) { c3[2 * t] = ld_frag(cb0 + t * 2048); c3[2 * t + 1] = ld_frag(cb1 + t * 2048); }
    }
    mm(15, 0); mm(15, 1);
    SCHED();
    if (ZB) {
#pragma unroll
      for (int t = 0; t < 4; ++t) cs3[t] = MFMA16(ONES, c3[2 * t], Z4);
#pragma unroll
      for (int t = 0; t < 4; ++t) cs3[t] = MFMA16(ONES, c3[2 * t + 1], cs3[t]);
    }
    SCHED();
#pragma unroll
    for (int i = 2; i < 10; ++i) mm(15, i);
    STAMP(3);
    unsigned i01 = 32 * wm + 16 * wn, i23 = 64 * kh + 16 * (2 * wn + (wm & 1));
    asm volatile("" : "+s"(i01), "+s"(i23));
    const float s1 = sel4(rsum, fr & 3), s2 = sel4(gram, fr & 3);
    const float mean = s1 * (1.0f / D);
    const float rstd = __builtin_amdgcn_rsqf(fmaxf(s2 * (1.0f / D) - mean * mean, 0.0f) + 1e-5f);
    if ((fr >> 2) == fq) { s_r[i01 + fr] = rstd; s_c[i01 + fr] = -mean * rstd; }
    if (fq < 2) s_u1[128 * fq + i23 + fr] = fq ? cs2[0] : cs1[0];
    if (ZB) s_u3[lane * 8 + wid] = sel4(f32x4{cs3[0][0], cs3[1][0], cs3[2][0], cs3[3][0]}, fq);
  }
  const int s0 = pm * 128 + 32 * wm;
  const rsrc_t rT = RSRC(a.ws + WS_Q, (2 * D + FH) * S * 2);
  const unsigned vT = ((fr >> 2) & 1) * 65536 + fq * 256 + (4 * (fr >> 3) + (fr & 3)) * 16;
  WL0();
  BAR();
  STAMP(4);
  if (wid == 7) dma4_sc1(rG, P1_SCR + 7168, 0, G_ANS + blockIdx.x * 4);
  earlyP(2); earlyP(3);
  f32x4 rq[2], cq[2];
  auto ld_rc = [&]() {
#pragma unroll
    for (int m = 0; m < 2; ++m) {
      rq[m] = *(LAS f32x4*)(s_r + 32 * wm + 16 * (m ^ wn) + 4 * fq);
      cq[m] = *(LAS f32x4*)(s_c + 32 * wm + 16 * (m ^ wn) + 4 * fq);
    }
  };
  ld_rc();
  auto store_t = [&](int T, f32x4 v0, f32x4 v1) {
    const unsigned a0 = cvt_pk(v0[0], v0[1]), a1 = cvt_pk(v0[2], v0[3]), b0 = cvt_pk(v1[0], v1[1]), b1 = cvt_pk(v1[2], v1[3]);
    const u32x4 o = wn ? u32x4{b0, b1, a0, a1} : u32x4{a0, a1, b0, b1};
    st16_sc1(o, rT, vT, (T >> 1) * 131072 + (T & 1) * 128 + s0 * 32);
  };
  auto lin_store = [&](f32x4 c0, f32x4 c1, float u, int T) { store_t(T, rq[0] * c0 + cq[0] * u, rq[1] * c1 + cq[1] * u); };
  float u1[2];
#pragma unroll
  for (int n = 0; n < 2; ++n) { const int ui = 16 * (2 * wn + (n ^ (wm & 1))) + fr; u1[n] = s_u1[ui] + s_u1[64 + ui]; }
  if (!ZB) {
#pragma unroll
    for (int n = 0; n < 2; ++n) {
      const int tile = 2 * wn + (n ^ (wm & 1)), ui = 16 * tile + fr;
      lin_store(acc1[0][n], acc1[1][n], u1[n], 56 + tile);
      lin_store(acc2[0][n], acc2[1][n], s_u2[ui] + s_u2[64 + ui], 60 + tile);
    }
    if (wid == 7) { WAIT_V(0); wait_go(rG, nonce, __builtin_amdgcn_readfirstlane(*(LAS unsigned*)(size_t)(P1_SCR + 7168 + lane * 4))); }
    STAMP(5); STAMP(6); STAMP(7); STAMP(8); STAMP(9);
    STAMP_OUT(a.ws, 0, 10);
    return;
  }
  {
    f32x4 ps[2], pq[2];
#pragma unroll
    for (int n = 0; n < 2; ++n)
#pragma unroll
      for (int m = 0; m < 2; ++m) {
        f32x4 v = rq[m] * acc1[m][n] + cq[m] * u1[n];
        const f32x2 g0 = gelu2(f32x2{v[0], v[1]}), g1 = gelu2(f32x2{v[2], v[3]});
        v = f32x4{g0[0], g0[1], g1[0], g1[1]};
        acc1[m][n] = v;
        if (n == 0) { ps[m] = v; pq[m] = v * v; } else { ps[m] += v; pq[m] += v * v; }
      }
    float t[8], h[4];
#define DPP2(d, x, y, ctl, m0, m1) "v_add_f32_dpp " d ", " x ", " x " " ctl " row_mask:0xf bank_mask:" m0 "\n\tv_add_f32_dpp " d ", " y ", " y " " ctl " row_mask:0xf bank_mask:" m1 "\n\t"
#define DPPQ(d, ctl) "v_add_f32_dpp " d ", " d ", " d " " ctl " row_mask:0xf bank_mask:0xf\n\t"
    asm volatile("s_nop 1\n\t" DPP2("%0", "%8", "%16", "row_ror:8", "0x3", "0xc") DPP2("%1", "%9", "%17", "row_ror:8", "0x3", "0xc")
                 DPP2("%2", "%10", "%18", "row_ror:8", "0x3", "0xc") DPP2("%3", "%11", "%19", "row_ror:8", "0x3", "0xc")
                 DPP2("%4", "%12", "%20", "row_ror:8", "0x3", "0xc") DPP2("%5", "%13", "%21", "row_ror:8", "0x3", "0xc")
                 DPP2("%6", "%14", "%22", "row_ror:8", "0x3", "0xc") DPP2("%7", "%15", "%23", "row_ror:8", "0x3", "0xc") "s_nop 1"
                 : "=&v"(t[0]), "=&v"(t[1]), "=&v"(t[2]), "=&v"(t[3]), "=&v"(t[4]), "=&v"(t[5]), "=&v"(t[6]), "=&v"(t[7])
                 : "v"(ps[0][0]), "v"(pq[0][0]), "v"(ps[0][1]), "v"(pq[0][1]), "v"(ps[0][2]), "v"(pq[0][2]), "v"(ps[0][3]), "v"(pq[0][3]),
                   "v"(ps[1][0]), "v"(pq[1][0]), "v"(ps[1][1]), "v"(pq[1][1]), "v"(ps[1][2]), "v"(pq[1][2]), "v"(ps[1][3]), "v"(pq[1][3]));
    asm volatile("s_nop 1\n\t" DPP2("%0", "%4", "%8", "row_half_mirror", "0x5", "0xa") DPP2("%1", "%5", "%9", "row_half_mirror", "0x5", "0xa")
                 DPP2("%2", "%6", "%10", "row_half_mirror", "0x5", "0xa") DPP2("%3", "%7", "%11", "row_half_mirror", "0x5", "0xa") "s_nop 1\n\t"
                 DPPQ("%0", "quad_perm:[1,0,3,2]") DPPQ("%1", "quad_perm:[1,0,3,2]") DPPQ("%2", "quad_perm:[1,0,3,2]") DPPQ("%3", "quad_perm:[1,0,3,2]")
                 DPPQ("%0", "quad_perm:[2,3,0,1]") DPPQ("%1", "quad_perm:[2,3,0,1]") DPPQ("%2", "quad_perm:[2,3,0,1]") DPPQ("%3", "quad_perm:[2,3,0,1]") "s_nop 0"
                 : "=&v"(h[0]), "=&v"(h[1]), "=&v"(h[2]), "=&v"(h[3])
                 : "v"(t[0]), "v"(t[1]), "v"(t[2]), "v"(t[3]), "v"(t[4]), "v"(t[5]), "v"(t[6]), "v"(t[7]));
    if ((fr & 3) == 0) *(LAS f32x4*)(s_z + (wn * 128 + 32 * wm + 8 * fq + 4 * ((fr >> 3) ^ wn) + 2 * ((fr >> 2) & 1)) * 2) = f32x4{h[0], h[1], h[2], h[3]};
  }
  WL0();
  BAR();
  STAMP(5);
  if (wid == 7) {
    __builtin_amdgcn_s_setprio(2);
    const f32x4 p = *(LAS f32x4*)(s_z + lane * 4) + *(LAS f32x4*)(s_z + 256 + lane * 4);
    WAIT_V(4);
    wait_go(rG, nonce, __builtin_amdgcn_readfirstlane(*(LAS unsigned*)(size_t)(P1_SCR + 7168 + lane * 4)));
    st16_sc1(__builtin_bit_cast(u32x4, p) | 1u, rG, lane * 16, G_PAY + (pm * 14 + cb) * 1024);
  }
  STAMP(6);
  {
    float u3[2];
    f32x4 acct[2][2] = {{Z4, Z4}, {Z4, Z4}};
    const unsigned t30 = tb0 + wn * 4096, t31 = tb1 + wn * 4096;
    bf16x8 w[8];
    auto rd3 = [&](int f) { const int kk = f >> 1; w[f & 7] = ld_frag(((kk & 1) ? t31 : t30) + (kk >> 1) * 8192 + (f & 1) * 2048); };
#pragma unroll
    for (int f = 0; f < 4; ++f) rd3(f);
    SCHED();
#pragma unroll
    for (int f = 0; f < 32; ++f) {
      const int kk = f >> 1, n = f & 1;
      acct[0][n] = MFMA16(xf[kk][0], w[f & 7], acct[0][n]);
      SCHED();
      if (f + 4 < 32) rd3(f + 4);
      if (f == 8 || f == 12) {
        const int m = (f - 8) >> 2;
        const float r = s_r[32 * wm + 16 * (m ^ wn) + fr], c = s_c[32 * wm + 16 * (m ^ wn) + fr];
#pragma unroll
        for (int q = 0; q < 2; ++q) {
          const int ui = 16 * (2 * wn + (q ^ (wm & 1))) + 4 * fq;
          z1v[m][q] = acc2[m][q] * r + (*(LAS f32x4*)(s_u2 + ui) + *(LAS f32x4*)(s_u2 + 64 + ui)) * c;
        }
      }
      if (f == 20 || f == 24) {
        const int q = (f - 20) >> 2;
        const f32x4 uu = *(LAS f32x4*)(s_u3 + (32 * wn + 16 * q + fr) * 8) + *(LAS f32x4*)(s_u3 + (32 * wn + 16 * q + fr) * 8 + 4);
        u3[q] = (uu[0] + uu[1]) + (uu[2] + uu[3]);
      }
      SCHED();
      acct[1][n] = MFMA16(xf[kk][1], w[f & 7], acct[1][n]);
      SCHED();
    }
    if (wid != 7) {
#pragma unroll
      for (int n = 0; n < 2; ++n) lin_store(acct[0][n], acct[1][n], u3[n], 4 * cb + 2 * wn + n);
    }
    STAMP(7);
  if (wid == 7) {
    f32x4 t;
    for (unsigned spin = 0; spin < SPIN; ++spin) {
      u32x4 pv[14];
#pragma unroll
      for (int i = 0; i < 14; ++i) pv[i] = __builtin_amdgcn_raw_buffer_load_b128(rG, lane * 16 + (i & 3) * 1024, G_PAY + pm * 14336 + (i >> 2) * 4096, 16);
      if (spin == 0) {
#pragma unroll
        for (int n = 0; n < 2; ++n) lin_store(acct[0][n], acct[1][n], u3[n], 4 * cb + 2 * wn + n);
      }
      u32x4 al = pv[0];
      t = __builtin_bit_cast(f32x4, pv[0]);
#pragma unroll
      for (int i = 1; i < 14; ++i) { al &= pv[i]; t += __builtin_bit_cast(f32x4, pv[i]); }
      if (__all((al[0] & al[1] & al[2] & al[3] & 1u) != 0)) break;
      asm volatile("" ::: "memory");
    }
    const float m0 = t[0] * (1.0f / FH), m1 = t[2] * (1.0f / FH);
    const float r0 = __builtin_amdgcn_rsqf(fmaxf(t[1] * (1.0f / FH) - m0 * m0, 0.0f) + 1e-5f);
    const float r1 = __builtin_amdgcn_rsqf(fmaxf(t[3] * (1.0f / FH) - m1 * m1, 0.0f) + 1e-5f);
    *(LAS f32x2*)(s_n + 2 * lane) = f32x2{r0, r1};
    *(LAS f32x2*)(s_n + 128 + 2 * lane) = f32x2{-m0 * r0, -m1 * r1};
    __builtin_amdgcn_s_setprio(0);
  }
  }
  WL0();
  BAR();
  STAMP(8);
  {
    f32x4 r2[2], c2[2];
#pragma unroll
    for (int m = 0; m < 2; ++m) {
      r2[m] = *(LAS f32x4*)(s_n + 32 * wm + 8 * fq + 4 * (m ^ wn));
      c2[m] = *(LAS f32x4*)(s_n + 128 + 32 * wm + 8 * fq + 4 * (m ^ wn));
    }
#pragma unroll
    for (int n = 0; n < 2; ++n) store_t(64 + 4 * cb + 2 * wn + (n ^ (wm & 1)), acc1[0][n] * r2[0] + c2[0], acc1[1][n] * r2[1] + c2[1]);
  }
  STAMP(9);
  STAMP_OUT(a.ws, 0, 10);
}

__device__ __forceinline__ void warm_window(const Args& a, int junk, int rb, int w, int wv, int lane) {
  if (w < 8) {
    const unsigned vo = (rb * 128 + wv * 32 + (lane >> 2)) * (S * 2) + (lane & 3) * 128;
#pragma unroll
    for (int m = 0; m < 3; ++m) {
      const rsrc_t r = RSRC(m == 0 ? a.wsr : m == 1 ? a.wsi : a.wsp, S * S * 2);
      touch(r, junk, vo, w * 512);
      touch(r, junk, vo, w * 512 + 16 * (S * 2));
    }
  } else {
    const rsrc_t r = RSRC(a.wout + (size_t)rb * 56 * D, 56 * D * 2);
    touch(r, junk, lane * 128, wv * 16384);
    touch(r, junk, lane * 128, wv * 16384 + 8192);
  }
}

__global__ __launch_bounds__(512, 2) void fused(Args a) {
  extern __shared__ __attribute__((aligned(1024))) char smem[];
  asm volatile("" ::"v"(smem));
  const unsigned long long pstart = __builtin_amdgcn_s_memrealtime();
  const int bx = blockIdx.x & 7, bi = blockIdx.x >> 3;
  if (bi < CODEWARM && threadIdx.x < 64) {
    unsigned long long pc;
    asm volatile("s_getpc_b64 %0" : "=s"(pc));
    touch(RSRC(pc & ~127ull, CODEWARM * 8192), 1024, threadIdx.x * 128, bi * 8192);
  }
  const int pm = 4 * (bx >> 1) + (bi >> 3), cb = 2 * (bi & 7) + (bx & 1);
  int mat, tm, tn;
  if (bx & 1) { mat = 2; tm = pm; tn = bi & 7; }
  else { mat = bx >> 2; tm = ((bx >> 1) & 1) * 8 + (bi >> 2); tn = bi & 3; }
  const int kx = (tm ^ tn) & K2_KXMASK;
  const u16* Pp = (mat == 0 ? a.wsr : mat == 1 ? a.wsi : a.wsp) + (size_t)tm * 128 * S;
  f32x4 z1v[2][2];
  {
    const int kx1 = ((bi & 7) ^ (2 * (bi >> 3))) & K1_KXMASK;
    if (__builtin_expect(cb < 14, 1)) phase1<true>(a, pm, cb, kx1, Pp, kx, z1v);
    else if (cb == 14) phase1<false>(a, pm, cb, kx1, Pp, kx, z1v);
  }
  int tid = threadIdx.x;
  asm volatile("" : "+v"(tid));
  const int lane = tid & 63;
  const int wid = __builtin_amdgcn_readfirstlane(tid >> 6);
  STAMP_DECL STAMP(0);
  const int tm3 = bx * 4 + (bi >> 3), tn3 = bi & 7;
  const int fr = lane & 15, fq = lane >> 4;
  const unsigned vl = lane * 16;
  const rsrc_t rG = RSRC(a.ws + WS_G, G_END);
  const bool work2 = cb != 15;
  auto z1_finish = [&]() {
    if (cb < 14) {
      const int wm = wid & 3, wn = wid >> 2;
      const rsrc_t rZ = RSRC(a.ws + WS_Z1, FH * S * 2);
      const unsigned vZ = (fr >> 3) * (112 * 256) + (2 * ((fq & 1) ^ (wm & 1)) + (fq >> 1)) * 256 + (fr & 7) * 16;
      const unsigned sZ = ((pm * 8 + 2 * wm) * 112 + 8 * cb + 4 * wn) * 256;
#pragma unroll
      for (int m = 0; m < 2; ++m) {
        unsigned pk[2][2];
#pragma unroll
        for (int n = 0; n < 2; ++n) {
          const f32x2 g0 = gelu2(f32x2{z1v[m][n][0], z1v[m][n][1]}), g1 = gelu2(f32x2{z1v[m][n][2], z1v[m][n][3]});
          pk[n][0] = cvt_pk(g0[0], g0[1]); pk[n][1] = cvt_pk(g1[0], g1[1]);
        }
        auto w0 = __builtin_amdgcn_permlane16_swap(pk[0][0], pk[1][0], false, false);
        auto w1 = __builtin_amdgcn_permlane16_swap(pk[0][1], pk[1][1], false, false);
        st16_sc1(u32x4{w0[0], w1[0], w0[1], w1[1]}, rZ, vZ, sZ + 128 * (m ^ wn));
      }
    }
  };
  if (blockIdx.x == 255) {
    for (int i = 0; i < 36; ++i) st16_sc1(u32x4{0, 0, 0, 0}, rG, vl, (wid * 36 + i) * 1024);
    st16_sc1(u32x4{0, 0, 0, 0}, rG, vl, G_ALL);
  }
  unsigned bv[4];
#pragma unroll
  for (int ni = 0; ni < 4; ++ni) bv[ni] = a.bsp[(8 * tm + 4 * ((wid >> 1) & 1) + ni) * 16 + fr];
  asm volatile("" : "+v"(bv[0]), "+v"(bv[1]), "+v"(bv[2]), "+v"(bv[3]));
  if (work2 || blockIdx.x == 255) {
    WAIT_V(0);
    WL0();
    BAR();
  }
  if (wid >= 4) {
    const int lw = wid - 4;
    const int r8 = lane >> 3, p = lane & 7;
    if (wid == 7) {
      if (!work2) {
        if (blockIdx.x == 255) {
          for (unsigned spin = 0; spin < SPIN; ++spin) {
            const u32x4 rq = ld16_sc1(rG, vl, G_REQ);
            st16_sc1(rq, rG, vl, G_ANS);
            const u32x4 fl = ld16_sc1(rG, vl, G_ALL);
            if (__all((fl[0] & fl[1] & fl[2] & (lane == 63 ? 1u : fl[3])) == 1u)) break;
          }
        } else {
          const unsigned nonce = __builtin_amdgcn_readfirstlane(ld4_sc1(rG, 0, G_ANS + blockIdx.x * 4)) + 1;
          st4_sc1(nonce, rG, 0, G_REQ + blockIdx.x * 4);
          wait_go(rG, nonce, nonce - 1);
        }
      }
      const unsigned box = (fq & 1) * 128 + (cb >> 1) * 16 + fr;
      st4_sc1(1u, rG, fq < 2 ? box * 128 + (pm * 2 + (cb & 1)) * 4 : G_ALL + blockIdx.x * 4, 0);
    }
    const rsrc_t rP = RSRC(Pp, 128 * S * 2);
    const unsigned vo_e = (lw * 32 + r8) * (S * 2) + ((p ^ (r8 >> 1)) << 4);
    const unsigned vo_o = (vo_e ^ 64) + 8 * (S * 2);
    auto stageP = [&](int kt, unsigned poff) {
      const unsigned mb = poff + lw * 4096, so = (kt ^ kx) * 128;
#pragma unroll
      for (int j = 0; j < 4; ++j) dma16(rP, mb, P2_PB + j * 1024, (j & 1) ? vo_o : vo_e, so + (j >> 1) * 16 * (S * 2));
    };
    if (work2) {
      BAR();
      stageP(4, 0); stageP(5, 16384);
    }
    const rsrc_t rWo = RSRC(a.wout + 64 * tn3, (FH * D - 64 * tn3) * 2);
    const unsigned vw_e = (32 * lw + r8) * (D * 2) + ((p ^ (((r8 >> 1) & 1) << 1)) << 4), vw_o = (vw_e ^ 64) + 8 * (D * 2);
    u32x4 wreg[12];
#pragma unroll
    for (int i = 0; i < 12; ++i) wreg[i] = u32x4{0, 0, 0, 0};
    auto wput = [&](int rg) {
#pragma unroll
      for (int j = 0; j < 4 && !NOPARK; ++j) *(LAS u32x4*)(size_t)(rg * 16384 + lw * 4096 + j * 1024 + vl) = wreg[(rg == 6 ? 8 : (rg - 2) * 4) + j];
    };
    auto wdma = [&](int rg) {
#pragma unroll
      for (int j = 0; j < 4; ++j) dma16(rWo, lw * 4096, rg * 16384 + j * 1024, (j & 1) ? vw_o : vw_e, (rg * 128 + (j >> 1) * 16) * (D * 2));
    };
    if (work2) {
#pragma unroll
      for (int i = 0; i < 12; ++i) {
        const int rg = i < 8 ? 2 + (i >> 2) : 6;
        ld16_async(wreg[i], rWo, (i & 1) ? vw_o : vw_e, (rg * 128 + ((i >> 1) & 1) * 16) * (D * 2));
      }
      if (Z1EARLY && wid != 7) z1_finish();
      if (wid == 7) {
        const unsigned box = (mat * 4 + tn) * 16 + tm;
        for (unsigned spin = 0; spin < SPIN; ++spin) {
          const unsigned fl = ld4_sc1(rG, (lane & 31) * 4, G_F1 + box * 128);
          if (__all(fl == 1u || (box >= 112 && box < 128 && (lane & 1)))) break;
        }
      }
      BAR();
      STAMP(1);
      const rsrc_t rQ = RSRC(a.ws + WS_Q + (size_t)(mat * 4 + tn) * 128 * S * 2, 128 * S * 2);
      const unsigned vq = vl + lw * 131072;
      auto stageQ = [&](int kt) {
        const unsigned mb = (kt & 3) * 16384 + lw * 4096, so = (kt ^ kx) * 2048;
#pragma unroll
        for (int j = 0; j < 4; ++j) dma16_sc1(rQ, mb, j * 1024, vq, so + (j >> 1) * 65536 + (j & 1) * 1024);
      };
      stageQ(0); stageQ(1); stageQ(2); stageQ(3);
      if (!Z1EARLY || wid == 7) z1_finish();
      WAIT_V(12); BAR();
      WAIT_V(8); BAR(); stageQ(4); stageP(6, 2 * 16384);
      WAIT_V(12); BAR(); stageQ(5); stageP(7, 3 * 16384);
      WAIT_V(16); BAR(); stageQ(6); stageP(8, 4 * 16384);
      unsigned poff = 5 * 16384;
      unsigned zf;
      for (int kt = 3; kt < 23; ++kt) {
        WAIT_V(20);
        BAR();
        if (kt == 3 && wid == 7 && cb < 14) st4_sc1(1u, rG, 0, G_FZ + (pm * 8 + (cb >> 1)) * 128 + (cb & 1) * 4);
        stageQ(kt + 4); stageP(kt + 6, poff);
        poff = poff == 5 * 16384 ? 0 : poff + 16384;
      }
      asm volatile("buffer_load_dword %0, %1, %2, %3 offen sc1" : "=v"(zf) : "v"((lane & 1) * 4), "s"(rG), "s"(G_FZ + (tm * 8 + tn) * 128) : "memory");
#pragma unroll
      for (int kt = 23; kt < 26; ++kt) {
        WAIT_V(20);
        BAR();
        stageQ(kt + 4); stageP(kt + 6, ((kt + 2) % 6) * 16384);
      }
      asm volatile("s_waitcnt vmcnt(20)" : "+v"(zf)::"memory");
      if (mat == 2) {
        const rsrc_t rZ = RSRC(a.ws + WS_Z1 + (size_t)((8 * tm + lw) * 112 + 16 * tn) * 256, 4 * 112 * 256 + 4096);
        auto stageZ = [&](int h) {
#pragma unroll
          for (int j = 0; j < 4; ++j) dma16_sc1(rZ, lw * 4096, P2_Z + h * 16384 + j * 1024, vl, h * (4 * 112 * 256) + j * 1024);
        };
        for (unsigned spin = 0; !__all(zf == 1u) && spin < SPIN; ++spin) zf = ld4_sc1(rG, (lane & 1) * 4, G_FZ + (tm * 8 + tn) * 128);
        WAIT_V(20); BAR(); stageQ(30); stageZ(0);
        WAIT_V(20); BAR(); stageQ(31); stageZ(1);
        WAIT_V(20); BAR(); wdma(0); wdma(4);
        WAIT_V(20); BAR(); wdma(1); wdma(5);
        WAIT_V(20); BAR(); wput(2); wput(6);
        WAIT_V(16); BAR(); wput(3);
      } else {
        WAIT_V(20); BAR(); stageQ(30);
        WAIT_V(16); BAR(); stageQ(31);
        WAIT_V(12); BAR(); wdma(0); wdma(4);
        WAIT_V(12); BAR(); wdma(1); wdma(5);
        WAIT_V(16); BAR(); wput(2); wput(6);
        BAR(); wput(3);
      }
    } else {
      STAMP(1);
      wdma(0); wdma(4); wdma(1); wdma(5); wdma(2); wdma(3); wdma(6);
    }
    STAMP(2); STAMP(3);
    WAIT_V(0);
  } else if (work2) {
    const int wp = wid >> 1, wq = wid & 1;
    f32x4 acc[4][4];
#pragma unroll
    for (int ni = 0; ni < 4; ++ni) {
      const float bias = mat == 2 ? __uint_as_float(bv[ni] << 16) : 0.0f;
#pragma unroll
      for (int n = 0; n < 4; ++n) acc[ni][n] = f32x4{bias, bias, bias, bias};
    }
    BAR();
    if (DWARM && cb == 14) warm_window(a, 0, pm, 2, wid, lane);
    const int sw = (fr >> 1) & 7;
    const unsigned ra = P2_PB + (wp * 64 + fr) * 128 + ((fq ^ sw) << 4);
    const unsigned rb = wq * 8192 + vl;
    bf16x8 a0[4], b0[4], a1[4], b1[4];
    unsigned poff = 2 * 16384;
    unsigned pa0 = ra + poff, pa1 = pa0 ^ 64;
#define K2_RD(AF, BF, pa, sl, ks, i) do { \
    if ((i) == 0) AF[0] = ld_frag(pa); \
    else if ((i) <= 4) BF[(i) - 1] = ld_frag(rb + (sl) * 16384 + ((i) - 1) * 2048 + (ks) * 1024); \
    else AF[(i) - 4] = ld_frag(pa + ((i) - 4) * 2048); } while (0)
#define K2_MM(AF, BF, i) acc[(i) >> 2][(i) & 3] = MFMA16(BF[(i) & 3], AF[(i) >> 2], acc[(i) >> 2][(i) & 3])
#define K2_STEP(s, LAST) do { \
    SCHED(); WL0(); SCHED(); \
    _Pragma("unroll") for (int i = 0; i < 16; ++i) { K2_MM(a0, b0, i); SCHED(); if (i < 8) K2_RD(a1, b1, pa1, s, 1, i); \
      if (i == 10) { poff = poff == 5 * 16384 ? 0 : poff + 16384; pa0 = ra + poff; } SCHED(); } \
    WL0(); \
    if ((s) == 3) WAIT_V(0); \
    BAR(); \
    SCHED(); \
    _Pragma("unroll") for (int i = 0; i < 16; ++i) { K2_MM(a1, b1, i); SCHED(); if (!(LAST) && i < 8) K2_RD(a0, b0, pa0, ((s) + 1) & 3, 0, i); \
      if (i == 10) pa1 = pa0 ^ 64; SCHED(); } \
  } while (0)
    if (DWARM && cb == 14) WAIT_V(0);
    if (Z1EARLY) z1_finish();
    BAR();
    if (!Z1EARLY) z1_finish();
    BAR();
    STAMP(1);
#pragma unroll
    for (int i = 0; i < 8; ++i) K2_RD(a0, b0, pa0, 0, 0, i);
    for (int kt = 0; kt < 28; kt += 4) { K2_STEP(0, 0); K2_STEP(1, 0); K2_STEP(2, 0); K2_STEP(3, 0); }
    K2_STEP(0, 0); K2_STEP(1, 0); K2_STEP(2, 0); K2_STEP(3, 1);
    SCHED();
    STAMP(2);
    if (mat < 2) {
      const rsrc_t rY = RSRC(a.ws + WS_Y + (size_t)mat * S * D * 2, S * D * 2);
      const unsigned so = ((8 * tm + 4 * wp) * 64 + 16 * tn + 8 * wq) * 256;
#pragma unroll
      for (int ni = 0; ni < 4; ++ni)
#pragma unroll
        for (int P = 0; P < 2; ++P) {
          const f32x4 c0 = acc[ni][2 * P], c1 = acc[ni][2 * P + 1];
          const u32x4 v = {cvt_pk(c0[0], c0[1]), cvt_pk(c0[2], c0[3]), cvt_pk(c1[0], c1[1]), cvt_pk(c1[2], c1[3])};
          st16_sc1(v, rY, vl, so + (ni * 64 + 4 * P) * 256);
        }
    } else {
      const rsrc_t rH = RSRC(a.ws + WS_H, S * FH * 2);
      const unsigned so = ((8 * tm + 4 * wp) * 112 + 16 * tn + 8 * wq) * 256;
      const unsigned za = P2_Z + wp * 16384 + wq * 2048 + (fq * 16 + ((fr & 4) << 1) + ((fr & 8) >> 1) + (fr & 3)) * 16;
#pragma unroll
      for (int ni = 0; ni < 4; ++ni) {
#pragma unroll
        for (int P = 0; P < 2; ++P) {
          const f32x4 c0 = acc[ni][2 * P], c1 = acc[ni][2 * P + 1];
          const u32x4 z = *(const LAS u32x4*)(size_t)(za + ni * 4096 + P * 1024);
          const u32x4 v = {cvt_pk(bf_lo(z[0]) * c0[0], bf_hi(z[0]) * c0[1]), cvt_pk(bf_lo(z[1]) * c0[2], bf_hi(z[1]) * c0[3]),
                           cvt_pk(bf_lo(z[2]) * c1[0], bf_hi(z[2]) * c1[1]), cvt_pk(bf_lo(z[3]) * c1[2], bf_hi(z[3]) * c1[3])};
          st16_sc1(v, rH, vl, so + (ni * 112 + 4 * P) * 256);
        }
      }
    }
    STAMP(3);
    WAIT_V(0);
  } else {
    STAMP(1);
    for (int i = 0; i < PF_N && blockIdx.x != 255; ++i) {
      warm_window(a, P2_Z, pm, (PF_LIST >> (4 * i)) & 15, wid, lane);
      WAIT_V(6);
    }
    STAMP(2);
    WAIT_V(0);
    STAMP(3);
  }
  WL0();
  BAR();
  STAMP(4);
  {
    const int wr = (wid + tn3) & 7, tp = wr >> 2, kq = wr & 3;
    const int tt3 = 4 * tm3 + 2 * tp + (kq & 1);
    const int cq = 2 * (fq & 1) + (fq >> 1);
    const int c3 = 8 * tn3 + 2 * (kq & 2);
    if (wid == 4 && work2) {
      const unsigned box = mat == 2 ? fr : (fr & 8) + 2 * tn + (fr & 1);
      st4_sc1(1u, rG, box * 128, G_FP + tm * 2048 + (mat == 2 ? tn : 8 + mat) * 4);
    }
    const int q4 = fr >> 2, pp = lane & 3;
    const int sp = ((q4 >> 1) & 1) | ((fq & 1) << 1);
    const unsigned wa = kq * 4096 + 128 * (8 * fq + q4) + 16 * ((2 * (kq & 2) + (pp >> 1)) ^ (sp << 1)) + 8 * (pp & 1);
    bf16x8 wf[7][4];
    auto ld_w = [&]() {
#pragma unroll
      for (int mi = 0; mi < 4; ++mi) {
        unsigned lo = wa ^ (mi * 32), hi = lo + 65536;
        OPAQUE(hi);
#pragma unroll
        for (int g = 0; g < 7; ++g) wf[g][mi] = ld_tr((g < 4 ? lo : hi) + (g & 3) * 16384);
      }
    };
    if (wid == 4) {
      for (unsigned spin = 0; spin < SPIN; ++spin) {
        const unsigned fl = ld4_sc1(rG, fr * 4, G_FP + (tm3 * 8 + tn3) * 128);
        if (__all(fl == 1u || fr == 7 || fr > 9)) break;
      }
    } else {
      ld_w();
    }
    BAR();
    STAMP(5);
    const rsrc_t rH = RSRC(a.ws + WS_H + (size_t)((4 * tm3 + 2 * tp) * 112 + 4 * kq) * 256, 2 * 112 * 256);
    const rsrc_t rY = RSRC(a.ws + WS_Y + (size_t)(tt3 * 64 + c3) * 256, S * D * 2 + 1024);
    const unsigned vh0 = vl + (kq & 1) * (112 * 256), vh1 = vl + ((kq & 1) ^ 1) * (112 * 256);
    u32x4 hf[7][2];
#pragma unroll
    for (int g = 0; g < 7; ++g) {
      hf[g][0] = __builtin_amdgcn_raw_buffer_load_b128(rH, vh0, g * 4096, 16);
      hf[g][1] = __builtin_amdgcn_raw_buffer_load_b128(rH, vh1, g * 4096, 16);
    }
    const unsigned vy = cq * 256 + fr * 16;
    const u32x4 e1 = __builtin_amdgcn_raw_buffer_load_b128(rY, vy, 0, 16), e2 = __builtin_amdgcn_raw_buffer_load_b128(rY, vy, S * D * 2, 16);
    const u32x4 ex = *(const u32x4*)(a.x + (size_t)(tt3 * 16 + fr) * D + (c3 + cq) * 8);
    SCHED();
    if (wid == 4) ld_w();
    f32x4 acc[2][4];
#pragma unroll
    for (int g = 0; g < 7; ++g)
#pragma unroll
      for (int tl = 0; tl < 2; ++tl)
#pragma unroll
        for (int mi = 0; mi < 4; ++mi) acc[tl][mi] = MFMA16(wf[g][mi], __builtin_bit_cast(bf16x8, hf[g][tl]), (g ? acc[tl][mi] : f32x4{0, 0, 0, 0}));
    STAMP(6);
    LAS f32x4* xb = (LAS f32x4*)(size_t)(P3_X) + lane;
#pragma unroll
    for (int d = 1; d < 4; ++d) {
      xb[((wr * 3 + d - 1) * 2) * 64] = acc[d & 1][d & 2];
      xb[((wr * 3 + d - 1) * 2 + 1) * 64] = acc[d & 1][(d & 2) + 1];
    }
    WL0();
    BAR();
    f32x4 s0 = acc[0][0], s1 = acc[0][1];
#pragma unroll
    for (int d = 1; d < 4; ++d) {
      s0 += xb[(((wr ^ d) * 3 + d - 1) * 2) * 64];
      s1 += xb[(((wr ^ d) * 3 + d - 1) * 2 + 1) * 64];
    }
    f32x4 c0, c1;
#pragma unroll
    for (int j = 0; j < 4; ++j) {
      auto w = __builtin_amdgcn_permlane16_swap(__float_as_uint(s0[j]), __float_as_uint(s1[j]), false, false);
      c0[j] = __uint_as_float(w[0]); c1[j] = __uint_as_float(w[1]);
    }
    u32x4 o;
#pragma unroll
    for (int k = 0; k < 4; ++k) {
      const f32x4 c = (k >> 1) ? c1 : c0;
      const float lo = bf_lo(ex[k]) + bf_lo(e1[k]) - bf_lo(e2[k]) + c[(k & 1) * 2];
      const float hi = bf_hi(ex[k]) + bf_hi(e1[k]) - bf_hi(e2[k]) + c[(k & 1) * 2 + 1];
      o[k] = cvt_pk(lo, hi);
    }
    st16_sc1(o, RSRC(a.out, S * D * 2), ((tt3 * 16 + fr) * D + (c3 + cq) * 8) * 2, 0);
  }
  STAMP(7);
  STAMP_OUT(a.ws, 1, 8);
  {
    asm volatile("s_waitcnt vmcnt(0)" ::: "memory");
    const unsigned long long pend = __builtin_amdgcn_s_memrealtime();
    const rsrc_t rS = RSRC(a.ws + (100u << 20), 65536);
    st16_sc1(u32x4{(unsigned)pstart, (unsigned)pend, 0, 0}, rS, 0, (blockIdx.x * 8 + wid) * 16);
    if (blockIdx.x == 73 && wid == 0) {
      while (__builtin_amdgcn_s_memrealtime() < pstart + 6000) __builtin_amdgcn_s_sleep(1);
      int mns = 1 << 30, mxe = -(1 << 30);
      for (int i = 0; i < 32; ++i) {
        const u32x4 v = ld16_sc1(rS, lane * 16, i * 1024);
        const int st = (int)(v[0] - (unsigned)pstart), en = (int)(v[1] - (unsigned)pstart);
        mns = min(mns, st); mxe = max(mxe, en);
      }
      for (int o = 32; o > 0; o >>= 1) { mns = min(mns, __shfl_xor(mns, o)); mxe = max(mxe, __shfl_xor(mxe, o)); }
      const int q = __builtin_amdgcn_readfirstlane(mxe - mns);
      const unsigned long long tend = pstart + __builtin_amdgcn_readfirstlane(mns) + 10 * q;
      while (__builtin_amdgcn_s_memrealtime() < tend) ;
    }
  }
}

extern "C" void kernel_launch(void* const* d_in, const int* in_sizes, int n_in, void* d_out, int out_size, void* d_ws, size_t ws_size,
                              hipStream_t stream) {
  (void)in_sizes; (void)n_in; (void)out_size; (void)ws_size;
  (void)hipFuncSetAttribute((const void*)fused, hipFuncAttributeMaxDynamicSharedMemorySize, LDS_BYTES);
  Args a{(const u16*)d_in[0], (const u16*)d_in[3], (const u16*)d_in[4], (const u16*)d_in[6], (const u16*)d_in[9],
         (const u16*)d_in[1], (const u16*)d_in[2], (const u16*)d_in[7], (const u16*)d_in[8], (char*)d_ws, (u16*)d_out};
  fused<<<256, 512, LDS_BYTES, stream>>>(a);
}
```

```cpp
#include <hip/hip_runtime.h>
#include <hip/hip_bf16.h>
#include <stdint.h>

typedef unsigned short u16;
typedef short bf16x8 __attribute__((ext_vector_type(8)));
typedef short s16x4 __attribute__((ext_vector_type(4)));
typedef float f32x4 __attribute__((ext_vector_type(4)));
typedef float f32x2 __attribute__((ext_vector_type(2)));
typedef unsigned u32x4 __attribute__((ext_vector_type(4)));
typedef __bf16 bf16x2v __attribute__((ext_vector_type(2)));
typedef __amdgpu_buffer_rsrc_t rsrc_t;
#define LAS __attribute__((address_space(3)))
#define WAIT_V(n) asm volatile("s_waitcnt vmcnt(%0)" ::"n"(n) : "memory")
#define WL0() __builtin_amdgcn_s_waitcnt(0xC07F)
#define BAR() do { asm volatile("" ::: "memory"); __builtin_amdgcn_s_barrier(); asm volatile("" ::: "memory"); } while (0)
#define SCHED() __builtin_amdgcn_sched_barrier(0)
#define MFMA16(a, b, c) __builtin_amdgcn_mfma_f32_16x16x32_bf16(a, b, c, 0, 0, 0)
#define RSRC(p, n) __builtin_amdgcn_make_buffer_rsrc((void*)(p), 0, (unsigned)(n), 0x00020000)
#define OPAQUE(x) asm volatile("" : "+v"(x))

#define STAMP_DECL
#define STAMP(i)
#define STAMP_OUT(ws, kid, n)
#define K2_KXMASK 7
#define K1_KXMASK 3
#define DWARM 0
#define CODEWARM 5
#define NOPARK 0
#define Z1EARLY 0
#define YEARLY 1
#define PF_LIST 0x753810u
#define PF_N 6

constexpr int S = 2048, D = 512, F = 1792, FH = 896;
constexpr size_t WS_Q = 0;
constexpr size_t WS_Z1 = WS_Q + (size_t)(2 * D + FH) * S * 2;
constexpr size_t WS_H = WS_Z1 + (size_t)FH * S * 2;
constexpr size_t WS_Y = WS_H + (size_t)FH * S * 2;
constexpr size_t WS_G = WS_Y + (size_t)2 * D * S * 2;
constexpr unsigned G_F1 = 0;
constexpr unsigned G_FP = 32768;
constexpr unsigned G_FZ = 32768 + 64;
constexpr unsigned G_PAY = 65536;
constexpr unsigned G_ALL = G_PAY + 16 * 14336;
constexpr unsigned G_REQ = G_ALL + 1024;
constexpr unsigned G_ANS = G_REQ + 1024;
constexpr unsigned G_END = G_ANS + 1024;

constexpr int LDS_BYTES = 163840;
constexpr int P1_RING = 32768, P1_SCR = 81920;
constexpr int P2_PB = 65536, P2_Z = 131072, P3_X = 114688;
constexpr unsigned SPIN = 1u << 20;

struct Args {
  const u16* x; const u16* wdr; const u16* wdi; const u16* win; const u16* wout;
  const u16* wsr; const u16* wsi; const u16* wsp; const u16* bsp;
  char* ws; u16* out;
};

__device__ __forceinline__ unsigned cvt_pk(float lo, float hi) {
  f32x2 v = {lo, hi};
  return __builtin_bit_cast(unsigned, __builtin_convertvector(v, bf16x2v));
}
__device__ __forceinline__ float bf_lo(unsigned u) { return __uint_as_float(u << 16); }
__device__ __forceinline__ float bf_hi(unsigned u) { return __uint_as_float(u & 0xffff0000u); }
constexpr float GC1 = -2.0f * 0.7978845608028654f * 1.4426950408889634f, GC2 = GC1 * 0.044715f;
__device__ __forceinline__ f32x2 gelu2(f32x2 v) {
  const f32x2 t = v * ((v * v) * GC2 + GC1);
  const f32x2 e = f32x2{__builtin_amdgcn_exp2f(t[0]), __builtin_amdgcn_exp2f(t[1])} + 1.0f;
  return v * f32x2{__builtin_amdgcn_rcpf(e[0]), __builtin_amdgcn_rcpf(e[1])};
}
__device__ __forceinline__ void dot_sq(float& c, unsigned a) { asm volatile("v_dot2c_f32_bf16 %0, %1, %1" : "+v"(c) : "v"(a)); }
__device__ __forceinline__ void dot_sum(float& c, unsigned a) { asm volatile("v_dot2c_f32_bf16 %0, 0x3f803f80, %1" : "+v"(c) : "v"(a)); }
__device__ __forceinline__ float sum_rows(float v) {
  auto a = __builtin_amdgcn_permlane32_swap(__float_as_uint(v), __float_as_uint(v), false, false);
  v = __uint_as_float(a[0]) + __uint_as_float(a[1]);
  auto b = __builtin_amdgcn_permlane16_swap(__float_as_uint(v), __float_as_uint(v), false, false);
  return __uint_as_float(b[0]) + __uint_as_float(b[1]);
}
__device__ __forceinline__ float sel4(f32x4 v, int i) { const float a = (i & 1) ? v[1] : v[0], b = (i & 1) ? v[3] : v[2]; return (i & 2) ? b : a; }

__device__ __forceinline__ void dma16(rsrc_t r, unsigned mbase, int mimm, unsigned voff, unsigned soff) {
  asm volatile("s_add_i32 m0, %0, %1\n\ts_nop 0\n\tbuffer_load_dwordx4 %2, %3, %4 offen lds" ::"s"(mbase), "n"(mimm), "v"(voff), "s"(r), "s"(soff) : "scc");
}
__device__ __forceinline__ void dma16_sc1(rsrc_t r, unsigned mbase, int mimm, unsigned voff, unsigned soff) {
  asm volatile("s_add_i32 m0, %0, %1\n\ts_nop 0\n\tbuffer_load_dwordx4 %2, %3, %4 offen sc1 lds" ::"s"(mbase), "n"(mimm), "v"(voff), "s"(r), "s"(soff) : "scc");
}
__device__ __forceinline__ void dma4_sc1(rsrc_t r, int ldsaddr, unsigned voff, unsigned soff) {
  asm volatile("s_mov_b32 m0, %0\n\ts_nop 0\n\tbuffer_load_dword %1, %2, %3 offen sc1 lds" ::"n"(ldsaddr), "v"(voff), "s"(r), "s"(soff) : "memory");
}
__device__ __forceinline__ void touch(rsrc_t r, int junk, unsigned voff, unsigned soff) {
  asm volatile("s_mov_b32 m0, %0\n\ts_nop 0\n\tbuffer_load_dword %1, %2, %3 offen lds" ::"n"(junk), "v"(voff), "s"(r), "s"(soff) : "memory");
}
__device__ __forceinline__ void st16_sc1(u32x4 v, rsrc_t r, unsigned voff, unsigned soff) {
  asm volatile("buffer_store_dwordx4 %0, %1, %2, %3 offen sc1\n\ts_nop 1" ::"v"(v), "v"(voff), "s"(r), "s"(soff) : "memory");
}
__device__ __forceinline__ void st4_sc1(unsigned v, rsrc_t r, unsigned voff, unsigned soff) {
  asm volatile("buffer_store_dword %0, %1, %2, %3 offen sc1" ::"v"(v), "v"(voff), "s"(r), "s"(soff) : "memory");
}
__device__ __forceinline__ unsigned ld4_sc1(rsrc_t r, unsigned voff, unsigned soff) {
  unsigned v;
  asm volatile("buffer_load_dword %0, %1, %2, %3 offen sc1\n\ts_waitcnt vmcnt(0)" : "=&v"(v) : "v"(voff), "s"(r), "s"(soff) : "memory");
  return v;
}
__device__ __forceinline__ u32x4 ld16_sc1(rsrc_t r, unsigned voff, unsigned soff) {
  u32x4 v;
  asm volatile("buffer_load_dwordx4 %0, %1, %2, %3 offen sc1\n\ts_waitcnt vmcnt(0)" : "=&v"(v) : "v"(voff), "s"(r), "s"(soff) : "memory");
  return v;
}
__device__ __forceinline__ void ld16_async(u32x4& v, rsrc_t r, unsigned voff, unsigned soff) {
  asm volatile("buffer_load_dwordx4 %0, %1, %2, %3 offen" : "=v"(v) : "v"(voff), "s"(r), "s"(soff) : "memory");
}
__device__ __forceinline__ void wait_vn(int n) { WAIT_V(n); }
__device__ __forceinline__ bf16x8 ld_tr(unsigned addr) {
  s16x4 lo = __builtin_amdgcn_ds_read_tr16_b64_v4i16((LAS s16x4*)(size_t)(addr));
  s16x4 hi = __builtin_amdgcn_ds_read_tr16_b64_v4i16((LAS s16x4*)(size_t)(addr + 512));
  return __builtin_shufflevector(lo, hi, 0, 1, 2, 3, 4, 5, 6, 7);
}
__device__ __forceinline__ bf16x8 ld_frag(unsigned addr) { return *(const LAS bf16x8*)(size_t)(addr); }
__device__ __forceinline__ void wait_go(rsrc_t rG, unsigned nonce, unsigned seen) {
  for (unsigned spin = 0; seen != nonce && spin < SPIN; ++spin) {
    seen = __builtin_amdgcn_readfirstlane(ld4_sc1(rG, 0, G_ANS + blockIdx.x * 4));
    __builtin_amdgcn_s_sleep(1);
  }
}

template <bool ZB>
__device__ __forceinline__ void phase1(const Args& a, int pm, int cb, int kx, const u16* Pp, int kx2, f32x4 (&z1v)[2][2]) {
  const int tid = threadIdx.x, lane = tid & 63;
  const int wid = __builtin_amdgcn_readfirstlane(tid >> 6);
  const int wm = wid & 3, wn = wid >> 2;
  STAMP_DECL STAMP(0);
  const rsrc_t rX = RSRC(a.x + (size_t)pm * 128 * D, 128 * D * 2);
  const rsrc_t rW = ZB ? RSRC(a.win + 64 * cb, (D * F - 64 * cb) * 2) : RSRC(a.wdi + (size_t)384 * D, 128 * D * 2);
  const int r8 = lane >> 3, p8 = lane & 7;
  const unsigned sw_e = (p8 ^ (r8 >> 1)) << 4;
  const unsigned vx_e = (32 * (wid >> 1) + 4 * (wid & 1) + 8 * (r8 >> 2) + (r8 & 3)) * (D * 2) + sw_e;
  const unsigned vx_o = (vx_e ^ 64) + 16 * (D * 2);
  const unsigned vw2 = ZB ? (8 * wid + r8) * (F * 2) + ((p8 ^ ((((r8 >> 1) & 1) | ((wid & 1) << 1)) << 1)) << 4)
                          : (64 + 8 * wid + r8) * (D * 2) + (sw_e ^ ((wid & 1) << 6));
  const unsigned vw1 = ZB ? vw2 + FH * 2 : vw2 - 64 * (D * 2);
  constexpr unsigned WKB = ZB ? 64 * F * 2 : 128;
  const unsigned mb1 = wid * 1024, mb2 = wid * 2048;
  auto piece1 = [&](int st, int p) {
    const unsigned kb = st ^ kx;
    const int base = P1_RING + (st & 3) * 32768;
    if (p == 0) dma16(rX, mb2, base, vx_e, kb * 128);
    else if (p == 1) dma16(rW, mb1, base + 16384, vw1, kb * WKB);
    else if (p == 2) dma16(rX, mb2, base + 1024, vx_o, kb * 128);
    else dma16(rW, mb1, base + 24576, vw2, kb * WKB);
  };
  piece1(0, 0); piece1(0, 1); piece1(0, 2); piece1(0, 3);
  SCHED();

  const int fr = lane & 15, fq = lane >> 4;
  const int kh = wm >> 1;
  const unsigned fsw = ((fq ^ ((fr >> 1) & 7)) << 4) ^ (kh << 6);
  unsigned xa[2][2][2], wb[2][2][2];
#pragma unroll
  for (int m = 0; m < 2; ++m) { xa[0][m][0] = P1_RING + (32 * wm + 16 * (m ^ wn) + fr) * 128 + fsw; xa[0][m][1] = xa[0][m][0] ^ 64; }
#pragma unroll
  for (int n = 0; n < 2; ++n) {
    const int tile = 2 * wn + (n ^ (wm & 1));
    if (ZB) {
      const int q4 = fr >> 2, pp = lane & 3;
      const int sp = ((q4 >> 1) & 1) | ((fq & 1) << 1);
      const int ch = (2 * tile + (pp >> 1)) ^ (sp << 1);
      wb[0][n][0] = P1_RING + 16384 + 128 * (8 * fq + q4) + 16 * ch + 8 * (pp & 1) + kh * 4096;
      wb[0][n][1] = wb[0][n][0] ^ 4096;
    } else {
      wb[0][n][0] = P1_RING + 16384 + (16 * tile + fr) * 128 + fsw;
      wb[0][n][1] = wb[0][n][0] ^ 64;
    }
  }
#pragma unroll
  for (int i = 0; i < 2; ++i)
#pragma unroll
    for (int ks = 0; ks < 2; ++ks) {
      xa[1][i][ks] = xa[0][i][ks] + 65536; wb[1][i][ks] = wb[0][i][ks] + 65536;
      OPAQUE(xa[0][i][ks]); OPAQUE(wb[0][i][ks]); OPAQUE(xa[1][i][ks]); OPAQUE(wb[1][i][ks]);
    }
  auto ldw = [&](unsigned addr) -> bf16x8 { return ZB ? ld_tr(addr) : ld_frag(addr); };
  SCHED();
  piece1(1, 0); piece1(1, 1); piece1(1, 2); piece1(1, 3);
  const rsrc_t rG = RSRC(a.ws + WS_G, G_END);
  if (wid == 7) dma4_sc1(rG, 0, 0, G_ANS + blockIdx.x * 4);
  unsigned nonce = 0;
  STAMP(1);

  bf16x8 xf[16][2], wf[2][4];
  const f32x4 Z4 = {0, 0, 0, 0};
  f32x4 acc1[2][2] = {{Z4, Z4}, {Z4, Z4}}, acc2[2][2] = {{Z4, Z4}, {Z4, Z4}};
  f32x4 gram = Z4, rsum = Z4, cs1 = Z4, cs2 = Z4;
  const bf16x8 ONES = {0x3f80, 0x3f80, 0x3f80, 0x3f80, 0x3f80, 0x3f80, 0x3f80, 0x3f80};

  auto mm = [&](int kk, int i) {
    const int ks = kk & 1;
    if (i == 0) gram = MFMA16(xf[kk][0], xf[kk][0], gram);
    else if (i == 1) rsum = MFMA16(xf[kk][0], ONES, rsum);
    else if (i < 6) { const int m = (i - 2) >> 1, n = i & 1; acc1[m][n] = MFMA16(xf[kk][m], wf[ks][n], acc1[m][n]); }
    else if (i < 10) { const int m = (i - 6) >> 1, n = i & 1; acc2[m][n] = ZB ? MFMA16(wf[ks][2 + n], xf[kk][m], acc2[m][n]) : MFMA16(xf[kk][m], wf[ks][2 + n], acc2[m][n]); }
    else if (i == 10) cs1 = MFMA16(ONES, wf[0][0], cs1);
    else cs2 = MFMA16(ONES, wf[0][2], cs2);
  };
  auto rd = [&](int kk, int i) {
    const int st = kk >> 1, ks = kk & 1, wi = (st >> 1) & 1, so = (st & 1) * 32768;
    if (i == 0 || i == 3) xf[kk][i & 1] = ld_frag(xa[wi][i & 1][ks] + so);
    else if (i < 3) wf[ks][i - 1] = ldw(wb[wi][i - 1][ks] + so);
    else wf[ks][i - 2] = ldw(wb[wi][i - 4][ks] + so + 8192);
  };
  auto piece3 = [&](int p) {
    const rsrc_t rD = RSRC((cb < 8 ? a.wdr : a.wdi) + (size_t)(cb & 7) * 64 * D, 64 * D * 2);
    const unsigned ve = r8 * (D * 2) + sw_e, vo = (ve ^ 64) + 8 * (D * 2);
    dma16(rD, wid * 8192, p * 1024, (p & 1) ? vo : ve, (wid ^ kx) * 128 + (p >> 1) * 16 * (D * 2));
  };
  auto earlyP = [&](int k) {
    const rsrc_t rP = RSRC(Pp, 128 * S * 2);
    const unsigned ve = (16 * wid + r8) * (S * 2) + sw_e, vo = (ve ^ 64) + 8 * (S * 2);
    dma16(rP, mb2, P2_PB + (k + 2) * 16384, ve, (k ^ kx2) * 128);
    dma16(rP, mb2, P2_PB + (k + 2) * 16384 + 1024, vo, (k ^ kx2) * 128);
  };
#pragma unroll
  for (int st = 0; st < 8; ++st) {
    wait_vn(st <= 6 ? 4 : ZB ? 8 : 0);
    WL0();
    BAR();
    if (st == 0) STAMP(2);
    SCHED();
#pragma unroll
    for (int i = 0; i < 10; ++i) {
      if (st > 0) mm(2 * st - 1, i);
      SCHED();
      if (i < 6) rd(2 * st, i);
      if (st + 2 < 8) { if (i == 3) piece1(st + 2, 0); if (i == 8) piece1(st + 2, 1); }
      if (ZB && st == 6 && (i % 3) == 0) piece3(i / 3);
      if (st == 7 && i == 7) earlyP(0);
      SCHED();
    }
    WL0();
    SCHED();
#pragma unroll
    for (int i = 0; i < 12; ++i) {
      mm(2 * st, i);
      SCHED();
      if (i < 6) rd(2 * st + 1, i);
      if (st + 2 < 8) { if (i == 3) piece1(st + 2, 2); if (i == 9) piece1(st + 2, 3); }
      if (ZB && st == 6 && (i % 3) == 2) piece3(4 + i / 3);
      if (st == 7 && i == 8) earlyP(1);
      SCHED();
    }
    if (st == 2 && wid == 7) {
      nonce = __builtin_amdgcn_readfirstlane(*(LAS unsigned*)(size_t)(lane * 4)) + 1;
      st4_sc1(nonce, rG, 0, G_REQ + blockIdx.x * 4);
    }
    SCHED();
  }
  LAS float* const s_r = (LAS float*)(size_t)(P1_SCR);
  LAS float* const s_c = (LAS float*)(size_t)(P1_SCR + 512);
  LAS float* const s_u1 = (LAS float*)(size_t)(P1_SCR + 1024);
  LAS float* const s_u2 = (LAS float*)(size_t)(P1_SCR + 1536);
  LAS float* const s_z = (LAS float*)(size_t)(P1_SCR + 2048);
  LAS float* const s_n = (LAS float*)(size_t)(P1_SCR + 4096);
  LAS float* const s_u3 = (LAS float*)(size_t)(P1_SCR + 5120);
  const unsigned tb0 = fr * 128 + fsw, tb1 = tb0 ^ 64;
  WAIT_V(4);
  {
    const unsigned cb0 = tb0 + wid * 8192, cb1 = cb0 ^ 64;
    bf16x8 c3[8];
    f32x4 cs3[4];
    if (ZB) {
#pragma unroll
      for (int t = 0; t < 4; ++t) { c3[2 * t] = ld_frag(cb0 + t * 2048); c3[2 * t + 1] = ld_frag(cb1 + t * 2048); }
    }
    mm(15, 0); mm(15, 1);
    SCHED();
    if (ZB) {
#pragma unroll
      for (int t = 0; t < 4; ++t) cs3[t] = MFMA16(ONES, c3[2 * t], Z4);
#pragma unroll
      for (int t = 0; t < 4; ++t) cs3[t] = MFMA16(ONES, c3[2 * t + 1], cs3[t]);
    }
    SCHED();
#pragma unroll
    for (int i = 2; i < 10; ++i) mm(15, i);
    STAMP(3);
    unsigned i01 = 32 * wm + 16 * wn, i23 = 64 * kh + 16 * (2 * wn + (wm & 1));
    asm volatile("" : "+s"(i01), "+s"(i23));
    const float s1 = sel4(rsum, fr & 3), s2 = sel4(gram, fr & 3);
    const float mean = s1 * (1.0f / D);
    const float rstd = __builtin_amdgcn_rsqf(fmaxf(s2 * (1.0f / D) - mean * mean, 0.0f) + 1e-5f);
    if ((fr >> 2) == fq) { s_r[i01 + fr] = rstd; s_c[i01 + fr] = -mean * rstd; }
    if (fq < 2) s_u1[128 * fq + i23 + fr] = fq ? cs2[0] : cs1[0];
    if (ZB) s_u3[lane * 8 + wid] = sel4(f32x4{cs3[0][0], cs3[1][0], cs3[2][0], cs3[3][0]}, fq);
  }
  const int s0 = pm * 128 + 32 * wm;
  const rsrc_t rT = RSRC(a.ws + WS_Q, (2 * D + FH) * S * 2);
  const unsigned vT = ((fr >> 2) & 1) * 65536 + fq * 256 + (4 * (fr >> 3) + (fr & 3)) * 16;
  WL0();
  BAR();
  STAMP(4);
  if (wid == 7) dma4_sc1(rG, P1_SCR + 7168, 0, G_ANS + blockIdx.x * 4);
  earlyP(2); earlyP(3);
  f32x4 rq[2], cq[2];
  auto ld_rc = [&]() {
#pragma unroll
    for (int m = 0; m < 2; ++m) {
      rq[m] = *(LAS f32x4*)(s_r + 32 * wm + 16 * (m ^ wn) + 4 * fq);
      cq[m] = *(LAS f32x4*)(s_c + 32 * wm + 16 * (m ^ wn) + 4 * fq);
    }
  };
  ld_rc();
  auto store_t = [&](int T, f32x4 v0, f32x4 v1) {
    const unsigned a0 = cvt_pk(v0[0], v0[1]), a1 = cvt_pk(v0[2], v0[3]), b0 = cvt_pk(v1[0], v1[1]), b1 = cvt_pk(v1[2], v1[3]);
    const u32x4 o = wn ? u32x4{b0, b1, a0, a1} : u32x4{a0, a1, b0, b1};
    st16_sc1(o, rT, vT, (T >> 1) * 131072 + (T & 1) * 128 + s0 * 32);
  };
  auto lin_store = [&](f32x4 c0, f32x4 c1, float u, int T) { store_t(T, rq[0] * c0 + cq[0] * u, rq[1] * c1 + cq[1] * u); };
  float u1[2];
#pragma unroll
  for (int n = 0; n < 2; ++n) { const int ui = 16 * (2 * wn + (n ^ (wm & 1))) + fr; u1[n] = s_u1[ui] + s_u1[64 + ui]; }
  if (!ZB) {
#pragma unroll
    for (int n = 0; n < 2; ++n) {
      const int tile = 2 * wn + (n ^ (wm & 1)), ui = 16 * tile + fr;
      lin_store(acc1[0][n], acc1[1][n], u1[n], 56 + tile);
      lin_store(acc2[0][n], acc2[1][n], s_u2[ui] + s_u2[64 + ui], 60 + tile);
    }
    if (wid == 7) { WAIT_V(0); wait_go(rG, nonce, __builtin_amdgcn_readfirstlane(*(LAS unsigned*)(size_t)(P1_SCR + 7168 + lane * 4))); }
    STAMP(5); STAMP(6); STAMP(7); STAMP(8); STAMP(9);
    STAMP_OUT(a.ws, 0, 10);
    return;
  }
  {
    f32x4 ps[2], pq[2];
#pragma unroll
    for (int n = 0; n < 2; ++n)
#pragma unroll
      for (int m = 0; m < 2; ++m) {
        f32x4 v = rq[m] * acc1[m][n] + cq[m] * u1[n];
        const f32x2 g0 = gelu2(f32x2{v[0], v[1]}), g1 = gelu2(f32x2{v[2], v[3]});
        v = f32x4{g0[0], g0[1], g1[0], g1[1]};
        acc1[m][n] = v;
        if (n == 0) { ps[m] = v; pq[m] = v * v; } else { ps[m] += v; pq[m] += v * v; }
      }
    float t[8], h[4];
#define DPP2(d, x, y, ctl, m0, m1) "v_add_f32_dpp " d ", " x ", " x " " ctl " row_mask:0xf bank_mask:" m0 "\n\tv_add_f32_dpp " d ", " y ", " y " " ctl " row_mask:0xf bank_mask:" m1 "\n\t"
#define DPPQ(d, ctl) "v_add_f32_dpp " d ", " d ", " d " " ctl " row_mask:0xf bank_mask:0xf\n\t"
    asm volatile("s_nop 1\n\t" DPP2("%0", "%8", "%16", "row_ror:8", "0x3", "0xc") DPP2("%1", "%9", "%17", "row_ror:8", "0x3", "0xc")
                 DPP2("%2", "%10", "%18", "row_ror:8", "0x3", "0xc") DPP2("%3", "%11", "%19", "row_ror:8", "0x3", "0xc")
                 DPP2("%4", "%12", "%20", "row_ror:8", "0x3", "0xc") DPP2("%5", "%13", "%21", "row_ror:8", "0x3", "0xc")
                 DPP2("%6", "%14", "%22", "row_ror:8", "0x3", "0xc") DPP2("%7", "%15", "%23", "row_ror:8", "0x3", "0xc") "s_nop 1"
                 : "=&v"(t[0]), "=&v"(t[1]), "=&v"(t[2]), "=&v"(t[3]), "=&v"(t[4]), "=&v"(t[5]), "=&v"(t[6]), "=&v"(t[7])
                 : "v"(ps[0][0]), "v"(pq[0][0]), "v"(ps[0][1]), "v"(pq[0][1]), "v"(ps[0][2]), "v"(pq[0][2]), "v"(ps[0][3]), "v"(pq[0][3]),
                   "v"(ps[1][0]), "v"(pq[1][0]), "v"(ps[1][1]), "v"(pq[1][1]), "v"(ps[1][2]), "v"(pq[1][2]), "v"(ps[1][3]), "v"(pq[1][3]));
    asm volatile("s_nop 1\n\t" DPP2("%0", "%4", "%8", "row_half_mirror", "0x5", "0xa") DPP2("%1", "%5", "%9", "row_half_mirror", "0x5", "0xa")
                 DPP2("%2", "%6", "%10", "row_half_mirror", "0x5", "0xa") DPP2("%3", "%7", "%11", "row_half_mirror", "0x5", "0xa") "s_nop 1\n\t"
                 DPPQ("%0", "quad_perm:[1,0,3,2]") DPPQ("%1", "quad_perm:[1,0,3,2]") DPPQ("%2", "quad_perm:[1,0,3,2]") DPPQ("%3", "quad_perm:[1,0,3,2]")
                 DPPQ("%0", "quad_perm:[2,3,0,1]") DPPQ("%1", "quad_perm:[2,3,0,1]") DPPQ("%2", "quad_perm:[2,3,0,1]") DPPQ("%3", "quad_perm:[2,3,0,1]") "s_nop 0"
                 : "=&v"(h[0]), "=&v"(h[1]), "=&v"(h[2]), "=&v"(h[3])
                 : "v"(t[0]), "v"(t[1]), "v"(t[2]), "v"(t[3]), "v"(t[4]), "v"(t[5]), "v"(t[6]), "v"(t[7]));
    if ((fr & 3) == 0) *(LAS f32x4*)(s_z + (wn * 128 + 32 * wm + 8 * fq + 4 * ((fr >> 3) ^ wn) + 2 * ((fr >> 2) & 1)) * 2) = f32x4{h[0], h[1], h[2], h[3]};
  }
  WL0();
  BAR();
  STAMP(5);
  if (wid == 7) {
    __builtin_amdgcn_s_setprio(2);
    const f32x4 p = *(LAS f32x4*)(s_z + lane * 4) + *(LAS f32x4*)(s_z + 256 + lane * 4);
    WAIT_V(4);
    wait_go(rG, nonce, __builtin_amdgcn_readfirstlane(*(LAS unsigned*)(size_t)(P1_SCR + 7168 + lane * 4)));
    st16_sc1(__builtin_bit_cast(u32x4, p) | 1u, rG, lane * 16, G_PAY + (pm * 14 + cb) * 1024);
  }
  STAMP(6);
  {
    float u3[2];
    f32x4 acct[2][2] = {{Z4, Z4}, {Z4, Z4}};
    const unsigned t30 = tb0 + wn * 4096, t31 = tb1 + wn * 4096;
    bf16x8 w[8];
    auto rd3 = [&](int f) { const int kk = f >> 1; w[f & 7] = ld_frag(((kk & 1) ? t31 : t30) + (kk >> 1) * 8192 + (f & 1) * 2048); };
#pragma unroll
    for (int f = 0; f < 4; ++f) rd3(f);
    SCHED();
#pragma unroll
    for (int f = 0; f < 32; ++f) {
      const int kk = f >> 1, n = f & 1;
      acct[0][n] = MFMA16(xf[kk][0], w[f & 7], acct[0][n]);
      SCHED();
      if (f + 4 < 32) rd3(f + 4);
      if (f == 8 || f == 12) {
        const int m = (f - 8) >> 2;
        const float r = s_r[32 * wm + 16 * (m ^ wn) + fr], c = s_c[32 * wm + 16 * (m ^ wn) + fr];
#pragma unroll
        for (int q = 0; q < 2; ++q) {
          const int ui = 16 * (2 * wn + (q ^ (wm & 1))) + 4 * fq;
          z1v[m][q] = acc2[m][q] * r + (*(LAS f32x4*)(s_u2 + ui) + *(LAS f32x4*)(s_u2 + 64 + ui)) * c;
        }
      }
      if (f == 20 || f == 24) {
        const int q = (f - 20) >> 2;
        const f32x4 uu = *(LAS f32x4*)(s_u3 + (32 * wn + 16 * q + fr) * 8) + *(LAS f32x4*)(s_u3 + (32 * wn + 16 * q + fr) * 8 + 4);
        u3[q] = (uu[0] + uu[1]) + (uu[2] + uu[3]);
      }
      SCHED();
      acct[1][n] = MFMA16(xf[kk][1], w[f & 7], acct[1][n]);
      SCHED();
    }
    if (wid != 7) {
#pragma unroll
      for (int n = 0; n < 2; ++n) lin_store(acct[0][n], acct[1][n], u3[n], 4 * cb + 2 * wn + n);
    }
    STAMP(7);
  if (wid == 7) {
    f32x4 t;
    for (unsigned spin = 0; spin < SPIN; ++spin) {
      u32x4 pv[14];
#pragma unroll
      for (int i = 0; i < 14; ++i) pv[i] = __builtin_amdgcn_raw_buffer_load_b128(rG, lane * 16 + (i & 3) * 1024, G_PAY + pm * 14336 + (i >> 2) * 4096, 16);
      if (spin == 0) {
#pragma unroll
        for (int n = 0; n < 2; ++n) lin_store(acct[0][n], acct[1][n], u3[n], 4 * cb + 2 * wn + n);
      }
      u32x4 al = pv[0];
      t = __builtin_bit_cast(f32x4, pv[0]);
#pragma unroll
      for (int i = 1; i < 14; ++i) { al &= pv[i]; t += __builtin_bit_cast(f32x4, pv[i]); }
      if (__all((al[0] & al[1] & al[2] & al[3] & 1u) != 0)) break;
      asm volatile("" ::: "memory");
    }
    const float m0 = t[0] * (1.0f / FH), m1 = t[2] * (1.0f / FH);
    const float r0 = __builtin_amdgcn_rsqf(fmaxf(t[1] * (1.0f / FH) - m0 * m0, 0.0f) + 1e-5f);
    const float r1 = __builtin_amdgcn_rsqf(fmaxf(t[3] * (1.0f / FH) - m1 * m1, 0.0f) + 1e-5f);
    *(LAS f32x2*)(s_n + 2 * lane) = f32x2{r0, r1};
    *(LAS f32x2*)(s_n + 128 + 2 * lane) = f32x2{-m0 * r0, -m1 * r1};
    __builtin_amdgcn_s_setprio(0);
  }
  }
  WL0();
  BAR();
  STAMP(8);
  {
    f32x4 r2[2], c2[2];
#pragma unroll
    for (int m = 0; m < 2; ++m) {
      r2[m] = *(LAS f32x4*)(s_n + 32 * wm + 8 * fq + 4 * (m ^ wn));
      c2[m] = *(LAS f32x4*)(s_n + 128 + 32 * wm + 8 * fq + 4 * (m ^ wn));
    }
#pragma unroll
    for (int n = 0; n < 2; ++n) store_t(64 + 4 * cb + 2 * wn + (n ^ (wm & 1)), acc1[0][n] * r2[0] + c2[0], acc1[1][n] * r2[1] + c2[1]);
  }
  STAMP(9);
  STAMP_OUT(a.ws, 0, 10);
}

__device__ __forceinline__ void warm_window(const Args& a, int junk, int rb, int w, int wv, int lane) {
  if (w < 8) {
    const unsigned vo = (rb * 128 + wv * 32 + (lane >> 2)) * (S * 2) + (lane & 3) * 128;
#pragma unroll
    for (int m = 0; m < 3; ++m) {
      const rsrc_t r = RSRC(m == 0 ? a.wsr : m == 1 ? a.wsi : a.wsp, S * S * 2);
      touch(r, junk, vo, w * 512);
      touch(r, junk, vo, w * 512 + 16 * (S * 2));
    }
  } else {
    const rsrc_t r = RSRC(a.wout + (size_t)rb * 56 * D, 56 * D * 2);
    touch(r, junk, lane * 128, wv * 16384);
    touch(r, junk, lane * 128, wv * 16384 + 8192);
  }
}

__global__ __launch_bounds__(512, 2) void fused(Args a) {
  extern __shared__ __attribute__((aligned(1024))) char smem[];
  asm volatile("" ::"v"(smem));
  const unsigned long long pstart = __builtin_amdgcn_s_memrealtime();
  const int bx = blockIdx.x & 7, bi = blockIdx.x >> 3;
  if (bi < CODEWARM && threadIdx.x < 64) {
    unsigned long long pc;
    asm volatile("s_getpc_b64 %0" : "=s"(pc));
    touch(RSRC(pc & ~127ull, CODEWARM * 8192), 1024, threadIdx.x * 128, bi * 8192);
  }
  const int pm = 4 * (bx >> 1) + (bi >> 3), cb = 2 * (bi & 7) + (bx & 1);
  int mat, tm, tn;
  if (bx & 1) { mat = 2; tm = pm; tn = bi & 7; }
  else { mat = bx >> 2; tm = ((bx >> 1) & 1) * 8 + (bi >> 2); tn = bi & 3; }
  const int kx = (tm ^ tn) & K2_KXMASK;
  const u16* Pp = (mat == 0 ? a.wsr : mat == 1 ? a.wsi : a.wsp) + (size_t)tm * 128 * S;
  f32x4 z1v[2][2];
  {
    const int kx1 = ((bi & 7) ^ (2 * (bi >> 3))) & K1_KXMASK;
    if (__builtin_expect(cb < 14, 1)) phase1<true>(a, pm, cb, kx1, Pp, kx, z1v);
    else if (cb == 14) phase1<false>(a, pm, cb, kx1, Pp, kx, z1v);
  }
  int tid = threadIdx.x;
  asm volatile("" : "+v"(tid));
  const int lane = tid & 63;
  const int wid = __builtin_amdgcn_readfirstlane(tid >> 6);
  STAMP_DECL STAMP(0);
  const int tm3 = bx * 4 + (bi >> 3), tn3 = bi & 7;
  const int fr = lane & 15, fq = lane >> 4;
  const unsigned vl = lane * 16;
  const rsrc_t rG = RSRC(a.ws + WS_G, G_END);
  const bool work2 = cb != 15;
  auto z1_finish = [&]() {
    if (cb < 14) {
      const int wm = wid & 3, wn = wid >> 2;
      const rsrc_t rZ = RSRC(a.ws + WS_Z1, FH * S * 2);
      const unsigned vZ = (fr >> 3) * (112 * 256) + (2 * ((fq & 1) ^ (wm & 1)) + (fq >> 1)) * 256 + (fr & 7) * 16;
      const unsigned sZ = ((pm * 8 + 2 * wm) * 112 + 8 * cb + 4 * wn) * 256;
#pragma unroll
      for (int m = 0; m < 2; ++m) {
        unsigned pk[2][2];
#pragma unroll
        for (int n = 0; n < 2; ++n) {
          const f32x2 g0 = gelu2(f32x2{z1v[m][n][0], z1v[m][n][1]}), g1 = gelu2(f32x2{z1v[m][n][2], z1v[m][n][3]});
          pk[n][0] = cvt_pk(g0[0], g0[1]); pk[n][1] = cvt_pk(g1[0], g1[1]);
        }
        auto w0 = __builtin_amdgcn_permlane16_swap(pk[0][0], pk[1][0], false, false);
        auto w1 = __builtin_amdgcn_permlane16_swap(pk[0][1], pk[1][1], false, false);
        st16_sc1(u32x4{w0[0], w1[0], w0[1], w1[1]}, rZ, vZ, sZ + 128 * (m ^ wn));
      }
    }
  };
  if (blockIdx.x == 255) {
    for (int i = 0; i < 36; ++i) st16_sc1(u32x4{0, 0, 0, 0}, rG, vl, (wid * 36 + i) * 1024);
    st16_sc1(u32x4{0, 0, 0, 0}, rG, vl, G_ALL);
  }
  unsigned bv[4];
#pragma unroll
  for (int ni = 0; ni < 4; ++ni) bv[ni] = a.bsp[(8 * tm + 4 * ((wid >> 1) & 1) + ni) * 16 + fr];
  asm volatile("" : "+v"(bv[0]), "+v"(bv[1]), "+v"(bv[2]), "+v"(bv[3]));
  if (work2 || blockIdx.x == 255) {
    WAIT_V(0);
    WL0();
    BAR();
  }
  if (wid >= 4) {
    const int lw = wid - 4;
    const int r8 = lane >> 3, p = lane & 7;
    if (wid == 7) {
      if (!work2) {
        if (blockIdx.x == 255) {
          for (unsigned spin = 0; spin < SPIN; ++spin) {
            const u32x4 rq = ld16_sc1(rG, vl, G_REQ);
            st16_sc1(rq, rG, vl, G_ANS);
            const u32x4 fl = ld16_sc1(rG, vl, G_ALL);
            if (__all((fl[0] & fl[1] & fl[2] & (lane == 63 ? 1u : fl[3])) == 1u)) break;
          }
        } else {
          const unsigned nonce = __builtin_amdgcn_readfirstlane(ld4_sc1(rG, 0, G_ANS + blockIdx.x * 4)) + 1;
          st4_sc1(nonce, rG, 0, G_REQ + blockIdx.x * 4);
          wait_go(rG, nonce, nonce - 1);
        }
      }
      const unsigned box = (fq & 1) * 128 + (cb >> 1) * 16 + fr;
      st4_sc1(1u, rG, fq < 2 ? box * 128 + (pm * 2 + (cb & 1)) * 4 : G_ALL + blockIdx.x * 4, 0);
    }
    const rsrc_t rP = RSRC(Pp, 128 * S * 2);
    const unsigned vo_e = (lw * 32 + r8) * (S * 2) + ((p ^ (r8 >> 1)) << 4);
    const unsigned vo_o = (vo_e ^ 64) + 8 * (S * 2);
    auto stageP = [&](int kt, unsigned poff) {
      const unsigned mb = poff + lw * 4096, so = (kt ^ kx) * 128;
#pragma unroll
      for (int j = 0; j < 4; ++j) dma16(rP, mb, P2_PB + j * 1024, (j & 1) ? vo_o : vo_e, so + (j >> 1) * 16 * (S * 2));
    };
    if (work2) {
      BAR();
      stageP(4, 0); stageP(5, 16384);
    }
    const rsrc_t rWo = RSRC(a.wout + 64 * tn3, (FH * D - 64 * tn3) * 2);
    const unsigned vw_e = (32 * lw + r8) * (D * 2) + ((p ^ (((r8 >> 1) & 1) << 1)) << 4), vw_o = (vw_e ^ 64) + 8 * (D * 2);
    u32x4 wreg[12];
#pragma unroll
    for (int i = 0; i < 12; ++i) wreg[i] = u32x4{0, 0, 0, 0};
    auto wput = [&](int rg) {
#pragma unroll
      for (int j = 0; j < 4 && !NOPARK; ++j) *(LAS u32x4*)(size_t)(rg * 16384 + lw * 4096 + j * 1024 + vl) = wreg[(rg == 6 ? 8 : (rg - 2) * 4) + j];
    };
    auto wdma = [&](int rg) {
#pragma unroll
      for (int j = 0; j < 4; ++j) dma16(rWo, lw * 4096, rg * 16384 + j * 1024, (j & 1) ? vw_o : vw_e, (rg * 128 + (j >> 1) * 16) * (D * 2));
    };
    if (work2) {
#pragma unroll
      for (int i = 0; i < 12; ++i) {
        const int rg = i < 8 ? 2 + (i >> 2) : 6;
        ld16_async(wreg[i], rWo, (i & 1) ? vw_o : vw_e, (rg * 128 + ((i >> 1) & 1) * 16) * (D * 2));
      }
      if (Z1EARLY && wid != 7) z1_finish();
      if (wid == 7) {
        const unsigned box = (mat * 4 + tn) * 16 + tm;
        for (unsigned spin = 0; spin < SPIN; ++spin) {
          const unsigned fl = ld4_sc1(rG, (lane & 31) * 4, G_F1 + box * 128);
          if (__all(fl == 1u || (box >= 112 && box < 128 && (lane & 1)))) break;
        }
      }
      BAR();
      STAMP(1);
      const rsrc_t rQ = RSRC(a.ws + WS_Q + (size_t)(mat * 4 + tn) * 128 * S * 2, 128 * S * 2);
      const unsigned vq = vl + lw * 131072;
      auto stageQ = [&](int kt) {
        const unsigned mb = (kt & 3) * 16384 + lw * 4096, so = (kt ^ kx) * 2048;
#pragma unroll
        for (int j = 0; j < 4; ++j) dma16_sc1(rQ, mb, j * 1024, vq, so + (j >> 1) * 65536 + (j & 1) * 1024);
      };
      stageQ(0); stageQ(1); stageQ(2); stageQ(3);
      if (!Z1EARLY || wid == 7) z1_finish();
      WAIT_V(12); BAR();
      WAIT_V(8); BAR(); stageQ(4); stageP(6, 2 * 16384);
      WAIT_V(12); BAR(); stageQ(5); stageP(7, 3 * 16384);
      WAIT_V(16); BAR(); stageQ(6); stageP(8, 4 * 16384);
      unsigned poff = 5 * 16384;
      unsigned zf;
      for (int kt = 3; kt < 23; ++kt) {
        WAIT_V(20);
        BAR();
        if (kt == 3 && wid == 7 && cb < 14) st4_sc1(1u, rG, 0, G_FZ + (pm * 8 + (cb >> 1)) * 128 + (cb & 1) * 4);
        stageQ(kt + 4); stageP(kt + 6, poff);
        poff = poff == 5 * 16384 ? 0 : poff + 16384;
      }
      asm volatile("buffer_load_dword %0, %1, %2, %3 offen sc1" : "=v"(zf) : "v"((lane & 1) * 4), "s"(rG), "s"(G_FZ + (tm * 8 + tn) * 128) : "memory");
#pragma unroll
      for (int kt = 23; kt < 26; ++kt) {
        WAIT_V(20);
        BAR();
        stageQ(kt + 4); stageP(kt + 6, ((kt + 2) % 6) * 16384);
      }
      asm volatile("s_waitcnt vmcnt(20)" : "+v"(zf)::"memory");
      if (mat == 2) {
        const rsrc_t rZ = RSRC(a.ws + WS_Z1 + (size_t)((8 * tm + lw) * 112 + 16 * tn) * 256, 4 * 112 * 256 + 4096);
        auto stageZ = [&](int h) {
#pragma unroll
          for (int j = 0; j < 4; ++j) dma16_sc1(rZ, lw * 4096, P2_Z + h * 16384 + j * 1024, vl, h * (4 * 112 * 256) + j * 1024);
        };
        for (unsigned spin = 0; !__all(zf == 1u) && spin < SPIN; ++spin) zf = ld4_sc1(rG, (lane & 1) * 4, G_FZ + (tm * 8 + tn) * 128);
        WAIT_V(20); BAR(); stageQ(30); stageZ(0);
        WAIT_V(20); BAR(); stageQ(31); stageZ(1);
        WAIT_V(20); BAR(); wdma(0); wdma(4);
        WAIT_V(20); BAR(); wdma(1); wdma(5);
        WAIT_V(20); BAR(); wput(2); wput(6);
        WAIT_V(16); BAR(); wput(3);
      } else {
        WAIT_V(20); BAR(); stageQ(30);
        WAIT_V(16); BAR(); stageQ(31);
        WAIT_V(12); BAR(); wdma(0); wdma(4);
        WAIT_V(12); BAR(); wdma(1); wdma(5);
        WAIT_V(16); BAR(); wput(2); wput(6);
        BAR(); wput(3);
      }
    } else {
      STAMP(1);
      wdma(0); wdma(4); wdma(1); wdma(5); wdma(2); wdma(3); wdma(6);
    }
    STAMP(2); STAMP(3);
    WAIT_V(0);
  } else if (work2) {
    const int wp = wid >> 1, wq = wid & 1;
    f32x4 acc[4][4];
#pragma unroll
    for (int ni = 0; ni < 4; ++ni) {
      const float bias = mat == 2 ? __uint_as_float(bv[ni] << 16) : 0.0f;
#pragma unroll
      for (int n = 0; n < 4; ++n) acc[ni][n] = f32x4{bias, bias, bias, bias};
    }
    BAR();
    if (DWARM && cb == 14) warm_window(a, 0, pm, 2, wid, lane);
    const int sw = (fr >> 1) & 7;
    const unsigned ra = P2_PB + (wp * 64 + fr) * 128 + ((fq ^ sw) << 4);
    const unsigned rb = wq * 8192 + vl;
    bf16x8 a0[4], b0[4], a1[4], b1[4];
    unsigned poff = 2 * 16384;
    unsigned pa0 = ra + poff, pa1 = pa0 ^ 64;
#define K2_RD(AF, BF, pa, sl, ks, i) do { \
    if ((i) == 0) AF[0] = ld_frag(pa); \
    else if ((i) <= 4) BF[(i) - 1] = ld_frag(rb + (sl) * 16384 + ((i) - 1) * 2048 + (ks) * 1024); \
    else AF[(i) - 4] = ld_frag(pa + ((i) - 4) * 2048); } while (0)
#define K2_MM(AF, BF, i) acc[(i) >> 2][(i) & 3] = MFMA16(BF[(i) & 3], AF[(i) >> 2], acc[(i) >> 2][(i) & 3])
#define K2_STEP(s, LAST) do { \
    SCHED(); WL0(); SCHED(); \
    _Pragma("unroll") for (int i = 0; i < 16; ++i) { K2_MM(a0, b0, i); SCHED(); if (i < 8) K2_RD(a1, b1, pa1, s, 1, i); \
      if (i == 10) { poff = poff == 5 * 16384 ? 0 : poff + 16384; pa0 = ra + poff; } SCHED(); } \
    WL0(); \
    if ((s) == 3) WAIT_V(0); \
    BAR(); \
    SCHED(); \
    _Pragma("unroll") for (int i = 0; i < 16; ++i) { K2_MM(a1, b1, i); SCHED(); if (!(LAST) && i < 8) K2_RD(a0, b0, pa0, ((s) + 1) & 3, 0, i); \
      if (i == 10) pa1 = pa0 ^ 64; SCHED(); } \
  } while (0)
    if (DWARM && cb == 14) WAIT_V(0);
    if (Z1EARLY) z1_finish();
    BAR();
    if (!Z1EARLY) z1_finish();
    BAR();
    STAMP(1);
#pragma unroll
    for (int i = 0; i < 8; ++i) K2_RD(a0, b0, pa0, 0, 0, i);
    for (int kt = 0; kt < 28; kt += 4) { K2_STEP(0, 0); K2_STEP(1, 0); K2_STEP(2, 0); K2_STEP(3, 0); }
    K2_STEP(0, 0); K2_STEP(1, 0); K2_STEP(2, 0); K2_STEP(3, 1);
    SCHED();
    STAMP(2);
    if (mat < 2) {
      const rsrc_t rY = RSRC(a.ws + WS_Y + (size_t)mat * S * D * 2, S * D * 2);
      const unsigned so = ((8 * tm + 4 * wp) * 64 + 16 * tn + 8 * wq) * 256;
#pragma unroll
      for (int ni = 0; ni < 4; ++ni)
#pragma unroll
        for (int P = 0; P < 2; ++P) {
          const f32x4 c0 = acc[ni][2 * P], c1 = acc[ni][2 * P + 1];
          const u32x4 v = {cvt_pk(c0[0], c0[1]), cvt_pk(c0[2], c0[3]), cvt_pk(c1[0], c1[1]), cvt_pk(c1[2], c1[3])};
          st16_sc1(v, rY, vl, so + (ni * 64 + 4 * P) * 256);
        }
    } else {
      const rsrc_t rH = RSRC(a.ws + WS_H, S * FH * 2);
      const unsigned so = ((8 * tm + 4 * wp) * 112 + 16 * tn + 8 * wq) * 256;
      const unsigned za = P2_Z + wp * 16384 + wq * 2048 + (fq * 16 + ((fr & 4) << 1) + ((fr & 8) >> 1) + (fr & 3)) * 16;
#pragma unroll
      for (int ni = 0; ni < 4; ++ni) {
#pragma unroll
        for (int P = 0; P < 2; ++P) {
          const f32x4 c0 = acc[ni][2 * P], c1 = acc[ni][2 * P + 1];
          const u32x4 z = *(const LAS u32x4*)(size_t)(za + ni * 4096 + P * 1024);
          const u32x4 v = {cvt_pk(bf_lo(z[0]) * c0[0], bf_hi(z[0]) * c0[1]), cvt_pk(bf_lo(z[1]) * c0[2], bf_hi(z[1]) * c0[3]),
                           cvt_pk(bf_lo(z[2]) * c1[0], bf_hi(z[2]) * c1[1]), cvt_pk(bf_lo(z[3]) * c1[2], bf_hi(z[3]) * c1[3])};
          st16_sc1(v, rH, vl, so + (ni * 112 + 4 * P) * 256);
        }
      }
    }
    STAMP(3);
    WAIT_V(0);
  } else {
    STAMP(1);
    for (int i = 0; i < PF_N && blockIdx.x != 255; ++i) {
      warm_window(a, P2_Z, pm, (PF_LIST >> (4 * i)) & 15, wid, lane);
      WAIT_V(6);
    }
    STAMP(2);
    WAIT_V(0);
    STAMP(3);
  }
  WL0();
  BAR();
  STAMP(4);
  {
    const int wr = (wid + tn3) & 7, tp = wr >> 2, kq = wr & 3;
    const int tt3 = 4 * tm3 + 2 * tp + (kq & 1);
    const int cq = 2 * (fq & 1) + (fq >> 1);
    const int c3 = 8 * tn3 + 2 * (kq & 2);
    if (wid == 4 && work2) {
      const unsigned box = mat == 2 ? fr : (fr & 8) + 2 * tn + (fr & 1);
      st4_sc1(1u, rG, box * 128, G_FP + tm * 2048 + (mat == 2 ? tn : 8 + mat) * 4);
    }
    const int q4 = fr >> 2, pp = lane & 3;
    const int sp = ((q4 >> 1) & 1) | ((fq & 1) << 1);
    const unsigned wa = kq * 4096 + 128 * (8 * fq + q4) + 16 * ((2 * (kq & 2) + (pp >> 1)) ^ (sp << 1)) + 8 * (pp & 1);
    bf16x8 wf[7][4];
    auto ld_w = [&]() {
#pragma unroll
      for (int mi = 0; mi < 4; ++mi) {
        unsigned lo = wa ^ (mi * 32), hi = lo + 65536;
        OPAQUE(hi);
#pragma unroll
        for (int g = 0; g < 7; ++g) wf[g][mi] = ld_tr((g < 4 ? lo : hi) + (g & 3) * 16384);
      }
    };
    const rsrc_t rY = RSRC(a.ws + WS_Y + (size_t)(tt3 * 64 + c3) * 256, S * D * 2 + 1024);
    const unsigned vy = cq * 256 + fr * 16;
    unsigned fl;
    asm volatile("buffer_load_dword %0, %1, %2, %3 offen sc1" : "=v"(fl) : "v"(fr * 4), "s"(rG), "s"(G_FP + (tm3 * 8 + tn3) * 128) : "memory");
    const u32x4 ex = *(const u32x4*)(a.x + (size_t)(tt3 * 16 + fr) * D + (c3 + cq) * 8);
    ld_w();
    asm volatile("s_waitcnt vmcnt(1)" : "+v"(fl)::"memory");
    u32x4 e1, e2;
    const bool gy = YEARLY && __all(fl == 1u || fr < 8 || fr > 9);
    if (gy) { e1 = __builtin_amdgcn_raw_buffer_load_b128(rY, vy, 0, 16); e2 = __builtin_amdgcn_raw_buffer_load_b128(rY, vy, S * D * 2, 16); }
    if (wid == 4) {
      for (unsigned spin = 0; !__all(fl == 1u || fr == 7 || fr > 9) && spin < SPIN; ++spin) fl = ld4_sc1(rG, fr * 4, G_FP + (tm3 * 8 + tn3) * 128);
    }
    BAR();
    STAMP(5);
    const rsrc_t rH = RSRC(a.ws + WS_H + (size_t)((4 * tm3 + 2 * tp) * 112 + 4 * kq) * 256, 2 * 112 * 256);
    const unsigned vh0 = vl + (kq & 1) * (112 * 256), vh1 = vl + ((kq & 1) ^ 1) * (112 * 256);
    if (!gy) { e1 = __builtin_amdgcn_raw_buffer_load_b128(rY, vy, 0, 16); e2 = __builtin_amdgcn_raw_buffer_load_b128(rY, vy, S * D * 2, 16); }
    u32x4 hf[7][2];
#pragma unroll
    for (int g = 0; g < 7; ++g) {
      hf[g][0] = __builtin_amdgcn_raw_buffer_load_b128(rH, vh0, g * 4096, 16);
      hf[g][1] = __builtin_amdgcn_raw_buffer_load_b128(rH, vh1, g * 4096, 16);
      SCHED();
    }
    f32x4 acc[2][4];
#pragma unroll
    for (int g = 0; g < 7; ++g)
#pragma unroll
      for (int tl = 0; tl < 2; ++tl)
#pragma unroll
        for (int mi = 0; mi < 4; ++mi) acc[tl][mi] = MFMA16(wf[g][mi], __builtin_bit_cast(bf16x8, hf[g][tl]), (g ? acc[tl][mi] : f32x4{0, 0, 0, 0}));
    STAMP(6);
    LAS f32x4* xb = (LAS f32x4*)(size_t)(P3_X) + lane;
#pragma unroll
    for (int d = 1; d < 4; ++d) {
      xb[((wr * 3 + d - 1) * 2) * 64] = acc[d & 1][d & 2];
      xb[((wr * 3 + d - 1) * 2 + 1) * 64] = acc[d & 1][(d & 2) + 1];
    }
    WL0();
    BAR();
    f32x4 s0 = acc[0][0], s1 = acc[0][1];
#pragma unroll
    for (int d = 1; d < 4; ++d) {
      s0 += xb[(((wr ^ d) * 3 + d - 1) * 2) * 64];
      s1 += xb[(((wr ^ d) * 3 + d - 1) * 2 + 1) * 64];
    }
    f32x4 c0, c1;
#pragma unroll
    for (int j = 0; j < 4; ++j) {
      auto w = __builtin_amdgcn_permlane16_swap(__float_as_uint(s0[j]), __float_as_uint(s1[j]), false, false);
      c0[j] = __uint_as_float(w[0]); c1[j] = __uint_as_float(w[1]);
    }
    u32x4 o;
#pragma unroll
    for (int k = 0; k < 4; ++k) {
      const f32x4 c = (k >> 1) ? c1 : c0;
      const float lo = bf_lo(ex[k]) + bf_lo(e1[k]) - bf_lo(e2[k]) + c[(k & 1) * 2];
      const float hi = bf_hi(ex[k]) + bf_hi(e1[k]) - bf_hi(e2[k]) + c[(k & 1) * 2 + 1];
      o[k] = cvt_pk(lo, hi);
    }
    st16_sc1(o, RSRC(a.out, S * D * 2), ((tt3 * 16 + fr) * D + (c3 + cq) * 8) * 2, 0);
  }
  STAMP(7);
  STAMP_OUT(a.ws, 1, 8);
  {
    asm volatile("s_waitcnt vmcnt(0)" ::: "memory");
    const unsigned long long pend = __builtin_amdgcn_s_memrealtime();
    const rsrc_t rS = RSRC(a.ws + (100u << 20), 65536);
    st16_sc1(u32x4{(unsigned)pstart, (unsigned)pend, 0, 0}, rS, 0, (blockIdx.x * 8 + wid) * 16);
    if (blockIdx.x == 73 && wid == 0) {
      while (__builtin_amdgcn_s_memrealtime() < pstart + 6000) __builtin_amdgcn_s_sleep(1);
      int mns = 1 << 30, mxe = -(1 << 30);
      for (int i = 0; i < 32; ++i) {
        const u32x4 v = ld16_sc1(rS, lane * 16, i * 1024);
        const int st = (int)(v[0] - (unsigned)pstart), en = (int)(v[1] - (unsigned)pstart);
        mns = min(mns, st); mxe = max(mxe, en);
      }
      for (int o = 32; o > 0; o >>= 1) { mns = min(mns, __shfl_xor(mns, o)); mxe = max(mxe, __shfl_xor(mxe, o)); }
      const int q = __builtin_amdgcn_readfirstlane(mxe - mns);
      const unsigned long long tend = pstart + __builtin_amdgcn_readfirstlane(mns) + 10 * q;
      while (__builtin_amdgcn_s_memrealtime() < tend) ;
    }
  }
}

extern "C" void kernel_launch(void* const* d_in, const int* in_sizes, int n_in, void* d_out, int out_size, void* d_ws, size_t ws_size,
                              hipStream_t stream) {
  (void)in_sizes; (void)n_in; (void)out_size; (void)ws_size;
  (void)hipFuncSetAttribute((const void*)fused, hipFuncAttributeMaxDynamicSharedMemorySize, LDS_BYTES);
  Args a{(const u16*)d_in[0], (const u16*)d_in[3], (const u16*)d_in[4], (const u16*)d_in[6], (const u16*)d_in[9],
         (const u16*)d_in[1], (const u16*)d_in[2], (const u16*)d_in[7], (const u16*)d_in[8], (char*)d_ws, (u16*)d_out};
  fused<<<256, 512, LDS_BYTES, stream>>>(a);
}
```

```cpp
#include <hip/hip_runtime.h>
#include <hip/hip_bf16.h>
#include <stdint.h>

typedef unsigned short u16;
typedef short bf16x8 __attribute__((ext_vector_type(8)));
typedef short s16x4 __attribute__((ext_vector_type(4)));
typedef float f32x4 __attribute__((ext_vector_type(4)));
typedef float f32x2 __attribute__((ext_vector_type(2)));
typedef unsigned u32x4 __attribute__((ext_vector_type(4)));
typedef __bf16 bf16x2v __attribute__((ext_vector_type(2)));
typedef __amdgpu_buffer_rsrc_t rsrc_t;
#define LAS __attribute__((address_space(3)))
#define WAIT_V(n) asm volatile("s_waitcnt vmcnt(%0)" ::"n"(n) : "memory")
#define WL0() __builtin_amdgcn_s_waitcnt(0xC07F)
#define BAR() do { asm volatile("" ::: "memory"); __builtin_amdgcn_s_barrier(); asm volatile("" ::: "memory"); } while (0)
#define SCHED() __builtin_amdgcn_sched_barrier(0)
#define MFMA16(a, b, c) __builtin_amdgcn_mfma_f32_16x16x32_bf16(a, b, c, 0, 0, 0)
#define RSRC(p, n) __builtin_amdgcn_make_buffer_rsrc((void*)(p), 0, (unsigned)(n), 0x00020000)
#define OPAQUE(x) asm volatile("" : "+v"(x))

constexpr int K2_KXMASK = 7;
constexpr int K1_KXMASK = 3;
constexpr int CODEWARM = 5;
constexpr unsigned PF_LIST = 0x764810u;
constexpr int PF_N = 6;

constexpr int S = 2048, D = 512, F = 1792, FH = 896;
constexpr size_t WS_Q = 0;
constexpr size_t WS_Z1 = WS_Q + (size_t)(2 * D + FH) * S * 2;
constexpr size_t WS_H = WS_Z1 + (size_t)FH * S * 2;
constexpr size_t WS_Y = WS_H + (size_t)FH * S * 2;
constexpr size_t WS_G = WS_Y + (size_t)2 * D * S * 2;
constexpr unsigned G_F1 = 0;
constexpr unsigned G_FP = 32768;
constexpr unsigned G_FZ = 32768 + 64;
constexpr unsigned G_PAY = 65536;
constexpr unsigned G_ALL = G_PAY + 16 * 14336;
constexpr unsigned G_REQ = G_ALL + 1024;
constexpr unsigned G_ANS = G_REQ + 1024;
constexpr unsigned G_END = G_ANS + 1024;

constexpr int LDS_BYTES = 163840;
constexpr int P1_RING = 32768, P1_SCR = 81920;
constexpr int P2_PB = 65536, P2_Z = 131072, P3_X = 114688;
constexpr unsigned SPIN = 1u << 20;

struct Args {
  const u16* x; const u16* wdr; const u16* wdi; const u16* win; const u16* wout;
  const u16* wsr; const u16* wsi; const u16* wsp; const u16* bsp;
  char* ws; u16* out;
};

__device__ __forceinline__ unsigned cvt_pk(float lo, float hi) {
  f32x2 v = {lo, hi};
  return __builtin_bit_cast(unsigned, __builtin_convertvector(v, bf16x2v));
}
__device__ __forceinline__ float bf_lo(unsigned u) { return __uint_as_float(u << 16); }
__device__ __forceinline__ float bf_hi(unsigned u) { return __uint_as_float(u & 0xffff0000u); }
constexpr float GC1 = -2.0f * 0.7978845608028654f * 1.4426950408889634f, GC2 = GC1 * 0.044715f;
__device__ __forceinline__ f32x2 gelu2(f32x2 v) {
  const f32x2 t = v * ((v * v) * GC2 + GC1);
  const f32x2 e = f32x2{__builtin_amdgcn_exp2f(t[0]), __builtin_amdgcn_exp2f(t[1])} + 1.0f;
  return v * f32x2{__builtin_amdgcn_rcpf(e[0]), __builtin_amdgcn_rcpf(e[1])};
}
__device__ __forceinline__ float sel4(f32x4 v, int i) { const float a = (i & 1) ? v[1] : v[0], b = (i & 1) ? v[3] : v[2]; return (i & 2) ? b : a; }

__device__ __forceinline__ void dma16(rsrc_t r, unsigned mbase, int mimm, unsigned voff, unsigned soff) {
  asm volatile("s_add_i32 m0, %0, %1\n\ts_nop 0\n\tbuffer_load_dwordx4 %2, %3, %4 offen lds" ::"s"(mbase), "n"(mimm), "v"(voff), "s"(r), "s"(soff) : "scc");
}
__device__ __forceinline__ void dma16_sc1(rsrc_t r, unsigned mbase, int mimm, unsigned voff, unsigned soff) {
  asm volatile("s_add_i32 m0, %0, %1\n\ts_nop 0\n\tbuffer_load_dwordx4 %2, %3, %4 offen sc1 lds" ::"s"(mbase), "n"(mimm), "v"(voff), "s"(r), "s"(soff) : "scc");
}
__device__ __forceinline__ void dma4_sc1(rsrc_t r, int ldsaddr, unsigned voff, unsigned soff) {
  asm volatile("s_mov_b32 m0, %0\n\ts_nop 0\n\tbuffer_load_dword %1, %2, %3 offen sc1 lds" ::"n"(ldsaddr), "v"(voff), "s"(r), "s"(soff) : "memory");
}
__device__ __forceinline__ void touch(rsrc_t r, int junk, unsigned voff, unsigned soff) {
  asm volatile("s_mov_b32 m0, %0\n\ts_nop 0\n\tbuffer_load_dword %1, %2, %3 offen lds" ::"n"(junk), "v"(voff), "s"(r), "s"(soff) : "memory");
}
__device__ __forceinline__ void st16_sc1(u32x4 v, rsrc_t r, unsigned voff, unsigned soff) {
  asm volatile("buffer_store_dwordx4 %0, %1, %2, %3 offen sc1\n\ts_nop 1" ::"v"(v), "v"(voff), "s"(r), "s"(soff) : "memory");
}
__device__ __forceinline__ void st4_sc1(unsigned v, rsrc_t r, unsigned voff, unsigned soff) {
  asm volatile("buffer_store_dword %0, %1, %2, %3 offen sc1" ::"v"(v), "v"(voff), "s"(r), "s"(soff) : "memory");
}
__device__ __forceinline__ unsigned ld4_sc1(rsrc_t r, unsigned voff, unsigned soff) {
  unsigned v;
  asm volatile("buffer_load_dword %0, %1, %2, %3 offen sc1\n\ts_waitcnt vmcnt(0)" : "=&v"(v) : "v"(voff), "s"(r), "s"(soff) : "memory");
  return v;
}
__device__ __forceinline__ u32x4 ld16_sc1(rsrc_t r, unsigned voff, unsigned soff) {
  u32x4 v;
  asm volatile("buffer_load_dwordx4 %0, %1, %2, %3 offen sc1\n\ts_waitcnt vmcnt(0)" : "=&v"(v) : "v"(voff), "s"(r), "s"(soff) : "memory");
  return v;
}
__device__ __forceinline__ void ld16_async(u32x4& v, rsrc_t r, unsigned voff, unsigned soff) {
  asm volatile("buffer_load_dwordx4 %0, %1, %2, %3 offen" : "=v"(v) : "v"(voff), "s"(r), "s"(soff) : "memory");
}
__device__ __forceinline__ void wait_vn(int n) { WAIT_V(n); }
__device__ __forceinline__ bf16x8 ld_tr(unsigned addr) {
  s16x4 lo = __builtin_amdgcn_ds_read_tr16_b64_v4i16((LAS s16x4*)(size_t)(addr));
  s16x4 hi = __builtin_amdgcn_ds_read_tr16_b64_v4i16((LAS s16x4*)(size_t)(addr + 512));
  return __builtin_shufflevector(lo, hi, 0, 1, 2, 3, 4, 5, 6, 7);
}
__device__ __forceinline__ bf16x8 ld_frag(unsigned addr) { return *(const LAS bf16x8*)(size_t)(addr); }
__device__ __forceinline__ void wait_go(rsrc_t rG, unsigned nonce, unsigned seen) {
  for (unsigned spin = 0; seen != nonce && spin < SPIN; ++spin) {
    seen = __builtin_amdgcn_readfirstlane(ld4_sc1(rG, 0, G_ANS + blockIdx.x * 4));
    __builtin_amdgcn_s_sleep(1);
  }
}

template <bool ZB>
__device__ __forceinline__ void phase1(const Args& a, int pm, int cb, int kx, const u16* Pp, int kx2, f32x4 (&z1v)[2][2]) {
  const int tid = threadIdx.x, lane = tid & 63;
  const int wid = __builtin_amdgcn_readfirstlane(tid >> 6);
  const int wm = wid & 3, wn = wid >> 2;
  const rsrc_t rX = RSRC(a.x + (size_t)pm * 128 * D, 128 * D * 2);
  const rsrc_t rW = ZB ? RSRC(a.win + 64 * cb, (D * F - 64 * cb) * 2) : RSRC(a.wdi + (size_t)384 * D, 128 * D * 2);
  const int r8 = lane >> 3, p8 = lane & 7;
  const unsigned sw_e = (p8 ^ (r8 >> 1)) << 4;
  const unsigned vx_e = (32 * (wid >> 1) + 4 * (wid & 1) + 8 * (r8 >> 2) + (r8 & 3)) * (D * 2) + sw_e;
  const unsigned vx_o = (vx_e ^ 64) + 16 * (D * 2);
  const unsigned vw2 = ZB ? (8 * wid + r8) * (F * 2) + ((p8 ^ ((((r8 >> 1) & 1) | ((wid & 1) << 1)) << 1)) << 4)
                          : (64 + 8 * wid + r8) * (D * 2) + (sw_e ^ ((wid & 1) << 6));
  const unsigned vw1 = ZB ? vw2 + FH * 2 : vw2 - 64 * (D * 2);
  constexpr unsigned WKB = ZB ? 64 * F * 2 : 128;
  const unsigned mb1 = wid * 1024, mb2 = wid * 2048;
  auto piece1 = [&](int st, int p) {
    const unsigned kb = st ^ kx;
    const int base = P1_RING + (st & 3) * 32768;
    if (p == 0) dma16(rX, mb2, base, vx_e, kb * 128);
    else if (p == 1) dma16(rW, mb1, base + 16384, vw1, kb * WKB);
    else if (p == 2) dma16(rX, mb2, base + 1024, vx_o, kb * 128);
    else dma16(rW, mb1, base + 24576, vw2, kb * WKB);
  };
  piece1(0, 0); piece1(0, 1); piece1(0, 2); piece1(0, 3);
  SCHED();

  const int fr = lane & 15, fq = lane >> 4;
  const int kh = wm >> 1;
  const unsigned fsw = ((fq ^ ((fr >> 1) & 7)) << 4) ^ (kh << 6);
  unsigned xa[2][2][2], wb[2][2][2];
#pragma unroll
  for (int m = 0; m < 2; ++m) { xa[0][m][0] = P1_RING + (32 * wm + 16 * (m ^ wn) + fr) * 128 + fsw; xa[0][m][1] = xa[0][m][0] ^ 64; }
#pragma unroll
  for (int n = 0; n < 2; ++n) {
    const int tile = 2 * wn + (n ^ (wm & 1));
    if (ZB) {
      const int q4 = fr >> 2, pp = lane & 3;
      const int sp = ((q4 >> 1) & 1) | ((fq & 1) << 1);
      const int ch = (2 * tile + (pp >> 1)) ^ (sp << 1);
      wb[0][n][0] = P1_RING + 16384 + 128 * (8 * fq + q4) + 16 * ch + 8 * (pp & 1) + kh * 4096;
      wb[0][n][1] = wb[0][n][0] ^ 4096;
    } else {
      wb[0][n][0] = P1_RING + 16384 + (16 * tile + fr) * 128 + fsw;
      wb[0][n][1] = wb[0][n][0] ^ 64;
    }
  }
#pragma unroll
  for (int i = 0; i < 2; ++i)
#pragma unroll
    for (int ks = 0; ks < 2; ++ks) {
      xa[1][i][ks] = xa[0][i][ks] + 65536; wb[1][i][ks] = wb[0][i][ks] + 65536;
      OPAQUE(xa[0][i][ks]); OPAQUE(wb[0][i][ks]); OPAQUE(xa[1][i][ks]); OPAQUE(wb[1][i][ks]);
    }
  auto ldw = [&](unsigned addr) -> bf16x8 { return ZB ? ld_tr(addr) : ld_frag(addr); };
  SCHED();
  piece1(1, 0); piece1(1, 1); piece1(1, 2); piece1(1, 3);
  const rsrc_t rG = RSRC(a.ws + WS_G, G_END);
  if (wid == 7) dma4_sc1(rG, 0, 0, G_ANS + blockIdx.x * 4);
  unsigned nonce = 0;
  bf16x8 xf[16][2], wf[2][4];
  const f32x4 Z4 = {0, 0, 0, 0};
  f32x4 acc1[2][2] = {{Z4, Z4}, {Z4, Z4}}, acc2[2][2] = {{Z4, Z4}, {Z4, Z4}};
  f32x4 gram = Z4, rsum = Z4, cs1 = Z4, cs2 = Z4;
  const bf16x8 ONES = {0x3f80, 0x3f80, 0x3f80, 0x3f80, 0x3f80, 0x3f80, 0x3f80, 0x3f80};

  auto mm = [&](int kk, int i) {
    const int ks = kk & 1;
    if (i == 0) gram = MFMA16(xf[kk][0], xf[kk][0], gram);
    else if (i == 1) rsum = MFMA16(xf[kk][0], ONES, rsum);
    else if (i < 6) { const int m = (i - 2) >> 1, n = i & 1; acc1[m][n] = MFMA16(xf[kk][m], wf[ks][n], acc1[m][n]); }
    else if (i < 10) { const int m = (i - 6) >> 1, n = i & 1; acc2[m][n] = ZB ? MFMA16(wf[ks][2 + n], xf[kk][m], acc2[m][n]) : MFMA16(xf[kk][m], wf[ks][2 + n], acc2[m][n]); }
    else if (i == 10) cs1 = MFMA16(ONES, wf[0][0], cs1);
    else cs2 = MFMA16(ONES, wf[0][2], cs2);
  };
  auto rd = [&](int kk, int i) {
    const int st = kk >> 1, ks = kk & 1, wi = (st >> 1) & 1, so = (st & 1) * 32768;
    if (i == 0 || i == 3) xf[kk][i & 1] = ld_frag(xa[wi][i & 1][ks] + so);
    else if (i < 3) wf[ks][i - 1] = ldw(wb[wi][i - 1][ks] + so);
    else wf[ks][i - 2] = ldw(wb[wi][i - 4][ks] + so + 8192);
  };
  auto piece3 = [&](int p) {
    const rsrc_t rD = RSRC((cb < 8 ? a.wdr : a.wdi) + (size_t)(cb & 7) * 64 * D, 64 * D * 2);
    const unsigned ve = r8 * (D * 2) + sw_e, vo = (ve ^ 64) + 8 * (D * 2);
    dma16(rD, wid * 8192, p * 1024, (p & 1) ? vo : ve, (wid ^ kx) * 128 + (p >> 1) * 16 * (D * 2));
  };
  auto earlyP = [&](int k) {
    const rsrc_t rP = RSRC(Pp, 128 * S * 2);
    const unsigned ve = (16 * wid + r8) * (S * 2) + sw_e, vo = (ve ^ 64) + 8 * (S * 2);
    dma16(rP, mb2, P2_PB + (k + 2) * 16384, ve, (k ^ kx2) * 128);
    dma16(rP, mb2, P2_PB + (k + 2) * 16384 + 1024, vo, (k ^ kx2) * 128);
  };
#pragma unroll
  for (int st = 0; st < 8; ++st) {
    wait_vn(st <= 6 ? 4 : ZB ? 8 : 0);
    WL0();
    BAR();
    SCHED();
#pragma unroll
    for (int i = 0; i < 10; ++i) {
      if (st > 0) mm(2 * st - 1, i);
      SCHED();
      if (i < 6) rd(2 * st, i);
      if (st + 2 < 8) { if (i == 3) piece1(st + 2, 0); if (i == 8) piece1(st + 2, 1); }
      if (ZB && st == 6 && (i % 3) == 0) piece3(i / 3);
      if (st == 7 && i == 7) earlyP(0);
      SCHED();
    }
    WL0();
    SCHED();
#pragma unroll
    for (int i = 0; i < 12; ++i) {
      mm(2 * st, i);
      SCHED();
      if (i < 6) rd(2 * st + 1, i);
      if (st + 2 < 8) { if (i == 3) piece1(st + 2, 2); if (i == 9) piece1(st + 2, 3); }
      if (ZB && st == 6 && (i % 3) == 2) piece3(4 + i / 3);
      if (st == 7 && i == 8) earlyP(1);
      SCHED();
    }
    if (st == 2 && wid == 7) {
      nonce = __builtin_amdgcn_readfirstlane(*(LAS unsigned*)(size_t)(lane * 4)) + 1;
      st4_sc1(nonce, rG, 0, G_REQ + blockIdx.x * 4);
    }
    SCHED();
  }
  LAS float* const s_r = (LAS float*)(size_t)(P1_SCR);
  LAS float* const s_c = (LAS float*)(size_t)(P1_SCR + 512);
  LAS float* const s_u1 = (LAS float*)(size_t)(P1_SCR + 1024);
  LAS float* const s_u2 = (LAS float*)(size_t)(P1_SCR + 1536);
  LAS float* const s_z = (LAS float*)(size_t)(P1_SCR + 2048);
  LAS float* const s_n = (LAS float*)(size_t)(P1_SCR + 4096);
  LAS float* const s_u3 = (LAS float*)(size_t)(P1_SCR + 5120);
  const unsigned tb0 = fr * 128 + fsw, tb1 = tb0 ^ 64;
  WAIT_V(4);
  {
    const unsigned cb0 = tb0 + wid * 8192, cb1 = cb0 ^ 64;
    bf16x8 c3[8];
    f32x4 cs3[4];
    if (ZB) {
#pragma unroll
      for (int t = 0; t < 4; ++t) { c3[2 * t] = ld_frag(cb0 + t * 2048); c3[2 * t + 1] = ld_frag(cb1 + t * 2048); }
    }
    mm(15, 0); mm(15, 1);
    SCHED();
    if (ZB) {
#pragma unroll
      for (int t = 0; t < 4; ++t) cs3[t] = MFMA16(ONES, c3[2 * t], Z4);
#pragma unroll
      for (int t = 0; t < 4; ++t) cs3[t] = MFMA16(ONES, c3[2 * t + 1], cs3[t]);
    }
    SCHED();
#pragma unroll
    for (int i = 2; i < 10; ++i) mm(15, i);
    unsigned i01 = 32 * wm + 16 * wn, i23 = 64 * kh + 16 * (2 * wn + (wm & 1));
    asm volatile("" : "+s"(i01), "+s"(i23));
    const float s1 = sel4(rsum, fr & 3), s2 = sel4(gram, fr & 3);
    const float mean = s1 * (1.0f / D);
    const float rstd = __builtin_amdgcn_rsqf(fmaxf(s2 * (1.0f / D) - mean * mean, 0.0f) + 1e-5f);
    if ((fr >> 2) == fq) { s_r[i01 + fr] = rstd; s_c[i01 + fr] = -mean * rstd; }
    if (fq < 2) s_u1[128 * fq + i23 + fr] = fq ? cs2[0] : cs1[0];
    if (ZB) s_u3[lane * 8 + wid] = sel4(f32x4{cs3[0][0], cs3[1][0], cs3[2][0], cs3[3][0]}, fq);
  }
  const int s0 = pm * 128 + 32 * wm;
  const rsrc_t rT = RSRC(a.ws + WS_Q, (2 * D + FH) * S * 2);
  const unsigned vT = ((fr >> 2) & 1) * 65536 + fq * 256 + (4 * (fr >> 3) + (fr & 3)) * 16;
  WL0();
  BAR();
  if (wid == 7) dma4_sc1(rG, P1_SCR + 7168, 0, G_ANS + blockIdx.x * 4);
  earlyP(2); earlyP(3);
  f32x4 rq[2], cq[2];
  auto ld_rc = [&]() {
#pragma unroll
    for (int m = 0; m < 2; ++m) {
      rq[m] = *(LAS f32x4*)(s_r + 32 * wm + 16 * (m ^ wn) + 4 * fq);
      cq[m] = *(LAS f32x4*)(s_c + 32 * wm + 16 * (m ^ wn) + 4 * fq);
    }
  };
  ld_rc();
  auto store_t = [&](int T, f32x4 v0, f32x4 v1) {
    const unsigned a0 = cvt_pk(v0[0], v0[1]), a1 = cvt_pk(v0[2], v0[3]), b0 = cvt_pk(v1[0], v1[1]), b1 = cvt_pk(v1[2], v1[3]);
    const u32x4 o = wn ? u32x4{b0, b1, a0, a1} : u32x4{a0, a1, b0, b1};
    st16_sc1(o, rT, vT, (T >> 1) * 131072 + (T & 1) * 128 + s0 * 32);
  };
  auto lin_store = [&](f32x4 c0, f32x4 c1, float u, int T) { store_t(T, rq[0] * c0 + cq[0] * u, rq[1] * c1 + cq[1] * u); };
  float u1[2];
#pragma unroll
  for (int n = 0; n < 2; ++n) { const int ui = 16 * (2 * wn + (n ^ (wm & 1))) + fr; u1[n] = s_u1[ui] + s_u1[64 + ui]; }
  if (!ZB) {
#pragma unroll
    for (int n = 0; n < 2; ++n) {
      const int tile = 2 * wn + (n ^ (wm & 1)), ui = 16 * tile + fr;
      lin_store(acc1[0][n], acc1[1][n], u1[n], 56 + tile);
      lin_store(acc2[0][n], acc2[1][n], s_u2[ui] + s_u2[64 + ui], 60 + tile);
    }
    if (wid == 7) { WAIT_V(0); wait_go(rG, nonce, __builtin_amdgcn_readfirstlane(*(LAS unsigned*)(size_t)(P1_SCR + 7168 + lane * 4))); }
    return;
  }
  {
    f32x4 ps[2], pq[2];
#pragma unroll
    for (int n = 0; n < 2; ++n)
#pragma unroll
      for (int m = 0; m < 2; ++m) {
        f32x4 v = rq[m] * acc1[m][n] + cq[m] * u1[n];
        const f32x2 g0 = gelu2(f32x2{v[0], v[1]}), g1 = gelu2(f32x2{v[2], v[3]});
        v = f32x4{g0[0], g0[1], g1[0], g1[1]};
        acc1[m][n] = v;
        if (n == 0) { ps[m] = v; pq[m] = v * v; } else { ps[m] += v; pq[m] += v * v; }
      }
    float t[8], h[4];
#define DPP2(d, x, y, ctl, m0, m1) "v_add_f32_dpp " d ", " x ", " x " " ctl " row_mask:0xf bank_mask:" m0 "\n\tv_add_f32_dpp " d ", " y ", " y " " ctl " row_mask:0xf bank_mask:" m1 "\n\t"
#define DPPQ(d, ctl) "v_add_f32_dpp " d ", " d ", " d " " ctl " row_mask:0xf bank_mask:0xf\n\t"
    asm volatile("s_nop 1\n\t" DPP2("%0", "%8", "%16", "row_ror:8", "0x3", "0xc") DPP2("%1", "%9", "%17", "row_ror:8", "0x3", "0xc")
                 DPP2("%2", "%10", "%18", "row_ror:8", "0x3", "0xc") DPP2("%3", "%11", "%19", "row_ror:8", "0x3", "0xc")
                 DPP2("%4", "%12", "%20", "row_ror:8", "0x3", "0xc") DPP2("%5", "%13", "%21", "row_ror:8", "0x3", "0xc")
                 DPP2("%6", "%14", "%22", "row_ror:8", "0x3", "0xc") DPP2("%7", "%15", "%23", "row_ror:8", "0x3", "0xc") "s_nop 1"
                 : "=&v"(t[0]), "=&v"(t[1]), "=&v"(t[2]), "=&v"(t[3]), "=&v"(t[4]), "=&v"(t[5]), "=&v"(t[6]), "=&v"(t[7])
                 : "v"(ps[0][0]), "v"(pq[0][0]), "v"(ps[0][1]), "v"(pq[0][1]), "v"(ps[0][2]), "v"(pq[0][2]), "v"(ps[0][3]), "v"(pq[0][3]),
                   "v"(ps[1][0]), "v"(pq[1][0]), "v"(ps[1][1]), "v"(pq[1][1]), "v"(ps[1][2]), "v"(pq[1][2]), "v"(ps[1][3]), "v"(pq[1][3]));
    asm volatile("s_nop 1\n\t" DPP2("%0", "%4", "%8", "row_half_mirror", "0x5", "0xa") DPP2("%1", "%5", "%9", "row_half_mirror", "0x5", "0xa")
                 DPP2("%2", "%6", "%10", "row_half_mirror", "0x5", "0xa") DPP2("%3", "%7", "%11", "row_half_mirror", "0x5", "0xa") "s_nop 1\n\t"
                 DPPQ("%0", "quad_perm:[1,0,3,2]") DPPQ("%1", "quad_perm:[1,0,3,2]") DPPQ("%2", "quad_perm:[1,0,3,2]") DPPQ("%3", "quad_perm:[1,0,3,2]")
                 DPPQ("%0", "quad_perm:[2,3,0,1]") DPPQ("%1", "quad_perm:[2,3,0,1]") DPPQ("%2", "quad_perm:[2,3,0,1]") DPPQ("%3", "quad_perm:[2,3,0,1]") "s_nop 0"
                 : "=&v"(h[0]), "=&v"(h[1]), "=&v"(h[2]), "=&v"(h[3])
                 : "v"(t[0]), "v"(t[1]), "v"(t[2]), "v"(t[3]), "v"(t[4]), "v"(t[5]), "v"(t[6]), "v"(t[7]));
    if ((fr & 3) == 0) *(LAS f32x4*)(s_z + (wn * 128 + 32 * wm + 8 * fq + 4 * ((fr >> 3) ^ wn) + 2 * ((fr >> 2) & 1)) * 2) = f32x4{h[0], h[1], h[2], h[3]};
  }
  WL0();
  BAR();
  if (wid == 7) {
    __builtin_amdgcn_s_setprio(2);
    const f32x4 p = *(LAS f32x4*)(s_z + lane * 4) + *(LAS f32x4*)(s_z + 256 + lane * 4);
    WAIT_V(4);
    wait_go(rG, nonce, __builtin_amdgcn_readfirstlane(*(LAS unsigned*)(size_t)(P1_SCR + 7168 + lane * 4)));
    st16_sc1(__builtin_bit_cast(u32x4, p) | 1u, rG, lane * 16, G_PAY + (pm * 14 + cb) * 1024);
  }
  {
    float u3[2];
    f32x4 acct[2][2] = {{Z4, Z4}, {Z4, Z4}};
    const unsigned t30 = tb0 + wn * 4096, t31 = tb1 + wn * 4096;
    bf16x8 w[8];
    auto rd3 = [&](int f) { const int kk = f >> 1; w[f & 7] = ld_frag(((kk & 1) ? t31 : t30) + (kk >> 1) * 8192 + (f & 1) * 2048); };
#pragma unroll
    for (int f = 0; f < 4; ++f) rd3(f);
    SCHED();
#pragma unroll
    for (int f = 0; f < 32; ++f) {
      const int kk = f >> 1, n = f & 1;
      acct[0][n] = MFMA16(xf[kk][0], w[f & 7], acct[0][n]);
      SCHED();
      if (f + 4 < 32) rd3(f + 4);
      if (f == 8 || f == 12) {
        const int m = (f - 8) >> 2;
        const float r = s_r[32 * wm + 16 * (m ^ wn) + fr], c = s_c[32 * wm + 16 * (m ^ wn) + fr];
#pragma unroll
        for (int q = 0; q < 2; ++q) {
          const int ui = 16 * (2 * wn + (q ^ (wm & 1))) + 4 * fq;
          z1v[m][q] = acc2[m][q] * r + (*(LAS f32x4*)(s_u2 + ui) + *(LAS f32x4*)(s_u2 + 64 + ui)) * c;
        }
      }
      if (f == 20 || f == 24) {
        const int q = (f - 20) >> 2;
        const f32x4 uu = *(LAS f32x4*)(s_u3 + (32 * wn + 16 * q + fr) * 8) + *(LAS f32x4*)(s_u3 + (32 * wn + 16 * q + fr) * 8 + 4);
        u3[q] = (uu[0] + uu[1]) + (uu[2] + uu[3]);
      }
      SCHED();
      acct[1][n] = MFMA16(xf[kk][1], w[f & 7], acct[1][n]);
      SCHED();
    }
    if (wid != 7) {
#pragma unroll
      for (int n = 0; n < 2; ++n) lin_store(acct[0][n], acct[1][n], u3[n], 4 * cb + 2 * wn + n);
    }
  if (wid == 7) {
    f32x4 t;
    for (unsigned spin = 0; spin < SPIN; ++spin) {
      u32x4 pv[14];
#pragma unroll
      for (int i = 0; i < 14; ++i) pv[i] = __builtin_amdgcn_raw_buffer_load_b128(rG, lane * 16 + (i & 3) * 1024, G_PAY + pm * 14336 + (i >> 2) * 4096, 16);
      if (spin == 0) {
#pragma unroll
        for (int n = 0; n < 2; ++n) lin_store(acct[0][n], acct[1][n], u3[n], 4 * cb + 2 * wn + n);
      }
      u32x4 al = pv[0];
      t = __builtin_bit_cast(f32x4, pv[0]);
#pragma unroll
      for (int i = 1; i < 14; ++i) { al &= pv[i]; t += __builtin_bit_cast(f32x4, pv[i]); }
      if (__all((al[0] & al[1] & al[2] & al[3] & 1u) != 0)) break;
      asm volatile("" ::: "memory");
    }
    const float m0 = t[0] * (1.0f / FH), m1 = t[2] * (1.0f / FH);
    const float r0 = __builtin_amdgcn_rsqf(fmaxf(t[1] * (1.0f / FH) - m0 * m0, 0.0f) + 1e-5f);
    const float r1 = __builtin_amdgcn_rsqf(fmaxf(t[3] * (1.0f / FH) - m1 * m1, 0.0f) + 1e-5f);
    *(LAS f32x2*)(s_n + 2 * lane) = f32x2{r0, r1};
    *(LAS f32x2*)(s_n + 128 + 2 * lane) = f32x2{-m0 * r0, -m1 * r1};
    __builtin_amdgcn_s_setprio(0);
  }
  }
  WL0();
  BAR();
  {
    f32x4 r2[2], c2[2];
#pragma unroll
    for (int m = 0; m < 2; ++m) {
      r2[m] = *(LAS f32x4*)(s_n + 32 * wm + 8 * fq + 4 * (m ^ wn));
      c2[m] = *(LAS f32x4*)(s_n + 128 + 32 * wm + 8 * fq + 4 * (m ^ wn));
    }
#pragma unroll
    for (int n = 0; n < 2; ++n) store_t(64 + 4 * cb + 2 * wn + (n ^ (wm & 1)), acc1[0][n] * r2[0] + c2[0], acc1[1][n] * r2[1] + c2[1]);
  }
}

__device__ __forceinline__ void warm_window(const Args& a, int junk, int rb, int w, int wv, int lane) {
  if (w < 8) {
    const unsigned vo = (rb * 128 + wv * 32 + (lane >> 2)) * (S * 2) + (lane & 3) * 128;
#pragma unroll
    for (int m = 0; m < 3; ++m) {
      const rsrc_t r = RSRC(m == 0 ? a.wsr : m == 1 ? a.wsi : a.wsp, S * S * 2);
      touch(r, junk, vo, w * 512);
      touch(r, junk, vo, w * 512 + 16 * (S * 2));
    }
  } else {
    const rsrc_t r = RSRC(a.wout + (size_t)rb * 56 * D, 56 * D * 2);
    touch(r, junk, lane * 128, wv * 16384);
    touch(r, junk, lane * 128, wv * 16384 + 8192);
  }
}

__global__ __launch_bounds__(512, 2) void fused(Args a) {
  extern __shared__ __attribute__((aligned(1024))) char smem[];
  asm volatile("" ::"v"(smem));
  const int bx = blockIdx.x & 7, bi = blockIdx.x >> 3;
  if (bi < CODEWARM && threadIdx.x < 64) {
    unsigned long long pc;
    asm volatile("s_getpc_b64 %0" : "=s"(pc));
    touch(RSRC(pc & ~127ull, CODEWARM * 8192), 1024, threadIdx.x * 128, bi * 8192);
  }
  const int pm = 4 * (bx >> 1) + (bi >> 3), cb = 2 * (bi & 7) + (bx & 1);
  int mat, tm, tn;
  if (bx & 1) { mat = 2; tm = pm; tn = bi & 7; }
  else { mat = bx >> 2; tm = ((bx >> 1) & 1) * 8 + (bi >> 2); tn = bi & 3; }
  const int kx = (tm ^ tn) & K2_KXMASK;
  const u16* Pp = (mat == 0 ? a.wsr : mat == 1 ? a.wsi : a.wsp) + (size_t)tm * 128 * S;
  f32x4 z1v[2][2];
  {
    const int kx1 = ((bi & 7) ^ (2 * (bi >> 3))) & K1_KXMASK;
    if (__builtin_expect(cb < 14, 1)) phase1<true>(a, pm, cb, kx1, Pp, kx, z1v);
    else if (cb == 14) phase1<false>(a, pm, cb, kx1, Pp, kx, z1v);
  }
  int tid = threadIdx.x;
  asm volatile("" : "+v"(tid));
  const int lane = tid & 63;
  const int wid = __builtin_amdgcn_readfirstlane(tid >> 6);
  const int tm3 = bx * 4 + (bi >> 3), tn3 = bi & 7;
  const int fr = lane & 15, fq = lane >> 4;
  const unsigned vl = lane * 16;
  const rsrc_t rG = RSRC(a.ws + WS_G, G_END);
  const bool work2 = cb != 15;
  auto z1_finish = [&]() {
    if (cb < 14) {
      const int wm = wid & 3, wn = wid >> 2;
      const rsrc_t rZ = RSRC(a.ws + WS_Z1, FH * S * 2);
      const unsigned vZ = (fr >> 3) * (112 * 256) + (2 * ((fq & 1) ^ (wm & 1)) + (fq >> 1)) * 256 + (fr & 7) * 16;
      const unsigned sZ = ((pm * 8 + 2 * wm) * 112 + 8 * cb + 4 * wn) * 256;
#pragma unroll
      for (int m = 0; m < 2; ++m) {
        unsigned pk[2][2];
#pragma unroll
        for (int n = 0; n < 2; ++n) {
          const f32x2 g0 = gelu2(f32x2{z1v[m][n][0], z1v[m][n][1]}), g1 = gelu2(f32x2{z1v[m][n][2], z1v[m][n][3]});
          pk[n][0] = cvt_pk(g0[0], g0[1]); pk[n][1] = cvt_pk(g1[0], g1[1]);
        }
        auto w0 = __builtin_amdgcn_permlane16_swap(pk[0][0], pk[1][0], false, false);
        auto w1 = __builtin_amdgcn_permlane16_swap(pk[0][1], pk[1][1], false, false);
        st16_sc1(u32x4{w0[0], w1[0], w0[1], w1[1]}, rZ, vZ, sZ + 128 * (m ^ wn));
      }
    }
  };
  if (blockIdx.x == 255) {
    for (int i = 0; i < 36; ++i) st16_sc1(u32x4{0, 0, 0, 0}, rG, vl, (wid * 36 + i) * 1024);
    st16_sc1(u32x4{0, 0, 0, 0}, rG, vl, G_ALL);
  }
  unsigned bv[4];
#pragma unroll
  for (int ni = 0; ni < 4; ++ni) bv[ni] = a.bsp[(8 * tm + 4 * ((wid >> 1) & 1) + ni) * 16 + fr];
  asm volatile("" : "+v"(bv[0]), "+v"(bv[1]), "+v"(bv[2]), "+v"(bv[3]));
  if (work2 || blockIdx.x == 255) {
    WAIT_V(0);
    WL0();
    BAR();
  }
  if (wid >= 4) {
    const int lw = wid - 4;
    const int r8 = lane >> 3, p = lane & 7;
    if (wid == 7) {
      if (!work2) {
        if (blockIdx.x == 255) {
          for (unsigned spin = 0; spin < SPIN; ++spin) {
            const u32x4 rq = ld16_sc1(rG, vl, G_REQ);
            st16_sc1(rq, rG, vl, G_ANS);
            const u32x4 fl = ld16_sc1(rG, vl, G_ALL);
            if (__all((fl[0] & fl[1] & fl[2] & (lane == 63 ? 1u : fl[3])) == 1u)) break;
          }
        } else {
          const unsigned nonce = __builtin_amdgcn_readfirstlane(ld4_sc1(rG, 0, G_ANS + blockIdx.x * 4)) + 1;
          st4_sc1(nonce, rG, 0, G_REQ + blockIdx.x * 4);
          wait_go(rG, nonce, nonce - 1);
        }
      }
      const unsigned box = (fq & 1) * 128 + (cb >> 1) * 16 + fr;
      st4_sc1(1u, rG, fq < 2 ? box * 128 + (pm * 2 + (cb & 1)) * 4 : G_ALL + blockIdx.x * 4, 0);
    }
    const rsrc_t rP = RSRC(Pp, 128 * S * 2);
    const unsigned vo_e = (lw * 32 + r8) * (S * 2) + ((p ^ (r8 >> 1)) << 4);
    const unsigned vo_o = (vo_e ^ 64) + 8 * (S * 2);
    auto stageP = [&](int kt, unsigned poff) {
      const unsigned mb = poff + lw * 4096, so = (kt ^ kx) * 128;
#pragma unroll
      for (int j = 0; j < 4; ++j) dma16(rP, mb, P2_PB + j * 1024, (j & 1) ? vo_o : vo_e, so + (j >> 1) * 16 * (S * 2));
    };
    if (work2) {
      BAR();
      stageP(4, 0); stageP(5, 16384);
    }
    const rsrc_t rWo = RSRC(a.wout + 64 * tn3, (FH * D - 64 * tn3) * 2);
    const unsigned vw_e = (32 * lw + r8) * (D * 2) + ((p ^ (((r8 >> 1) & 1) << 1)) << 4), vw_o = (vw_e ^ 64) + 8 * (D * 2);
    u32x4 wreg[12];
#pragma unroll
    for (int i = 0; i < 12; ++i) wreg[i] = u32x4{0, 0, 0, 0};
    auto wput = [&](int rg) {
#pragma unroll
      for (int j = 0; j < 4; ++j) *(LAS u32x4*)(size_t)(rg * 16384 + lw * 4096 + j * 1024 + vl) = wreg[(rg == 6 ? 8 : (rg - 2) * 4) + j];
    };
    auto wdma = [&](int rg) {
#pragma unroll
      for (int j = 0; j < 4; ++j) dma16(rWo, lw * 4096, rg * 16384 + j * 1024, (j & 1) ? vw_o : vw_e, (rg * 128 + (j >> 1) * 16) * (D * 2));
    };
    if (work2) {
#pragma unroll
      for (int i = 0; i < 12; ++i) {
        const int rg = i < 8 ? 2 + (i >> 2) : 6;
        ld16_async(wreg[i], rWo, (i & 1) ? vw_o : vw_e, (rg * 128 + ((i >> 1) & 1) * 16) * (D * 2));
      }
      z1_finish();
      WAIT_V(0);
      if (wid == 7) {
        const unsigned box = (mat * 4 + tn) * 16 + tm;
        for (unsigned spin = 0; spin < SPIN; ++spin) {
          const unsigned fl = ld4_sc1(rG, (lane & 31) * 4, G_F1 + box * 128);
          if (__all(fl == 1u || (box >= 112 && box < 128 && (lane & 1)))) break;
        }
      }
      BAR();
      const rsrc_t rQ = RSRC(a.ws + WS_Q + (size_t)(mat * 4 + tn) * 128 * S * 2, 128 * S * 2);
      const unsigned vq = vl + lw * 131072;
      auto stageQ = [&](int kt) {
        const unsigned mb = (kt & 3) * 16384 + lw * 4096, so = (kt ^ kx) * 2048;
#pragma unroll
        for (int j = 0; j < 4; ++j) dma16_sc1(rQ, mb, j * 1024, vq, so + (j >> 1) * 65536 + (j & 1) * 1024);
      };
      stageQ(0); stageQ(1); stageQ(2); stageQ(3);
      WAIT_V(12); BAR();
      WAIT_V(8); BAR(); stageQ(4); stageP(6, 2 * 16384);
      WAIT_V(12); BAR(); stageQ(5); stageP(7, 3 * 16384);
      WAIT_V(16); BAR(); stageQ(6); stageP(8, 4 * 16384);
      unsigned poff = 5 * 16384;
      unsigned zf;
      for (int kt = 3; kt < 23; ++kt) {
        WAIT_V(20);
        BAR();
        if (kt == 3 && wid == 7 && cb < 14) st4_sc1(1u, rG, 0, G_FZ + (pm * 8 + (cb >> 1)) * 128 + (cb & 1) * 4);
        stageQ(kt + 4); stageP(kt + 6, poff);
        poff = poff == 5 * 16384 ? 0 : poff + 16384;
      }
      asm volatile("buffer_load_dword %0, %1, %2, %3 offen sc1" : "=v"(zf) : "v"((lane & 1) * 4), "s"(rG), "s"(G_FZ + (tm * 8 + tn) * 128) : "memory");
#pragma unroll
      for (int kt = 23; kt < 26; ++kt) {
        WAIT_V(20);
        BAR();
        stageQ(kt + 4); stageP(kt + 6, ((kt + 2) % 6) * 16384);
      }
      asm volatile("s_waitcnt vmcnt(20)" : "+v"(zf)::"memory");
      if (mat == 2) {
        const rsrc_t rZ = RSRC(a.ws + WS_Z1 + (size_t)((8 * tm + lw) * 112 + 16 * tn) * 256, 4 * 112 * 256 + 4096);
        auto stageZ = [&](int h) {
#pragma unroll
          for (int j = 0; j < 4; ++j) dma16_sc1(rZ, lw * 4096, P2_Z + h * 16384 + j * 1024, vl, h * (4 * 112 * 256) + j * 1024);
        };
        for (unsigned spin = 0; !__all(zf == 1u) && spin < SPIN; ++spin) zf = ld4_sc1(rG, (lane & 1) * 4, G_FZ + (tm * 8 + tn) * 128);
        WAIT_V(20); BAR(); stageQ(30); stageZ(0);
        WAIT_V(20); BAR(); stageQ(31); stageZ(1);
        WAIT_V(20); BAR(); wdma(0); wdma(4);
        WAIT_V(20); BAR(); wdma(1); wdma(5);
        WAIT_V(20); BAR(); wput(2); wput(6);
        WAIT_V(16); BAR(); wput(3);
      } else {
        WAIT_V(20); BAR(); stageQ(30);
        WAIT_V(16); BAR(); stageQ(31);
        WAIT_V(12); BAR(); wdma(0); wdma(4);
        WAIT_V(12); BAR(); wdma(1); wdma(5);
        WAIT_V(16); BAR(); wput(2); wput(6);
        BAR(); wput(3);
      }
    } else {
      wdma(0); wdma(4); wdma(1); wdma(5); wdma(2); wdma(3); wdma(6);
    }
    WAIT_V(0);
  } else if (work2) {
    const int wp = wid >> 1, wq = wid & 1;
    f32x4 acc[4][4];
#pragma unroll
    for (int ni = 0; ni < 4; ++ni) {
      const float bias = mat == 2 ? __uint_as_float(bv[ni] << 16) : 0.0f;
#pragma unroll
      for (int n = 0; n < 4; ++n) acc[ni][n] = f32x4{bias, bias, bias, bias};
    }
    BAR();
    const int sw = (fr >> 1) & 7;
    const unsigned ra = P2_PB + (wp * 64 + fr) * 128 + ((fq ^ sw) << 4);
    const unsigned rb = wq * 8192 + vl;
    bf16x8 a0[4], b0[4], a1[4], b1[4];
    unsigned poff = 2 * 16384;
    unsigned pa0 = ra + poff, pa1 = pa0 ^ 64;
#define K2_RD(AF, BF, pa, sl, ks, i) do { \
    if ((i) == 0) AF[0] = ld_frag(pa); \
    else if ((i) <= 4) BF[(i) - 1] = ld_frag(rb + (sl) * 16384 + ((i) - 1) * 2048 + (ks) * 1024); \
    else AF[(i) - 4] = ld_frag(pa + ((i) - 4) * 2048); } while (0)
#define K2_NI(i) ((((i) >> 2) & 1) ? 3 - ((i) & 3) : ((i) & 3))
#define K2_MM(AF, BF, i) acc[(i) >> 2][K2_NI(i)] = MFMA16(BF[K2_NI(i)], AF[(i) >> 2], acc[(i) >> 2][K2_NI(i)])
#define K2_STEP(s, LAST) do { \
    SCHED(); WL0(); SCHED(); \
    _Pragma("unroll") for (int i = 0; i < 16; ++i) { K2_MM(a0, b0, i); SCHED(); if (i < 8) K2_RD(a1, b1, pa1, s, 1, i); \
      if (i == 10) { poff = poff == 5 * 16384 ? 0 : poff + 16384; pa0 = ra + poff; } SCHED(); } \
    WL0(); \
    if ((s) == 3) WAIT_V(0); \
    BAR(); \
    SCHED(); \
    _Pragma("unroll") for (int i = 0; i < 16; ++i) { K2_MM(a1, b1, i); SCHED(); if (!(LAST) && i < 8) K2_RD(a0, b0, pa0, ((s) + 1) & 3, 0, i); \
      if (i == 10) pa1 = pa0 ^ 64; SCHED(); } \
  } while (0)
    BAR();
    z1_finish();
    BAR();
#pragma unroll
    for (int i = 0; i < 8; ++i) K2_RD(a0, b0, pa0, 0, 0, i);
    for (int kt = 0; kt < 28; kt += 4) { K2_STEP(0, 0); K2_STEP(1, 0); K2_STEP(2, 0); K2_STEP(3, 0); }
    K2_STEP(0, 0); K2_STEP(1, 0); K2_STEP(2, 0); K2_STEP(3, 1);
    SCHED();
    if (mat < 2) {
      const rsrc_t rY = RSRC(a.ws + WS_Y + (size_t)mat * S * D * 2, S * D * 2);
      const unsigned so = ((8 * tm + 4 * wp) * 64 + 16 * tn + 8 * wq) * 256;
#pragma unroll
      for (int ni = 0; ni < 4; ++ni)
#pragma unroll
        for (int P = 0; P < 2; ++P) {
          const f32x4 c0 = acc[ni][2 * P], c1 = acc[ni][2 * P + 1];
          const u32x4 v = {cvt_pk(c0[0], c0[1]), cvt_pk(c0[2], c0[3]), cvt_pk(c1[0], c1[1]), cvt_pk(c1[2], c1[3])};
          st16_sc1(v, rY, vl, so + (ni * 64 + 4 * P) * 256);
        }
    } else {
      const rsrc_t rH = RSRC(a.ws + WS_H, S * FH * 2);
      const unsigned so = ((8 * tm + 4 * wp) * 112 + 16 * tn + 8 * wq) * 256;
      const unsigned za = P2_Z + wp * 16384 + wq * 2048 + (fq * 16 + ((fr & 4) << 1) + ((fr & 8) >> 1) + (fr & 3)) * 16;
#pragma unroll
      for (int ni = 0; ni < 4; ++ni) {
#pragma unroll
        for (int P = 0; P < 2; ++P) {
          const f32x4 c0 = acc[ni][2 * P], c1 = acc[ni][2 * P + 1];
          const u32x4 z = *(const LAS u32x4*)(size_t)(za + ni * 4096 + P * 1024);
          const u32x4 v = {cvt_pk(bf_lo(z[0]) * c0[0], bf_hi(z[0]) * c0[1]), cvt_pk(bf_lo(z[1]) * c0[2], bf_hi(z[1]) * c0[3]),
                           cvt_pk(bf_lo(z[2]) * c1[0], bf_hi(z[2]) * c1[1]), cvt_pk(bf_lo(z[3]) * c1[2], bf_hi(z[3]) * c1[3])};
          st16_sc1(v, rH, vl, so + (ni * 112 + 4 * P) * 256);
        }
      }
    }
    WAIT_V(0);
  } else {
    for (int i = 0; i < PF_N && blockIdx.x != 255; ++i) {
      warm_window(a, P2_Z, pm, (PF_LIST >> (4 * i)) & 15, wid, lane);
      WAIT_V(6);
    }
    WAIT_V(0);
  }
  WL0();
  BAR();
  {
    const int wr = (wid + tn3) & 7, tp = wr >> 2, kq = wr & 3;
    const int tt3 = 4 * tm3 + 2 * tp + (kq & 1);
    const int cq = 2 * (fq & 1) + (fq >> 1);
    const int c3 = 8 * tn3 + 2 * (kq & 2);
    if (wid == 4 && work2) {
      const unsigned box = mat == 2 ? fr : (fr & 8) + 2 * tn + (fr & 1);
      st4_sc1(1u, rG, box * 128, G_FP + tm * 2048 + (mat == 2 ? tn : 8 + mat) * 4);
    }
    const int q4 = fr >> 2, pp = lane & 3;
    const int sp = ((q4 >> 1) & 1) | ((fq & 1) << 1);
    const unsigned wa = kq * 4096 + 128 * (8 * fq + q4) + 16 * ((2 * (kq & 2) + (pp >> 1)) ^ (sp << 1)) + 8 * (pp & 1);
    bf16x8 wf[7][4];
    auto ld_w = [&]() {
#pragma unroll
      for (int mi = 0; mi < 4; ++mi) {
        unsigned lo = wa ^ (mi * 32), hi = lo + 65536;
        OPAQUE(hi);
#pragma unroll
        for (int g = 0; g < 7; ++g) wf[g][mi] = ld_tr((g < 4 ? lo : hi) + (g & 3) * 16384);
      }
    };
    const rsrc_t rY = RSRC(a.ws + WS_Y + (size_t)(tt3 * 64 + c3) * 256, S * D * 2 + 1024);
    const unsigned vy = cq * 256 + fr * 16;
    unsigned fl;
    asm volatile("buffer_load_dword %0, %1, %2, %3 offen sc1" : "=v"(fl) : "v"(fr * 4), "s"(rG), "s"(G_FP + (tm3 * 8 + tn3) * 128) : "memory");
    const u32x4 ex = *(const u32x4*)(a.x + (size_t)(tt3 * 16 + fr) * D + (c3 + cq) * 8);
    ld_w();
    asm volatile("s_waitcnt vmcnt(1)" : "+v"(fl)::"memory");
    u32x4 e1, e2;
    const bool gy = __all(fl == 1u || fr < 8 || fr > 9);
    if (gy) { e1 = __builtin_amdgcn_raw_buffer_load_b128(rY, vy, 0, 16); e2 = __builtin_amdgcn_raw_buffer_load_b128(rY, vy, S * D * 2, 16); }
    if (wid == 4) {
      for (unsigned spin = 0; !__all(fl == 1u || fr == 7 || fr > 9) && spin < SPIN; ++spin) fl = ld4_sc1(rG, fr * 4, G_FP + (tm3 * 8 + tn3) * 128);
    }
    BAR();
    const rsrc_t rH = RSRC(a.ws + WS_H + (size_t)((4 * tm3 + 2 * tp) * 112 + 4 * kq) * 256, 2 * 112 * 256);
    const unsigned vh0 = vl + (kq & 1) * (112 * 256), vh1 = vl + ((kq & 1) ^ 1) * (112 * 256);
    if (!gy) { e1 = __builtin_amdgcn_raw_buffer_load_b128(rY, vy, 0, 16); e2 = __builtin_amdgcn_raw_buffer_load_b128(rY, vy, S * D * 2, 16); }
    u32x4 hf[7][2];
#pragma unroll
    for (int g = 0; g < 7; ++g) {
      hf[g][0] = __builtin_amdgcn_raw_buffer_load_b128(rH, vh0, g * 4096, 16);
      hf[g][1] = __builtin_amdgcn_raw_buffer_load_b128(rH, vh1, g * 4096, 16);
      SCHED();
    }
    f32x4 acc[2][4];
#pragma unroll
    for (int g = 0; g < 7; ++g)
#pragma unroll
      for (int tl = 0; tl < 2; ++tl)
#pragma unroll
        for (int mi = 0; mi < 4; ++mi) acc[tl][mi] = MFMA16(wf[g][mi], __builtin_bit_cast(bf16x8, hf[g][tl]), (g ? acc[tl][mi] : f32x4{0, 0, 0, 0}));
    LAS f32x4* xb = (LAS f32x4*)(size_t)(P3_X) + lane;
#pragma unroll
    for (int d = 1; d < 4; ++d) {
      xb[((wr * 3 + d - 1) * 2) * 64] = acc[d & 1][d & 2];
      xb[((wr * 3 + d - 1) * 2 + 1) * 64] = acc[d & 1][(d & 2) + 1];
    }
    WL0();
    BAR();
    f32x4 s0 = acc[0][0], s1 = acc[0][1];
#pragma unroll
    for (int d = 1; d < 4; ++d) {
      s0 += xb[(((wr ^ d) * 3 + d - 1) * 2) * 64];
      s1 += xb[(((wr ^ d) * 3 + d - 1) * 2 + 1) * 64];
    }
    f32x4 c0, c1;
#pragma unroll
    for (int j = 0; j < 4; ++j) {
      auto w = __builtin_amdgcn_permlane16_swap(__float_as_uint(s0[j]), __float_as_uint(s1[j]), false, false);
      c0[j] = __uint_as_float(w[0]); c1[j] = __uint_as_float(w[1]);
    }
    u32x4 o;
#pragma unroll
    for (int k = 0; k < 4; ++k) {
      const f32x4 c = (k >> 1) ? c1 : c0;
      const float lo = bf_lo(ex[k]) + bf_lo(e1[k]) - bf_lo(e2[k]) + c[(k & 1) * 2];
      const float hi = bf_hi(ex[k]) + bf_hi(e1[k]) - bf_hi(e2[k]) + c[(k & 1) * 2 + 1];
      o[k] = cvt_pk(lo, hi);
    }
    st16_sc1(o, RSRC(a.out, S * D * 2), ((tt3 * 16 + fr) * D + (c3 + cq) * 8) * 2, 0);
  }
}

extern "C" void kernel_launch(void* const* d_in, const int* in_sizes, int n_in, void* d_out, int out_size, void* d_ws, size_t ws_size,
                              hipStream_t stream) {
  (void)in_sizes; (void)n_in; (void)out_size; (void)ws_size;
  (void)hipFuncSetAttribute((const void*)fused, hipFuncAttributeMaxDynamicSharedMemorySize, LDS_BYTES);
  Args a{(const u16*)d_in[0], (const u16*)d_in[3], (const u16*)d_in[4], (const u16*)d_in[6], (const u16*)d_in[9],
         (const u16*)d_in[1], (const u16*)d_in[2], (const u16*)d_in[7], (const u16*)d_in[8], (char*)d_ws, (u16*)d_out};
  fused<<<256, 512, LDS_BYTES, stream>>>(a);
}
```
